```python
import torch

def _softmax(x, axis=-1):
    return torch.softmax(x, dim=axis)
def _gelu(x):
    return torch.nn.functional.gelu(x, approximate="tanh")
def _silu(x):
    return torch.nn.functional.silu(x)
def _sigmoid(x):
    return torch.sigmoid(x)
def _layernorm(x, axis=-1, eps=1e-5):
    m = x.mean(dim=axis, keepdim=True)
    v = x.var(dim=axis, keepdim=True, unbiased=False)
    return (x - m) / torch.sqrt(v + eps)
def _scatter_add(tgt, idx, src, axis=0):
    return tgt.clone().index_add_(axis, idx, src)
def _segment_sum(src, seg, n):
    out = torch.zeros((n,) + src.shape[1:], dtype=src.dtype)
    return out.index_add_(0, seg, src)
def _pad(x, pw):
    flat = tuple(v for pair in reversed(pw) for v in pair)
    return torch.nn.functional.pad(x, flat)

kernel_name = 'recurrent_scan__sliding_block_cached__glu_gated_bilinear__prompt'

def setup_inputs():

    _w = (lambda a: (lambda t: t.to(torch.bfloat16) if t.is_floating_point() else t)(torch.as_tensor(a)))
    def _A_setup():
        B, Sq, D = 1, 2048, 1024
        Hq, Hkv, Sk, Dh = 32, 4, 2048, 128
        assert Hq % Hkv == 0, (Hq, Hkv)
        return {
            "x": _w((lambda *s: torch.randn(*s, dtype=torch.bfloat16) * 0.05)(B, Sq, D)),
            "k_cache": _w((lambda *s: torch.randn(*s, dtype=torch.bfloat16) * 1.6)(B, Hkv, Sk, Dh)),
            "v_cache": _w((lambda *s: torch.randn(*s, dtype=torch.bfloat16) * 1.6)(B, Hkv, Sk, Dh)),
            "wq": _w((lambda *s: torch.randn(*s, dtype=torch.bfloat16) * 1.6)(D, Hq * Dh) * (D ** -0.5)),
            "wo": _w((lambda *s: torch.randn(*s, dtype=torch.bfloat16) * 1.6)(Hq * Dh, D) * ((Hq * Dh) ** -0.5)),
        }
    _A_in = _A_setup()

    def _B_setup():
        B, S, D, F = 1, 2048, 1024, 3584
        return {
            "x": _w((lambda *s: torch.randn(*s, dtype=torch.bfloat16) * 0.05)(B, S, D)),
            "w_gate": _w((lambda *s: torch.randn(*s, dtype=torch.bfloat16) * 0.2)(D, F) * (D ** -0.5)),
            "w_up": _w((lambda *s: torch.randn(*s, dtype=torch.bfloat16) * 0.2)(D, F) * (D ** -0.5)),
            "w_down": _w((lambda *s: torch.randn(*s, dtype=torch.bfloat16) * 0.2)(F, D) * (F ** -0.5)),
        }
    _B_in = _B_setup()

    return {
        "A_x": _A_in["x"],
        "A_k_cache": _A_in["k_cache"],
        "A_v_cache": _A_in["v_cache"],
        "A_wq": _A_in["wq"],
        "A_wo": _A_in["wo"],
        "B_x": _B_in["x"],
        "B_w_gate": _B_in["w_gate"],
        "B_w_up": _B_in["w_up"],
        "B_w_down": _B_in["w_down"],
    }

def reference(A_x, A_k_cache, A_v_cache, A_wq, A_wo, B_x, B_w_gate, B_w_up, B_w_down):
    def _A_ref(x, k_cache, v_cache, wq, wo):
        B, Sq, D = x.shape
        Hq, Dh = 32, 128
        Hkv, Sk = k_cache.shape[1], k_cache.shape[2]
        W = min(4096, max(256, Sk // 4))
        rep = Hq // Hkv
        q = (lambda x, *d: x.permute(*d))((x @ wq).reshape(B, Sq, Hq, Dh), 0, 2, 1, 3)
        k = (lambda x, n, axis: torch.repeat_interleave(x, n, dim=axis))(k_cache[:, :, Sk - W:, :], rep, axis=1)
        v = (lambda x, n, axis: torch.repeat_interleave(x, n, dim=axis))(v_cache[:, :, Sk - W:, :], rep, axis=1)
        scores = torch.einsum("bhqd,bhkd->bhqk", q, k) * (Dh ** -0.5)
        p = _softmax(scores, axis=-1)
        o = torch.einsum("bhqk,bhkd->bhqd", p, v)
        return (lambda x, *d: x.permute(*d))(o, 0, 2, 1, 3).reshape(B, Sq, Hq * Dh) @ wo

    def _B_ref(x, w_gate, w_up, w_down):
        g = torch.einsum("bsd,df->bsf", x, w_gate)
        u = torch.einsum("bsd,df->bsf", x, w_up)
        h = g * u
        return torch.einsum("bsf,fd->bsd", h, w_down)

    x = A_x
    xn = _layernorm(x, axis=-1)
    s = _A_ref(xn, A_k_cache, A_v_cache, A_wq, A_wo)
    g = _silu(_B_ref(xn, B_w_gate, B_w_up, B_w_down))
    return x + s * g
```

```cpp
#include <hip/hip_runtime.h>
#include <hip/hip_bf16.h>
typedef unsigned short u16;
__device__ __forceinline__ float bf2f(u16 b){ return __uint_as_float(((unsigned)b)<<16); }
__device__ __forceinline__ u16 f2bf(float f){ unsigned u=__float_as_uint(f); u += 0x7fffu + ((u>>16)&1u); return (u16)(u>>16); }
__device__ __forceinline__ float rb(float f){ return bf2f(f2bf(f)); }

__global__ void ln_k(const u16* x, u16* xn){
  int row=blockIdx.x; int t=threadIdx.x;
  __shared__ float red[256];
  float v[4]; float s=0;
  for(int i=0;i<4;i++){ v[i]=bf2f(x[row*1024+t*4+i]); s+=v[i]; }
  red[t]=s; __syncthreads();
  for(int o=128;o>0;o>>=1){ if(t<o) red[t]+=red[t+o]; __syncthreads(); }
  float mean=red[0]/1024.f; __syncthreads();
  float q=0; for(int i=0;i<4;i++){ float d=v[i]-mean; q+=d*d; }
  red[t]=q; __syncthreads();
  for(int o=128;o>0;o>>=1){ if(t<o) red[t]+=red[t+o]; __syncthreads(); }
  float var=red[0]/1024.f;
  float mb=rb(mean), vb=rb(var);
  float den=rb(sqrtf(rb(vb+1e-5f)));
  for(int i=0;i<4;i++){ xn[row*1024+t*4+i]=f2bf(rb(v[i]-mb)/den); }
}
template<int MODE>
__global__ void gemm_k(const u16* A,int lda,const u16* B,int ldb,u16* C,int ldc,int K){
  __shared__ float As[32][65], Bs[32][65];
  int tx=threadIdx.x&15, ty=threadIdx.x>>4; int bm=blockIdx.y*64, bn=blockIdx.x*64;
  float acc[4][4]={};
  for(int k0=0;k0<K;k0+=32){
    for(int i=threadIdx.x;i<64*32;i+=256){ int r=i>>5,c=i&31; As[c][r]=bf2f(A[(size_t)(bm+r)*lda+k0+c]); }
    for(int i=threadIdx.x;i<64*32;i+=256){ int r=i>>6,c=i&63; Bs[r][c]=bf2f(B[(size_t)(k0+r)*ldb+bn+c]); }
    __syncthreads();
    for(int k=0;k<32;k++){ float a[4],b[4];
      for(int i=0;i<4;i++){a[i]=As[k][ty*4+i]; b[i]=Bs[k][tx*4+i];}
      for(int i=0;i<4;i++)for(int j=0;j<4;j++)acc[i][j]+=a[i]*b[j]; }
    __syncthreads();
  }
  for(int i=0;i<4;i++)for(int j=0;j<4;j++) C[(size_t)(bm+ty*4+i)*ldc+bn+tx*4+j]=f2bf(acc[i][j]);
}
__global__ void mul_k(const u16* g,const u16* u,u16* h,int n){ int i=blockIdx.x*256+threadIdx.x; if(i<n) h[i]=f2bf(bf2f(g[i])*bf2f(u[i])); }
__global__ void attn_k(const u16* q,const u16* kc,const u16* vc,u16* o){
  int row=blockIdx.x, h=blockIdx.y, t=threadIdx.x; int kv=h>>3;
  __shared__ float qs[128], sc[512], red[128];
  qs[t]=bf2f(q[(size_t)row*4096+h*128+t]); __syncthreads();
  const u16* K=kc+((size_t)kv*2048+1536)*128; const u16* V=vc+((size_t)kv*2048+1536)*128;
  float mx=-1e30f;
  for(int j=t;j<512;j+=128){ float s=0; for(int d=0;d<128;d++) s+=qs[d]*bf2f(K[j*128+d]); s=rb(rb(s)*0.08838834764831845f); sc[j]=s; mx=fmaxf(mx,s);}
  red[t]=mx; __syncthreads();
  for(int of=64;of>0;of>>=1){ if(t<of) red[t]=fmaxf(red[t],red[t+of]); __syncthreads(); }
  mx=red[0]; __syncthreads();
  float sum=0; for(int j=t;j<512;j+=128){ float e=__expf(sc[j]-mx); sc[j]=e; sum+=e; }
  red[t]=sum; __syncthreads();
  for(int of=64;of>0;of>>=1){ if(t<of) red[t]+=red[t+of]; __syncthreads(); }
  float inv=1.f/red[0];
  float acc=0; for(int j=0;j<512;j++) acc+=rb(sc[j]*inv)*bf2f(V[j*128+t]);
  o[(size_t)row*4096+h*128+t]=f2bf(acc);
}
__global__ void fin_k(const u16* x,const u16* s,const u16* d,u16* out,int n){ int i=blockIdx.x*256+threadIdx.x; if(i<n){ float dv=bf2f(d[i]); float g=rb(dv/(1.f+__expf(-dv))); out[i]=f2bf(bf2f(x[i])+rb(bf2f(s[i])*g)); } }

extern "C" void kernel_launch(void* const* d_in, const int* in_sizes, int n_in, void* d_out, int out_size, void* d_ws, size_t ws_size, hipStream_t stream) {
  const u16* x=(const u16*)d_in[0]; const u16* kc=(const u16*)d_in[1]; const u16* vc=(const u16*)d_in[2];
  const u16* wq=(const u16*)d_in[3]; const u16* wo=(const u16*)d_in[4];
  const u16* wg=(const u16*)d_in[6]; const u16* wu=(const u16*)d_in[7]; const u16* wd=(const u16*)d_in[8];
  u16* ws=(u16*)d_ws;
  u16* xn=ws; u16* q=xn+2048*1024; u16* o=q+2048*4096; u16* g=o+2048*4096; u16* u=g+2048*3584; u16* h=u+2048*3584; u16* s=h+2048*3584; u16* d=s+2048*1024;
  ln_k<<<2048,256,0,stream>>>(x,xn);
  gemm_k<0><<<dim3(4096/64,2048/64),256,0,stream>>>(xn,1024,wq,4096,q,4096,1024);
  gemm_k<0><<<dim3(3584/64,2048/64),256,0,stream>>>(xn,1024,wg,3584,g,3584,1024);
  gemm_k<0><<<dim3(3584/64,2048/64),256,0,stream>>>(xn,1024,wu,3584,u,3584,1024);
  mul_k<<<2048*3584/256,256,0,stream>>>(g,u,h,2048*3584);
  attn_k<<<dim3(2048,32),128,0,stream>>>(q,kc,vc,o);
  gemm_k<0><<<dim3(1024/64,2048/64),256,0,stream>>>(o,4096,wo,1024,s,1024,4096);
  gemm_k<0><<<dim3(1024/64,2048/64),256,0,stream>>>(h,3584,wd,1024,d,1024,3584);
  fin_k<<<2048*1024/256,256,0,stream>>>(x,s,d,(u16*)d_out,2048*1024);
}
```

```cpp
#include <hip/hip_runtime.h>
typedef unsigned u32;

extern "C" __global__ __launch_bounds__(256, 1) __attribute__((aligned(4096)))
void main_kernel(const void* x, const void* kc, const void* vc, const void* wq, const void* wo, const void* wg, const void* wu, const void* wd, void* out, void* ws) {
  __shared__ __attribute__((aligned(16))) char lds[131072 + 64];
  u32 tid = threadIdx.x;
  asm volatile(
    ".macro PP\n"
    ".set pd_page, ((. - main_kernel) >> 12)\n"
    ".set pd_fine, ((. - main_kernel) >> 8)\n"
    ".set pd_n, pd_n + 1\n"
    ".if (pd_n & 7) == 0\n"
    "70: s_cbranch_execz 70f\n"
    ".endif\n"
    ".if (pd_n & 7) == 1\n"
    "71: s_cbranch_execz 71f\n"
    ".endif\n"
    ".if (pd_n & 7) == 2\n"
    "72: s_cbranch_execz 72f\n"
    ".endif\n"
    ".if (pd_n & 7) == 3\n"
    "73: s_cbranch_execz 73f\n"
    ".endif\n"
    ".if (pd_n & 7) == 4\n"
    "74: s_cbranch_execz 74f\n"
    ".endif\n"
    ".if (pd_n & 7) == 5\n"
    "75: s_cbranch_execz 75f\n"
    ".endif\n"
    ".if (pd_n & 7) == 6\n"
    "76: s_cbranch_execz 76f\n"
    ".endif\n"
    ".if (pd_n & 7) == 7\n"
    "77: s_cbranch_execz 77f\n"
    ".endif\n"
    ".endm\n"
    ".macro PD\n"
    ".if ((. - main_kernel) >> 12) != pd_page\n"
    "PP\n"
    ".endif\n"
    ".endm\n"
    ".macro PF\n"
    ".if ((. - main_kernel) >> 8) != pd_fine\n"
    "PP\n"
    ".endif\n"
    ".endm\n"
    ".set pd_page, 0\n"
    ".set pd_fine, 0\n"
    ".set pd_n, 0\n"
    "s_load_dwordx16 s[4:19], s[0:1], 0x0\n"
    "s_load_dwordx4 s[20:23], s[0:1], 0x40\n"
    "v_readfirstlane_b32 s36, v0\n"
    "v_and_b32 v0, 63, v0\n"
    "s_and_b32 s25, s2, 3\n"
    "s_xor_b32 s25, s25, 1\n"
    "s_lshr_b32 s24, s2, 5\n"
    "s_lshr_b32 s36, s36, 6\n"
    "s_and_b32 s62, s2, 4\n"
    "s_bfe_u32 s63, s2, 0x20003\n"
    "s_or_b32 s62, s62, s63\n"
    "s_lshl3_add_u32 s27, s25, s24\n"
    "s_lshl2_add_u32 s26, s62, s36\n"
    "s_lshl_b32 s63, s62, 5\n"
    "s_add_u32 s63, s63, s27\n"
    "s_lshl2_add_u32 s63, s63, s36\n"
    "s_lshl_b32 s63, s63, 12\n"
    "v_lshlrev_b32 v4, 4, v0\n"
    "v_add_u32 v56, s63, v4\n"
    "s_mul_i32 s64, s26, 141312\n"
    "s_lshl2_add_u32 s65, s62, s25\n"
    "s_mul_i32 s65, s65, 141312\n"
    "s_mov_b32 s83, 0x20000\n"
    "s_movk_i32 s82, 0x2800\n"
    "s_waitcnt lgkmcnt(0)\n"
    "global_load_dwordx4 v[64:67], v56, s[4:5]\n"
    "global_load_dwordx4 v[68:71], v56, s[4:5] offset:1024\n"
    "global_load_dwordx4 v[72:75], v56, s[4:5] offset:2048\n"
    "global_load_dwordx4 v[76:79], v56, s[4:5] offset:3072\n"
    "s_add_u32 s40, s22, s64\n"
    "s_addc_u32 s41, s23, 0\n"
    "s_add_u32 s80, s40, 0x20000\n"
    "s_addc_u32 s81, s41, 0\n"
    "buffer_load_dwordx2 v[34:35], off, s[80:83], 0 sc1\n"
    "s_lshr_b32 s62, s2, 3\n"
    "s_lshl_b32 s63, s36, 5\n"
    "s_add_u32 s62, s62, s63\n"
    "s_lshl_b32 s62, s62, 11\n"
    "s_cmp_ge_u32 s62, L_end-L_pc\n"
    "s_cbranch_scc1 L_noct\n"
    "s_getpc_b64 s[66:67]\n"
    "L_pc:\n"
    "v_lshl_add_u32 v56, v0, 5, s62\n"
    "v_min_u32 v56, L_end-L_pc-4, v56\n"
    "global_load_dword v7, v56, s[66:67]\n"
    "L_noct:\n"
    "s_mov_b64 exec, 0\n"
    "s_cmp_eq_u32 s36, 1\n"
    "s_cbranch_scc1 L_sp1\n"
    "s_cmp_eq_u32 s36, 2\n"
    "s_cbranch_scc1 L_sp2\n"
    "s_cmp_eq_u32 s36, 3\n"
    "s_cbranch_scc1 L_sp3\n"
    "L_spr:\n"
    "s_mov_b64 exec, -1\n"
    "s_lshr_b32 s62, s24, 1\n"
    "s_lshl_b32 s62, s62, 11\n"
    "s_add_u32 s65, s65, s62\n"
    "s_and_b32 s62, s24, 1\n"
    "s_lshl_b32 s62, s62, 7\n"
    "s_add_u32 s65, s65, s62\n"
    "s_lshl_b32 s62, s36, 13\n"
    "s_add_u32 s65, s65, s62\n"
    "s_add_u32 s52, s22, s65\n"
    "s_addc_u32 s53, s23, 0\n"
    "s_mov_b32 s54, 0x18800\n"
    "s_mov_b32 s55, 0x20000\n"
    "s_mov_b32 s47, 0x20000\n"
    "s_mov_b32 s51, 0x20000\n"
    "s_mul_i32 s62, s36, 4160\n"
    "s_add_u32 s62, s62, 81920\n"
    "v_add_u32 v40, s62, v4\n"
    "v_and_b32 v56, 7, v0\n"
    "v_bfe_u32 v57, v0, 3, 2\n"
    "v_lshrrev_b32 v58, 5, v0\n"
    "s_lshl_b32 s62, s36, 7\n"
    "s_add_u32 s62, s62, 81920\n"
    "v_mul_u32_u24 v41, 2080, v56\n"
    "v_lshl_add_u32 v41, v57, 3, v41\n"
    "v_lshl_add_u32 v41, v58, 6, v41\n"
    "v_add_u32 v41, s62, v41\n"
    "v_lshlrev_b32 v42, 4, v56\n"
    "v_lshl_add_u32 v42, v57, 8, v42\n"
    "v_lshl_add_u32 v42, v58, 10, v42\n"
    "s_xor_b32 s28, s25, 1\n"
    "s_mul_i32 s28, s28, 7\n"
    "s_add_u32 s28, s28, s24\n"
    "s_lshl_b32 s61, s36, 12\n"
    "s_cmp_lt_u32 s24, 7\n"
    "s_cselect_b32 s31, s25, 0\n"
    "s_andn2_b32 s31, 1, s31\n"
    "v_lshrrev_b32 v56, 4, v0\n"
    "v_and_b32 v57, 15, v0\n"
    "v_lshl_add_u32 v6, s36, 4, v56\n"
    "v_lshlrev_b32 v56, 1, v56\n"
    "v_xor_b32 v56, v56, v57\n"
    "v_lshlrev_b32 v32, 4, v56\n"
    "v_xor_b32 v33, 0x80, v32\n"
    "s_cmp_lg_u32 s31, 1\n"
    "s_cbranch_scc1 L_bn1\n"
    "s_lshl_b32 s62, s27, 8\n"
    "s_add_u32 s44, s10, s62\n"
    "s_addc_u32 s45, s11, 0\n"
    "s_sub_u32 s46, 8388608, s62\n"
    "s_mov_b32 s59, 1048576\n"
    "s_movk_i32 s68, 8192\n"
    "s_mov_b32 s57, 0\n"
    "v_mad_u32_u24 v24, v6, s68, v32\n"
    "v_mad_u32_u24 v25, v6, s68, v33\n"
    "v_add_u32 v26, 63488, v24\n"
    "v_add_u32 v27, 95232, v25\n"
    "v_add_u32 v25, 31744, v25\n"
    "s_mov_b64 s[48:49], s[44:45]\n"
    "s_mov_b32 s58, 524288\n"
    "s_mov_b32 s50, s46\n"
    "v_mov_b32 v28, v24\n"
    "v_mov_b32 v29, v25\n"
    "v_mov_b32 v30, v26\n"
    "v_mov_b32 v31, v27\n"
    "s_branch L_be1\n"
    "L_bn1:\n"
    "s_lshl_b32 s62, s28, 8\n"
    "s_add_u32 s44, s14, s62\n"
    "s_addc_u32 s45, s15, 0\n"
    "s_sub_u32 s46, 7340032, s62\n"
    "s_mov_b32 s59, 458752\n"
    "s_movk_i32 s68, 7168\n"
    "s_mov_b32 s57, 0\n"
    "v_mad_u32_u24 v24, v6, s68, v32\n"
    "v_mad_u32_u24 v25, v6, s68, v33\n"
    "v_add_u32 v26, 55296, v24\n"
    "v_add_u32 v27, 82944, v25\n"
    "v_add_u32 v25, 27648, v25\n"
    "s_lshl_b32 s62, s28, 8\n"
    "s_add_u32 s48, s16, s62\n"
    "s_addc_u32 s49, s17, 0\n"
    "s_mov_b32 s58, 0\n"
    "s_mov_b32 s50, s46\n"
    "v_mov_b32 v28, v24\n"
    "v_mov_b32 v29, v25\n"
    "v_mov_b32 v30, v26\n"
    "v_mov_b32 v31, v27\n"
    "L_be1:\n"
    "s_add_u32 m0, s61, 0\n"
    "s_nop 0\n"
    "buffer_load_dwordx4 v24, s[44:47], s57 offen offset:0 lds\n"
    "buffer_load_dwordx4 v25, s[44:47], s57 offen offset:1024 lds\n"
    "buffer_load_dwordx4 v26, s[44:47], s57 offen offset:2048 lds\n"
    "buffer_load_dwordx4 v27, s[44:47], s57 offen offset:3072 lds\n"
    "s_add_u32 s57, s57, s59\n"
    "s_add_u32 m0, s61, 16384\n"
    "s_nop 0\n"
    "buffer_load_dwordx4 v28, s[48:51], s58 offen offset:0 lds\n"
    "buffer_load_dwordx4 v29, s[48:51], s58 offen offset:1024 lds\n"
    "buffer_load_dwordx4 v30, s[48:51], s58 offen offset:2048 lds\n"
    "buffer_load_dwordx4 v31, s[48:51], s58 offen offset:3072 lds\n"
    "s_add_u32 s58, s58, s59\n"
    "s_mov_b32 s63, 0x8000\n"
    "s_mov_b32 s64, 0x10000\n"
    "s_mov_b32 s65, 0x18000\n"
    "L_sp1:\n"
    "s_cbranch_execz L_spr\n"
    "s_mov_b32 s62, 0xffff0000\n"
    "s_waitcnt vmcnt(12)\n"
    "v_lshlrev_b32 v128, 16, v64\n"
    "v_and_b32 v129, s62, v64\n"
    "v_lshlrev_b32 v130, 16, v65\n"
    "v_and_b32 v131, s62, v65\n"
    "v_lshlrev_b32 v132, 16, v66\n"
    "v_and_b32 v133, s62, v66\n"
    "v_lshlrev_b32 v134, 16, v67\n"
    "v_and_b32 v135, s62, v67\n"
    "s_waitcnt vmcnt(11)\n"
    "v_lshlrev_b32 v136, 16, v68\n"
    "v_and_b32 v137, s62, v68\n"
    "v_lshlrev_b32 v138, 16, v69\n"
    "v_and_b32 v139, s62, v69\n"
    "v_lshlrev_b32 v140, 16, v70\n"
    "v_and_b32 v141, s62, v70\n"
    "v_lshlrev_b32 v142, 16, v71\n"
    "v_and_b32 v143, s62, v71\n"
    "s_waitcnt vmcnt(10)\n"
    "v_lshlrev_b32 v144, 16, v72\n"
    "v_and_b32 v145, s62, v72\n"
    "v_lshlrev_b32 v146, 16, v73\n"
    "v_and_b32 v147, s62, v73\n"
    "v_lshlrev_b32 v148, 16, v74\n"
    "v_and_b32 v149, s62, v74\n"
    "v_lshlrev_b32 v150, 16, v75\n"
    "v_and_b32 v151, s62, v75\n"
    "s_waitcnt vmcnt(9)\n"
    "v_lshlrev_b32 v152, 16, v76\n"
    "v_and_b32 v153, s62, v76\n"
    "v_lshlrev_b32 v154, 16, v77\n"
    "v_and_b32 v155, s62, v77\n"
    "v_lshlrev_b32 v156, 16, v78\n"
    "v_and_b32 v157, s62, v78\n"
    "v_lshlrev_b32 v158, 16, v79\n"
    "v_and_b32 v159, s62, v79\n"
    "v_pk_add_f32 v[160:161], v[128:129], v[130:131]\n"
    "v_pk_add_f32 v[168:169], v[144:145], v[146:147]\n"
    "v_pk_add_f32 v[162:163], v[132:133], v[134:135]\n"
    "v_pk_add_f32 v[170:171], v[148:149], v[150:151]\n"
    "v_pk_add_f32 v[164:165], v[136:137], v[138:139]\n"
    "v_pk_add_f32 v[172:173], v[152:153], v[154:155]\n"
    "v_pk_add_f32 v[166:167], v[140:141], v[142:143]\n"
    "v_pk_add_f32 v[174:175], v[156:157], v[158:159]\n"
    "v_pk_add_f32 v[160:161], v[160:161], v[164:165]\n"
    "v_pk_add_f32 v[168:169], v[168:169], v[172:173]\n"
    "v_pk_add_f32 v[162:163], v[162:163], v[166:167]\n"
    "v_pk_add_f32 v[170:171], v[170:171], v[174:175]\n"
    "v_pk_add_f32 v[160:161], v[160:161], v[162:163]\n"
    "v_pk_add_f32 v[168:169], v[168:169], v[170:171]\n"
    "v_add_f32 v160, v160, v161\n"
    "v_add_f32 v168, v168, v169\n"
    "s_nop 0\n"
    "v_add_f32_dpp v160, v160, v160 row_ror:8 row_mask:0xf bank_mask:0xf\n"
    "v_add_f32_dpp v168, v168, v168 row_ror:8 row_mask:0xf bank_mask:0xf\n"
    "s_nop 0\n"
    "v_add_f32_dpp v160, v160, v160 row_ror:4 row_mask:0xf bank_mask:0xf\n"
    "v_add_f32_dpp v168, v168, v168 row_ror:4 row_mask:0xf bank_mask:0xf\n"
    "s_nop 0\n"
    "v_add_f32_dpp v160, v160, v160 row_ror:2 row_mask:0xf bank_mask:0xf\n"
    "v_add_f32_dpp v168, v168, v168 row_ror:2 row_mask:0xf bank_mask:0xf\n"
    "s_nop 0\n"
    "v_add_f32_dpp v160, v160, v160 row_ror:1 row_mask:0xf bank_mask:0xf\n"
    "v_add_f32_dpp v168, v168, v168 row_ror:1 row_mask:0xf bank_mask:0xf\n"
    "v_mov_b32 v176, v160\n"
    "v_mov_b32 v177, v168\n"
    "s_nop 0\n"
    "v_permlane16_swap_b32 v160, v176\n"
    "v_permlane16_swap_b32 v168, v177\n"
    "v_add_f32 v160, v160, v176\n"
    "v_add_f32 v168, v168, v177\n"
    "v_mov_b32 v176, v160\n"
    "v_mov_b32 v177, v168\n"
    "s_nop 0\n"
    "v_permlane32_swap_b32 v160, v176\n"
    "v_permlane32_swap_b32 v168, v177\n"
    "v_add_f32 v160, v160, v176\n"
    "v_add_f32 v168, v168, v177\n"
    "v_mul_f32 v160, 0xba800000, v160\n"
    "v_mul_f32 v168, 0xba800000, v168\n"
    "L_sp2:\n"
    "s_cbranch_execz L_spr\n"
    "v_pk_add_f32 v[128:129], v[128:129], v[160:161] op_sel_hi:[1,0]\n"
    "v_pk_add_f32 v[144:145], v[144:145], v[168:169] op_sel_hi:[1,0]\n"
    "v_pk_add_f32 v[130:131], v[130:131], v[160:161] op_sel_hi:[1,0]\n"
    "v_pk_add_f32 v[146:147], v[146:147], v[168:169] op_sel_hi:[1,0]\n"
    "v_pk_add_f32 v[132:133], v[132:133], v[160:161] op_sel_hi:[1,0]\n"
    "v_pk_add_f32 v[148:149], v[148:149], v[168:169] op_sel_hi:[1,0]\n"
    "v_pk_add_f32 v[134:135], v[134:135], v[160:161] op_sel_hi:[1,0]\n"
    "v_pk_add_f32 v[150:151], v[150:151], v[168:169] op_sel_hi:[1,0]\n"
    "v_pk_add_f32 v[136:137], v[136:137], v[160:161] op_sel_hi:[1,0]\n"
    "v_pk_add_f32 v[152:153], v[152:153], v[168:169] op_sel_hi:[1,0]\n"
    "v_pk_add_f32 v[138:139], v[138:139], v[160:161] op_sel_hi:[1,0]\n"
    "v_pk_add_f32 v[154:155], v[154:155], v[168:169] op_sel_hi:[1,0]\n"
    "v_pk_add_f32 v[140:141], v[140:141], v[160:161] op_sel_hi:[1,0]\n"
    "v_pk_add_f32 v[156:157], v[156:157], v[168:169] op_sel_hi:[1,0]\n"
    "v_pk_add_f32 v[142:143], v[142:143], v[160:161] op_sel_hi:[1,0]\n"
    "v_pk_add_f32 v[158:159], v[158:159], v[168:169] op_sel_hi:[1,0]\n"
    "v_pk_mul_f32 v[162:163], v[128:129], v[128:129]\n"
    "v_pk_mul_f32 v[170:171], v[144:145], v[144:145]\n"
    "v_pk_mul_f32 v[164:165], v[130:131], v[130:131]\n"
    "v_pk_mul_f32 v[172:173], v[146:147], v[146:147]\n"
    "v_pk_fma_f32 v[162:163], v[132:133], v[132:133], v[162:163]\n"
    "v_pk_fma_f32 v[170:171], v[148:149], v[148:149], v[170:171]\n"
    "v_pk_fma_f32 v[164:165], v[134:135], v[134:135], v[164:165]\n"
    "v_pk_fma_f32 v[172:173], v[150:151], v[150:151], v[172:173]\n"
    "v_pk_fma_f32 v[162:163], v[136:137], v[136:137], v[162:163]\n"
    "v_pk_fma_f32 v[170:171], v[152:153], v[152:153], v[170:171]\n"
    "v_pk_fma_f32 v[164:165], v[138:139], v[138:139], v[164:165]\n"
    "v_pk_fma_f32 v[172:173], v[154:155], v[154:155], v[172:173]\n"
    "v_pk_fma_f32 v[162:163], v[140:141], v[140:141], v[162:163]\n"
    "v_pk_fma_f32 v[170:171], v[156:157], v[156:157], v[170:171]\n"
    "v_pk_fma_f32 v[164:165], v[142:143], v[142:143], v[164:165]\n"
    "v_pk_fma_f32 v[172:173], v[158:159], v[158:159], v[172:173]\n"
    "v_pk_add_f32 v[162:163], v[162:163], v[164:165]\n"
    "v_pk_add_f32 v[170:171], v[170:171], v[172:173]\n"
    "v_add_f32 v162, v162, v163\n"
    "v_add_f32 v170, v170, v171\n"
    "s_nop 0\n"
    "v_add_f32_dpp v162, v162, v162 row_ror:8 row_mask:0xf bank_mask:0xf\n"
    "v_add_f32_dpp v170, v170, v170 row_ror:8 row_mask:0xf bank_mask:0xf\n"
    "s_nop 0\n"
    "v_add_f32_dpp v162, v162, v162 row_ror:4 row_mask:0xf bank_mask:0xf\n"
    "v_add_f32_dpp v170, v170, v170 row_ror:4 row_mask:0xf bank_mask:0xf\n"
    "s_nop 0\n"
    "v_add_f32_dpp v162, v162, v162 row_ror:2 row_mask:0xf bank_mask:0xf\n"
    "v_add_f32_dpp v170, v170, v170 row_ror:2 row_mask:0xf bank_mask:0xf\n"
    "s_nop 0\n"
    "v_add_f32_dpp v162, v162, v162 row_ror:1 row_mask:0xf bank_mask:0xf\n"
    "v_add_f32_dpp v170, v170, v170 row_ror:1 row_mask:0xf bank_mask:0xf\n"
    "v_mov_b32 v176, v162\n"
    "v_mov_b32 v177, v170\n"
    "s_nop 0\n"
    "v_permlane16_swap_b32 v162, v176\n"
    "v_permlane16_swap_b32 v170, v177\n"
    "v_add_f32 v162, v162, v176\n"
    "v_add_f32 v170, v170, v177\n"
    "v_mov_b32 v176, v162\n"
    "v_mov_b32 v177, v170\n"
    "s_nop 0\n"
    "v_permlane32_swap_b32 v162, v176\n"
    "v_permlane32_swap_b32 v170, v177\n"
    "v_add_f32 v162, v162, v176\n"
    "v_add_f32 v170, v170, v177\n"
    "v_mov_b32 v178, 0x3727c5ac\n"
    "v_fmamk_f32 v162, v162, 0x3a800000, v178\n"
    "v_fmamk_f32 v170, v170, 0x3a800000, v178\n"
    "v_rsq_f32 v162, v162\n"
    "v_rsq_f32 v170, v170\n"
    "s_nop 0\n"
    "L_sp3:\n"
    "s_cbranch_execz L_spr\n"
    "v_pk_mul_f32 v[128:129], v[128:129], v[162:163] op_sel_hi:[1,0]\n"
    "v_pk_mul_f32 v[130:131], v[130:131], v[162:163] op_sel_hi:[1,0]\n"
    "v_pk_mul_f32 v[132:133], v[132:133], v[162:163] op_sel_hi:[1,0]\n"
    "v_pk_mul_f32 v[134:135], v[134:135], v[162:163] op_sel_hi:[1,0]\n"
    "v_pk_mul_f32 v[136:137], v[136:137], v[162:163] op_sel_hi:[1,0]\n"
    "v_pk_mul_f32 v[138:139], v[138:139], v[162:163] op_sel_hi:[1,0]\n"
    "v_pk_mul_f32 v[140:141], v[140:141], v[162:163] op_sel_hi:[1,0]\n"
    "v_pk_mul_f32 v[142:143], v[142:143], v[162:163] op_sel_hi:[1,0]\n"
    "v_cvt_pk_bf16_f32 v64, v128, v129\n"
    "v_cvt_pk_bf16_f32 v65, v130, v131\n"
    "v_cvt_pk_bf16_f32 v66, v132, v133\n"
    "v_cvt_pk_bf16_f32 v67, v134, v135\n"
    "v_cvt_pk_bf16_f32 v68, v136, v137\n"
    "v_cvt_pk_bf16_f32 v69, v138, v139\n"
    "v_cvt_pk_bf16_f32 v70, v140, v141\n"
    "v_cvt_pk_bf16_f32 v71, v142, v143\n"
    "ds_write_b128 v40, v[64:67] offset:0\n"
    "ds_write_b128 v40, v[68:71] offset:1024\n"
    "v_pk_mul_f32 v[144:145], v[144:145], v[170:171] op_sel_hi:[1,0]\n"
    "v_pk_mul_f32 v[146:147], v[146:147], v[170:171] op_sel_hi:[1,0]\n"
    "v_pk_mul_f32 v[148:149], v[148:149], v[170:171] op_sel_hi:[1,0]\n"
    "v_pk_mul_f32 v[150:151], v[150:151], v[170:171] op_sel_hi:[1,0]\n"
    "v_pk_mul_f32 v[152:153], v[152:153], v[170:171] op_sel_hi:[1,0]\n"
    "v_pk_mul_f32 v[154:155], v[154:155], v[170:171] op_sel_hi:[1,0]\n"
    "v_pk_mul_f32 v[156:157], v[156:157], v[170:171] op_sel_hi:[1,0]\n"
    "v_pk_mul_f32 v[158:159], v[158:159], v[170:171] op_sel_hi:[1,0]\n"
    "v_cvt_pk_bf16_f32 v72, v144, v145\n"
    "v_cvt_pk_bf16_f32 v73, v146, v147\n"
    "v_cvt_pk_bf16_f32 v74, v148, v149\n"
    "v_cvt_pk_bf16_f32 v75, v150, v151\n"
    "v_cvt_pk_bf16_f32 v76, v152, v153\n"
    "v_cvt_pk_bf16_f32 v77, v154, v155\n"
    "v_cvt_pk_bf16_f32 v78, v156, v157\n"
    "v_cvt_pk_bf16_f32 v79, v158, v159\n"
    "ds_write_b128 v40, v[72:75] offset:2080\n"
    "ds_write_b128 v40, v[76:79] offset:3104\n"
    "s_waitcnt lgkmcnt(0)\n"
    "s_barrier\n"
    "ds_read2_b64 v[80:83], v41 offset0:0 offset1:4\n"
    "ds_read2_b64 v[84:87], v41 offset0:64 offset1:68\n"
    "ds_read2_b64 v[88:91], v41 offset0:128 offset1:132\n"
    "ds_read2_b64 v[92:95], v41 offset0:192 offset1:196\n"
    "s_waitcnt lgkmcnt(3)\n"
    "buffer_store_dwordx4 v[80:83], v42, s[52:55], 0 offen sc1\n"
    "s_waitcnt lgkmcnt(2)\n"
    "buffer_store_dwordx4 v[84:87], v42, s[52:55], s63 offen sc1\n"
    "s_waitcnt lgkmcnt(1)\n"
    "buffer_store_dwordx4 v[88:91], v42, s[52:55], s64 offen sc1\n"
    "s_waitcnt lgkmcnt(0)\n"
    "buffer_store_dwordx4 v[92:95], v42, s[52:55], s65 offen sc1\n"
    "s_waitcnt vmcnt(12)\n"
    "v_add_u32 v34, 1, v34\n"
    "v_mov_b32 v56, 0\n"
    "v_mov_b32 v57, 0\n"
    "v_mov_b32 v58, 0\n"
    "v_mov_b32 v59, 0\n"
    "v_min_u32 v38, 7, v0\n"
    "v_lshlrev_b32 v38, 7, v38\n"
    "v_mfma_f32_32x32x16_bf16 v[128:143], v[56:59], v[56:59], 0\n"
    "s_movk_i32 s86, 128\n"
    "s_mov_b32 s42, 0x20000\n"
    "s_mov_b32 s43, 0x20000\n"
    "v_mfma_f32_32x32x16_bf16 v[144:159], v[56:59], v[56:59], 0\n"
    "s_movk_i32 s69, 0x2000\n"
    "s_mov_b32 s56, 0\n"
    "v_mov_b32 v20, v4\n"
    "v_mfma_f32_32x32x16_bf16 v[160:175], v[56:59], v[56:59], 0\n"
    "v_add_u32 v21, 2048, v4\n"
    "v_add_u32 v22, 4096, v4\n"
    "v_add_u32 v23, 6144, v4\n"
    "v_mfma_f32_32x32x16_bf16 v[176:191], v[56:59], v[56:59], 0\n"
    "v_lshrrev_b32 v44, 2, v0\n"
    "v_and_b32 v45, 7, v44\n"
    "v_and_b32 v46, 3, v0\n"
    "v_mfma_f32_32x32x16_bf16 a[0:15], v[56:59], v[56:59], 0\n"
    "v_lshlrev_b32 v46, 3, v46\n"
    "v_lshl_add_u32 v46, v44, 8, v46\n"
    "v_lshl_add_u32 v8, v45, 5, v46\n"
    "v_mfma_f32_32x32x16_bf16 a[16:31], v[56:59], v[56:59], 0\n"
    "v_xor_b32 v9, 32, v8\n"
    "v_xor_b32 v10, 64, v8\n"
    "v_xor_b32 v11, 96, v8\n"
    "v_mfma_f32_32x32x16_bf16 a[32:47], v[56:59], v[56:59], 0\n"
    "v_xor_b32 v12, 128, v8\n"
    "v_xor_b32 v13, 160, v8\n"
    "v_xor_b32 v14, 192, v8\n"
    "v_mfma_f32_32x32x16_bf16 a[48:63], v[56:59], v[56:59], 0\n"
    "v_xor_b32 v15, 224, v8\n"
    "v_add_u32 v5, 0x1000, v4\n"
    "v_and_b32 v44, 15, v0\n"
    "v_mfma_f32_32x32x16_bf16 a[64:79], v[56:59], v[56:59], 0\n"
    "v_bfe_u32 v45, v0, 5, 1\n"
    "v_bfe_u32 v46, v0, 4, 1\n"
    "v_lshl_add_u32 v45, v46, 1, v45\n"
    "v_mfma_f32_32x32x16_bf16 a[80:95], v[56:59], v[56:59], 0\n"
    "v_xor_b32 v45, v45, v44\n"
    "v_lshlrev_b32 v45, 4, v45\n"
    "v_lshl_add_u32 v16, v44, 8, v45\n"
    "v_mfma_f32_32x32x16_bf16 a[96:111], v[56:59], v[56:59], 0\n"
    "v_xor_b32 v17, 64, v16\n"
    "v_xor_b32 v18, 128, v16\n"
    "v_xor_b32 v19, 192, v16\n"
    "v_mfma_f32_32x32x16_bf16 a[112:127], v[56:59], v[56:59], 0\n"
    "s_and_b32 s29, s2, 1\n"
    "s_lshl_b32 s30, s29, 8\n"
    "s_sub_u32 s30, 0x800, s30\n"
    "v_mfma_f32_32x32x16_bf16 v[192:207], v[56:59], v[56:59], 0\n"
    "s_bfe_u32 s62, s2, 0x10001\n"
    "s_lshl1_add_u32 s29, s29, s62\n"
    "s_mov_b32 s84, 0x40000000\n"
    "v_mfma_f32_32x32x16_bf16 v[208:223], v[56:59], v[56:59], 0\n"
    "s_mov_b32 s39, 0\n"
    "s_xor_b32 s99, s31, 1\n"
    "s_cmp_eq_u32 s24, 7\n"
    "v_mfma_f32_32x32x16_bf16 v[224:239], v[56:59], v[56:59], 0\n"
    "s_cmov_b32 s99, 2\n"
    "v_mfma_f32_32x32x16_bf16 v[240:255], v[56:59], v[56:59], 0\n"
    "s_lshl_b32 s62, s24, 7\n"
    "s_add_u32 s62, s62, 128\n"
    "s_waitcnt vmcnt(0)\n"
    "s_barrier\n"
    "s_cmp_lg_u32 s36, s25\n"
    "s_cbranch_scc1 L_nolns\n"
    "s_mov_b64 exec, 1\n"
    "buffer_store_dwordx2 v[34:35], off, s[80:83], s62 sc1\n"
    "s_mov_b64 exec, -1\n"
    "L_nolns:\n"
    "s_add_u32 m0, s61, 32768\n"
    "s_nop 0\n"
    "buffer_load_dwordx4 v24, s[44:47], s57 offen offset:0 lds\n"
    "buffer_load_dwordx4 v25, s[44:47], s57 offen offset:1024 lds\n"
    "buffer_load_dwordx4 v26, s[44:47], s57 offen offset:2048 lds\n"
    "buffer_load_dwordx4 v27, s[44:47], s57 offen offset:3072 lds\n"
    "s_add_u32 s57, s57, s59\n"
    "s_add_u32 m0, s61, 49152\n"
    "s_nop 0\n"
    "buffer_load_dwordx4 v28, s[48:51], s58 offen offset:0 lds\n"
    "buffer_load_dwordx4 v29, s[48:51], s58 offen offset:1024 lds\n"
    "buffer_load_dwordx4 v30, s[48:51], s58 offen offset:2048 lds\n"
    "buffer_load_dwordx4 v31, s[48:51], s58 offen offset:3072 lds\n"
    "s_add_u32 s58, s58, s59\n"
    "s_add_u32 m0, s61, 65536\n"
    "s_nop 0\n"
    "buffer_load_dwordx4 v24, s[44:47], s57 offen offset:0 lds\n"
    "buffer_load_dwordx4 v25, s[44:47], s57 offen offset:1024 lds\n"
    "buffer_load_dwordx4 v26, s[44:47], s57 offen offset:2048 lds\n"
    "buffer_load_dwordx4 v27, s[44:47], s57 offen offset:3072 lds\n"
    "s_add_u32 s57, s57, s59\n"
    "s_cmp_lg_u32 s24, 7\n"
    "s_cbranch_scc1 L_nop\n"
    "s_and_b32 s62, s2, 31\n"
    "s_lshl2_add_u32 s62, s62, s36\n"
    "v_lshrrev_b32 v42, 5, v0\n"
    "v_lshl_add_u32 v42, s62, 1, v42\n"
    "v_bfe_u32 v43, v0, 4, 1\n"
    "v_and_b32 v56, 15, v0\n"
    "v_lshlrev_b32 v40, 13, v42\n"
    "v_lshl_add_u32 v40, v43, 12, v40\n"
    "v_lshl_add_u32 v40, v56, 7, v40\n"
    "v_mul_u32_u24 v41, 0x1c00, v42\n"
    "v_mul_u32_u24 v43, 0xe00, v43\n"
    "v_min_u32 v56, 13, v56\n"
    "v_lshl_add_u32 v41, v56, 7, v41\n"
    "v_add_u32 v41, v41, v43\n"
    "v_add_u32 v42, 2097152, v40\n"
    "global_load_dword v7, v42, s[10:11]\n"
    "v_add_u32 v42, 4194304, v40\n"
    "global_load_dword v7, v42, s[10:11]\n"
    "v_add_u32 v42, 6291456, v40\n"
    "global_load_dword v7, v42, s[10:11]\n"
    "s_bitcmp1_b32 s62, 6\n"
    "s_cselect_b64 s[64:65], s[8:9], s[6:7]\n"
    "s_bfe_u32 s63, s62, 0x20004\n"
    "s_lshl_b32 s63, s63, 19\n"
    "s_and_b32 s66, s62, 15\n"
    "s_lshl_b32 s66, s66, 13\n"
    "s_add_u32 s63, s63, s66\n"
    "s_add_u32 s63, s63, 393216\n"
    "v_lshl_add_u32 v43, v0, 7, s63\n"
    "global_load_dword v7, v43, s[64:65]\n"
    "s_cmp_lg_u32 s36, 0\n"
    "s_cbranch_scc1 L_notl\n"
    "v_and_b32 v43, 31, v0\n"
    "v_add_u32 v43, 1, v43\n"
    "v_mul_u32_u24 v42, 525312, v43\n"
    "v_mul_u32_u24 v43, 459776, v43\n"
    "v_add_u32 v42, 5242816, v42\n"
    "v_add_u32 v43, 23068608, v43\n"
    "v_cmp_gt_u32 vcc, 32, v0\n"
    "s_nop 1\n"
    "v_cndmask_b32 v42, v43, v42, vcc\n"
    "global_load_dword v7, v42, s[22:23]\n"
    "v_mul_u32_u24 v42, 212992, v0\n"
    "v_add_u32 v42, 38810560, v42\n"
    "global_load_dword v7, v42, s[22:23]\n"
    "v_lshlrev_b32 v43, 17, v0\n"
    "v_min_u32 v42, 0x6fffc0, v43\n"
    "global_load_dword v7, v43, s[12:13]\n"
    "global_load_dword v7, v42, s[18:19]\n"
    "L_notl:\n"
    "global_load_dword v7, v40, s[10:11] offset:2048\n"
    "v_add_u32 v42, 2097152, v40\n"
    "global_load_dword v7, v42, s[10:11] offset:2048\n"
    "global_load_dword v7, v41, s[14:15] offset:1792\n"
    "global_load_dword v7, v41, s[16:17] offset:1792\n"
    "v_add_u32 v42, 4194304, v40\n"
    "global_load_dword v7, v42, s[10:11] offset:2048\n"
    "v_add_u32 v42, 6291456, v40\n"
    "global_load_dword v7, v42, s[10:11] offset:2048\n"
    "v_add_u32 v42, 1835008, v41\n"
    "global_load_dword v7, v42, s[14:15] offset:1792\n"
    "global_load_dword v7, v42, s[16:17] offset:1792\n"
    "v_add_u32 v42, 3670016, v41\n"
    "global_load_dword v7, v42, s[14:15] offset:1792\n"
    "global_load_dword v7, v42, s[16:17] offset:1792\n"
    "v_add_u32 v42, 5505024, v41\n"
    "global_load_dword v7, v42, s[14:15] offset:1792\n"
    "global_load_dword v7, v42, s[16:17] offset:1792\n"
    "L_nop:\n"
    "ds_read_b64_tr_b16 a[224:225], v8 offset:0\n"
    "ds_read_b64_tr_b16 a[226:227], v8 offset:4096\n"
    "ds_read_b64_tr_b16 a[228:229], v8 offset:8192\n"
    "ds_read_b64_tr_b16 a[230:231], v8 offset:12288\n"
    "ds_read_b64_tr_b16 a[232:233], v9 offset:0\n"
    "ds_read_b64_tr_b16 a[234:235], v9 offset:4096\n"
    "ds_read_b64_tr_b16 a[236:237], v9 offset:8192\n"
    "ds_read_b64_tr_b16 a[238:239], v9 offset:12288\n"
    "s_mov_b32 s54, 0\n"
    "s_cmp_ge_u32 s29, 2\n"
    "s_cselect_b64 s[34:35], s[18:19], s[12:13]\n"
    "s_cselect_b32 s64, 17825792, 0\n"
    "s_cselect_b32 s91, 4096, 0\n"
    "s_bitcmp1_b32 s29, 0\n"
    "s_cselect_b32 s65, s30, 0\n"
    "s_lshl_b32 s62, s30, 11\n"
    "s_lshl_b32 s63, s24, 8\n"
    "s_sub_u32 s90, s62, s63\n"
    "s_lshl_b32 s62, s65, 11\n"
    "s_add_u32 s62, s62, s63\n"
    "s_add_u32 s34, s34, s62\n"
    "s_addc_u32 s35, s35, 0\n"
    "s_lshl_b32 s62, s30, 8\n"
    "s_add_u32 s62, s62, 1024\n"
    "s_mul_i32 s62, s62, s26\n"
    "s_lshl_b32 s63, s65, 7\n"
    "s_add_u32 s62, s62, s63\n"
    "s_add_u32 s62, s62, s64\n"
    "s_add_u32 s62, s62, 5242880\n"
    "s_add_u32 s78, s22, s62\n"
    "s_addc_u32 s79, s23, 0\n"
    "s_lshr_b32 s62, s65, 0\n"
    "s_add_u32 s91, s91, s62\n"
    "s_add_u32 s91, s91, 1152\n"
    "s_mov_b64 exec, 0\n"
    "s_cmp_eq_u32 s36, 1\n"
    "s_cbranch_scc1 71f\n"
    "s_cmp_eq_u32 s36, 2\n"
    "s_cbranch_scc1 72f\n"
    "s_cmp_eq_u32 s36, 3\n"
    "s_cbranch_scc1 73f\n"
    "s_branch 70f\n"
    "L_retA:\n"
    "s_mov_b64 exec, -1\n"
    "s_mov_b64 exec, 0\n"
    "s_cmp_eq_u32 s36, 1\n"
    "s_cbranch_scc1 75f\n"
    "s_cmp_eq_u32 s36, 2\n"
    "s_cbranch_scc1 76f\n"
    "s_cmp_eq_u32 s36, 3\n"
    "s_cbranch_scc1 77f\n"
    "s_branch 74f\n"
    "L_retB:\n"
    "s_mov_b64 exec, -1\n"
    "buffer_load_dwordx2 v[36:37], v38, s[80:83], s86 offen sc1\n"
    "PD\n"
    "s_waitcnt vmcnt(0)\n"
    "s_mov_b32 s87, 0x40000\n"
    "PD\n"
    "L_S_pc:\n"
    "PD\n"
    "v_cmp_ne_u64 vcc, v[36:37], v[34:35]\n"
    "s_cbranch_vccz L_S_pd\n"
    "PD\n"
    "s_sleep 2\n"
    "PD\n"
    "buffer_load_dwordx2 v[36:37], v38, s[80:83], s86 offen sc1\n"
    "PD\n"
    "s_sub_u32 s87, s87, 1\n"
    "PD\n"
    "s_cmp_eq_u32 s87, 0\n"
    "PD\n"
    "s_waitcnt vmcnt(0)\n"
    "s_cbranch_scc0 L_S_pc\n"
    "PD\n"
    "s_mov_b64 vcc, 0\n"
    "PD\n"
    "L_S_pd:\n"
    "PD\n"
    "s_cmp_eq_u32 s31, 0\n"
    "PD\n"
    "s_cbranch_scc1 S_U\n"
    "PD\n"
    "buffer_load_dwordx4 a[128:131], v20, s[40:43], s56 offen offset:0 sc1\n"
    "PD\n"
    "buffer_load_dwordx4 a[144:147], v20, s[40:43], s56 offen offset:1024 sc1\n"
    "PD\n"
    "buffer_load_dwordx4 a[132:135], v21, s[40:43], s56 offen offset:0 sc1\n"
    "PD\n"
    "buffer_load_dwordx4 a[148:151], v21, s[40:43], s56 offen offset:1024 sc1\n"
    "PD\n"
    "buffer_load_dwordx4 a[136:139], v22, s[40:43], s56 offen offset:0 sc1\n"
    "PD\n"
    "buffer_load_dwordx4 a[152:155], v22, s[40:43], s56 offen offset:1024 sc1\n"
    "PD\n"
    "buffer_load_dwordx4 a[140:143], v23, s[40:43], s56 offen offset:0 sc1\n"
    "PD\n"
    "buffer_load_dwordx4 a[156:159], v23, s[40:43], s56 offen offset:1024 sc1\n"
    "PD\n"
    "s_add_u32 s56, s56, s69\n"
    "PD\n"
    "buffer_load_dwordx4 a[160:163], v20, s[40:43], s56 offen offset:0 sc1\n"
    "PD\n"
    "buffer_load_dwordx4 a[176:179], v20, s[40:43], s56 offen offset:1024 sc1\n"
    "PD\n"
    "buffer_load_dwordx4 a[164:167], v21, s[40:43], s56 offen offset:0 sc1\n"
    "PD\n"
    "buffer_load_dwordx4 a[180:183], v21, s[40:43], s56 offen offset:1024 sc1\n"
    "PD\n"
    "buffer_load_dwordx4 a[168:171], v22, s[40:43], s56 offen offset:0 sc1\n"
    "PD\n"
    "buffer_load_dwordx4 a[184:187], v22, s[40:43], s56 offen offset:1024 sc1\n"
    "PD\n"
    "buffer_load_dwordx4 a[172:175], v23, s[40:43], s56 offen offset:0 sc1\n"
    "PD\n"
    "buffer_load_dwordx4 a[188:191], v23, s[40:43], s56 offen offset:1024 sc1\n"
    "PD\n"
    "s_add_u32 s56, s56, s69\n"
    "PD\n"
    "buffer_load_dwordx4 a[192:195], v20, s[40:43], s56 offen offset:0 sc1\n"
    "PD\n"
    "buffer_load_dwordx4 a[208:211], v20, s[40:43], s56 offen offset:1024 sc1\n"
    "PD\n"
    "buffer_load_dwordx4 a[196:199], v21, s[40:43], s56 offen offset:0 sc1\n"
    "PD\n"
    "buffer_load_dwordx4 a[212:215], v21, s[40:43], s56 offen offset:1024 sc1\n"
    "PD\n"
    "buffer_load_dwordx4 a[200:203], v22, s[40:43], s56 offen offset:0 sc1\n"
    "PD\n"
    "buffer_load_dwordx4 a[216:219], v22, s[40:43], s56 offen offset:1024 sc1\n"
    "PD\n"
    "buffer_load_dwordx4 a[204:207], v23, s[40:43], s56 offen offset:0 sc1\n"
    "PD\n"
    "buffer_load_dwordx4 a[220:223], v23, s[40:43], s56 offen offset:1024 sc1\n"
    "PD\n"
    "s_branch E_A\n"
    "PD\n"
    "S_U:\n"
    "PD\n"
    "buffer_load_dwordx4 a[160:163], v20, s[40:43], s56 offen offset:0 sc1\n"
    "PD\n"
    "buffer_load_dwordx4 a[176:179], v20, s[40:43], s56 offen offset:1024 sc1\n"
    "PD\n"
    "buffer_load_dwordx4 a[164:167], v21, s[40:43], s56 offen offset:0 sc1\n"
    "PD\n"
    "buffer_load_dwordx4 a[180:183], v21, s[40:43], s56 offen offset:1024 sc1\n"
    "PD\n"
    "buffer_load_dwordx4 a[168:171], v22, s[40:43], s56 offen offset:0 sc1\n"
    "PD\n"
    "buffer_load_dwordx4 a[184:187], v22, s[40:43], s56 offen offset:1024 sc1\n"
    "PD\n"
    "buffer_load_dwordx4 a[172:175], v23, s[40:43], s56 offen offset:0 sc1\n"
    "PD\n"
    "buffer_load_dwordx4 a[188:191], v23, s[40:43], s56 offen offset:1024 sc1\n"
    "PD\n"
    "E_U:\n"
    "PP\n"
    "s_mov_b32 s37, 0\n"
    "PD\n"
    "s_xor_b32 s60, s61, 0x10000\n"
    "PD\n"
    "s_movk_i32 s56, 8192\n"
    "PD\n"
    "s_mov_b32 s38, 7\n"
    "PD\n"
    "s_mov_b32 s31, s99\n"
    "PD\n"
    "s_mov_b32 s99, 2\n"
    "PD\n"
    "s_waitcnt vmcnt(0) lgkmcnt(4)\n"
    "L_U:\n"
    "PD\n"
    "v_mfma_f32_16x16x32_bf16 v[128:131], a[224:227], a[160:163], v[128:131]\n"
    "PD\n"
    "ds_read_b64_tr_b16 a[240:241], v10 offset:0\n"
    "v_mfma_f32_16x16x32_bf16 v[128:131], a[228:231], a[176:179], v[128:131]\n"
    "PD\n"
    "buffer_load_dwordx4 a[192:195], v20, s[40:43], s56 offen offset:0 sc1\n"
    "v_mfma_f32_16x16x32_bf16 v[132:135], a[228:231], a[180:183], v[132:135]\n"
    "PD\n"
    "ds_read_b64_tr_b16 a[242:243], v10 offset:4096\n"
    "v_mfma_f32_16x16x32_bf16 v[132:135], a[224:227], a[164:167], v[132:135]\n"
    "PD\n"
    "v_mfma_f32_16x16x32_bf16 v[136:139], a[224:227], a[168:171], v[136:139]\n"
    "PD\n"
    "ds_read_b64_tr_b16 a[244:245], v10 offset:8192\n"
    "v_mfma_f32_16x16x32_bf16 v[136:139], a[228:231], a[184:187], v[136:139]\n"
    "PD\n"
    "buffer_load_dwordx4 a[208:211], v20, s[40:43], s56 offen offset:1024 sc1\n"
    "v_mfma_f32_16x16x32_bf16 v[140:143], a[228:231], a[188:191], v[140:143]\n"
    "PD\n"
    "ds_read_b64_tr_b16 a[246:247], v10 offset:12288\n"
    "v_mfma_f32_16x16x32_bf16 v[140:143], a[224:227], a[172:175], v[140:143]\n"
    "PD\n"
    "s_waitcnt lgkmcnt(4)\n"
    "v_mfma_f32_16x16x32_bf16 v[156:159], a[232:235], a[172:175], v[156:159]\n"
    "PD\n"
    "ds_read_b64_tr_b16 a[248:249], v11 offset:0\n"
    "v_mfma_f32_16x16x32_bf16 v[156:159], a[236:239], a[188:191], v[156:159]\n"
    "PD\n"
    "buffer_load_dwordx4 a[196:199], v21, s[40:43], s56 offen offset:0 sc1\n"
    "v_mfma_f32_16x16x32_bf16 v[152:155], a[236:239], a[184:187], v[152:155]\n"
    "PD\n"
    "ds_read_b64_tr_b16 a[250:251], v11 offset:4096\n"
    "v_mfma_f32_16x16x32_bf16 v[152:155], a[232:235], a[168:171], v[152:155]\n"
    "PD\n"
    "v_mfma_f32_16x16x32_bf16 v[148:151], a[232:235], a[164:167], v[148:151]\n"
    "PD\n"
    "ds_read_b64_tr_b16 a[252:253], v11 offset:8192\n"
    "v_mfma_f32_16x16x32_bf16 v[148:151], a[236:239], a[180:183], v[148:151]\n"
    "PD\n"
    "buffer_load_dwordx4 a[212:215], v21, s[40:43], s56 offen offset:1024 sc1\n"
    "v_mfma_f32_16x16x32_bf16 v[144:147], a[236:239], a[176:179], v[144:147]\n"
    "PD\n"
    "ds_read_b64_tr_b16 a[254:255], v11 offset:12288\n"
    "v_mfma_f32_16x16x32_bf16 v[144:147], a[232:235], a[160:163], v[144:147]\n"
    "PD\n"
    "s_waitcnt lgkmcnt(4)\n"
    "v_mfma_f32_16x16x32_bf16 v[160:163], a[240:243], a[160:163], v[160:163]\n"
    "PD\n"
    "ds_read_b64_tr_b16 a[224:225], v12 offset:0\n"
    "v_mfma_f32_16x16x32_bf16 v[160:163], a[244:247], a[176:179], v[160:163]\n"
    "PD\n"
    "buffer_load_dwordx4 a[200:203], v22, s[40:43], s56 offen offset:0 sc1\n"
    "v_mfma_f32_16x16x32_bf16 v[164:167], a[244:247], a[180:183], v[164:167]\n"
    "PD\n"
    "ds_read_b64_tr_b16 a[226:227], v12 offset:4096\n"
    "v_mfma_f32_16x16x32_bf16 v[164:167], a[240:243], a[164:167], v[164:167]\n"
    "PD\n"
    "v_mfma_f32_16x16x32_bf16 v[168:171], a[240:243], a[168:171], v[168:171]\n"
    "PD\n"
    "ds_read_b64_tr_b16 a[228:229], v12 offset:8192\n"
    "v_mfma_f32_16x16x32_bf16 v[168:171], a[244:247], a[184:187], v[168:171]\n"
    "PD\n"
    "buffer_load_dwordx4 a[216:219], v22, s[40:43], s56 offen offset:1024 sc1\n"
    "v_mfma_f32_16x16x32_bf16 v[172:175], a[244:247], a[188:191], v[172:175]\n"
    "PD\n"
    "ds_read_b64_tr_b16 a[230:231], v12 offset:12288\n"
    "v_mfma_f32_16x16x32_bf16 v[172:175], a[240:243], a[172:175], v[172:175]\n"
    "PD\n"
    "s_waitcnt lgkmcnt(4)\n"
    "v_mfma_f32_16x16x32_bf16 v[188:191], a[248:251], a[172:175], v[188:191]\n"
    "PD\n"
    "ds_read_b64_tr_b16 a[232:233], v13 offset:0\n"
    "v_mfma_f32_16x16x32_bf16 v[188:191], a[252:255], a[188:191], v[188:191]\n"
    "PD\n"
    "buffer_load_dwordx4 a[204:207], v23, s[40:43], s56 offen offset:0 sc1\n"
    "v_mfma_f32_16x16x32_bf16 v[184:187], a[252:255], a[184:187], v[184:187]\n"
    "PD\n"
    "ds_read_b64_tr_b16 a[234:235], v13 offset:4096\n"
    "v_mfma_f32_16x16x32_bf16 v[184:187], a[248:251], a[168:171], v[184:187]\n"
    "PD\n"
    "v_mfma_f32_16x16x32_bf16 v[180:183], a[248:251], a[164:167], v[180:183]\n"
    "PD\n"
    "ds_read_b64_tr_b16 a[236:237], v13 offset:8192\n"
    "v_mfma_f32_16x16x32_bf16 v[180:183], a[252:255], a[180:183], v[180:183]\n"
    "PD\n"
    "buffer_load_dwordx4 a[220:223], v23, s[40:43], s56 offen offset:1024 sc1\n"
    "v_mfma_f32_16x16x32_bf16 v[176:179], a[252:255], a[176:179], v[176:179]\n"
    "PD\n"
    "ds_read_b64_tr_b16 a[238:239], v13 offset:12288\n"
    "v_mfma_f32_16x16x32_bf16 v[176:179], a[248:251], a[160:163], v[176:179]\n"
    "PD\n"
    "s_waitcnt lgkmcnt(4)\n"
    "v_mfma_f32_16x16x32_bf16 v[192:195], a[224:227], a[160:163], v[192:195]\n"
    "PD\n"
    "ds_read_b64_tr_b16 a[240:241], v14 offset:0\n"
    "v_mfma_f32_16x16x32_bf16 v[192:195], a[228:231], a[176:179], v[192:195]\n"
    "PD\n"
    "s_add_u32 s56, s56, s69\n"
    "s_add_u32 m0, s60, 16384\n"
    "v_mfma_f32_16x16x32_bf16 v[196:199], a[228:231], a[180:183], v[196:199]\n"
    "PD\n"
    "ds_read_b64_tr_b16 a[242:243], v14 offset:4096\n"
    "v_mfma_f32_16x16x32_bf16 v[196:199], a[224:227], a[164:167], v[196:199]\n"
    "PD\n"
    "v_mfma_f32_16x16x32_bf16 v[200:203], a[224:227], a[168:171], v[200:203]\n"
    "PD\n"
    "ds_read_b64_tr_b16 a[244:245], v14 offset:8192\n"
    "v_mfma_f32_16x16x32_bf16 v[200:203], a[228:231], a[184:187], v[200:203]\n"
    "PD\n"
    "buffer_load_dwordx4 v28, s[48:51], s58 offen offset:0 lds\n"
    "v_mfma_f32_16x16x32_bf16 v[204:207], a[228:231], a[188:191], v[204:207]\n"
    "PD\n"
    "ds_read_b64_tr_b16 a[246:247], v14 offset:12288\n"
    "v_mfma_f32_16x16x32_bf16 v[204:207], a[224:227], a[172:175], v[204:207]\n"
    "PD\n"
    "s_waitcnt lgkmcnt(4)\n"
    "v_mfma_f32_16x16x32_bf16 v[220:223], a[232:235], a[172:175], v[220:223]\n"
    "PD\n"
    "ds_read_b64_tr_b16 a[248:249], v15 offset:0\n"
    "v_mfma_f32_16x16x32_bf16 v[220:223], a[236:239], a[188:191], v[220:223]\n"
    "PD\n"
    "buffer_load_dwordx4 v29, s[48:51], s58 offen offset:1024 lds\n"
    "v_mfma_f32_16x16x32_bf16 v[216:219], a[236:239], a[184:187], v[216:219]\n"
    "PD\n"
    "ds_read_b64_tr_b16 a[250:251], v15 offset:4096\n"
    "v_mfma_f32_16x16x32_bf16 v[216:219], a[232:235], a[168:171], v[216:219]\n"
    "PD\n"
    "v_mfma_f32_16x16x32_bf16 v[212:215], a[232:235], a[164:167], v[212:215]\n"
    "PD\n"
    "ds_read_b64_tr_b16 a[252:253], v15 offset:8192\n"
    "v_mfma_f32_16x16x32_bf16 v[212:215], a[236:239], a[180:183], v[212:215]\n"
    "PD\n"
    "v_mfma_f32_16x16x32_bf16 v[208:211], a[236:239], a[176:179], v[208:211]\n"
    "PD\n"
    "ds_read_b64_tr_b16 a[254:255], v15 offset:12288\n"
    "v_mfma_f32_16x16x32_bf16 v[208:211], a[232:235], a[160:163], v[208:211]\n"
    "PD\n"
    "s_waitcnt lgkmcnt(4)\n"
    "v_mfma_f32_16x16x32_bf16 v[224:227], a[240:243], a[160:163], v[224:227]\n"
    "PD\n"
    "ds_read_b64_tr_b16 a[224:225], v8 offset:16384\n"
    "v_mfma_f32_16x16x32_bf16 v[224:227], a[244:247], a[176:179], v[224:227]\n"
    "PD\n"
    "buffer_load_dwordx4 v30, s[48:51], s58 offen offset:2048 lds\n"
    "v_mfma_f32_16x16x32_bf16 v[228:231], a[244:247], a[180:183], v[228:231]\n"
    "PD\n"
    "ds_read_b64_tr_b16 a[226:227], v8 offset:20480\n"
    "v_mfma_f32_16x16x32_bf16 v[228:231], a[240:243], a[164:167], v[228:231]\n"
    "PD\n"
    "v_mfma_f32_16x16x32_bf16 v[232:235], a[240:243], a[168:171], v[232:235]\n"
    "PD\n"
    "ds_read_b64_tr_b16 a[228:229], v8 offset:24576\n"
    "v_mfma_f32_16x16x32_bf16 v[232:235], a[244:247], a[184:187], v[232:235]\n"
    "PD\n"
    "v_mfma_f32_16x16x32_bf16 v[236:239], a[244:247], a[188:191], v[236:239]\n"
    "PD\n"
    "ds_read_b64_tr_b16 a[230:231], v8 offset:28672\n"
    "v_mfma_f32_16x16x32_bf16 v[236:239], a[240:243], a[172:175], v[236:239]\n"
    "PD\n"
    "s_waitcnt lgkmcnt(4)\n"
    "v_mfma_f32_16x16x32_bf16 v[252:255], a[248:251], a[172:175], v[252:255]\n"
    "PD\n"
    "ds_read_b64_tr_b16 a[232:233], v9 offset:16384\n"
    "v_mfma_f32_16x16x32_bf16 v[252:255], a[252:255], a[188:191], v[252:255]\n"
    "PD\n"
    "buffer_load_dwordx4 v31, s[48:51], s58 offen offset:3072 lds\n"
    "v_mfma_f32_16x16x32_bf16 v[248:251], a[252:255], a[184:187], v[248:251]\n"
    "PD\n"
    "ds_read_b64_tr_b16 a[234:235], v9 offset:20480\n"
    "v_mfma_f32_16x16x32_bf16 v[248:251], a[248:251], a[168:171], v[248:251]\n"
    "PD\n"
    "s_add_u32 s58, s58, s59\n"
    "v_mfma_f32_16x16x32_bf16 v[244:247], a[248:251], a[164:167], v[244:247]\n"
    "PD\n"
    "ds_read_b64_tr_b16 a[236:237], v9 offset:24576\n"
    "v_mfma_f32_16x16x32_bf16 v[244:247], a[252:255], a[180:183], v[244:247]\n"
    "PD\n"
    "v_mfma_f32_16x16x32_bf16 v[240:243], a[252:255], a[176:179], v[240:243]\n"
    "PD\n"
    "ds_read_b64_tr_b16 a[238:239], v9 offset:28672\n"
    "v_mfma_f32_16x16x32_bf16 v[240:243], a[248:251], a[160:163], v[240:243]\n"
    "PD\n"
    "s_waitcnt lgkmcnt(4)\n"
    "v_mfma_f32_16x16x32_bf16 a[0:3], a[224:227], a[160:163], a[0:3]\n"
    "PD\n"
    "ds_read_b64_tr_b16 a[240:241], v10 offset:16384\n"
    "v_mfma_f32_16x16x32_bf16 a[0:3], a[228:231], a[176:179], a[0:3]\n"
    "PD\n"
    "v_mfma_f32_16x16x32_bf16 a[4:7], a[228:231], a[180:183], a[4:7]\n"
    "PD\n"
    "ds_read_b64_tr_b16 a[242:243], v10 offset:20480\n"
    "v_mfma_f32_16x16x32_bf16 a[4:7], a[224:227], a[164:167], a[4:7]\n"
    "PD\n"
    "s_add_u32 m0, s60, 32768\n"
    "v_mfma_f32_16x16x32_bf16 a[8:11], a[224:227], a[168:171], a[8:11]\n"
    "PD\n"
    "ds_read_b64_tr_b16 a[244:245], v10 offset:24576\n"
    "v_mfma_f32_16x16x32_bf16 a[8:11], a[228:231], a[184:187], a[8:11]\n"
    "PD\n"
    "buffer_load_dwordx4 v24, s[44:47], s57 offen offset:0 lds\n"
    "v_mfma_f32_16x16x32_bf16 a[12:15], a[228:231], a[188:191], a[12:15]\n"
    "PD\n"
    "ds_read_b64_tr_b16 a[246:247], v10 offset:28672\n"
    "v_mfma_f32_16x16x32_bf16 a[12:15], a[224:227], a[172:175], a[12:15]\n"
    "PD\n"
    "s_waitcnt lgkmcnt(4)\n"
    "v_mfma_f32_16x16x32_bf16 a[28:31], a[232:235], a[172:175], a[28:31]\n"
    "PD\n"
    "ds_read_b64_tr_b16 a[248:249], v11 offset:16384\n"
    "v_mfma_f32_16x16x32_bf16 a[28:31], a[236:239], a[188:191], a[28:31]\n"
    "PD\n"
    "v_mfma_f32_16x16x32_bf16 a[24:27], a[236:239], a[184:187], a[24:27]\n"
    "PD\n"
    "ds_read_b64_tr_b16 a[250:251], v11 offset:20480\n"
    "v_mfma_f32_16x16x32_bf16 a[24:27], a[232:235], a[168:171], a[24:27]\n"
    "PD\n"
    "v_mfma_f32_16x16x32_bf16 a[20:23], a[232:235], a[164:167], a[20:23]\n"
    "PD\n"
    "ds_read_b64_tr_b16 a[252:253], v11 offset:24576\n"
    "v_mfma_f32_16x16x32_bf16 a[20:23], a[236:239], a[180:183], a[20:23]\n"
    "PD\n"
    "buffer_load_dwordx4 v25, s[44:47], s57 offen offset:1024 lds\n"
    "v_mfma_f32_16x16x32_bf16 a[16:19], a[236:239], a[176:179], a[16:19]\n"
    "PD\n"
    "ds_read_b64_tr_b16 a[254:255], v11 offset:28672\n"
    "v_mfma_f32_16x16x32_bf16 a[16:19], a[232:235], a[160:163], a[16:19]\n"
    "PD\n"
    "s_waitcnt lgkmcnt(4)\n"
    "v_mfma_f32_16x16x32_bf16 a[32:35], a[240:243], a[160:163], a[32:35]\n"
    "PD\n"
    "ds_read_b64_tr_b16 a[224:225], v12 offset:16384\n"
    "v_mfma_f32_16x16x32_bf16 a[32:35], a[244:247], a[176:179], a[32:35]\n"
    "PD\n"
    "v_mfma_f32_16x16x32_bf16 a[36:39], a[244:247], a[180:183], a[36:39]\n"
    "PD\n"
    "ds_read_b64_tr_b16 a[226:227], v12 offset:20480\n"
    "v_mfma_f32_16x16x32_bf16 a[36:39], a[240:243], a[164:167], a[36:39]\n"
    "PD\n"
    "v_mfma_f32_16x16x32_bf16 a[40:43], a[240:243], a[168:171], a[40:43]\n"
    "PD\n"
    "ds_read_b64_tr_b16 a[228:229], v12 offset:24576\n"
    "v_mfma_f32_16x16x32_bf16 a[40:43], a[244:247], a[184:187], a[40:43]\n"
    "PD\n"
    "buffer_load_dwordx4 v26, s[44:47], s57 offen offset:2048 lds\n"
    "v_mfma_f32_16x16x32_bf16 a[44:47], a[244:247], a[188:191], a[44:47]\n"
    "PD\n"
    "ds_read_b64_tr_b16 a[230:231], v12 offset:28672\n"
    "v_mfma_f32_16x16x32_bf16 a[44:47], a[240:243], a[172:175], a[44:47]\n"
    "PD\n"
    "s_waitcnt lgkmcnt(4)\n"
    "v_mfma_f32_16x16x32_bf16 a[60:63], a[248:251], a[172:175], a[60:63]\n"
    "PD\n"
    "ds_read_b64_tr_b16 a[232:233], v13 offset:16384\n"
    "v_mfma_f32_16x16x32_bf16 a[60:63], a[252:255], a[188:191], a[60:63]\n"
    "PD\n"
    "v_mfma_f32_16x16x32_bf16 a[56:59], a[252:255], a[184:187], a[56:59]\n"
    "PD\n"
    "ds_read_b64_tr_b16 a[234:235], v13 offset:20480\n"
    "v_mfma_f32_16x16x32_bf16 a[56:59], a[248:251], a[168:171], a[56:59]\n"
    "PD\n"
    "v_mfma_f32_16x16x32_bf16 a[52:55], a[248:251], a[164:167], a[52:55]\n"
    "PD\n"
    "ds_read_b64_tr_b16 a[236:237], v13 offset:24576\n"
    "v_mfma_f32_16x16x32_bf16 a[52:55], a[252:255], a[180:183], a[52:55]\n"
    "PD\n"
    "buffer_load_dwordx4 v27, s[44:47], s57 offen offset:3072 lds\n"
    "v_mfma_f32_16x16x32_bf16 a[48:51], a[252:255], a[176:179], a[48:51]\n"
    "PD\n"
    "ds_read_b64_tr_b16 a[238:239], v13 offset:28672\n"
    "v_mfma_f32_16x16x32_bf16 a[48:51], a[248:251], a[160:163], a[48:51]\n"
    "PD\n"
    "s_waitcnt lgkmcnt(4)\n"
    "v_mfma_f32_16x16x32_bf16 a[64:67], a[224:227], a[160:163], a[64:67]\n"
    "PD\n"
    "ds_read_b64_tr_b16 a[240:241], v14 offset:16384\n"
    "v_mfma_f32_16x16x32_bf16 a[64:67], a[228:231], a[176:179], a[64:67]\n"
    "PD\n"
    "s_add_u32 s57, s57, s59\n"
    "v_mfma_f32_16x16x32_bf16 a[68:71], a[228:231], a[180:183], a[68:71]\n"
    "PD\n"
    "ds_read_b64_tr_b16 a[242:243], v14 offset:20480\n"
    "v_mfma_f32_16x16x32_bf16 a[68:71], a[224:227], a[164:167], a[68:71]\n"
    "PD\n"
    "v_mfma_f32_16x16x32_bf16 a[72:75], a[224:227], a[168:171], a[72:75]\n"
    "PD\n"
    "ds_read_b64_tr_b16 a[244:245], v14 offset:24576\n"
    "v_mfma_f32_16x16x32_bf16 a[72:75], a[228:231], a[184:187], a[72:75]\n"
    "PD\n"
    "v_mfma_f32_16x16x32_bf16 a[76:79], a[228:231], a[188:191], a[76:79]\n"
    "PD\n"
    "ds_read_b64_tr_b16 a[246:247], v14 offset:28672\n"
    "v_mfma_f32_16x16x32_bf16 a[76:79], a[224:227], a[172:175], a[76:79]\n"
    "PD\n"
    "s_waitcnt lgkmcnt(4)\n"
    "v_mfma_f32_16x16x32_bf16 a[92:95], a[232:235], a[172:175], a[92:95]\n"
    "PD\n"
    "ds_read_b64_tr_b16 a[248:249], v15 offset:16384\n"
    "v_mfma_f32_16x16x32_bf16 a[92:95], a[236:239], a[188:191], a[92:95]\n"
    "PD\n"
    "v_mfma_f32_16x16x32_bf16 a[88:91], a[236:239], a[184:187], a[88:91]\n"
    "PD\n"
    "ds_read_b64_tr_b16 a[250:251], v15 offset:20480\n"
    "v_mfma_f32_16x16x32_bf16 a[88:91], a[232:235], a[168:171], a[88:91]\n"
    "PD\n"
    "v_mfma_f32_16x16x32_bf16 a[84:87], a[232:235], a[164:167], a[84:87]\n"
    "PD\n"
    "ds_read_b64_tr_b16 a[252:253], v15 offset:24576\n"
    "v_mfma_f32_16x16x32_bf16 a[84:87], a[236:239], a[180:183], a[84:87]\n"
    "PD\n"
    "v_mfma_f32_16x16x32_bf16 a[80:83], a[236:239], a[176:179], a[80:83]\n"
    "PD\n"
    "ds_read_b64_tr_b16 a[254:255], v15 offset:28672\n"
    "v_mfma_f32_16x16x32_bf16 a[80:83], a[232:235], a[160:163], a[80:83]\n"
    "PD\n"
    "s_waitcnt lgkmcnt(4)\n"
    "s_waitcnt vmcnt(20)\n"
    "s_barrier\n"
    "v_mfma_f32_16x16x32_bf16 a[96:99], a[240:243], a[160:163], a[96:99]\n"
    "PD\n"
    "ds_read_b64_tr_b16 a[224:225], v8 offset:32768\n"
    "v_mfma_f32_16x16x32_bf16 a[96:99], a[244:247], a[176:179], a[96:99]\n"
    "PD\n"
    "v_mfma_f32_16x16x32_bf16 a[100:103], a[244:247], a[180:183], a[100:103]\n"
    "PD\n"
    "ds_read_b64_tr_b16 a[226:227], v8 offset:36864\n"
    "v_mfma_f32_16x16x32_bf16 a[100:103], a[240:243], a[164:167], a[100:103]\n"
    "PD\n"
    "v_mfma_f32_16x16x32_bf16 a[104:107], a[240:243], a[168:171], a[104:107]\n"
    "PD\n"
    "ds_read_b64_tr_b16 a[228:229], v8 offset:40960\n"
    "v_mfma_f32_16x16x32_bf16 a[104:107], a[244:247], a[184:187], a[104:107]\n"
    "PD\n"
    "v_mfma_f32_16x16x32_bf16 a[108:111], a[244:247], a[188:191], a[108:111]\n"
    "PD\n"
    "ds_read_b64_tr_b16 a[230:231], v8 offset:45056\n"
    "v_mfma_f32_16x16x32_bf16 a[108:111], a[240:243], a[172:175], a[108:111]\n"
    "PD\n"
    "s_waitcnt lgkmcnt(4)\n"
    "v_mfma_f32_16x16x32_bf16 a[124:127], a[248:251], a[172:175], a[124:127]\n"
    "PD\n"
    "ds_read_b64_tr_b16 a[232:233], v9 offset:32768\n"
    "v_mfma_f32_16x16x32_bf16 a[124:127], a[252:255], a[188:191], a[124:127]\n"
    "PD\n"
    "v_mfma_f32_16x16x32_bf16 a[120:123], a[252:255], a[184:187], a[120:123]\n"
    "PD\n"
    "ds_read_b64_tr_b16 a[234:235], v9 offset:36864\n"
    "v_mfma_f32_16x16x32_bf16 a[120:123], a[248:251], a[168:171], a[120:123]\n"
    "PD\n"
    "v_mfma_f32_16x16x32_bf16 a[116:119], a[248:251], a[164:167], a[116:119]\n"
    "PD\n"
    "ds_read_b64_tr_b16 a[236:237], v9 offset:40960\n"
    "v_mfma_f32_16x16x32_bf16 a[116:119], a[252:255], a[180:183], a[116:119]\n"
    "PD\n"
    "v_mfma_f32_16x16x32_bf16 a[112:115], a[252:255], a[176:179], a[112:115]\n"
    "PD\n"
    "ds_read_b64_tr_b16 a[238:239], v9 offset:45056\n"
    "v_mfma_f32_16x16x32_bf16 a[112:115], a[248:251], a[160:163], a[112:115]\n"
    "PD\n"
    "s_waitcnt vmcnt(8) lgkmcnt(4)\n"
    "v_mfma_f32_16x16x32_bf16 v[128:131], a[224:227], a[192:195], v[128:131]\n"
    "PD\n"
    "ds_read_b64_tr_b16 a[240:241], v10 offset:32768\n"
    "s_bitcmp1_b32 2, s37\n"
    "v_mfma_f32_16x16x32_bf16 v[128:131], a[228:231], a[208:211], v[128:131]\n"
    "PD\n"
    "s_cbranch_scc1 H_Um\n"
    "R_Um:\n"
    "buffer_load_dwordx4 a[160:163], v20, s[40:43], s56 offen offset:0 sc1\n"
    "v_mfma_f32_16x16x32_bf16 v[132:135], a[228:231], a[212:215], v[132:135]\n"
    "PD\n"
    "ds_read_b64_tr_b16 a[242:243], v10 offset:36864\n"
    "v_mfma_f32_16x16x32_bf16 v[132:135], a[224:227], a[196:199], v[132:135]\n"
    "PD\n"
    "v_mfma_f32_16x16x32_bf16 v[136:139], a[224:227], a[200:203], v[136:139]\n"
    "PD\n"
    "ds_read_b64_tr_b16 a[244:245], v10 offset:40960\n"
    "v_mfma_f32_16x16x32_bf16 v[136:139], a[228:231], a[216:219], v[136:139]\n"
    "PD\n"
    "buffer_load_dwordx4 a[176:179], v20, s[40:43], s56 offen offset:1024 sc1\n"
    "v_mfma_f32_16x16x32_bf16 v[140:143], a[228:231], a[220:223], v[140:143]\n"
    "PD\n"
    "ds_read_b64_tr_b16 a[246:247], v10 offset:45056\n"
    "v_mfma_f32_16x16x32_bf16 v[140:143], a[224:227], a[204:207], v[140:143]\n"
    "PD\n"
    "s_waitcnt lgkmcnt(4)\n"
    "v_mfma_f32_16x16x32_bf16 v[156:159], a[232:235], a[204:207], v[156:159]\n"
    "PD\n"
    "ds_read_b64_tr_b16 a[248:249], v11 offset:32768\n"
    "v_mfma_f32_16x16x32_bf16 v[156:159], a[236:239], a[220:223], v[156:159]\n"
    "PD\n"
    "buffer_load_dwordx4 a[164:167], v21, s[40:43], s56 offen offset:0 sc1\n"
    "v_mfma_f32_16x16x32_bf16 v[152:155], a[236:239], a[216:219], v[152:155]\n"
    "PD\n"
    "ds_read_b64_tr_b16 a[250:251], v11 offset:36864\n"
    "v_mfma_f32_16x16x32_bf16 v[152:155], a[232:235], a[200:203], v[152:155]\n"
    "PD\n"
    "v_mfma_f32_16x16x32_bf16 v[148:151], a[232:235], a[196:199], v[148:151]\n"
    "PD\n"
    "ds_read_b64_tr_b16 a[252:253], v11 offset:40960\n"
    "v_mfma_f32_16x16x32_bf16 v[148:151], a[236:239], a[212:215], v[148:151]\n"
    "PD\n"
    "buffer_load_dwordx4 a[180:183], v21, s[40:43], s56 offen offset:1024 sc1\n"
    "v_mfma_f32_16x16x32_bf16 v[144:147], a[236:239], a[208:211], v[144:147]\n"
    "PD\n"
    "ds_read_b64_tr_b16 a[254:255], v11 offset:45056\n"
    "v_mfma_f32_16x16x32_bf16 v[144:147], a[232:235], a[192:195], v[144:147]\n"
    "PD\n"
    "s_waitcnt lgkmcnt(4)\n"
    "v_mfma_f32_16x16x32_bf16 v[160:163], a[240:243], a[192:195], v[160:163]\n"
    "PD\n"
    "ds_read_b64_tr_b16 a[224:225], v12 offset:32768\n"
    "v_mfma_f32_16x16x32_bf16 v[160:163], a[244:247], a[208:211], v[160:163]\n"
    "PD\n"
    "buffer_load_dwordx4 a[168:171], v22, s[40:43], s56 offen offset:0 sc1\n"
    "v_mfma_f32_16x16x32_bf16 v[164:167], a[244:247], a[212:215], v[164:167]\n"
    "PD\n"
    "ds_read_b64_tr_b16 a[226:227], v12 offset:36864\n"
    "v_mfma_f32_16x16x32_bf16 v[164:167], a[240:243], a[196:199], v[164:167]\n"
    "PD\n"
    "v_mfma_f32_16x16x32_bf16 v[168:171], a[240:243], a[200:203], v[168:171]\n"
    "PD\n"
    "ds_read_b64_tr_b16 a[228:229], v12 offset:40960\n"
    "v_mfma_f32_16x16x32_bf16 v[168:171], a[244:247], a[216:219], v[168:171]\n"
    "PD\n"
    "buffer_load_dwordx4 a[184:187], v22, s[40:43], s56 offen offset:1024 sc1\n"
    "v_mfma_f32_16x16x32_bf16 v[172:175], a[244:247], a[220:223], v[172:175]\n"
    "PD\n"
    "ds_read_b64_tr_b16 a[230:231], v12 offset:45056\n"
    "v_mfma_f32_16x16x32_bf16 v[172:175], a[240:243], a[204:207], v[172:175]\n"
    "PD\n"
    "s_waitcnt lgkmcnt(4)\n"
    "v_mfma_f32_16x16x32_bf16 v[188:191], a[248:251], a[204:207], v[188:191]\n"
    "PD\n"
    "ds_read_b64_tr_b16 a[232:233], v13 offset:32768\n"
    "v_mfma_f32_16x16x32_bf16 v[188:191], a[252:255], a[220:223], v[188:191]\n"
    "PD\n"
    "buffer_load_dwordx4 a[172:175], v23, s[40:43], s56 offen offset:0 sc1\n"
    "v_mfma_f32_16x16x32_bf16 v[184:187], a[252:255], a[216:219], v[184:187]\n"
    "PD\n"
    "ds_read_b64_tr_b16 a[234:235], v13 offset:36864\n"
    "v_mfma_f32_16x16x32_bf16 v[184:187], a[248:251], a[200:203], v[184:187]\n"
    "PD\n"
    "v_mfma_f32_16x16x32_bf16 v[180:183], a[248:251], a[196:199], v[180:183]\n"
    "PD\n"
    "ds_read_b64_tr_b16 a[236:237], v13 offset:40960\n"
    "v_mfma_f32_16x16x32_bf16 v[180:183], a[252:255], a[212:215], v[180:183]\n"
    "PD\n"
    "buffer_load_dwordx4 a[188:191], v23, s[40:43], s56 offen offset:1024 sc1\n"
    "v_mfma_f32_16x16x32_bf16 v[176:179], a[252:255], a[208:211], v[176:179]\n"
    "PD\n"
    "ds_read_b64_tr_b16 a[238:239], v13 offset:45056\n"
    "v_mfma_f32_16x16x32_bf16 v[176:179], a[248:251], a[192:195], v[176:179]\n"
    "PD\n"
    "s_waitcnt lgkmcnt(4)\n"
    "v_mfma_f32_16x16x32_bf16 v[192:195], a[224:227], a[192:195], v[192:195]\n"
    "PD\n"
    "ds_read_b64_tr_b16 a[240:241], v14 offset:32768\n"
    "v_mfma_f32_16x16x32_bf16 v[192:195], a[228:231], a[208:211], v[192:195]\n"
    "PD\n"
    "s_add_u32 s56, s56, s69\n"
    "s_add_u32 m0, s60, 49152\n"
    "v_mfma_f32_16x16x32_bf16 v[196:199], a[228:231], a[212:215], v[196:199]\n"
    "PD\n"
    "ds_read_b64_tr_b16 a[242:243], v14 offset:36864\n"
    "v_mfma_f32_16x16x32_bf16 v[196:199], a[224:227], a[196:199], v[196:199]\n"
    "PD\n"
    "v_mfma_f32_16x16x32_bf16 v[200:203], a[224:227], a[200:203], v[200:203]\n"
    "PD\n"
    "ds_read_b64_tr_b16 a[244:245], v14 offset:40960\n"
    "v_mfma_f32_16x16x32_bf16 v[200:203], a[228:231], a[216:219], v[200:203]\n"
    "PD\n"
    "buffer_load_dwordx4 v28, s[48:51], s58 offen offset:0 lds\n"
    "v_mfma_f32_16x16x32_bf16 v[204:207], a[228:231], a[220:223], v[204:207]\n"
    "PD\n"
    "ds_read_b64_tr_b16 a[246:247], v14 offset:45056\n"
    "v_mfma_f32_16x16x32_bf16 v[204:207], a[224:227], a[204:207], v[204:207]\n"
    "PD\n"
    "s_waitcnt lgkmcnt(4)\n"
    "v_mfma_f32_16x16x32_bf16 v[220:223], a[232:235], a[204:207], v[220:223]\n"
    "PD\n"
    "ds_read_b64_tr_b16 a[248:249], v15 offset:32768\n"
    "v_mfma_f32_16x16x32_bf16 v[220:223], a[236:239], a[220:223], v[220:223]\n"
    "PD\n"
    "buffer_load_dwordx4 v29, s[48:51], s58 offen offset:1024 lds\n"
    "v_mfma_f32_16x16x32_bf16 v[216:219], a[236:239], a[216:219], v[216:219]\n"
    "PD\n"
    "ds_read_b64_tr_b16 a[250:251], v15 offset:36864\n"
    "v_mfma_f32_16x16x32_bf16 v[216:219], a[232:235], a[200:203], v[216:219]\n"
    "PD\n"
    "v_mfma_f32_16x16x32_bf16 v[212:215], a[232:235], a[196:199], v[212:215]\n"
    "PD\n"
    "ds_read_b64_tr_b16 a[252:253], v15 offset:40960\n"
    "v_mfma_f32_16x16x32_bf16 v[212:215], a[236:239], a[212:215], v[212:215]\n"
    "PD\n"
    "v_mfma_f32_16x16x32_bf16 v[208:211], a[236:239], a[208:211], v[208:211]\n"
    "PD\n"
    "ds_read_b64_tr_b16 a[254:255], v15 offset:45056\n"
    "v_mfma_f32_16x16x32_bf16 v[208:211], a[232:235], a[192:195], v[208:211]\n"
    "PD\n"
    "s_waitcnt lgkmcnt(4)\n"
    "v_mfma_f32_16x16x32_bf16 v[224:227], a[240:243], a[192:195], v[224:227]\n"
    "PD\n"
    "ds_read_b64_tr_b16 a[224:225], v8 offset:49152\n"
    "v_mfma_f32_16x16x32_bf16 v[224:227], a[244:247], a[208:211], v[224:227]\n"
    "PD\n"
    "buffer_load_dwordx4 v30, s[48:51], s58 offen offset:2048 lds\n"
    "v_mfma_f32_16x16x32_bf16 v[228:231], a[244:247], a[212:215], v[228:231]\n"
    "PD\n"
    "ds_read_b64_tr_b16 a[226:227], v8 offset:53248\n"
    "v_mfma_f32_16x16x32_bf16 v[228:231], a[240:243], a[196:199], v[228:231]\n"
    "PD\n"
    "v_mfma_f32_16x16x32_bf16 v[232:235], a[240:243], a[200:203], v[232:235]\n"
    "PD\n"
    "ds_read_b64_tr_b16 a[228:229], v8 offset:57344\n"
    "v_mfma_f32_16x16x32_bf16 v[232:235], a[244:247], a[216:219], v[232:235]\n"
    "PD\n"
    "v_mfma_f32_16x16x32_bf16 v[236:239], a[244:247], a[220:223], v[236:239]\n"
    "PD\n"
    "ds_read_b64_tr_b16 a[230:231], v8 offset:61440\n"
    "v_xor_b32 v8, 0x10000, v8\n"
    "v_mfma_f32_16x16x32_bf16 v[236:239], a[240:243], a[204:207], v[236:239]\n"
    "PD\n"
    "s_waitcnt lgkmcnt(4)\n"
    "v_mfma_f32_16x16x32_bf16 v[252:255], a[248:251], a[204:207], v[252:255]\n"
    "PD\n"
    "ds_read_b64_tr_b16 a[232:233], v9 offset:49152\n"
    "v_mfma_f32_16x16x32_bf16 v[252:255], a[252:255], a[220:223], v[252:255]\n"
    "PD\n"
    "buffer_load_dwordx4 v31, s[48:51], s58 offen offset:3072 lds\n"
    "v_mfma_f32_16x16x32_bf16 v[248:251], a[252:255], a[216:219], v[248:251]\n"
    "PD\n"
    "ds_read_b64_tr_b16 a[234:235], v9 offset:53248\n"
    "v_mfma_f32_16x16x32_bf16 v[248:251], a[248:251], a[200:203], v[248:251]\n"
    "PD\n"
    "s_add_u32 s58, s58, s59\n"
    "v_mfma_f32_16x16x32_bf16 v[244:247], a[248:251], a[196:199], v[244:247]\n"
    "PD\n"
    "ds_read_b64_tr_b16 a[236:237], v9 offset:57344\n"
    "v_mfma_f32_16x16x32_bf16 v[244:247], a[252:255], a[212:215], v[244:247]\n"
    "PD\n"
    "v_mfma_f32_16x16x32_bf16 v[240:243], a[252:255], a[208:211], v[240:243]\n"
    "PD\n"
    "ds_read_b64_tr_b16 a[238:239], v9 offset:61440\n"
    "v_xor_b32 v9, 0x10000, v9\n"
    "v_mfma_f32_16x16x32_bf16 v[240:243], a[248:251], a[192:195], v[240:243]\n"
    "PD\n"
    "s_waitcnt lgkmcnt(4)\n"
    "v_mfma_f32_16x16x32_bf16 a[0:3], a[224:227], a[192:195], a[0:3]\n"
    "PD\n"
    "ds_read_b64_tr_b16 a[240:241], v10 offset:49152\n"
    "v_mfma_f32_16x16x32_bf16 a[0:3], a[228:231], a[208:211], a[0:3]\n"
    "PD\n"
    "v_mfma_f32_16x16x32_bf16 a[4:7], a[228:231], a[212:215], a[4:7]\n"
    "PD\n"
    "ds_read_b64_tr_b16 a[242:243], v10 offset:53248\n"
    "v_mfma_f32_16x16x32_bf16 a[4:7], a[224:227], a[196:199], a[4:7]\n"
    "PD\n"
    "s_xor_b32 m0, s60, 0x10000\n"
    "v_mfma_f32_16x16x32_bf16 a[8:11], a[224:227], a[200:203], a[8:11]\n"
    "PD\n"
    "ds_read_b64_tr_b16 a[244:245], v10 offset:57344\n"
    "v_mfma_f32_16x16x32_bf16 a[8:11], a[228:231], a[216:219], a[8:11]\n"
    "PD\n"
    "buffer_load_dwordx4 v24, s[44:47], s57 offen offset:0 lds\n"
    "v_mfma_f32_16x16x32_bf16 a[12:15], a[228:231], a[220:223], a[12:15]\n"
    "PD\n"
    "ds_read_b64_tr_b16 a[246:247], v10 offset:61440\n"
    "v_xor_b32 v10, 0x10000, v10\n"
    "v_mfma_f32_16x16x32_bf16 a[12:15], a[224:227], a[204:207], a[12:15]\n"
    "PD\n"
    "s_waitcnt lgkmcnt(4)\n"
    "v_mfma_f32_16x16x32_bf16 a[28:31], a[232:235], a[204:207], a[28:31]\n"
    "PD\n"
    "ds_read_b64_tr_b16 a[248:249], v11 offset:49152\n"
    "v_mfma_f32_16x16x32_bf16 a[28:31], a[236:239], a[220:223], a[28:31]\n"
    "PD\n"
    "v_mfma_f32_16x16x32_bf16 a[24:27], a[236:239], a[216:219], a[24:27]\n"
    "PD\n"
    "ds_read_b64_tr_b16 a[250:251], v11 offset:53248\n"
    "v_mfma_f32_16x16x32_bf16 a[24:27], a[232:235], a[200:203], a[24:27]\n"
    "PD\n"
    "v_mfma_f32_16x16x32_bf16 a[20:23], a[232:235], a[196:199], a[20:23]\n"
    "PD\n"
    "ds_read_b64_tr_b16 a[252:253], v11 offset:57344\n"
    "v_mfma_f32_16x16x32_bf16 a[20:23], a[236:239], a[212:215], a[20:23]\n"
    "PD\n"
    "buffer_load_dwordx4 v25, s[44:47], s57 offen offset:1024 lds\n"
    "v_mfma_f32_16x16x32_bf16 a[16:19], a[236:239], a[208:211], a[16:19]\n"
    "PD\n"
    "ds_read_b64_tr_b16 a[254:255], v11 offset:61440\n"
    "v_xor_b32 v11, 0x10000, v11\n"
    "v_mfma_f32_16x16x32_bf16 a[16:19], a[232:235], a[192:195], a[16:19]\n"
    "PD\n"
    "s_waitcnt lgkmcnt(4)\n"
    "v_mfma_f32_16x16x32_bf16 a[32:35], a[240:243], a[192:195], a[32:35]\n"
    "PD\n"
    "ds_read_b64_tr_b16 a[224:225], v12 offset:49152\n"
    "v_mfma_f32_16x16x32_bf16 a[32:35], a[244:247], a[208:211], a[32:35]\n"
    "PD\n"
    "v_mfma_f32_16x16x32_bf16 a[36:39], a[244:247], a[212:215], a[36:39]\n"
    "PD\n"
    "ds_read_b64_tr_b16 a[226:227], v12 offset:53248\n"
    "v_mfma_f32_16x16x32_bf16 a[36:39], a[240:243], a[196:199], a[36:39]\n"
    "PD\n"
    "v_mfma_f32_16x16x32_bf16 a[40:43], a[240:243], a[200:203], a[40:43]\n"
    "PD\n"
    "ds_read_b64_tr_b16 a[228:229], v12 offset:57344\n"
    "v_mfma_f32_16x16x32_bf16 a[40:43], a[244:247], a[216:219], a[40:43]\n"
    "PD\n"
    "buffer_load_dwordx4 v26, s[44:47], s57 offen offset:2048 lds\n"
    "v_mfma_f32_16x16x32_bf16 a[44:47], a[244:247], a[220:223], a[44:47]\n"
    "PD\n"
    "ds_read_b64_tr_b16 a[230:231], v12 offset:61440\n"
    "v_xor_b32 v12, 0x10000, v12\n"
    "v_mfma_f32_16x16x32_bf16 a[44:47], a[240:243], a[204:207], a[44:47]\n"
    "PD\n"
    "s_waitcnt lgkmcnt(4)\n"
    "v_mfma_f32_16x16x32_bf16 a[60:63], a[248:251], a[204:207], a[60:63]\n"
    "PD\n"
    "ds_read_b64_tr_b16 a[232:233], v13 offset:49152\n"
    "v_mfma_f32_16x16x32_bf16 a[60:63], a[252:255], a[220:223], a[60:63]\n"
    "PD\n"
    "v_mfma_f32_16x16x32_bf16 a[56:59], a[252:255], a[216:219], a[56:59]\n"
    "PD\n"
    "ds_read_b64_tr_b16 a[234:235], v13 offset:53248\n"
    "v_mfma_f32_16x16x32_bf16 a[56:59], a[248:251], a[200:203], a[56:59]\n"
    "PD\n"
    "v_mfma_f32_16x16x32_bf16 a[52:55], a[248:251], a[196:199], a[52:55]\n"
    "PD\n"
    "ds_read_b64_tr_b16 a[236:237], v13 offset:57344\n"
    "v_mfma_f32_16x16x32_bf16 a[52:55], a[252:255], a[212:215], a[52:55]\n"
    "PD\n"
    "buffer_load_dwordx4 v27, s[44:47], s57 offen offset:3072 lds\n"
    "v_mfma_f32_16x16x32_bf16 a[48:51], a[252:255], a[208:211], a[48:51]\n"
    "PD\n"
    "ds_read_b64_tr_b16 a[238:239], v13 offset:61440\n"
    "v_xor_b32 v13, 0x10000, v13\n"
    "v_mfma_f32_16x16x32_bf16 a[48:51], a[248:251], a[192:195], a[48:51]\n"
    "PD\n"
    "s_waitcnt lgkmcnt(4)\n"
    "v_mfma_f32_16x16x32_bf16 a[64:67], a[224:227], a[192:195], a[64:67]\n"
    "PD\n"
    "ds_read_b64_tr_b16 a[240:241], v14 offset:49152\n"
    "v_mfma_f32_16x16x32_bf16 a[64:67], a[228:231], a[208:211], a[64:67]\n"
    "PD\n"
    "s_add_u32 s57, s57, s59\n"
    "v_mfma_f32_16x16x32_bf16 a[68:71], a[228:231], a[212:215], a[68:71]\n"
    "PD\n"
    "ds_read_b64_tr_b16 a[242:243], v14 offset:53248\n"
    "v_mfma_f32_16x16x32_bf16 a[68:71], a[224:227], a[196:199], a[68:71]\n"
    "PD\n"
    "s_add_u32 s37, s37, 1\n"
    "v_mfma_f32_16x16x32_bf16 a[72:75], a[224:227], a[200:203], a[72:75]\n"
    "PD\n"
    "ds_read_b64_tr_b16 a[244:245], v14 offset:57344\n"
    "v_mfma_f32_16x16x32_bf16 a[72:75], a[228:231], a[216:219], a[72:75]\n"
    "PD\n"
    "v_mfma_f32_16x16x32_bf16 a[76:79], a[228:231], a[220:223], a[76:79]\n"
    "PD\n"
    "ds_read_b64_tr_b16 a[246:247], v14 offset:61440\n"
    "v_xor_b32 v14, 0x10000, v14\n"
    "v_mfma_f32_16x16x32_bf16 a[76:79], a[224:227], a[204:207], a[76:79]\n"
    "PD\n"
    "s_waitcnt lgkmcnt(4)\n"
    "v_mfma_f32_16x16x32_bf16 a[92:95], a[232:235], a[204:207], a[92:95]\n"
    "PD\n"
    "ds_read_b64_tr_b16 a[248:249], v15 offset:49152\n"
    "v_mfma_f32_16x16x32_bf16 a[92:95], a[236:239], a[220:223], a[92:95]\n"
    "PD\n"
    "v_mfma_f32_16x16x32_bf16 a[88:91], a[236:239], a[216:219], a[88:91]\n"
    "PD\n"
    "ds_read_b64_tr_b16 a[250:251], v15 offset:53248\n"
    "v_mfma_f32_16x16x32_bf16 a[88:91], a[232:235], a[200:203], a[88:91]\n"
    "PD\n"
    "v_mfma_f32_16x16x32_bf16 a[84:87], a[232:235], a[196:199], a[84:87]\n"
    "PD\n"
    "ds_read_b64_tr_b16 a[252:253], v15 offset:57344\n"
    "v_mfma_f32_16x16x32_bf16 a[84:87], a[236:239], a[212:215], a[84:87]\n"
    "PD\n"
    "v_mfma_f32_16x16x32_bf16 a[80:83], a[236:239], a[208:211], a[80:83]\n"
    "PD\n"
    "ds_read_b64_tr_b16 a[254:255], v15 offset:61440\n"
    "v_xor_b32 v15, 0x10000, v15\n"
    "v_mfma_f32_16x16x32_bf16 a[80:83], a[232:235], a[192:195], a[80:83]\n"
    "PD\n"
    "s_waitcnt lgkmcnt(4)\n"
    "s_waitcnt vmcnt(20)\n"
    "s_barrier\n"
    "v_mfma_f32_16x16x32_bf16 a[96:99], a[240:243], a[192:195], a[96:99]\n"
    "PD\n"
    "ds_read_b64_tr_b16 a[224:225], v8 offset:0\n"
    "v_mfma_f32_16x16x32_bf16 a[96:99], a[244:247], a[208:211], a[96:99]\n"
    "PD\n"
    "v_mfma_f32_16x16x32_bf16 a[100:103], a[244:247], a[212:215], a[100:103]\n"
    "PD\n"
    "ds_read_b64_tr_b16 a[226:227], v8 offset:4096\n"
    "v_mfma_f32_16x16x32_bf16 a[100:103], a[240:243], a[196:199], a[100:103]\n"
    "PD\n"
    "s_xor_b32 s60, s60, 0x10000\n"
    "v_mfma_f32_16x16x32_bf16 a[104:107], a[240:243], a[200:203], a[104:107]\n"
    "PD\n"
    "ds_read_b64_tr_b16 a[228:229], v8 offset:8192\n"
    "v_mfma_f32_16x16x32_bf16 a[104:107], a[244:247], a[216:219], a[104:107]\n"
    "PD\n"
    "v_mfma_f32_16x16x32_bf16 a[108:111], a[244:247], a[220:223], a[108:111]\n"
    "PD\n"
    "ds_read_b64_tr_b16 a[230:231], v8 offset:12288\n"
    "v_mfma_f32_16x16x32_bf16 a[108:111], a[240:243], a[204:207], a[108:111]\n"
    "PD\n"
    "s_waitcnt lgkmcnt(4)\n"
    "v_mfma_f32_16x16x32_bf16 a[124:127], a[248:251], a[204:207], a[124:127]\n"
    "PD\n"
    "ds_read_b64_tr_b16 a[232:233], v9 offset:0\n"
    "v_mfma_f32_16x16x32_bf16 a[124:127], a[252:255], a[220:223], a[124:127]\n"
    "PD\n"
    "v_mfma_f32_16x16x32_bf16 a[120:123], a[252:255], a[216:219], a[120:123]\n"
    "PD\n"
    "ds_read_b64_tr_b16 a[234:235], v9 offset:4096\n"
    "v_mfma_f32_16x16x32_bf16 a[120:123], a[248:251], a[200:203], a[120:123]\n"
    "PD\n"
    "v_mfma_f32_16x16x32_bf16 a[116:119], a[248:251], a[196:199], a[116:119]\n"
    "PD\n"
    "ds_read_b64_tr_b16 a[236:237], v9 offset:8192\n"
    "v_mfma_f32_16x16x32_bf16 a[116:119], a[252:255], a[212:215], a[116:119]\n"
    "PD\n"
    "v_mfma_f32_16x16x32_bf16 a[112:115], a[252:255], a[208:211], a[112:115]\n"
    "PD\n"
    "ds_read_b64_tr_b16 a[238:239], v9 offset:12288\n"
    "s_cmp_lg_u32 s37, s38\n"
    "v_mfma_f32_16x16x32_bf16 a[112:115], a[248:251], a[192:195], a[112:115]\n"
    "PD\n"
    "s_waitcnt vmcnt(8) lgkmcnt(4)\n"
    "s_cbranch_scc1 L_U\n"
    "PP\n"
    "v_mfma_f32_16x16x32_bf16 v[128:131], a[224:227], a[160:163], v[128:131]\n"
    "PD\n"
    "ds_read_b64_tr_b16 a[240:241], v8 offset:16384\n"
    "s_lshl_b32 s62, s27, 8\n"
    "v_mfma_f32_16x16x32_bf16 v[128:131], a[228:231], a[176:179], v[128:131]\n"
    "PD\n"
    "buffer_load_dwordx4 a[192:195], v20, s[40:43], s56 offen offset:0 sc1\n"
    "s_add_u32 s44, s10, s62\n"
    "s_addc_u32 s45, s11, 0\n"
    "v_mfma_f32_16x16x32_bf16 v[132:135], a[228:231], a[180:183], v[132:135]\n"
    "PD\n"
    "ds_read_b64_tr_b16 a[242:243], v8 offset:20480\n"
    "s_mov_b64 s[48:49], s[44:45]\n"
    "v_mfma_f32_16x16x32_bf16 v[132:135], a[224:227], a[164:167], v[132:135]\n"
    "PD\n"
    "buffer_load_dwordx4 a[208:211], v20, s[40:43], s56 offen offset:1024 sc1\n"
    "s_mov_b32 s59, 1048576\n"
    "s_mov_b32 s58, 524288\n"
    "v_mfma_f32_16x16x32_bf16 v[136:139], a[224:227], a[168:171], v[136:139]\n"
    "PD\n"
    "ds_read_b64_tr_b16 a[244:245], v8 offset:24576\n"
    "s_sub_u32 s46, 8388608, s62\n"
    "v_mfma_f32_16x16x32_bf16 v[136:139], a[228:231], a[184:187], v[136:139]\n"
    "PD\n"
    "buffer_load_dwordx4 a[196:199], v21, s[40:43], s56 offen offset:0 sc1\n"
    "s_movk_i32 s68, 8192\n"
    "s_cmp_eq_u32 s31, 2\n"
    "v_mfma_f32_16x16x32_bf16 v[140:143], a[228:231], a[188:191], v[140:143]\n"
    "PD\n"
    "ds_read_b64_tr_b16 a[246:247], v8 offset:28672\n"
    "s_cmov_b64 s[44:45], s[34:35]\n"
    "v_mfma_f32_16x16x32_bf16 v[140:143], a[224:227], a[172:175], v[140:143]\n"
    "PD\n"
    "s_waitcnt lgkmcnt(4)\n"
    "s_cmov_b64 s[48:49], s[34:35]\n"
    "s_cmov_b32 s46, s90\n"
    "v_mfma_f32_16x16x32_bf16 v[156:159], a[232:235], a[172:175], v[156:159]\n"
    "PD\n"
    "ds_read_b64_tr_b16 a[248:249], v9 offset:16384\n"
    "s_cmovk_i32 s68, 2048\n"
    "v_mfma_f32_16x16x32_bf16 v[156:159], a[236:239], a[188:191], v[156:159]\n"
    "PD\n"
    "buffer_load_dwordx4 a[212:215], v21, s[40:43], s56 offen offset:1024 sc1\n"
    "s_cmov_b32 s59, 262144\n"
    "s_cmov_b32 s58, 131072\n"
    "v_mfma_f32_16x16x32_bf16 v[152:155], a[236:239], a[184:187], v[152:155]\n"
    "PD\n"
    "ds_read_b64_tr_b16 a[250:251], v9 offset:20480\n"
    "s_mov_b32 s50, s46\n"
    "v_mfma_f32_16x16x32_bf16 v[152:155], a[232:235], a[168:171], v[152:155]\n"
    "PD\n"
    "buffer_load_dwordx4 a[200:203], v22, s[40:43], s56 offen offset:0 sc1\n"
    "s_mov_b32 s57, 0\n"
    "s_lshl_b32 s62, s68, 2\n"
    "v_mfma_f32_16x16x32_bf16 v[148:151], a[232:235], a[164:167], v[148:151]\n"
    "PD\n"
    "ds_read_b64_tr_b16 a[252:253], v9 offset:24576\n"
    "s_sub_u32 s62, s62, 0x400\n"
    "v_mfma_f32_16x16x32_bf16 v[148:151], a[236:239], a[180:183], v[148:151]\n"
    "PD\n"
    "buffer_load_dwordx4 a[216:219], v22, s[40:43], s56 offen offset:1024 sc1\n"
    "s_lshl_b32 s63, s62, 1\n"
    "v_mad_u32_u24 v24, v6, s68, v32\n"
    "v_mfma_f32_16x16x32_bf16 v[144:147], a[236:239], a[176:179], v[144:147]\n"
    "PD\n"
    "ds_read_b64_tr_b16 a[254:255], v9 offset:28672\n"
    "v_mad_u32_u24 v25, v6, s68, v33\n"
    "v_mfma_f32_16x16x32_bf16 v[144:147], a[232:235], a[160:163], v[144:147]\n"
    "PD\n"
    "s_waitcnt lgkmcnt(4)\n"
    "v_add_u32 v25, s62, v25\n"
    "v_add_u32 v26, s63, v24\n"
    "v_mfma_f32_16x16x32_bf16 a[0:3], a[240:243], a[160:163], a[0:3]\n"
    "PD\n"
    "ds_read_b64_tr_b16 a[224:225], v10 offset:0\n"
    "v_add_u32 v27, s63, v25\n"
    "v_mfma_f32_16x16x32_bf16 a[0:3], a[244:247], a[176:179], a[0:3]\n"
    "PD\n"
    "buffer_load_dwordx4 a[204:207], v23, s[40:43], s56 offen offset:0 sc1\n"
    "v_mov_b32 v28, v24\n"
    "v_mov_b32 v29, v25\n"
    "v_mfma_f32_16x16x32_bf16 a[4:7], a[244:247], a[180:183], a[4:7]\n"
    "PD\n"
    "ds_read_b64_tr_b16 a[226:227], v10 offset:4096\n"
    "v_mov_b32 v30, v26\n"
    "v_mfma_f32_16x16x32_bf16 a[4:7], a[240:243], a[164:167], a[4:7]\n"
    "PD\n"
    "buffer_load_dwordx4 a[220:223], v23, s[40:43], s56 offen offset:1024 sc1\n"
    "v_mov_b32 v31, v27\n"
    "s_mov_b32 s86, s91\n"
    "v_mfma_f32_16x16x32_bf16 a[8:11], a[240:243], a[168:171], a[8:11]\n"
    "PD\n"
    "ds_read_b64_tr_b16 a[228:229], v10 offset:8192\n"
    "v_min_u32 v38, 6, v0\n"
    "v_mfma_f32_16x16x32_bf16 a[8:11], a[244:247], a[184:187], a[8:11]\n"
    "PD\n"
    "v_lshlrev_b32 v38, 7, v38\n"
    "v_mfma_f32_16x16x32_bf16 a[12:15], a[244:247], a[188:191], a[12:15]\n"
    "PD\n"
    "ds_read_b64_tr_b16 a[230:231], v10 offset:12288\n"
    "v_mfma_f32_16x16x32_bf16 a[12:15], a[240:243], a[172:175], a[12:15]\n"
    "PD\n"
    "s_waitcnt lgkmcnt(4)\n"
    "v_mfma_f32_16x16x32_bf16 a[28:31], a[248:251], a[172:175], a[28:31]\n"
    "PD\n"
    "ds_read_b64_tr_b16 a[232:233], v11 offset:0\n"
    "v_mfma_f32_16x16x32_bf16 a[28:31], a[252:255], a[188:191], a[28:31]\n"
    "PD\n"
    "buffer_load_dwordx2 v[36:37], v38, s[80:83], s86 offen sc1\n"
    "v_mfma_f32_16x16x32_bf16 a[24:27], a[252:255], a[184:187], a[24:27]\n"
    "PD\n"
    "ds_read_b64_tr_b16 a[234:235], v11 offset:4096\n"
    "s_mul_i32 s62, s26, 459776\n"
    "v_mfma_f32_16x16x32_bf16 a[24:27], a[248:251], a[168:171], a[24:27]\n"
    "PD\n"
    "s_lshl_b32 s63, s28, 14\n"
    "s_add_u32 s62, s62, s63\n"
    "s_add_u32 s62, s62, 23068672\n"
    "v_mfma_f32_16x16x32_bf16 a[20:23], a[248:251], a[164:167], a[20:23]\n"
    "PD\n"
    "ds_read_b64_tr_b16 a[236:237], v11 offset:8192\n"
    "s_lshl_b32 s84, s28, 7\n"
    "v_mfma_f32_16x16x32_bf16 a[20:23], a[252:255], a[180:183], a[20:23]\n"
    "PD\n"
    "s_add_u32 s84, s84, 5248\n"
    "s_add_u32 s52, s22, s62\n"
    "s_addc_u32 s53, s23, 0\n"
    "v_mfma_f32_16x16x32_bf16 a[16:19], a[252:255], a[176:179], a[16:19]\n"
    "PD\n"
    "ds_read_b64_tr_b16 a[238:239], v11 offset:12288\n"
    "s_movk_i32 s54, 0x4000\n"
    "v_mfma_f32_16x16x32_bf16 a[16:19], a[248:251], a[160:163], a[16:19]\n"
    "PD\n"
    "s_waitcnt lgkmcnt(4)\n"
    "v_mov_b32 v56, 0\n"
    "v_mov_b32 v57, 0\n"
    "v_mfma_f32_16x16x32_bf16 v[160:163], a[224:227], a[160:163], v[160:163]\n"
    "PD\n"
    "ds_read_b64_tr_b16 a[240:241], v10 offset:16384\n"
    "v_mov_b32 v58, 0\n"
    "v_mfma_f32_16x16x32_bf16 v[160:163], a[228:231], a[176:179], v[160:163]\n"
    "PD\n"
    "v_mov_b32 v59, 0\n"
    "s_add_u32 m0, s60, 0\n"
    "v_mfma_f32_16x16x32_bf16 v[164:167], a[228:231], a[180:183], v[164:167]\n"
    "PD\n"
    "ds_read_b64_tr_b16 a[242:243], v10 offset:20480\n"
    "v_mfma_f32_16x16x32_bf16 v[164:167], a[224:227], a[164:167], v[164:167]\n"
    "PD\n"
    "buffer_load_dwordx4 v24, s[44:47], s57 offen offset:0 lds\n"
    "v_mfma_f32_16x16x32_bf16 v[168:171], a[224:227], a[168:171], v[168:171]\n"
    "PD\n"
    "ds_read_b64_tr_b16 a[244:245], v10 offset:24576\n"
    "v_mfma_f32_16x16x32_bf16 v[168:171], a[228:231], a[184:187], v[168:171]\n"
    "PD\n"
    "v_mfma_f32_16x16x32_bf16 v[172:175], a[228:231], a[188:191], v[172:175]\n"
    "PD\n"
    "ds_read_b64_tr_b16 a[246:247], v10 offset:28672\n"
    "v_mfma_f32_16x16x32_bf16 v[172:175], a[224:227], a[172:175], v[172:175]\n"
    "PD\n"
    "s_waitcnt lgkmcnt(4)\n"
    "v_mfma_f32_16x16x32_bf16 v[188:191], a[232:235], a[172:175], v[188:191]\n"
    "PD\n"
    "ds_read_b64_tr_b16 a[248:249], v11 offset:16384\n"
    "v_mfma_f32_16x16x32_bf16 v[188:191], a[236:239], a[188:191], v[188:191]\n"
    "PD\n"
    "buffer_load_dwordx4 v25, s[44:47], s57 offen offset:1024 lds\n"
    "v_mfma_f32_16x16x32_bf16 v[184:187], a[236:239], a[184:187], v[184:187]\n"
    "PD\n"
    "ds_read_b64_tr_b16 a[250:251], v11 offset:20480\n"
    "v_mfma_f32_16x16x32_bf16 v[184:187], a[232:235], a[168:171], v[184:187]\n"
    "PD\n"
    "v_mfma_f32_16x16x32_bf16 v[180:183], a[232:235], a[164:167], v[180:183]\n"
    "PD\n"
    "ds_read_b64_tr_b16 a[252:253], v11 offset:24576\n"
    "v_mfma_f32_16x16x32_bf16 v[180:183], a[236:239], a[180:183], v[180:183]\n"
    "PD\n"
    "buffer_load_dwordx4 v26, s[44:47], s57 offen offset:2048 lds\n"
    "v_mfma_f32_16x16x32_bf16 v[176:179], a[236:239], a[176:179], v[176:179]\n"
    "PD\n"
    "ds_read_b64_tr_b16 a[254:255], v11 offset:28672\n"
    "v_mfma_f32_16x16x32_bf16 v[176:179], a[232:235], a[160:163], v[176:179]\n"
    "PD\n"
    "s_waitcnt lgkmcnt(4)\n"
    "s_waitcnt vmcnt(16)\n"
    "s_barrier\n"
    "v_mfma_f32_16x16x32_bf16 a[32:35], a[240:243], a[160:163], a[32:35]\n"
    "PD\n"
    "ds_read_b64_tr_b16 a[224:225], v8 offset:32768\n"
    "v_mfma_f32_16x16x32_bf16 a[32:35], a[244:247], a[176:179], a[32:35]\n"
    "PD\n"
    "buffer_load_dwordx4 v27, s[44:47], s57 offen offset:3072 lds\n"
    "v_mfma_f32_16x16x32_bf16 a[36:39], a[244:247], a[180:183], a[36:39]\n"
    "PD\n"
    "ds_read_b64_tr_b16 a[226:227], v8 offset:36864\n"
    "s_add_u32 s57, s57, s59\n"
    "v_mfma_f32_16x16x32_bf16 a[36:39], a[240:243], a[164:167], a[36:39]\n"
    "PD\n"
    "s_add_u32 m0, s60, 16384\n"
    "v_mfma_f32_16x16x32_bf16 a[40:43], a[240:243], a[168:171], a[40:43]\n"
    "PD\n"
    "ds_read_b64_tr_b16 a[228:229], v8 offset:40960\n"
    "v_mfma_f32_16x16x32_bf16 a[40:43], a[244:247], a[184:187], a[40:43]\n"
    "PD\n"
    "buffer_load_dwordx4 v28, s[48:51], s58 offen offset:0 lds\n"
    "v_mfma_f32_16x16x32_bf16 a[44:47], a[244:247], a[188:191], a[44:47]\n"
    "PD\n"
    "ds_read_b64_tr_b16 a[230:231], v8 offset:45056\n"
    "v_mfma_f32_16x16x32_bf16 a[44:47], a[240:243], a[172:175], a[44:47]\n"
    "PD\n"
    "s_waitcnt lgkmcnt(4)\n"
    "v_mfma_f32_16x16x32_bf16 a[60:63], a[248:251], a[172:175], a[60:63]\n"
    "PD\n"
    "ds_read_b64_tr_b16 a[232:233], v9 offset:32768\n"
    "v_mfma_f32_16x16x32_bf16 a[60:63], a[252:255], a[188:191], a[60:63]\n"
    "PD\n"
    "buffer_load_dwordx4 v29, s[48:51], s58 offen offset:1024 lds\n"
    "v_mfma_f32_16x16x32_bf16 a[56:59], a[252:255], a[184:187], a[56:59]\n"
    "PD\n"
    "ds_read_b64_tr_b16 a[234:235], v9 offset:36864\n"
    "v_mfma_f32_16x16x32_bf16 a[56:59], a[248:251], a[168:171], a[56:59]\n"
    "PD\n"
    "v_mfma_f32_16x16x32_bf16 a[52:55], a[248:251], a[164:167], a[52:55]\n"
    "PD\n"
    "ds_read_b64_tr_b16 a[236:237], v9 offset:40960\n"
    "v_mfma_f32_16x16x32_bf16 a[52:55], a[252:255], a[180:183], a[52:55]\n"
    "PD\n"
    "buffer_load_dwordx4 v30, s[48:51], s58 offen offset:2048 lds\n"
    "v_mfma_f32_16x16x32_bf16 a[48:51], a[252:255], a[176:179], a[48:51]\n"
    "PD\n"
    "ds_read_b64_tr_b16 a[238:239], v9 offset:45056\n"
    "v_mfma_f32_16x16x32_bf16 a[48:51], a[248:251], a[160:163], a[48:51]\n"
    "PD\n"
    "s_waitcnt vmcnt(8) lgkmcnt(4)\n"
    "v_mfma_f32_16x16x32_bf16 v[128:131], a[224:227], a[192:195], v[128:131]\n"
    "PD\n"
    "ds_read_b64_tr_b16 a[240:241], v8 offset:49152\n"
    "v_mfma_f32_16x16x32_bf16 v[128:131], a[228:231], a[208:211], v[128:131]\n"
    "PD\n"
    "buffer_load_dwordx4 v31, s[48:51], s58 offen offset:3072 lds\n"
    "v_mfma_f32_16x16x32_bf16 v[132:135], a[228:231], a[212:215], v[132:135]\n"
    "PD\n"
    "ds_read_b64_tr_b16 a[242:243], v8 offset:53248\n"
    "s_add_u32 s58, s58, s59\n"
    "v_mfma_f32_16x16x32_bf16 v[132:135], a[224:227], a[196:199], v[132:135]\n"
    "PD\n"
    "v_mfma_f32_16x16x32_bf16 v[136:139], a[224:227], a[200:203], v[136:139]\n"
    "PD\n"
    "ds_read_b64_tr_b16 a[244:245], v8 offset:57344\n"
    "v_mfma_f32_16x16x32_bf16 v[136:139], a[228:231], a[216:219], v[136:139]\n"
    "PD\n"
    "v_mfma_f32_16x16x32_bf16 v[140:143], a[228:231], a[220:223], v[140:143]\n"
    "PD\n"
    "ds_read_b64_tr_b16 a[246:247], v8 offset:61440\n"
    "v_xor_b32 v8, 0x10000, v8\n"
    "v_mfma_f32_16x16x32_bf16 v[140:143], a[224:227], a[204:207], v[140:143]\n"
    "PD\n"
    "s_waitcnt lgkmcnt(4)\n"
    "v_mfma_f32_16x16x32_bf16 v[156:159], a[232:235], a[204:207], v[156:159]\n"
    "PD\n"
    "ds_read_b64_tr_b16 a[248:249], v9 offset:49152\n"
    "v_mfma_f32_16x16x32_bf16 v[156:159], a[236:239], a[220:223], v[156:159]\n"
    "PD\n"
    "v_mfma_f32_16x16x32_bf16 v[152:155], a[236:239], a[216:219], v[152:155]\n"
    "PD\n"
    "ds_read_b64_tr_b16 a[250:251], v9 offset:53248\n"
    "v_mfma_f32_16x16x32_bf16 v[152:155], a[232:235], a[200:203], v[152:155]\n"
    "PD\n"
    "v_mfma_f32_16x16x32_bf16 v[148:151], a[232:235], a[196:199], v[148:151]\n"
    "PD\n"
    "ds_read_b64_tr_b16 a[252:253], v9 offset:57344\n"
    "v_mfma_f32_16x16x32_bf16 v[148:151], a[236:239], a[212:215], v[148:151]\n"
    "PD\n"
    "v_mfma_f32_16x16x32_bf16 v[144:147], a[236:239], a[208:211], v[144:147]\n"
    "PD\n"
    "ds_read_b64_tr_b16 a[254:255], v9 offset:61440\n"
    "v_xor_b32 v9, 0x10000, v9\n"
    "v_mfma_f32_16x16x32_bf16 v[144:147], a[232:235], a[192:195], v[144:147]\n"
    "PD\n"
    "s_waitcnt lgkmcnt(4)\n"
    "v_mfma_f32_16x16x32_bf16 a[0:3], a[240:243], a[192:195], a[0:3]\n"
    "PD\n"
    "ds_read_b64_tr_b16 a[224:225], v10 offset:32768\n"
    "v_mfma_f32_16x16x32_bf16 a[0:3], a[244:247], a[208:211], a[0:3]\n"
    "PD\n"
    "v_mfma_f32_16x16x32_bf16 a[4:7], a[244:247], a[212:215], a[4:7]\n"
    "PD\n"
    "ds_read_b64_tr_b16 a[226:227], v10 offset:36864\n"
    "s_waitcnt vmcnt(8)\n"
    "v_cmp_ne_u64 vcc, v[36:37], v[34:35]\n"
    "v_mfma_f32_16x16x32_bf16 a[4:7], a[240:243], a[196:199], a[4:7]\n"
    "PD\n"
    "s_lshl_b32 s62, s29, 11\n"
    "s_lshl_b32 s63, s30, 7\n"
    "s_cmp_lg_u32 s31, 2\n"
    "v_mfma_f32_16x16x32_bf16 a[8:11], a[240:243], a[200:203], a[8:11]\n"
    "PD\n"
    "ds_read_b64_tr_b16 a[228:229], v10 offset:40960\n"
    "s_cmov_b32 s62, 0\n"
    "v_mfma_f32_16x16x32_bf16 a[8:11], a[244:247], a[216:219], a[8:11]\n"
    "PD\n"
    "s_cmov_b64 vcc, 0\n"
    "s_mov_b32 s56, 0\n"
    "s_cbranch_vccnz H_Ux\n"
    "R_Ux:\n"
    "v_mfma_f32_16x16x32_bf16 a[12:15], a[244:247], a[220:223], a[12:15]\n"
    "PD\n"
    "ds_read_b64_tr_b16 a[230:231], v10 offset:45056\n"
    "s_cmp_eq_u32 s31, 2\n"
    "v_mfma_f32_16x16x32_bf16 a[12:15], a[240:243], a[204:207], a[12:15]\n"
    "PD\n"
    "s_waitcnt lgkmcnt(4)\n"
    "s_cmov_b64 s[40:41], s[78:79]\n"
    "s_cmov_b32 s42, s63\n"
    "v_mfma_f32_16x16x32_bf16 a[28:31], a[248:251], a[204:207], a[28:31]\n"
    "PD\n"
    "ds_read_b64_tr_b16 a[232:233], v11 offset:32768\n"
    "v_xor_b32 v20, s62, v4\n"
    "v_mfma_f32_16x16x32_bf16 a[28:31], a[252:255], a[220:223], a[28:31]\n"
    "PD\n"
    "v_xor_b32 v21, 2048, v20\n"
    "v_xor_b32 v22, 4096, v20\n"
    "v_mfma_f32_16x16x32_bf16 a[24:27], a[252:255], a[216:219], a[24:27]\n"
    "PD\n"
    "ds_read_b64_tr_b16 a[234:235], v11 offset:36864\n"
    "v_xor_b32 v23, 6144, v20\n"
    "v_mfma_f32_16x16x32_bf16 a[24:27], a[248:251], a[200:203], a[24:27]\n"
    "PD\n"
    "s_add_u32 m0, s60, 32768\n"
    "v_mfma_f32_16x16x32_bf16 a[20:23], a[248:251], a[196:199], a[20:23]\n"
    "PD\n"
    "ds_read_b64_tr_b16 a[236:237], v11 offset:40960\n"
    "v_mfma_f32_16x16x32_bf16 a[20:23], a[252:255], a[212:215], a[20:23]\n"
    "PD\n"
    "buffer_load_dwordx4 v24, s[44:47], s57 offen offset:0 lds\n"
    "v_mfma_f32_16x16x32_bf16 a[16:19], a[252:255], a[208:211], a[16:19]\n"
    "PD\n"
    "ds_read_b64_tr_b16 a[238:239], v11 offset:45056\n"
    "v_mfma_f32_16x16x32_bf16 a[16:19], a[248:251], a[192:195], a[16:19]\n"
    "PD\n"
    "s_waitcnt lgkmcnt(4)\n"
    "v_mfma_f32_16x16x32_bf16 v[160:163], a[224:227], a[192:195], v[160:163]\n"
    "PD\n"
    "ds_read_b64_tr_b16 a[240:241], v10 offset:49152\n"
    "v_mfma_f32_16x16x32_bf16 v[160:163], a[228:231], a[208:211], v[160:163]\n"
    "PD\n"
    "buffer_load_dwordx4 v25, s[44:47], s57 offen offset:1024 lds\n"
    "v_mfma_f32_16x16x32_bf16 v[164:167], a[228:231], a[212:215], v[164:167]\n"
    "PD\n"
    "ds_read_b64_tr_b16 a[242:243], v10 offset:53248\n"
    "v_mfma_f32_16x16x32_bf16 v[164:167], a[224:227], a[196:199], v[164:167]\n"
    "PD\n"
    "v_mfma_f32_16x16x32_bf16 v[168:171], a[224:227], a[200:203], v[168:171]\n"
    "PD\n"
    "ds_read_b64_tr_b16 a[244:245], v10 offset:57344\n"
    "v_accvgpr_read_b32 v40, a0\n"
    "v_mfma_f32_16x16x32_bf16 v[168:171], a[228:231], a[216:219], v[168:171]\n"
    "PD\n"
    "buffer_load_dwordx4 v26, s[44:47], s57 offen offset:2048 lds\n"
    "v_accvgpr_read_b32 v41, a1\n"
    "v_accvgpr_read_b32 v42, a2\n"
    "v_mfma_f32_16x16x32_bf16 v[172:175], a[228:231], a[220:223], v[172:175]\n"
    "PD\n"
    "ds_read_b64_tr_b16 a[246:247], v10 offset:61440\n"
    "v_xor_b32 v10, 0x10000, v10\n"
    "v_mfma_f32_16x16x32_bf16 v[172:175], a[224:227], a[204:207], v[172:175]\n"
    "PD\n"
    "s_waitcnt lgkmcnt(4)\n"
    "v_accvgpr_read_b32 v43, a3\n"
    "v_accvgpr_read_b32 v44, a16\n"
    "v_mfma_f32_16x16x32_bf16 v[188:191], a[232:235], a[204:207], v[188:191]\n"
    "PD\n"
    "ds_read_b64_tr_b16 a[248:249], v11 offset:49152\n"
    "v_accvgpr_read_b32 v45, a17\n"
    "v_mfma_f32_16x16x32_bf16 v[188:191], a[236:239], a[220:223], v[188:191]\n"
    "PD\n"
    "buffer_load_dwordx4 v27, s[44:47], s57 offen offset:3072 lds\n"
    "v_accvgpr_read_b32 v46, a18\n"
    "v_accvgpr_read_b32 v47, a19\n"
    "v_mfma_f32_16x16x32_bf16 v[184:187], a[236:239], a[216:219], v[184:187]\n"
    "PD\n"
    "ds_read_b64_tr_b16 a[250:251], v11 offset:53248\n"
    "v_mul_f32 v40, v40, v128\n"
    "v_mfma_f32_16x16x32_bf16 v[184:187], a[232:235], a[200:203], v[184:187]\n"
    "PD\n"
    "s_add_u32 s57, s57, s59\n"
    "v_mul_f32 v41, v41, v129\n"
    "v_mul_f32 v42, v42, v130\n"
    "v_mfma_f32_16x16x32_bf16 v[180:183], a[232:235], a[196:199], v[180:183]\n"
    "PD\n"
    "ds_read_b64_tr_b16 a[252:253], v11 offset:57344\n"
    "v_mul_f32 v43, v43, v131\n"
    "v_mfma_f32_16x16x32_bf16 v[180:183], a[236:239], a[212:215], v[180:183]\n"
    "PD\n"
    "buffer_load_dwordx4 a[128:131], v20, s[40:43], s56 offen offset:0 sc1\n"
    "v_mul_f32 v44, v44, v144\n"
    "v_mul_f32 v45, v45, v145\n"
    "v_mfma_f32_16x16x32_bf16 v[176:179], a[236:239], a[208:211], v[176:179]\n"
    "PD\n"
    "ds_read_b64_tr_b16 a[254:255], v11 offset:61440\n"
    "v_xor_b32 v11, 0x10000, v11\n"
    "v_mfma_f32_16x16x32_bf16 v[176:179], a[232:235], a[192:195], v[176:179]\n"
    "PD\n"
    "s_waitcnt lgkmcnt(4)\n"
    "v_mul_f32 v46, v46, v146\n"
    "v_mul_f32 v47, v47, v147\n"
    "v_mfma_f32_16x16x32_bf16 a[32:35], a[240:243], a[192:195], a[32:35]\n"
    "PD\n"
    "ds_read_b64_tr_b16 a[224:225], v12 offset:0\n"
    "v_cvt_pk_bf16_f32 v64, v40, v41\n"
    "v_mfma_f32_16x16x32_bf16 a[32:35], a[244:247], a[208:211], a[32:35]\n"
    "PD\n"
    "buffer_load_dwordx4 a[144:147], v20, s[40:43], s56 offen offset:1024 sc1\n"
    "v_cvt_pk_bf16_f32 v65, v42, v43\n"
    "v_cvt_pk_bf16_f32 v66, v44, v45\n"
    "v_mfma_f32_16x16x32_bf16 a[36:39], a[244:247], a[212:215], a[36:39]\n"
    "PD\n"
    "ds_read_b64_tr_b16 a[226:227], v12 offset:4096\n"
    "v_cvt_pk_bf16_f32 v67, v46, v47\n"
    "v_mfma_f32_16x16x32_bf16 a[36:39], a[240:243], a[196:199], a[36:39]\n"
    "PD\n"
    "buffer_store_dwordx4 v[64:67], v4, s[52:55], 0 offen offset:0 sc1\n"
    "v_accvgpr_read_b32 v48, a4\n"
    "v_accvgpr_read_b32 v49, a5\n"
    "v_mfma_f32_16x16x32_bf16 a[40:43], a[240:243], a[200:203], a[40:43]\n"
    "PD\n"
    "ds_read_b64_tr_b16 a[228:229], v12 offset:8192\n"
    "v_accvgpr_read_b32 v50, a6\n"
    "v_mfma_f32_16x16x32_bf16 a[40:43], a[244:247], a[216:219], a[40:43]\n"
    "PD\n"
    "buffer_load_dwordx4 a[132:135], v21, s[40:43], s56 offen offset:0 sc1\n"
    "v_accvgpr_read_b32 v51, a7\n"
    "v_accvgpr_read_b32 v52, a20\n"
    "v_mfma_f32_16x16x32_bf16 a[44:47], a[244:247], a[220:223], a[44:47]\n"
    "PD\n"
    "ds_read_b64_tr_b16 a[230:231], v12 offset:12288\n"
    "v_accvgpr_read_b32 v53, a21\n"
    "v_mfma_f32_16x16x32_bf16 a[44:47], a[240:243], a[204:207], a[44:47]\n"
    "PD\n"
    "s_waitcnt lgkmcnt(4)\n"
    "v_accvgpr_read_b32 v54, a22\n"
    "v_accvgpr_read_b32 v55, a23\n"
    "v_mfma_f32_16x16x32_bf16 a[60:63], a[248:251], a[204:207], a[60:63]\n"
    "PD\n"
    "ds_read_b64_tr_b16 a[232:233], v13 offset:0\n"
    "v_mul_f32 v48, v48, v132\n"
    "v_mfma_f32_16x16x32_bf16 a[60:63], a[252:255], a[220:223], a[60:63]\n"
    "PD\n"
    "buffer_load_dwordx4 a[148:151], v21, s[40:43], s56 offen offset:1024 sc1\n"
    "v_mul_f32 v49, v49, v133\n"
    "v_mul_f32 v50, v50, v134\n"
    "v_mfma_f32_16x16x32_bf16 a[56:59], a[252:255], a[216:219], a[56:59]\n"
    "PD\n"
    "ds_read_b64_tr_b16 a[234:235], v13 offset:4096\n"
    "v_mul_f32 v51, v51, v135\n"
    "v_mfma_f32_16x16x32_bf16 a[56:59], a[248:251], a[200:203], a[56:59]\n"
    "PD\n"
    "v_mul_f32 v52, v52, v148\n"
    "v_mul_f32 v53, v53, v149\n"
    "v_mfma_f32_16x16x32_bf16 a[52:55], a[248:251], a[196:199], a[52:55]\n"
    "PD\n"
    "ds_read_b64_tr_b16 a[236:237], v13 offset:8192\n"
    "v_mul_f32 v54, v54, v150\n"
    "v_mfma_f32_16x16x32_bf16 a[52:55], a[252:255], a[212:215], a[52:55]\n"
    "PD\n"
    "buffer_load_dwordx4 a[136:139], v22, s[40:43], s56 offen offset:0 sc1\n"
    "v_mul_f32 v55, v55, v151\n"
    "v_cvt_pk_bf16_f32 v68, v48, v49\n"
    "v_mfma_f32_16x16x32_bf16 a[48:51], a[252:255], a[208:211], a[48:51]\n"
    "PD\n"
    "ds_read_b64_tr_b16 a[238:239], v13 offset:12288\n"
    "v_cvt_pk_bf16_f32 v69, v50, v51\n"
    "v_mfma_f32_16x16x32_bf16 a[48:51], a[248:251], a[192:195], a[48:51]\n"
    "PD\n"
    "s_waitcnt lgkmcnt(4)\n"
    "v_cvt_pk_bf16_f32 v70, v52, v53\n"
    "v_cvt_pk_bf16_f32 v71, v54, v55\n"
    "v_mfma_f32_16x16x32_bf16 v[192:195], a[224:227], a[160:163], v[192:195]\n"
    "PD\n"
    "ds_read_b64_tr_b16 a[240:241], v12 offset:16384\n"
    "v_accvgpr_read_b32 v40, a8\n"
    "v_mfma_f32_16x16x32_bf16 v[192:195], a[228:231], a[176:179], v[192:195]\n"
    "PD\n"
    "buffer_load_dwordx4 a[152:155], v22, s[40:43], s56 offen offset:1024 sc1\n"
    "v_accvgpr_read_b32 v41, a9\n"
    "v_accvgpr_read_b32 v42, a10\n"
    "v_mfma_f32_16x16x32_bf16 v[196:199], a[228:231], a[180:183], v[196:199]\n"
    "PD\n"
    "ds_read_b64_tr_b16 a[242:243], v12 offset:20480\n"
    "v_accvgpr_read_b32 v43, a11\n"
    "v_mfma_f32_16x16x32_bf16 v[196:199], a[224:227], a[164:167], v[196:199]\n"
    "PD\n"
    "v_accvgpr_read_b32 v44, a24\n"
    "v_accvgpr_read_b32 v45, a25\n"
    "v_mfma_f32_16x16x32_bf16 v[200:203], a[224:227], a[168:171], v[200:203]\n"
    "PD\n"
    "ds_read_b64_tr_b16 a[244:245], v12 offset:24576\n"
    "v_accvgpr_read_b32 v46, a26\n"
    "v_mfma_f32_16x16x32_bf16 v[200:203], a[228:231], a[184:187], v[200:203]\n"
    "PD\n"
    "buffer_load_dwordx4 a[140:143], v23, s[40:43], s56 offen offset:0 sc1\n"
    "v_accvgpr_read_b32 v47, a27\n"
    "v_mul_f32 v40, v40, v136\n"
    "v_mfma_f32_16x16x32_bf16 v[204:207], a[228:231], a[188:191], v[204:207]\n"
    "PD\n"
    "ds_read_b64_tr_b16 a[246:247], v12 offset:28672\n"
    "v_mul_f32 v41, v41, v137\n"
    "v_mfma_f32_16x16x32_bf16 v[204:207], a[224:227], a[172:175], v[204:207]\n"
    "PD\n"
    "s_waitcnt lgkmcnt(4)\n"
    "buffer_store_dwordx4 v[68:71], v4, s[52:55], 0 offen offset:2048 sc1\n"
    "v_mul_f32 v42, v42, v138\n"
    "v_mfma_f32_16x16x32_bf16 v[220:223], a[232:235], a[172:175], v[220:223]\n"
    "PD\n"
    "ds_read_b64_tr_b16 a[248:249], v13 offset:16384\n"
    "v_mul_f32 v43, v43, v139\n"
    "v_mfma_f32_16x16x32_bf16 v[220:223], a[236:239], a[188:191], v[220:223]\n"
    "PD\n"
    "buffer_load_dwordx4 a[156:159], v23, s[40:43], s56 offen offset:1024 sc1\n"
    "v_mul_f32 v44, v44, v152\n"
    "v_mul_f32 v45, v45, v153\n"
    "v_mfma_f32_16x16x32_bf16 v[216:219], a[236:239], a[184:187], v[216:219]\n"
    "PD\n"
    "ds_read_b64_tr_b16 a[250:251], v13 offset:20480\n"
    "v_mul_f32 v46, v46, v154\n"
    "v_mfma_f32_16x16x32_bf16 v[216:219], a[232:235], a[168:171], v[216:219]\n"
    "PD\n"
    "s_add_u32 s56, s56, s69\n"
    "v_mul_f32 v47, v47, v155\n"
    "v_cvt_pk_bf16_f32 v72, v40, v41\n"
    "v_mfma_f32_16x16x32_bf16 v[212:215], a[232:235], a[164:167], v[212:215]\n"
    "PD\n"
    "ds_read_b64_tr_b16 a[252:253], v13 offset:24576\n"
    "v_cvt_pk_bf16_f32 v73, v42, v43\n"
    "v_mfma_f32_16x16x32_bf16 v[212:215], a[236:239], a[180:183], v[212:215]\n"
    "PD\n"
    "s_add_u32 m0, s60, 49152\n"
    "v_cvt_pk_bf16_f32 v74, v44, v45\n"
    "v_cvt_pk_bf16_f32 v75, v46, v47\n"
    "v_mfma_f32_16x16x32_bf16 v[208:211], a[236:239], a[176:179], v[208:211]\n"
    "PD\n"
    "ds_read_b64_tr_b16 a[254:255], v13 offset:28672\n"
    "v_accvgpr_read_b32 v48, a12\n"
    "v_mfma_f32_16x16x32_bf16 v[208:211], a[232:235], a[160:163], v[208:211]\n"
    "PD\n"
    "s_waitcnt lgkmcnt(4)\n"
    "v_accvgpr_read_b32 v49, a13\n"
    "v_accvgpr_read_b32 v50, a14\n"
    "v_mfma_f32_16x16x32_bf16 a[64:67], a[240:243], a[160:163], a[64:67]\n"
    "PD\n"
    "ds_read_b64_tr_b16 a[224:225], v12 offset:32768\n"
    "v_accvgpr_read_b32 v51, a15\n"
    "v_mfma_f32_16x16x32_bf16 a[64:67], a[244:247], a[176:179], a[64:67]\n"
    "PD\n"
    "buffer_load_dwordx4 v28, s[48:51], s58 offen offset:0 lds\n"
    "v_accvgpr_read_b32 v52, a28\n"
    "v_accvgpr_read_b32 v53, a29\n"
    "v_mfma_f32_16x16x32_bf16 a[68:71], a[244:247], a[180:183], a[68:71]\n"
    "PD\n"
    "ds_read_b64_tr_b16 a[226:227], v12 offset:36864\n"
    "v_accvgpr_read_b32 v54, a30\n"
    "v_mfma_f32_16x16x32_bf16 a[68:71], a[240:243], a[164:167], a[68:71]\n"
    "PD\n"
    "v_accvgpr_read_b32 v55, a31\n"
    "v_mul_f32 v48, v48, v140\n"
    "v_mfma_f32_16x16x32_bf16 a[72:75], a[240:243], a[168:171], a[72:75]\n"
    "PD\n"
    "ds_read_b64_tr_b16 a[228:229], v12 offset:40960\n"
    "v_mul_f32 v49, v49, v141\n"
    "v_mfma_f32_16x16x32_bf16 a[72:75], a[244:247], a[184:187], a[72:75]\n"
    "PD\n"
    "buffer_load_dwordx4 v29, s[48:51], s58 offen offset:1024 lds\n"
    "v_mul_f32 v50, v50, v142\n"
    "v_mul_f32 v51, v51, v143\n"
    "v_mfma_f32_16x16x32_bf16 a[76:79], a[244:247], a[188:191], a[76:79]\n"
    "PD\n"
    "ds_read_b64_tr_b16 a[230:231], v12 offset:45056\n"
    "v_mul_f32 v52, v52, v156\n"
    "v_mfma_f32_16x16x32_bf16 a[76:79], a[240:243], a[172:175], a[76:79]\n"
    "PD\n"
    "s_waitcnt lgkmcnt(4)\n"
    "v_mul_f32 v53, v53, v157\n"
    "v_mul_f32 v54, v54, v158\n"
    "v_mfma_f32_16x16x32_bf16 a[92:95], a[248:251], a[172:175], a[92:95]\n"
    "PD\n"
    "ds_read_b64_tr_b16 a[232:233], v13 offset:32768\n"
    "v_mul_f32 v55, v55, v159\n"
    "v_mfma_f32_16x16x32_bf16 a[92:95], a[252:255], a[188:191], a[92:95]\n"
    "PD\n"
    "buffer_load_dwordx4 v30, s[48:51], s58 offen offset:2048 lds\n"
    "v_cvt_pk_bf16_f32 v76, v48, v49\n"
    "v_cvt_pk_bf16_f32 v77, v50, v51\n"
    "v_mfma_f32_16x16x32_bf16 a[88:91], a[252:255], a[184:187], a[88:91]\n"
    "PD\n"
    "ds_read_b64_tr_b16 a[234:235], v13 offset:36864\n"
    "v_cvt_pk_bf16_f32 v78, v52, v53\n"
    "v_mfma_f32_16x16x32_bf16 a[88:91], a[248:251], a[168:171], a[88:91]\n"
    "PD\n"
    "buffer_store_dwordx4 v[72:75], v5, s[52:55], 0 offen offset:0 sc1\n"
    "v_cvt_pk_bf16_f32 v79, v54, v55\n"
    "v_accvgpr_read_b32 v40, a32\n"
    "v_mfma_f32_16x16x32_bf16 a[84:87], a[248:251], a[164:167], a[84:87]\n"
    "PD\n"
    "ds_read_b64_tr_b16 a[236:237], v13 offset:40960\n"
    "v_accvgpr_read_b32 v41, a33\n"
    "v_mfma_f32_16x16x32_bf16 a[84:87], a[252:255], a[180:183], a[84:87]\n"
    "PD\n"
    "buffer_load_dwordx4 v31, s[48:51], s58 offen offset:3072 lds\n"
    "v_accvgpr_read_b32 v42, a34\n"
    "v_accvgpr_read_b32 v43, a35\n"
    "v_mfma_f32_16x16x32_bf16 a[80:83], a[252:255], a[176:179], a[80:83]\n"
    "PD\n"
    "ds_read_b64_tr_b16 a[238:239], v13 offset:45056\n"
    "v_accvgpr_read_b32 v44, a48\n"
    "v_mfma_f32_16x16x32_bf16 a[80:83], a[248:251], a[160:163], a[80:83]\n"
    "PD\n"
    "s_waitcnt lgkmcnt(4)\n"
    "v_accvgpr_read_b32 v45, a49\n"
    "v_accvgpr_read_b32 v46, a50\n"
    "v_mfma_f32_16x16x32_bf16 v[192:195], a[224:227], a[192:195], v[192:195]\n"
    "PD\n"
    "ds_read_b64_tr_b16 a[240:241], v12 offset:49152\n"
    "v_accvgpr_read_b32 v47, a51\n"
    "v_mfma_f32_16x16x32_bf16 v[192:195], a[228:231], a[208:211], v[192:195]\n"
    "PD\n"
    "s_add_u32 s58, s58, s59\n"
    "v_mul_f32 v40, v40, v160\n"
    "v_mul_f32 v41, v41, v161\n"
    "v_mfma_f32_16x16x32_bf16 v[196:199], a[228:231], a[212:215], v[196:199]\n"
    "PD\n"
    "ds_read_b64_tr_b16 a[242:243], v12 offset:53248\n"
    "v_mul_f32 v42, v42, v162\n"
    "v_mfma_f32_16x16x32_bf16 v[196:199], a[224:227], a[196:199], v[196:199]\n"
    "PD\n"
    "v_mul_f32 v43, v43, v163\n"
    "v_mul_f32 v44, v44, v176\n"
    "v_mfma_f32_16x16x32_bf16 v[200:203], a[224:227], a[200:203], v[200:203]\n"
    "PD\n"
    "ds_read_b64_tr_b16 a[244:245], v12 offset:57344\n"
    "v_mul_f32 v45, v45, v177\n"
    "v_mfma_f32_16x16x32_bf16 v[200:203], a[228:231], a[216:219], v[200:203]\n"
    "PD\n"
    "v_mul_f32 v46, v46, v178\n"
    "v_mul_f32 v47, v47, v179\n"
    "v_mfma_f32_16x16x32_bf16 v[204:207], a[228:231], a[220:223], v[204:207]\n"
    "PD\n"
    "ds_read_b64_tr_b16 a[246:247], v12 offset:61440\n"
    "v_xor_b32 v12, 0x10000, v12\n"
    "v_mfma_f32_16x16x32_bf16 v[204:207], a[224:227], a[204:207], v[204:207]\n"
    "PD\n"
    "s_waitcnt lgkmcnt(4)\n"
    "v_cvt_pk_bf16_f32 v80, v40, v41\n"
    "v_cvt_pk_bf16_f32 v81, v42, v43\n"
    "v_mfma_f32_16x16x32_bf16 v[220:223], a[232:235], a[204:207], v[220:223]\n"
    "PD\n"
    "ds_read_b64_tr_b16 a[248:249], v13 offset:49152\n"
    "v_cvt_pk_bf16_f32 v82, v44, v45\n"
    "v_mfma_f32_16x16x32_bf16 v[220:223], a[236:239], a[220:223], v[220:223]\n"
    "PD\n"
    "v_cvt_pk_bf16_f32 v83, v46, v47\n"
    "v_accvgpr_read_b32 v48, a36\n"
    "v_mfma_f32_16x16x32_bf16 v[216:219], a[236:239], a[216:219], v[216:219]\n"
    "PD\n"
    "ds_read_b64_tr_b16 a[250:251], v13 offset:53248\n"
    "v_accvgpr_read_b32 v49, a37\n"
    "v_mfma_f32_16x16x32_bf16 v[216:219], a[232:235], a[200:203], v[216:219]\n"
    "PD\n"
    "v_accvgpr_read_b32 v50, a38\n"
    "v_accvgpr_read_b32 v51, a39\n"
    "v_mfma_f32_16x16x32_bf16 v[212:215], a[232:235], a[196:199], v[212:215]\n"
    "PD\n"
    "ds_read_b64_tr_b16 a[252:253], v13 offset:57344\n"
    "v_accvgpr_read_b32 v52, a52\n"
    "v_mfma_f32_16x16x32_bf16 v[212:215], a[236:239], a[212:215], v[212:215]\n"
    "PD\n"
    "v_accvgpr_read_b32 v53, a53\n"
    "v_accvgpr_read_b32 v54, a54\n"
    "v_mfma_f32_16x16x32_bf16 v[208:211], a[236:239], a[208:211], v[208:211]\n"
    "PD\n"
    "ds_read_b64_tr_b16 a[254:255], v13 offset:61440\n"
    "v_xor_b32 v13, 0x10000, v13\n"
    "v_mfma_f32_16x16x32_bf16 v[208:211], a[232:235], a[192:195], v[208:211]\n"
    "PD\n"
    "s_waitcnt lgkmcnt(4)\n"
    "buffer_store_dwordx4 v[76:79], v5, s[52:55], 0 offen offset:2048 sc1\n"
    "v_accvgpr_read_b32 v55, a55\n"
    "v_mfma_f32_16x16x32_bf16 a[64:67], a[240:243], a[192:195], a[64:67]\n"
    "PD\n"
    "ds_read_b64_tr_b16 a[224:225], v14 offset:0\n"
    "v_mul_f32 v48, v48, v164\n"
    "v_mfma_f32_16x16x32_bf16 a[64:67], a[244:247], a[208:211], a[64:67]\n"
    "PD\n"
    "v_mfma_f32_32x32x16_bf16 v[128:143], v[56:59], v[56:59], 0\n"
    "v_mul_f32 v49, v49, v165\n"
    "v_mul_f32 v50, v50, v166\n"
    "v_mul_f32 v51, v51, v167\n"
    "v_mul_f32 v52, v52, v180\n"
    "v_mul_f32 v53, v53, v181\n"
    "v_mul_f32 v54, v54, v182\n"
    "PD\n"
    "v_mfma_f32_16x16x32_bf16 a[68:71], a[244:247], a[212:215], a[68:71]\n"
    "PD\n"
    "ds_read_b64_tr_b16 a[226:227], v14 offset:4096\n"
    "v_mul_f32 v55, v55, v183\n"
    "v_mfma_f32_16x16x32_bf16 a[68:71], a[240:243], a[196:199], a[68:71]\n"
    "PD\n"
    "v_mfma_f32_32x32x16_bf16 v[144:159], v[56:59], v[56:59], 0\n"
    "v_cvt_pk_bf16_f32 v84, v48, v49\n"
    "v_cvt_pk_bf16_f32 v85, v50, v51\n"
    "v_cvt_pk_bf16_f32 v86, v52, v53\n"
    "v_cvt_pk_bf16_f32 v87, v54, v55\n"
    "v_accvgpr_read_b32 v40, a40\n"
    "v_accvgpr_read_b32 v41, a41\n"
    "PD\n"
    "v_mfma_f32_16x16x32_bf16 a[72:75], a[240:243], a[200:203], a[72:75]\n"
    "PD\n"
    "ds_read_b64_tr_b16 a[228:229], v14 offset:8192\n"
    "v_accvgpr_read_b32 v42, a42\n"
    "v_mfma_f32_16x16x32_bf16 a[72:75], a[244:247], a[216:219], a[72:75]\n"
    "PD\n"
    "v_accvgpr_read_b32 v43, a43\n"
    "v_accvgpr_read_b32 v44, a56\n"
    "v_mfma_f32_16x16x32_bf16 a[76:79], a[244:247], a[220:223], a[76:79]\n"
    "PD\n"
    "ds_read_b64_tr_b16 a[230:231], v14 offset:12288\n"
    "v_accvgpr_read_b32 v45, a57\n"
    "v_mfma_f32_16x16x32_bf16 a[76:79], a[240:243], a[204:207], a[76:79]\n"
    "PD\n"
    "s_waitcnt lgkmcnt(4)\n"
    "v_accvgpr_read_b32 v46, a58\n"
    "v_accvgpr_read_b32 v47, a59\n"
    "v_mfma_f32_16x16x32_bf16 a[92:95], a[248:251], a[204:207], a[92:95]\n"
    "PD\n"
    "ds_read_b64_tr_b16 a[232:233], v15 offset:0\n"
    "v_mul_f32 v40, v40, v168\n"
    "v_mfma_f32_16x16x32_bf16 a[92:95], a[252:255], a[220:223], a[92:95]\n"
    "PD\n"
    "v_mul_f32 v41, v41, v169\n"
    "v_mul_f32 v42, v42, v170\n"
    "v_mfma_f32_16x16x32_bf16 a[88:91], a[252:255], a[216:219], a[88:91]\n"
    "PD\n"
    "ds_read_b64_tr_b16 a[234:235], v15 offset:4096\n"
    "v_mul_f32 v43, v43, v171\n"
    "v_mfma_f32_16x16x32_bf16 a[88:91], a[248:251], a[200:203], a[88:91]\n"
    "PD\n"
    "v_mul_f32 v44, v44, v184\n"
    "v_mul_f32 v45, v45, v185\n"
    "v_mfma_f32_16x16x32_bf16 a[84:87], a[248:251], a[196:199], a[84:87]\n"
    "PD\n"
    "ds_read_b64_tr_b16 a[236:237], v15 offset:8192\n"
    "v_mul_f32 v46, v46, v186\n"
    "v_mfma_f32_16x16x32_bf16 a[84:87], a[252:255], a[212:215], a[84:87]\n"
    "PD\n"
    "v_mul_f32 v47, v47, v187\n"
    "v_cvt_pk_bf16_f32 v88, v40, v41\n"
    "v_mfma_f32_16x16x32_bf16 a[80:83], a[252:255], a[208:211], a[80:83]\n"
    "PD\n"
    "ds_read_b64_tr_b16 a[238:239], v15 offset:12288\n"
    "v_cvt_pk_bf16_f32 v89, v42, v43\n"
    "v_mfma_f32_16x16x32_bf16 a[80:83], a[248:251], a[192:195], a[80:83]\n"
    "PD\n"
    "s_waitcnt lgkmcnt(4)\n"
    "v_cvt_pk_bf16_f32 v90, v44, v45\n"
    "v_cvt_pk_bf16_f32 v91, v46, v47\n"
    "v_mfma_f32_16x16x32_bf16 v[224:227], a[224:227], a[160:163], v[224:227]\n"
    "PD\n"
    "ds_read_b64_tr_b16 a[240:241], v14 offset:16384\n"
    "v_accvgpr_read_b32 v48, a44\n"
    "v_mfma_f32_16x16x32_bf16 v[224:227], a[228:231], a[176:179], v[224:227]\n"
    "PD\n"
    "v_accvgpr_read_b32 v49, a45\n"
    "v_accvgpr_read_b32 v50, a46\n"
    "v_mfma_f32_16x16x32_bf16 v[228:231], a[228:231], a[180:183], v[228:231]\n"
    "PD\n"
    "ds_read_b64_tr_b16 a[242:243], v14 offset:20480\n"
    "v_accvgpr_read_b32 v51, a47\n"
    "v_mfma_f32_16x16x32_bf16 v[228:231], a[224:227], a[164:167], v[228:231]\n"
    "PD\n"
    "buffer_store_dwordx4 v[80:83], v4, s[52:55], 0 offen offset:1024 sc1\n"
    "v_accvgpr_read_b32 v52, a60\n"
    "v_accvgpr_read_b32 v53, a61\n"
    "v_mfma_f32_16x16x32_bf16 v[232:235], a[224:227], a[168:171], v[232:235]\n"
    "PD\n"
    "ds_read_b64_tr_b16 a[244:245], v14 offset:24576\n"
    "v_accvgpr_read_b32 v54, a62\n"
    "v_mfma_f32_16x16x32_bf16 v[232:235], a[228:231], a[184:187], v[232:235]\n"
    "PD\n"
    "v_accvgpr_read_b32 v55, a63\n"
    "v_mul_f32 v48, v48, v172\n"
    "v_mfma_f32_16x16x32_bf16 v[236:239], a[228:231], a[188:191], v[236:239]\n"
    "PD\n"
    "ds_read_b64_tr_b16 a[246:247], v14 offset:28672\n"
    "v_mul_f32 v49, v49, v173\n"
    "v_mfma_f32_16x16x32_bf16 v[236:239], a[224:227], a[172:175], v[236:239]\n"
    "PD\n"
    "s_waitcnt lgkmcnt(4)\n"
    "v_mul_f32 v50, v50, v174\n"
    "v_mul_f32 v51, v51, v175\n"
    "v_mfma_f32_16x16x32_bf16 v[252:255], a[232:235], a[172:175], v[252:255]\n"
    "PD\n"
    "ds_read_b64_tr_b16 a[248:249], v15 offset:16384\n"
    "v_mul_f32 v52, v52, v188\n"
    "v_mfma_f32_16x16x32_bf16 v[252:255], a[236:239], a[188:191], v[252:255]\n"
    "PD\n"
    "v_mul_f32 v53, v53, v189\n"
    "v_mul_f32 v54, v54, v190\n"
    "v_mfma_f32_16x16x32_bf16 v[248:251], a[236:239], a[184:187], v[248:251]\n"
    "PD\n"
    "ds_read_b64_tr_b16 a[250:251], v15 offset:20480\n"
    "v_mul_f32 v55, v55, v191\n"
    "v_mfma_f32_16x16x32_bf16 v[248:251], a[232:235], a[168:171], v[248:251]\n"
    "PD\n"
    "v_cvt_pk_bf16_f32 v92, v48, v49\n"
    "v_cvt_pk_bf16_f32 v93, v50, v51\n"
    "v_mfma_f32_16x16x32_bf16 v[244:247], a[232:235], a[164:167], v[244:247]\n"
    "PD\n"
    "ds_read_b64_tr_b16 a[252:253], v15 offset:24576\n"
    "v_cvt_pk_bf16_f32 v94, v52, v53\n"
    "v_mfma_f32_16x16x32_bf16 v[244:247], a[236:239], a[180:183], v[244:247]\n"
    "PD\n"
    "v_cvt_pk_bf16_f32 v95, v54, v55\n"
    "v_accvgpr_read_b32 v40, a64\n"
    "v_mfma_f32_16x16x32_bf16 v[240:243], a[236:239], a[176:179], v[240:243]\n"
    "PD\n"
    "ds_read_b64_tr_b16 a[254:255], v15 offset:28672\n"
    "v_accvgpr_read_b32 v41, a65\n"
    "v_mfma_f32_16x16x32_bf16 v[240:243], a[232:235], a[160:163], v[240:243]\n"
    "PD\n"
    "s_waitcnt lgkmcnt(4)\n"
    "v_accvgpr_read_b32 v42, a66\n"
    "v_accvgpr_read_b32 v43, a67\n"
    "v_mfma_f32_16x16x32_bf16 a[96:99], a[240:243], a[160:163], a[96:99]\n"
    "PD\n"
    "ds_read_b64_tr_b16 a[224:225], v14 offset:32768\n"
    "v_accvgpr_read_b32 v44, a80\n"
    "v_mfma_f32_16x16x32_bf16 a[96:99], a[244:247], a[176:179], a[96:99]\n"
    "PD\n"
    "v_accvgpr_read_b32 v45, a81\n"
    "v_accvgpr_read_b32 v46, a82\n"
    "v_mfma_f32_16x16x32_bf16 a[100:103], a[244:247], a[180:183], a[100:103]\n"
    "PD\n"
    "ds_read_b64_tr_b16 a[226:227], v14 offset:36864\n"
    "v_accvgpr_read_b32 v47, a83\n"
    "v_mfma_f32_16x16x32_bf16 a[100:103], a[240:243], a[164:167], a[100:103]\n"
    "PD\n"
    "v_mul_f32 v40, v40, v192\n"
    "v_mul_f32 v41, v41, v193\n"
    "v_mfma_f32_16x16x32_bf16 a[104:107], a[240:243], a[168:171], a[104:107]\n"
    "PD\n"
    "ds_read_b64_tr_b16 a[228:229], v14 offset:40960\n"
    "v_mul_f32 v42, v42, v194\n"
    "v_mfma_f32_16x16x32_bf16 a[104:107], a[244:247], a[184:187], a[104:107]\n"
    "PD\n"
    "v_mul_f32 v43, v43, v195\n"
    "v_mul_f32 v44, v44, v208\n"
    "v_mfma_f32_16x16x32_bf16 a[108:111], a[244:247], a[188:191], a[108:111]\n"
    "PD\n"
    "ds_read_b64_tr_b16 a[230:231], v14 offset:45056\n"
    "v_mul_f32 v45, v45, v209\n"
    "v_mfma_f32_16x16x32_bf16 a[108:111], a[240:243], a[172:175], a[108:111]\n"
    "PD\n"
    "s_waitcnt lgkmcnt(4)\n"
    "buffer_store_dwordx4 v[84:87], v4, s[52:55], 0 offen offset:3072 sc1\n"
    "v_mul_f32 v46, v46, v210\n"
    "v_mfma_f32_16x16x32_bf16 a[124:127], a[248:251], a[172:175], a[124:127]\n"
    "PD\n"
    "ds_read_b64_tr_b16 a[232:233], v15 offset:32768\n"
    "v_mul_f32 v47, v47, v211\n"
    "v_mfma_f32_16x16x32_bf16 a[124:127], a[252:255], a[188:191], a[124:127]\n"
    "PD\n"
    "v_cvt_pk_bf16_f32 v96, v40, v41\n"
    "v_cvt_pk_bf16_f32 v97, v42, v43\n"
    "v_mfma_f32_16x16x32_bf16 a[120:123], a[252:255], a[184:187], a[120:123]\n"
    "PD\n"
    "ds_read_b64_tr_b16 a[234:235], v15 offset:36864\n"
    "v_cvt_pk_bf16_f32 v98, v44, v45\n"
    "v_mfma_f32_16x16x32_bf16 a[120:123], a[248:251], a[168:171], a[120:123]\n"
    "PD\n"
    "v_cvt_pk_bf16_f32 v99, v46, v47\n"
    "v_accvgpr_read_b32 v48, a68\n"
    "v_mfma_f32_16x16x32_bf16 a[116:119], a[248:251], a[164:167], a[116:119]\n"
    "PD\n"
    "ds_read_b64_tr_b16 a[236:237], v15 offset:40960\n"
    "v_accvgpr_read_b32 v49, a69\n"
    "v_mfma_f32_16x16x32_bf16 a[116:119], a[252:255], a[180:183], a[116:119]\n"
    "PD\n"
    "v_accvgpr_read_b32 v50, a70\n"
    "v_accvgpr_read_b32 v51, a71\n"
    "v_mfma_f32_16x16x32_bf16 a[112:115], a[252:255], a[176:179], a[112:115]\n"
    "PD\n"
    "ds_read_b64_tr_b16 a[238:239], v15 offset:45056\n"
    "v_accvgpr_read_b32 v52, a84\n"
    "v_mfma_f32_16x16x32_bf16 a[112:115], a[248:251], a[160:163], a[112:115]\n"
    "PD\n"
    "s_waitcnt lgkmcnt(4)\n"
    "v_accvgpr_read_b32 v53, a85\n"
    "v_accvgpr_read_b32 v54, a86\n"
    "v_mfma_f32_16x16x32_bf16 v[224:227], a[224:227], a[192:195], v[224:227]\n"
    "PD\n"
    "ds_read_b64_tr_b16 a[240:241], v14 offset:49152\n"
    "v_accvgpr_read_b32 v55, a87\n"
    "v_mfma_f32_16x16x32_bf16 v[224:227], a[228:231], a[208:211], v[224:227]\n"
    "PD\n"
    "buffer_load_dwordx4 a[160:163], v20, s[40:43], s56 offen offset:0 sc1\n"
    "v_mul_f32 v48, v48, v196\n"
    "v_mul_f32 v49, v49, v197\n"
    "v_mfma_f32_16x16x32_bf16 v[228:231], a[228:231], a[212:215], v[228:231]\n"
    "PD\n"
    "ds_read_b64_tr_b16 a[242:243], v14 offset:53248\n"
    "v_mul_f32 v50, v50, v198\n"
    "v_mfma_f32_16x16x32_bf16 v[228:231], a[224:227], a[196:199], v[228:231]\n"
    "PD\n"
    "buffer_load_dwordx4 a[176:179], v20, s[40:43], s56 offen offset:1024 sc1\n"
    "v_mul_f32 v51, v51, v199\n"
    "v_mul_f32 v52, v52, v212\n"
    "v_mfma_f32_16x16x32_bf16 v[232:235], a[224:227], a[200:203], v[232:235]\n"
    "PD\n"
    "ds_read_b64_tr_b16 a[244:245], v14 offset:57344\n"
    "v_mul_f32 v53, v53, v213\n"
    "v_mfma_f32_16x16x32_bf16 v[232:235], a[228:231], a[216:219], v[232:235]\n"
    "PD\n"
    "buffer_load_dwordx4 a[164:167], v21, s[40:43], s56 offen offset:0 sc1\n"
    "v_mul_f32 v54, v54, v214\n"
    "v_mul_f32 v55, v55, v215\n"
    "v_mfma_f32_16x16x32_bf16 v[236:239], a[228:231], a[220:223], v[236:239]\n"
    "PD\n"
    "ds_read_b64_tr_b16 a[246:247], v14 offset:61440\n"
    "v_xor_b32 v14, 0x10000, v14\n"
    "v_mfma_f32_16x16x32_bf16 v[236:239], a[224:227], a[204:207], v[236:239]\n"
    "PD\n"
    "s_waitcnt lgkmcnt(4)\n"
    "v_cvt_pk_bf16_f32 v100, v48, v49\n"
    "v_cvt_pk_bf16_f32 v101, v50, v51\n"
    "v_mfma_f32_16x16x32_bf16 v[252:255], a[232:235], a[204:207], v[252:255]\n"
    "PD\n"
    "ds_read_b64_tr_b16 a[248:249], v15 offset:49152\n"
    "v_cvt_pk_bf16_f32 v102, v52, v53\n"
    "v_mfma_f32_16x16x32_bf16 v[252:255], a[236:239], a[220:223], v[252:255]\n"
    "PD\n"
    "buffer_load_dwordx4 a[180:183], v21, s[40:43], s56 offen offset:1024 sc1\n"
    "v_cvt_pk_bf16_f32 v103, v54, v55\n"
    "v_accvgpr_read_b32 v40, a72\n"
    "v_mfma_f32_16x16x32_bf16 v[248:251], a[236:239], a[216:219], v[248:251]\n"
    "PD\n"
    "ds_read_b64_tr_b16 a[250:251], v15 offset:53248\n"
    "v_accvgpr_read_b32 v41, a73\n"
    "v_mfma_f32_16x16x32_bf16 v[248:251], a[232:235], a[200:203], v[248:251]\n"
    "PD\n"
    "buffer_store_dwordx4 v[88:91], v5, s[52:55], 0 offen offset:1024 sc1\n"
    "v_accvgpr_read_b32 v42, a74\n"
    "v_accvgpr_read_b32 v43, a75\n"
    "v_mfma_f32_16x16x32_bf16 v[244:247], a[232:235], a[196:199], v[244:247]\n"
    "PD\n"
    "ds_read_b64_tr_b16 a[252:253], v15 offset:57344\n"
    "v_accvgpr_read_b32 v44, a88\n"
    "v_mfma_f32_16x16x32_bf16 v[244:247], a[236:239], a[212:215], v[244:247]\n"
    "PD\n"
    "v_accvgpr_read_b32 v45, a89\n"
    "v_accvgpr_read_b32 v46, a90\n"
    "v_mfma_f32_16x16x32_bf16 v[240:243], a[236:239], a[208:211], v[240:243]\n"
    "PD\n"
    "ds_read_b64_tr_b16 a[254:255], v15 offset:61440\n"
    "v_xor_b32 v15, 0x10000, v15\n"
    "v_mfma_f32_16x16x32_bf16 v[240:243], a[232:235], a[192:195], v[240:243]\n"
    "PD\n"
    "s_waitcnt lgkmcnt(4)\n"
    "s_waitcnt vmcnt(20)\n"
    "s_barrier\n"
    "v_accvgpr_read_b32 v47, a91\n"
    "v_mfma_f32_16x16x32_bf16 a[96:99], a[240:243], a[192:195], a[96:99]\n"
    "PD\n"
    "ds_read_b64_tr_b16 a[224:225], v8 offset:0\n"
    "v_mul_f32 v40, v40, v200\n"
    "v_mfma_f32_16x16x32_bf16 a[96:99], a[244:247], a[208:211], a[96:99]\n"
    "PD\n"
    "s_xor_b32 m0, s60, 65536\n"
    "v_mul_f32 v41, v41, v201\n"
    "v_mul_f32 v42, v42, v202\n"
    "v_mfma_f32_16x16x32_bf16 a[100:103], a[244:247], a[212:215], a[100:103]\n"
    "PD\n"
    "ds_read_b64_tr_b16 a[226:227], v8 offset:4096\n"
    "v_mul_f32 v43, v43, v203\n"
    "v_mfma_f32_16x16x32_bf16 a[100:103], a[240:243], a[196:199], a[100:103]\n"
    "PD\n"
    "buffer_load_dwordx4 v24, s[44:47], s57 offen offset:0 lds\n"
    "v_mul_f32 v44, v44, v216\n"
    "v_mul_f32 v45, v45, v217\n"
    "v_mfma_f32_16x16x32_bf16 a[104:107], a[240:243], a[200:203], a[104:107]\n"
    "PD\n"
    "ds_read_b64_tr_b16 a[228:229], v8 offset:8192\n"
    "v_mul_f32 v46, v46, v218\n"
    "v_mfma_f32_16x16x32_bf16 a[104:107], a[244:247], a[216:219], a[104:107]\n"
    "PD\n"
    "buffer_load_dwordx4 v25, s[44:47], s57 offen offset:1024 lds\n"
    "v_mul_f32 v47, v47, v219\n"
    "v_cvt_pk_bf16_f32 v104, v40, v41\n"
    "v_mfma_f32_16x16x32_bf16 a[108:111], a[244:247], a[220:223], a[108:111]\n"
    "PD\n"
    "ds_read_b64_tr_b16 a[230:231], v8 offset:12288\n"
    "v_cvt_pk_bf16_f32 v105, v42, v43\n"
    "v_mfma_f32_16x16x32_bf16 a[108:111], a[240:243], a[204:207], a[108:111]\n"
    "PD\n"
    "s_waitcnt lgkmcnt(4)\n"
    "v_cvt_pk_bf16_f32 v106, v44, v45\n"
    "v_cvt_pk_bf16_f32 v107, v46, v47\n"
    "v_mfma_f32_16x16x32_bf16 a[124:127], a[248:251], a[204:207], a[124:127]\n"
    "PD\n"
    "ds_read_b64_tr_b16 a[232:233], v9 offset:0\n"
    "v_accvgpr_read_b32 v48, a76\n"
    "v_mfma_f32_16x16x32_bf16 a[124:127], a[252:255], a[220:223], a[124:127]\n"
    "PD\n"
    "buffer_load_dwordx4 v26, s[44:47], s57 offen offset:2048 lds\n"
    "v_accvgpr_read_b32 v49, a77\n"
    "v_accvgpr_read_b32 v50, a78\n"
    "v_mfma_f32_16x16x32_bf16 a[120:123], a[252:255], a[216:219], a[120:123]\n"
    "PD\n"
    "ds_read_b64_tr_b16 a[234:235], v9 offset:4096\n"
    "v_accvgpr_read_b32 v51, a79\n"
    "v_mfma_f32_16x16x32_bf16 a[120:123], a[248:251], a[200:203], a[120:123]\n"
    "PD\n"
    "buffer_load_dwordx4 v27, s[44:47], s57 offen offset:3072 lds\n"
    "v_accvgpr_read_b32 v52, a92\n"
    "v_accvgpr_read_b32 v53, a93\n"
    "v_mfma_f32_16x16x32_bf16 a[116:119], a[248:251], a[196:199], a[116:119]\n"
    "PD\n"
    "ds_read_b64_tr_b16 a[236:237], v9 offset:8192\n"
    "v_accvgpr_read_b32 v54, a94\n"
    "v_mfma_f32_16x16x32_bf16 a[116:119], a[252:255], a[212:215], a[116:119]\n"
    "PD\n"
    "s_add_u32 s57, s57, s59\n"
    "v_accvgpr_read_b32 v55, a95\n"
    "v_mul_f32 v48, v48, v204\n"
    "v_mfma_f32_16x16x32_bf16 a[112:115], a[252:255], a[208:211], a[112:115]\n"
    "PD\n"
    "ds_read_b64_tr_b16 a[238:239], v9 offset:12288\n"
    "v_mul_f32 v49, v49, v205\n"
    "v_mfma_f32_16x16x32_bf16 a[112:115], a[248:251], a[192:195], a[112:115]\n"
    "PD\n"
    "buffer_store_dwordx4 v[92:95], v5, s[52:55], 0 offen offset:3072 sc1\n"
    "v_mul_f32 v50, v50, v206\n"
    "v_mul_f32 v51, v51, v207\n"
    "v_mfma_f32_32x32x16_bf16 v[160:175], v[56:59], v[56:59], 0\n"
    "v_mfma_f32_32x32x16_bf16 v[176:191], v[56:59], v[56:59], 0\n"
    "v_mul_f32 v52, v52, v220\n"
    "v_mul_f32 v53, v53, v221\n"
    "PD\n"
    "v_mul_f32 v54, v54, v222\n"
    "PD\n"
    "v_mul_f32 v55, v55, v223\n"
    "PD\n"
    "v_cvt_pk_bf16_f32 v108, v48, v49\n"
    "PD\n"
    "v_cvt_pk_bf16_f32 v109, v50, v51\n"
    "PD\n"
    "v_cvt_pk_bf16_f32 v110, v52, v53\n"
    "PD\n"
    "v_cvt_pk_bf16_f32 v111, v54, v55\n"
    "PD\n"
    "v_mfma_f32_32x32x16_bf16 v[192:207], v[56:59], v[56:59], 0\n"
    "PD\n"
    "v_mfma_f32_32x32x16_bf16 v[208:223], v[56:59], v[56:59], 0\n"
    "PD\n"
    "s_nop 4\n"
    "PD\n"
    "buffer_load_dwordx4 a[168:171], v22, s[40:43], s56 offen offset:0 sc1\n"
    "PD\n"
    "v_accvgpr_read_b32 v40, a96\n"
    "PD\n"
    "v_accvgpr_read_b32 v41, a97\n"
    "PD\n"
    "v_accvgpr_read_b32 v42, a98\n"
    "PD\n"
    "v_accvgpr_read_b32 v43, a99\n"
    "PD\n"
    "buffer_load_dwordx4 a[184:187], v22, s[40:43], s56 offen offset:1024 sc1\n"
    "PD\n"
    "v_accvgpr_read_b32 v44, a112\n"
    "PD\n"
    "v_accvgpr_read_b32 v45, a113\n"
    "PD\n"
    "v_accvgpr_read_b32 v46, a114\n"
    "PD\n"
    "v_accvgpr_read_b32 v47, a115\n"
    "PD\n"
    "v_pk_mul_f32 v[40:41], v[40:41], v[224:225]\n"
    "PD\n"
    "v_pk_mul_f32 v[42:43], v[42:43], v[226:227]\n"
    "PD\n"
    "v_pk_mul_f32 v[44:45], v[44:45], v[240:241]\n"
    "PD\n"
    "v_pk_mul_f32 v[46:47], v[46:47], v[242:243]\n"
    "PD\n"
    "buffer_load_dwordx4 a[172:175], v23, s[40:43], s56 offen offset:0 sc1\n"
    "PD\n"
    "v_cvt_pk_bf16_f32 v112, v40, v41\n"
    "PD\n"
    "v_cvt_pk_bf16_f32 v113, v42, v43\n"
    "PD\n"
    "v_cvt_pk_bf16_f32 v114, v44, v45\n"
    "PD\n"
    "v_cvt_pk_bf16_f32 v115, v46, v47\n"
    "PD\n"
    "buffer_load_dwordx4 a[188:191], v23, s[40:43], s56 offen offset:1024 sc1\n"
    "PD\n"
    "v_accvgpr_read_b32 v48, a100\n"
    "PD\n"
    "v_accvgpr_read_b32 v49, a101\n"
    "PD\n"
    "v_accvgpr_read_b32 v50, a102\n"
    "PD\n"
    "v_accvgpr_read_b32 v51, a103\n"
    "PD\n"
    "s_add_u32 s56, s56, s69\n"
    "PD\n"
    "v_accvgpr_read_b32 v52, a116\n"
    "PD\n"
    "v_accvgpr_read_b32 v53, a117\n"
    "PD\n"
    "v_accvgpr_read_b32 v54, a118\n"
    "PD\n"
    "v_accvgpr_read_b32 v55, a119\n"
    "PD\n"
    "v_pk_mul_f32 v[48:49], v[48:49], v[228:229]\n"
    "PD\n"
    "v_pk_mul_f32 v[50:51], v[50:51], v[230:231]\n"
    "PD\n"
    "v_pk_mul_f32 v[52:53], v[52:53], v[244:245]\n"
    "PD\n"
    "v_pk_mul_f32 v[54:55], v[54:55], v[246:247]\n"
    "PD\n"
    "buffer_load_dwordx4 a[192:195], v20, s[40:43], s56 offen offset:0 sc1\n"
    "PD\n"
    "v_cvt_pk_bf16_f32 v116, v48, v49\n"
    "PD\n"
    "v_cvt_pk_bf16_f32 v117, v50, v51\n"
    "PD\n"
    "v_cvt_pk_bf16_f32 v118, v52, v53\n"
    "PD\n"
    "v_cvt_pk_bf16_f32 v119, v54, v55\n"
    "PD\n"
    "buffer_load_dwordx4 a[208:211], v20, s[40:43], s56 offen offset:1024 sc1\n"
    "PD\n"
    "v_accvgpr_read_b32 v40, a104\n"
    "PD\n"
    "v_accvgpr_read_b32 v41, a105\n"
    "PD\n"
    "v_accvgpr_read_b32 v42, a106\n"
    "PD\n"
    "v_accvgpr_read_b32 v43, a107\n"
    "PD\n"
    "buffer_load_dwordx4 a[196:199], v21, s[40:43], s56 offen offset:0 sc1\n"
    "PD\n"
    "v_accvgpr_read_b32 v44, a120\n"
    "PD\n"
    "v_accvgpr_read_b32 v45, a121\n"
    "PD\n"
    "v_accvgpr_read_b32 v46, a122\n"
    "PD\n"
    "v_accvgpr_read_b32 v47, a123\n"
    "PD\n"
    "v_pk_mul_f32 v[40:41], v[40:41], v[232:233]\n"
    "PD\n"
    "v_pk_mul_f32 v[42:43], v[42:43], v[234:235]\n"
    "PD\n"
    "v_pk_mul_f32 v[44:45], v[44:45], v[248:249]\n"
    "PD\n"
    "v_pk_mul_f32 v[46:47], v[46:47], v[250:251]\n"
    "PD\n"
    "buffer_load_dwordx4 a[212:215], v21, s[40:43], s56 offen offset:1024 sc1\n"
    "PD\n"
    "v_cvt_pk_bf16_f32 v120, v40, v41\n"
    "PD\n"
    "v_cvt_pk_bf16_f32 v121, v42, v43\n"
    "PD\n"
    "v_cvt_pk_bf16_f32 v122, v44, v45\n"
    "PD\n"
    "v_cvt_pk_bf16_f32 v123, v46, v47\n"
    "PD\n"
    "buffer_load_dwordx4 a[200:203], v22, s[40:43], s56 offen offset:0 sc1\n"
    "PD\n"
    "v_accvgpr_read_b32 v48, a108\n"
    "PD\n"
    "v_accvgpr_read_b32 v49, a109\n"
    "PD\n"
    "v_accvgpr_read_b32 v50, a110\n"
    "PD\n"
    "v_accvgpr_read_b32 v51, a111\n"
    "PD\n"
    "buffer_load_dwordx4 a[216:219], v22, s[40:43], s56 offen offset:1024 sc1\n"
    "PD\n"
    "v_accvgpr_read_b32 v52, a124\n"
    "PD\n"
    "v_accvgpr_read_b32 v53, a125\n"
    "PD\n"
    "v_accvgpr_read_b32 v54, a126\n"
    "PD\n"
    "v_accvgpr_read_b32 v55, a127\n"
    "PD\n"
    "v_pk_mul_f32 v[48:49], v[48:49], v[236:237]\n"
    "PD\n"
    "v_pk_mul_f32 v[50:51], v[50:51], v[238:239]\n"
    "PD\n"
    "v_pk_mul_f32 v[52:53], v[52:53], v[252:253]\n"
    "PD\n"
    "v_pk_mul_f32 v[54:55], v[54:55], v[254:255]\n"
    "PD\n"
    "buffer_load_dwordx4 a[204:207], v23, s[40:43], s56 offen offset:0 sc1\n"
    "PD\n"
    "v_cvt_pk_bf16_f32 v124, v48, v49\n"
    "PD\n"
    "v_cvt_pk_bf16_f32 v125, v50, v51\n"
    "PD\n"
    "v_cvt_pk_bf16_f32 v126, v52, v53\n"
    "PD\n"
    "v_cvt_pk_bf16_f32 v127, v54, v55\n"
    "PD\n"
    "v_mfma_f32_32x32x16_bf16 v[224:239], v[56:59], v[56:59], 0\n"
    "PD\n"
    "v_mfma_f32_32x32x16_bf16 v[240:255], v[56:59], v[56:59], 0\n"
    "PD\n"
    "buffer_load_dwordx4 a[220:223], v23, s[40:43], s56 offen offset:1024 sc1\n"
    "PD\n"
    "s_cmp_eq_u32 s31, 2\n"
    "PD\n"
    "s_cbranch_scc1 E_O\n"
    "PD\n"
    "E_A:\n"
    "PP\n"
    "s_mov_b32 s37, 0\n"
    "PD\n"
    "s_xor_b32 s60, s61, 0x10000\n"
    "PD\n"
    "s_movk_i32 s56, 24576\n"
    "PD\n"
    "s_mov_b32 s38, 3\n"
    "PD\n"
    "s_mov_b32 s31, s99\n"
    "PD\n"
    "s_mov_b32 s99, 2\n"
    "PD\n"
    "s_waitcnt vmcnt(16) lgkmcnt(4)\n"
    "L_Q:\n"
    "PD\n"
    "v_mfma_f32_16x16x32_bf16 v[128:131], a[224:227], a[128:131], v[128:131]\n"
    "PD\n"
    "ds_read_b64_tr_b16 a[240:241], v10 offset:0\n"
    "v_mfma_f32_16x16x32_bf16 v[128:131], a[228:231], a[144:147], v[128:131]\n"
    "PD\n"
    "buffer_load_dwordx4 a[96:99], v20, s[40:43], s56 offen offset:0 sc1\n"
    "v_mfma_f32_16x16x32_bf16 v[132:135], a[228:231], a[148:151], v[132:135]\n"
    "PD\n"
    "ds_read_b64_tr_b16 a[242:243], v10 offset:4096\n"
    "v_mfma_f32_16x16x32_bf16 v[132:135], a[224:227], a[132:135], v[132:135]\n"
    "PD\n"
    "v_mfma_f32_16x16x32_bf16 v[136:139], a[224:227], a[136:139], v[136:139]\n"
    "PD\n"
    "ds_read_b64_tr_b16 a[244:245], v10 offset:8192\n"
    "v_mfma_f32_16x16x32_bf16 v[136:139], a[228:231], a[152:155], v[136:139]\n"
    "PD\n"
    "buffer_load_dwordx4 a[112:115], v20, s[40:43], s56 offen offset:1024 sc1\n"
    "v_mfma_f32_16x16x32_bf16 v[140:143], a[228:231], a[156:159], v[140:143]\n"
    "PD\n"
    "ds_read_b64_tr_b16 a[246:247], v10 offset:12288\n"
    "v_mfma_f32_16x16x32_bf16 v[140:143], a[224:227], a[140:143], v[140:143]\n"
    "PD\n"
    "s_waitcnt lgkmcnt(4)\n"
    "v_mfma_f32_16x16x32_bf16 v[156:159], a[232:235], a[140:143], v[156:159]\n"
    "PD\n"
    "ds_read_b64_tr_b16 a[248:249], v11 offset:0\n"
    "v_mfma_f32_16x16x32_bf16 v[156:159], a[236:239], a[156:159], v[156:159]\n"
    "PD\n"
    "buffer_load_dwordx4 a[100:103], v21, s[40:43], s56 offen offset:0 sc1\n"
    "v_mfma_f32_16x16x32_bf16 v[152:155], a[236:239], a[152:155], v[152:155]\n"
    "PD\n"
    "ds_read_b64_tr_b16 a[250:251], v11 offset:4096\n"
    "v_mfma_f32_16x16x32_bf16 v[152:155], a[232:235], a[136:139], v[152:155]\n"
    "PD\n"
    "v_mfma_f32_16x16x32_bf16 v[148:151], a[232:235], a[132:135], v[148:151]\n"
    "PD\n"
    "ds_read_b64_tr_b16 a[252:253], v11 offset:8192\n"
    "v_mfma_f32_16x16x32_bf16 v[148:151], a[236:239], a[148:151], v[148:151]\n"
    "PD\n"
    "buffer_load_dwordx4 a[116:119], v21, s[40:43], s56 offen offset:1024 sc1\n"
    "v_mfma_f32_16x16x32_bf16 v[144:147], a[236:239], a[144:147], v[144:147]\n"
    "PD\n"
    "ds_read_b64_tr_b16 a[254:255], v11 offset:12288\n"
    "v_mfma_f32_16x16x32_bf16 v[144:147], a[232:235], a[128:131], v[144:147]\n"
    "PD\n"
    "s_waitcnt lgkmcnt(4)\n"
    "v_mfma_f32_16x16x32_bf16 v[160:163], a[240:243], a[128:131], v[160:163]\n"
    "PD\n"
    "ds_read_b64_tr_b16 a[224:225], v12 offset:0\n"
    "v_mfma_f32_16x16x32_bf16 v[160:163], a[244:247], a[144:147], v[160:163]\n"
    "PD\n"
    "buffer_load_dwordx4 a[104:107], v22, s[40:43], s56 offen offset:0 sc1\n"
    "v_mfma_f32_16x16x32_bf16 v[164:167], a[244:247], a[148:151], v[164:167]\n"
    "PD\n"
    "ds_read_b64_tr_b16 a[226:227], v12 offset:4096\n"
    "v_mfma_f32_16x16x32_bf16 v[164:167], a[240:243], a[132:135], v[164:167]\n"
    "PD\n"
    "v_mfma_f32_16x16x32_bf16 v[168:171], a[240:243], a[136:139], v[168:171]\n"
    "PD\n"
    "ds_read_b64_tr_b16 a[228:229], v12 offset:8192\n"
    "v_mfma_f32_16x16x32_bf16 v[168:171], a[244:247], a[152:155], v[168:171]\n"
    "PD\n"
    "buffer_load_dwordx4 a[120:123], v22, s[40:43], s56 offen offset:1024 sc1\n"
    "v_mfma_f32_16x16x32_bf16 v[172:175], a[244:247], a[156:159], v[172:175]\n"
    "PD\n"
    "ds_read_b64_tr_b16 a[230:231], v12 offset:12288\n"
    "v_mfma_f32_16x16x32_bf16 v[172:175], a[240:243], a[140:143], v[172:175]\n"
    "PD\n"
    "s_waitcnt lgkmcnt(4)\n"
    "v_mfma_f32_16x16x32_bf16 v[188:191], a[248:251], a[140:143], v[188:191]\n"
    "PD\n"
    "ds_read_b64_tr_b16 a[232:233], v13 offset:0\n"
    "v_mfma_f32_16x16x32_bf16 v[188:191], a[252:255], a[156:159], v[188:191]\n"
    "PD\n"
    "buffer_load_dwordx4 a[108:111], v23, s[40:43], s56 offen offset:0 sc1\n"
    "v_mfma_f32_16x16x32_bf16 v[184:187], a[252:255], a[152:155], v[184:187]\n"
    "PD\n"
    "ds_read_b64_tr_b16 a[234:235], v13 offset:4096\n"
    "v_mfma_f32_16x16x32_bf16 v[184:187], a[248:251], a[136:139], v[184:187]\n"
    "PD\n"
    "v_mfma_f32_16x16x32_bf16 v[180:183], a[248:251], a[132:135], v[180:183]\n"
    "PD\n"
    "ds_read_b64_tr_b16 a[236:237], v13 offset:8192\n"
    "v_mfma_f32_16x16x32_bf16 v[180:183], a[252:255], a[148:151], v[180:183]\n"
    "PD\n"
    "buffer_load_dwordx4 a[124:127], v23, s[40:43], s56 offen offset:1024 sc1\n"
    "v_mfma_f32_16x16x32_bf16 v[176:179], a[252:255], a[144:147], v[176:179]\n"
    "PD\n"
    "ds_read_b64_tr_b16 a[238:239], v13 offset:12288\n"
    "v_mfma_f32_16x16x32_bf16 v[176:179], a[248:251], a[128:131], v[176:179]\n"
    "PD\n"
    "s_waitcnt lgkmcnt(4)\n"
    "v_mfma_f32_16x16x32_bf16 v[192:195], a[224:227], a[128:131], v[192:195]\n"
    "PD\n"
    "ds_read_b64_tr_b16 a[240:241], v14 offset:0\n"
    "v_mfma_f32_16x16x32_bf16 v[192:195], a[228:231], a[144:147], v[192:195]\n"
    "PD\n"
    "s_add_u32 s56, s56, s69\n"
    "s_add_u32 m0, s60, 16384\n"
    "v_mfma_f32_16x16x32_bf16 v[196:199], a[228:231], a[148:151], v[196:199]\n"
    "PD\n"
    "ds_read_b64_tr_b16 a[242:243], v14 offset:4096\n"
    "v_mfma_f32_16x16x32_bf16 v[196:199], a[224:227], a[132:135], v[196:199]\n"
    "PD\n"
    "v_mfma_f32_16x16x32_bf16 v[200:203], a[224:227], a[136:139], v[200:203]\n"
    "PD\n"
    "ds_read_b64_tr_b16 a[244:245], v14 offset:8192\n"
    "v_mfma_f32_16x16x32_bf16 v[200:203], a[228:231], a[152:155], v[200:203]\n"
    "PD\n"
    "buffer_load_dwordx4 v28, s[48:51], s58 offen offset:0 lds\n"
    "v_mfma_f32_16x16x32_bf16 v[204:207], a[228:231], a[156:159], v[204:207]\n"
    "PD\n"
    "ds_read_b64_tr_b16 a[246:247], v14 offset:12288\n"
    "v_mfma_f32_16x16x32_bf16 v[204:207], a[224:227], a[140:143], v[204:207]\n"
    "PD\n"
    "s_waitcnt lgkmcnt(4)\n"
    "v_mfma_f32_16x16x32_bf16 v[220:223], a[232:235], a[140:143], v[220:223]\n"
    "PD\n"
    "ds_read_b64_tr_b16 a[248:249], v15 offset:0\n"
    "v_mfma_f32_16x16x32_bf16 v[220:223], a[236:239], a[156:159], v[220:223]\n"
    "PD\n"
    "buffer_load_dwordx4 v29, s[48:51], s58 offen offset:1024 lds\n"
    "v_mfma_f32_16x16x32_bf16 v[216:219], a[236:239], a[152:155], v[216:219]\n"
    "PD\n"
    "ds_read_b64_tr_b16 a[250:251], v15 offset:4096\n"
    "v_mfma_f32_16x16x32_bf16 v[216:219], a[232:235], a[136:139], v[216:219]\n"
    "PD\n"
    "v_mfma_f32_16x16x32_bf16 v[212:215], a[232:235], a[132:135], v[212:215]\n"
    "PD\n"
    "ds_read_b64_tr_b16 a[252:253], v15 offset:8192\n"
    "v_mfma_f32_16x16x32_bf16 v[212:215], a[236:239], a[148:151], v[212:215]\n"
    "PD\n"
    "v_mfma_f32_16x16x32_bf16 v[208:211], a[236:239], a[144:147], v[208:211]\n"
    "PD\n"
    "ds_read_b64_tr_b16 a[254:255], v15 offset:12288\n"
    "v_mfma_f32_16x16x32_bf16 v[208:211], a[232:235], a[128:131], v[208:211]\n"
    "PD\n"
    "s_waitcnt lgkmcnt(4)\n"
    "v_mfma_f32_16x16x32_bf16 v[224:227], a[240:243], a[128:131], v[224:227]\n"
    "PD\n"
    "ds_read_b64_tr_b16 a[224:225], v8 offset:16384\n"
    "v_mfma_f32_16x16x32_bf16 v[224:227], a[244:247], a[144:147], v[224:227]\n"
    "PD\n"
    "buffer_load_dwordx4 v30, s[48:51], s58 offen offset:2048 lds\n"
    "v_mfma_f32_16x16x32_bf16 v[228:231], a[244:247], a[148:151], v[228:231]\n"
    "PD\n"
    "ds_read_b64_tr_b16 a[226:227], v8 offset:20480\n"
    "v_mfma_f32_16x16x32_bf16 v[228:231], a[240:243], a[132:135], v[228:231]\n"
    "PD\n"
    "v_mfma_f32_16x16x32_bf16 v[232:235], a[240:243], a[136:139], v[232:235]\n"
    "PD\n"
    "ds_read_b64_tr_b16 a[228:229], v8 offset:24576\n"
    "v_mfma_f32_16x16x32_bf16 v[232:235], a[244:247], a[152:155], v[232:235]\n"
    "PD\n"
    "v_mfma_f32_16x16x32_bf16 v[236:239], a[244:247], a[156:159], v[236:239]\n"
    "PD\n"
    "ds_read_b64_tr_b16 a[230:231], v8 offset:28672\n"
    "v_mfma_f32_16x16x32_bf16 v[236:239], a[240:243], a[140:143], v[236:239]\n"
    "PD\n"
    "s_waitcnt lgkmcnt(4)\n"
    "v_mfma_f32_16x16x32_bf16 v[252:255], a[248:251], a[140:143], v[252:255]\n"
    "PD\n"
    "ds_read_b64_tr_b16 a[232:233], v9 offset:16384\n"
    "v_mfma_f32_16x16x32_bf16 v[252:255], a[252:255], a[156:159], v[252:255]\n"
    "PD\n"
    "buffer_load_dwordx4 v31, s[48:51], s58 offen offset:3072 lds\n"
    "v_mfma_f32_16x16x32_bf16 v[248:251], a[252:255], a[152:155], v[248:251]\n"
    "PD\n"
    "ds_read_b64_tr_b16 a[234:235], v9 offset:20480\n"
    "v_mfma_f32_16x16x32_bf16 v[248:251], a[248:251], a[136:139], v[248:251]\n"
    "PD\n"
    "s_add_u32 s58, s58, s59\n"
    "v_mfma_f32_16x16x32_bf16 v[244:247], a[248:251], a[132:135], v[244:247]\n"
    "PD\n"
    "ds_read_b64_tr_b16 a[236:237], v9 offset:24576\n"
    "v_mfma_f32_16x16x32_bf16 v[244:247], a[252:255], a[148:151], v[244:247]\n"
    "PD\n"
    "v_mfma_f32_16x16x32_bf16 v[240:243], a[252:255], a[144:147], v[240:243]\n"
    "PD\n"
    "ds_read_b64_tr_b16 a[238:239], v9 offset:28672\n"
    "v_mfma_f32_16x16x32_bf16 v[240:243], a[248:251], a[128:131], v[240:243]\n"
    "PD\n"
    "s_waitcnt vmcnt(20) lgkmcnt(4)\n"
    "v_mfma_f32_16x16x32_bf16 v[128:131], a[224:227], a[160:163], v[128:131]\n"
    "PD\n"
    "ds_read_b64_tr_b16 a[240:241], v10 offset:16384\n"
    "v_mfma_f32_16x16x32_bf16 v[128:131], a[228:231], a[176:179], v[128:131]\n"
    "PD\n"
    "buffer_load_dwordx4 a[128:131], v20, s[40:43], s56 offen offset:0 sc1\n"
    "v_mfma_f32_16x16x32_bf16 v[132:135], a[228:231], a[180:183], v[132:135]\n"
    "PD\n"
    "ds_read_b64_tr_b16 a[242:243], v10 offset:20480\n"
    "v_mfma_f32_16x16x32_bf16 v[132:135], a[224:227], a[164:167], v[132:135]\n"
    "PD\n"
    "v_mfma_f32_16x16x32_bf16 v[136:139], a[224:227], a[168:171], v[136:139]\n"
    "PD\n"
    "ds_read_b64_tr_b16 a[244:245], v10 offset:24576\n"
    "v_mfma_f32_16x16x32_bf16 v[136:139], a[228:231], a[184:187], v[136:139]\n"
    "PD\n"
    "buffer_load_dwordx4 a[144:147], v20, s[40:43], s56 offen offset:1024 sc1\n"
    "v_mfma_f32_16x16x32_bf16 v[140:143], a[228:231], a[188:191], v[140:143]\n"
    "PD\n"
    "ds_read_b64_tr_b16 a[246:247], v10 offset:28672\n"
    "v_mfma_f32_16x16x32_bf16 v[140:143], a[224:227], a[172:175], v[140:143]\n"
    "PD\n"
    "s_waitcnt lgkmcnt(4)\n"
    "v_mfma_f32_16x16x32_bf16 v[156:159], a[232:235], a[172:175], v[156:159]\n"
    "PD\n"
    "ds_read_b64_tr_b16 a[248:249], v11 offset:16384\n"
    "v_mfma_f32_16x16x32_bf16 v[156:159], a[236:239], a[188:191], v[156:159]\n"
    "PD\n"
    "buffer_load_dwordx4 a[132:135], v21, s[40:43], s56 offen offset:0 sc1\n"
    "v_mfma_f32_16x16x32_bf16 v[152:155], a[236:239], a[184:187], v[152:155]\n"
    "PD\n"
    "ds_read_b64_tr_b16 a[250:251], v11 offset:20480\n"
    "v_mfma_f32_16x16x32_bf16 v[152:155], a[232:235], a[168:171], v[152:155]\n"
    "PD\n"
    "v_mfma_f32_16x16x32_bf16 v[148:151], a[232:235], a[164:167], v[148:151]\n"
    "PD\n"
    "ds_read_b64_tr_b16 a[252:253], v11 offset:24576\n"
    "v_mfma_f32_16x16x32_bf16 v[148:151], a[236:239], a[180:183], v[148:151]\n"
    "PD\n"
    "buffer_load_dwordx4 a[148:151], v21, s[40:43], s56 offen offset:1024 sc1\n"
    "v_mfma_f32_16x16x32_bf16 v[144:147], a[236:239], a[176:179], v[144:147]\n"
    "PD\n"
    "ds_read_b64_tr_b16 a[254:255], v11 offset:28672\n"
    "v_mfma_f32_16x16x32_bf16 v[144:147], a[232:235], a[160:163], v[144:147]\n"
    "PD\n"
    "s_waitcnt lgkmcnt(4)\n"
    "v_mfma_f32_16x16x32_bf16 v[160:163], a[240:243], a[160:163], v[160:163]\n"
    "PD\n"
    "ds_read_b64_tr_b16 a[224:225], v12 offset:16384\n"
    "v_mfma_f32_16x16x32_bf16 v[160:163], a[244:247], a[176:179], v[160:163]\n"
    "PD\n"
    "buffer_load_dwordx4 a[136:139], v22, s[40:43], s56 offen offset:0 sc1\n"
    "v_mfma_f32_16x16x32_bf16 v[164:167], a[244:247], a[180:183], v[164:167]\n"
    "PD\n"
    "ds_read_b64_tr_b16 a[226:227], v12 offset:20480\n"
    "v_mfma_f32_16x16x32_bf16 v[164:167], a[240:243], a[164:167], v[164:167]\n"
    "PD\n"
    "v_mfma_f32_16x16x32_bf16 v[168:171], a[240:243], a[168:171], v[168:171]\n"
    "PD\n"
    "ds_read_b64_tr_b16 a[228:229], v12 offset:24576\n"
    "v_mfma_f32_16x16x32_bf16 v[168:171], a[244:247], a[184:187], v[168:171]\n"
    "PD\n"
    "buffer_load_dwordx4 a[152:155], v22, s[40:43], s56 offen offset:1024 sc1\n"
    "v_mfma_f32_16x16x32_bf16 v[172:175], a[244:247], a[188:191], v[172:175]\n"
    "PD\n"
    "ds_read_b64_tr_b16 a[230:231], v12 offset:28672\n"
    "v_mfma_f32_16x16x32_bf16 v[172:175], a[240:243], a[172:175], v[172:175]\n"
    "PD\n"
    "s_waitcnt lgkmcnt(4)\n"
    "v_mfma_f32_16x16x32_bf16 v[188:191], a[248:251], a[172:175], v[188:191]\n"
    "PD\n"
    "ds_read_b64_tr_b16 a[232:233], v13 offset:16384\n"
    "v_mfma_f32_16x16x32_bf16 v[188:191], a[252:255], a[188:191], v[188:191]\n"
    "PD\n"
    "buffer_load_dwordx4 a[140:143], v23, s[40:43], s56 offen offset:0 sc1\n"
    "v_mfma_f32_16x16x32_bf16 v[184:187], a[252:255], a[184:187], v[184:187]\n"
    "PD\n"
    "ds_read_b64_tr_b16 a[234:235], v13 offset:20480\n"
    "v_mfma_f32_16x16x32_bf16 v[184:187], a[248:251], a[168:171], v[184:187]\n"
    "PD\n"
    "v_mfma_f32_16x16x32_bf16 v[180:183], a[248:251], a[164:167], v[180:183]\n"
    "PD\n"
    "ds_read_b64_tr_b16 a[236:237], v13 offset:24576\n"
    "v_mfma_f32_16x16x32_bf16 v[180:183], a[252:255], a[180:183], v[180:183]\n"
    "PD\n"
    "buffer_load_dwordx4 a[156:159], v23, s[40:43], s56 offen offset:1024 sc1\n"
    "v_mfma_f32_16x16x32_bf16 v[176:179], a[252:255], a[176:179], v[176:179]\n"
    "PD\n"
    "ds_read_b64_tr_b16 a[238:239], v13 offset:28672\n"
    "v_mfma_f32_16x16x32_bf16 v[176:179], a[248:251], a[160:163], v[176:179]\n"
    "PD\n"
    "s_waitcnt lgkmcnt(4)\n"
    "v_mfma_f32_16x16x32_bf16 v[192:195], a[224:227], a[160:163], v[192:195]\n"
    "PD\n"
    "ds_read_b64_tr_b16 a[240:241], v14 offset:16384\n"
    "v_mfma_f32_16x16x32_bf16 v[192:195], a[228:231], a[176:179], v[192:195]\n"
    "PD\n"
    "s_add_u32 s56, s56, s69\n"
    "s_add_u32 m0, s60, 32768\n"
    "v_mfma_f32_16x16x32_bf16 v[196:199], a[228:231], a[180:183], v[196:199]\n"
    "PD\n"
    "ds_read_b64_tr_b16 a[242:243], v14 offset:20480\n"
    "v_mfma_f32_16x16x32_bf16 v[196:199], a[224:227], a[164:167], v[196:199]\n"
    "PD\n"
    "v_mfma_f32_16x16x32_bf16 v[200:203], a[224:227], a[168:171], v[200:203]\n"
    "PD\n"
    "ds_read_b64_tr_b16 a[244:245], v14 offset:24576\n"
    "v_mfma_f32_16x16x32_bf16 v[200:203], a[228:231], a[184:187], v[200:203]\n"
    "PD\n"
    "buffer_load_dwordx4 v24, s[44:47], s57 offen offset:0 lds\n"
    "v_mfma_f32_16x16x32_bf16 v[204:207], a[228:231], a[188:191], v[204:207]\n"
    "PD\n"
    "ds_read_b64_tr_b16 a[246:247], v14 offset:28672\n"
    "v_mfma_f32_16x16x32_bf16 v[204:207], a[224:227], a[172:175], v[204:207]\n"
    "PD\n"
    "s_waitcnt lgkmcnt(4)\n"
    "v_mfma_f32_16x16x32_bf16 v[220:223], a[232:235], a[172:175], v[220:223]\n"
    "PD\n"
    "ds_read_b64_tr_b16 a[248:249], v15 offset:16384\n"
    "v_mfma_f32_16x16x32_bf16 v[220:223], a[236:239], a[188:191], v[220:223]\n"
    "PD\n"
    "buffer_load_dwordx4 v25, s[44:47], s57 offen offset:1024 lds\n"
    "v_mfma_f32_16x16x32_bf16 v[216:219], a[236:239], a[184:187], v[216:219]\n"
    "PD\n"
    "ds_read_b64_tr_b16 a[250:251], v15 offset:20480\n"
    "v_mfma_f32_16x16x32_bf16 v[216:219], a[232:235], a[168:171], v[216:219]\n"
    "PD\n"
    "v_mfma_f32_16x16x32_bf16 v[212:215], a[232:235], a[164:167], v[212:215]\n"
    "PD\n"
    "ds_read_b64_tr_b16 a[252:253], v15 offset:24576\n"
    "v_mfma_f32_16x16x32_bf16 v[212:215], a[236:239], a[180:183], v[212:215]\n"
    "PD\n"
    "v_mfma_f32_16x16x32_bf16 v[208:211], a[236:239], a[176:179], v[208:211]\n"
    "PD\n"
    "ds_read_b64_tr_b16 a[254:255], v15 offset:28672\n"
    "v_mfma_f32_16x16x32_bf16 v[208:211], a[232:235], a[160:163], v[208:211]\n"
    "PD\n"
    "s_waitcnt lgkmcnt(4)\n"
    "s_barrier\n"
    "v_mfma_f32_16x16x32_bf16 v[224:227], a[240:243], a[160:163], v[224:227]\n"
    "PD\n"
    "ds_read_b64_tr_b16 a[224:225], v8 offset:32768\n"
    "v_mfma_f32_16x16x32_bf16 v[224:227], a[244:247], a[176:179], v[224:227]\n"
    "PD\n"
    "buffer_load_dwordx4 v26, s[44:47], s57 offen offset:2048 lds\n"
    "v_mfma_f32_16x16x32_bf16 v[228:231], a[244:247], a[180:183], v[228:231]\n"
    "PD\n"
    "ds_read_b64_tr_b16 a[226:227], v8 offset:36864\n"
    "v_mfma_f32_16x16x32_bf16 v[228:231], a[240:243], a[164:167], v[228:231]\n"
    "PD\n"
    "v_mfma_f32_16x16x32_bf16 v[232:235], a[240:243], a[168:171], v[232:235]\n"
    "PD\n"
    "ds_read_b64_tr_b16 a[228:229], v8 offset:40960\n"
    "v_mfma_f32_16x16x32_bf16 v[232:235], a[244:247], a[184:187], v[232:235]\n"
    "PD\n"
    "v_mfma_f32_16x16x32_bf16 v[236:239], a[244:247], a[188:191], v[236:239]\n"
    "PD\n"
    "ds_read_b64_tr_b16 a[230:231], v8 offset:45056\n"
    "v_mfma_f32_16x16x32_bf16 v[236:239], a[240:243], a[172:175], v[236:239]\n"
    "PD\n"
    "s_waitcnt lgkmcnt(4)\n"
    "v_mfma_f32_16x16x32_bf16 v[252:255], a[248:251], a[172:175], v[252:255]\n"
    "PD\n"
    "ds_read_b64_tr_b16 a[232:233], v9 offset:32768\n"
    "v_mfma_f32_16x16x32_bf16 v[252:255], a[252:255], a[188:191], v[252:255]\n"
    "PD\n"
    "buffer_load_dwordx4 v27, s[44:47], s57 offen offset:3072 lds\n"
    "v_mfma_f32_16x16x32_bf16 v[248:251], a[252:255], a[184:187], v[248:251]\n"
    "PD\n"
    "ds_read_b64_tr_b16 a[234:235], v9 offset:36864\n"
    "v_mfma_f32_16x16x32_bf16 v[248:251], a[248:251], a[168:171], v[248:251]\n"
    "PD\n"
    "s_add_u32 s57, s57, s59\n"
    "v_mfma_f32_16x16x32_bf16 v[244:247], a[248:251], a[164:167], v[244:247]\n"
    "PD\n"
    "ds_read_b64_tr_b16 a[236:237], v9 offset:40960\n"
    "v_mfma_f32_16x16x32_bf16 v[244:247], a[252:255], a[180:183], v[244:247]\n"
    "PD\n"
    "v_mfma_f32_16x16x32_bf16 v[240:243], a[252:255], a[176:179], v[240:243]\n"
    "PD\n"
    "ds_read_b64_tr_b16 a[238:239], v9 offset:45056\n"
    "v_mfma_f32_16x16x32_bf16 v[240:243], a[248:251], a[160:163], v[240:243]\n"
    "PD\n"
    "s_waitcnt vmcnt(24) lgkmcnt(4)\n"
    "v_mfma_f32_16x16x32_bf16 v[128:131], a[224:227], a[192:195], v[128:131]\n"
    "PD\n"
    "ds_read_b64_tr_b16 a[240:241], v10 offset:32768\n"
    "v_mfma_f32_16x16x32_bf16 v[128:131], a[228:231], a[208:211], v[128:131]\n"
    "PD\n"
    "buffer_load_dwordx4 a[160:163], v20, s[40:43], s56 offen offset:0 sc1\n"
    "v_mfma_f32_16x16x32_bf16 v[132:135], a[228:231], a[212:215], v[132:135]\n"
    "PD\n"
    "ds_read_b64_tr_b16 a[242:243], v10 offset:36864\n"
    "v_mfma_f32_16x16x32_bf16 v[132:135], a[224:227], a[196:199], v[132:135]\n"
    "PD\n"
    "v_mfma_f32_16x16x32_bf16 v[136:139], a[224:227], a[200:203], v[136:139]\n"
    "PD\n"
    "ds_read_b64_tr_b16 a[244:245], v10 offset:40960\n"
    "v_mfma_f32_16x16x32_bf16 v[136:139], a[228:231], a[216:219], v[136:139]\n"
    "PD\n"
    "buffer_load_dwordx4 a[176:179], v20, s[40:43], s56 offen offset:1024 sc1\n"
    "v_mfma_f32_16x16x32_bf16 v[140:143], a[228:231], a[220:223], v[140:143]\n"
    "PD\n"
    "ds_read_b64_tr_b16 a[246:247], v10 offset:45056\n"
    "v_mfma_f32_16x16x32_bf16 v[140:143], a[224:227], a[204:207], v[140:143]\n"
    "PD\n"
    "s_waitcnt lgkmcnt(4)\n"
    "v_mfma_f32_16x16x32_bf16 v[156:159], a[232:235], a[204:207], v[156:159]\n"
    "PD\n"
    "ds_read_b64_tr_b16 a[248:249], v11 offset:32768\n"
    "v_mfma_f32_16x16x32_bf16 v[156:159], a[236:239], a[220:223], v[156:159]\n"
    "PD\n"
    "buffer_load_dwordx4 a[164:167], v21, s[40:43], s56 offen offset:0 sc1\n"
    "v_mfma_f32_16x16x32_bf16 v[152:155], a[236:239], a[216:219], v[152:155]\n"
    "PD\n"
    "ds_read_b64_tr_b16 a[250:251], v11 offset:36864\n"
    "v_mfma_f32_16x16x32_bf16 v[152:155], a[232:235], a[200:203], v[152:155]\n"
    "PD\n"
    "v_mfma_f32_16x16x32_bf16 v[148:151], a[232:235], a[196:199], v[148:151]\n"
    "PD\n"
    "ds_read_b64_tr_b16 a[252:253], v11 offset:40960\n"
    "v_mfma_f32_16x16x32_bf16 v[148:151], a[236:239], a[212:215], v[148:151]\n"
    "PD\n"
    "buffer_load_dwordx4 a[180:183], v21, s[40:43], s56 offen offset:1024 sc1\n"
    "v_mfma_f32_16x16x32_bf16 v[144:147], a[236:239], a[208:211], v[144:147]\n"
    "PD\n"
    "ds_read_b64_tr_b16 a[254:255], v11 offset:45056\n"
    "v_mfma_f32_16x16x32_bf16 v[144:147], a[232:235], a[192:195], v[144:147]\n"
    "PD\n"
    "s_waitcnt lgkmcnt(4)\n"
    "v_mfma_f32_16x16x32_bf16 v[160:163], a[240:243], a[192:195], v[160:163]\n"
    "PD\n"
    "ds_read_b64_tr_b16 a[224:225], v12 offset:32768\n"
    "v_mfma_f32_16x16x32_bf16 v[160:163], a[244:247], a[208:211], v[160:163]\n"
    "PD\n"
    "buffer_load_dwordx4 a[168:171], v22, s[40:43], s56 offen offset:0 sc1\n"
    "v_mfma_f32_16x16x32_bf16 v[164:167], a[244:247], a[212:215], v[164:167]\n"
    "PD\n"
    "ds_read_b64_tr_b16 a[226:227], v12 offset:36864\n"
    "v_mfma_f32_16x16x32_bf16 v[164:167], a[240:243], a[196:199], v[164:167]\n"
    "PD\n"
    "v_mfma_f32_16x16x32_bf16 v[168:171], a[240:243], a[200:203], v[168:171]\n"
    "PD\n"
    "ds_read_b64_tr_b16 a[228:229], v12 offset:40960\n"
    "v_mfma_f32_16x16x32_bf16 v[168:171], a[244:247], a[216:219], v[168:171]\n"
    "PD\n"
    "buffer_load_dwordx4 a[184:187], v22, s[40:43], s56 offen offset:1024 sc1\n"
    "v_mfma_f32_16x16x32_bf16 v[172:175], a[244:247], a[220:223], v[172:175]\n"
    "PD\n"
    "ds_read_b64_tr_b16 a[230:231], v12 offset:45056\n"
    "v_mfma_f32_16x16x32_bf16 v[172:175], a[240:243], a[204:207], v[172:175]\n"
    "PD\n"
    "s_waitcnt lgkmcnt(4)\n"
    "v_mfma_f32_16x16x32_bf16 v[188:191], a[248:251], a[204:207], v[188:191]\n"
    "PD\n"
    "ds_read_b64_tr_b16 a[232:233], v13 offset:32768\n"
    "v_mfma_f32_16x16x32_bf16 v[188:191], a[252:255], a[220:223], v[188:191]\n"
    "PD\n"
    "buffer_load_dwordx4 a[172:175], v23, s[40:43], s56 offen offset:0 sc1\n"
    "v_mfma_f32_16x16x32_bf16 v[184:187], a[252:255], a[216:219], v[184:187]\n"
    "PD\n"
    "ds_read_b64_tr_b16 a[234:235], v13 offset:36864\n"
    "v_mfma_f32_16x16x32_bf16 v[184:187], a[248:251], a[200:203], v[184:187]\n"
    "PD\n"
    "v_mfma_f32_16x16x32_bf16 v[180:183], a[248:251], a[196:199], v[180:183]\n"
    "PD\n"
    "ds_read_b64_tr_b16 a[236:237], v13 offset:40960\n"
    "v_mfma_f32_16x16x32_bf16 v[180:183], a[252:255], a[212:215], v[180:183]\n"
    "PD\n"
    "buffer_load_dwordx4 a[188:191], v23, s[40:43], s56 offen offset:1024 sc1\n"
    "v_mfma_f32_16x16x32_bf16 v[176:179], a[252:255], a[208:211], v[176:179]\n"
    "PD\n"
    "ds_read_b64_tr_b16 a[238:239], v13 offset:45056\n"
    "v_mfma_f32_16x16x32_bf16 v[176:179], a[248:251], a[192:195], v[176:179]\n"
    "PD\n"
    "s_waitcnt lgkmcnt(4)\n"
    "v_mfma_f32_16x16x32_bf16 v[192:195], a[224:227], a[192:195], v[192:195]\n"
    "PD\n"
    "ds_read_b64_tr_b16 a[240:241], v14 offset:32768\n"
    "v_mfma_f32_16x16x32_bf16 v[192:195], a[228:231], a[208:211], v[192:195]\n"
    "PD\n"
    "s_add_u32 s56, s56, s69\n"
    "s_add_u32 m0, s60, 49152\n"
    "v_mfma_f32_16x16x32_bf16 v[196:199], a[228:231], a[212:215], v[196:199]\n"
    "PD\n"
    "ds_read_b64_tr_b16 a[242:243], v14 offset:36864\n"
    "v_mfma_f32_16x16x32_bf16 v[196:199], a[224:227], a[196:199], v[196:199]\n"
    "PD\n"
    "v_mfma_f32_16x16x32_bf16 v[200:203], a[224:227], a[200:203], v[200:203]\n"
    "PD\n"
    "ds_read_b64_tr_b16 a[244:245], v14 offset:40960\n"
    "v_mfma_f32_16x16x32_bf16 v[200:203], a[228:231], a[216:219], v[200:203]\n"
    "PD\n"
    "buffer_load_dwordx4 v28, s[48:51], s58 offen offset:0 lds\n"
    "v_mfma_f32_16x16x32_bf16 v[204:207], a[228:231], a[220:223], v[204:207]\n"
    "PD\n"
    "ds_read_b64_tr_b16 a[246:247], v14 offset:45056\n"
    "v_mfma_f32_16x16x32_bf16 v[204:207], a[224:227], a[204:207], v[204:207]\n"
    "PD\n"
    "s_waitcnt lgkmcnt(4)\n"
    "v_mfma_f32_16x16x32_bf16 v[220:223], a[232:235], a[204:207], v[220:223]\n"
    "PD\n"
    "ds_read_b64_tr_b16 a[248:249], v15 offset:32768\n"
    "v_mfma_f32_16x16x32_bf16 v[220:223], a[236:239], a[220:223], v[220:223]\n"
    "PD\n"
    "buffer_load_dwordx4 v29, s[48:51], s58 offen offset:1024 lds\n"
    "v_mfma_f32_16x16x32_bf16 v[216:219], a[236:239], a[216:219], v[216:219]\n"
    "PD\n"
    "ds_read_b64_tr_b16 a[250:251], v15 offset:36864\n"
    "v_mfma_f32_16x16x32_bf16 v[216:219], a[232:235], a[200:203], v[216:219]\n"
    "PD\n"
    "v_mfma_f32_16x16x32_bf16 v[212:215], a[232:235], a[196:199], v[212:215]\n"
    "PD\n"
    "ds_read_b64_tr_b16 a[252:253], v15 offset:40960\n"
    "v_mfma_f32_16x16x32_bf16 v[212:215], a[236:239], a[212:215], v[212:215]\n"
    "PD\n"
    "v_mfma_f32_16x16x32_bf16 v[208:211], a[236:239], a[208:211], v[208:211]\n"
    "PD\n"
    "ds_read_b64_tr_b16 a[254:255], v15 offset:45056\n"
    "v_mfma_f32_16x16x32_bf16 v[208:211], a[232:235], a[192:195], v[208:211]\n"
    "PD\n"
    "s_waitcnt lgkmcnt(4)\n"
    "v_mfma_f32_16x16x32_bf16 v[224:227], a[240:243], a[192:195], v[224:227]\n"
    "PD\n"
    "ds_read_b64_tr_b16 a[224:225], v8 offset:49152\n"
    "v_mfma_f32_16x16x32_bf16 v[224:227], a[244:247], a[208:211], v[224:227]\n"
    "PD\n"
    "buffer_load_dwordx4 v30, s[48:51], s58 offen offset:2048 lds\n"
    "v_mfma_f32_16x16x32_bf16 v[228:231], a[244:247], a[212:215], v[228:231]\n"
    "PD\n"
    "ds_read_b64_tr_b16 a[226:227], v8 offset:53248\n"
    "v_mfma_f32_16x16x32_bf16 v[228:231], a[240:243], a[196:199], v[228:231]\n"
    "PD\n"
    "v_mfma_f32_16x16x32_bf16 v[232:235], a[240:243], a[200:203], v[232:235]\n"
    "PD\n"
    "ds_read_b64_tr_b16 a[228:229], v8 offset:57344\n"
    "v_mfma_f32_16x16x32_bf16 v[232:235], a[244:247], a[216:219], v[232:235]\n"
    "PD\n"
    "v_mfma_f32_16x16x32_bf16 v[236:239], a[244:247], a[220:223], v[236:239]\n"
    "PD\n"
    "ds_read_b64_tr_b16 a[230:231], v8 offset:61440\n"
    "v_xor_b32 v8, 0x10000, v8\n"
    "v_mfma_f32_16x16x32_bf16 v[236:239], a[240:243], a[204:207], v[236:239]\n"
    "PD\n"
    "s_waitcnt lgkmcnt(4)\n"
    "v_mfma_f32_16x16x32_bf16 v[252:255], a[248:251], a[204:207], v[252:255]\n"
    "PD\n"
    "ds_read_b64_tr_b16 a[232:233], v9 offset:49152\n"
    "v_mfma_f32_16x16x32_bf16 v[252:255], a[252:255], a[220:223], v[252:255]\n"
    "PD\n"
    "buffer_load_dwordx4 v31, s[48:51], s58 offen offset:3072 lds\n"
    "v_mfma_f32_16x16x32_bf16 v[248:251], a[252:255], a[216:219], v[248:251]\n"
    "PD\n"
    "ds_read_b64_tr_b16 a[234:235], v9 offset:53248\n"
    "v_mfma_f32_16x16x32_bf16 v[248:251], a[248:251], a[200:203], v[248:251]\n"
    "PD\n"
    "s_add_u32 s58, s58, s59\n"
    "v_mfma_f32_16x16x32_bf16 v[244:247], a[248:251], a[196:199], v[244:247]\n"
    "PD\n"
    "ds_read_b64_tr_b16 a[236:237], v9 offset:57344\n"
    "v_mfma_f32_16x16x32_bf16 v[244:247], a[252:255], a[212:215], v[244:247]\n"
    "PD\n"
    "v_mfma_f32_16x16x32_bf16 v[240:243], a[252:255], a[208:211], v[240:243]\n"
    "PD\n"
    "ds_read_b64_tr_b16 a[238:239], v9 offset:61440\n"
    "v_xor_b32 v9, 0x10000, v9\n"
    "v_mfma_f32_16x16x32_bf16 v[240:243], a[248:251], a[192:195], v[240:243]\n"
    "PD\n"
    "s_waitcnt vmcnt(28) lgkmcnt(4)\n"
    "v_mfma_f32_16x16x32_bf16 v[128:131], a[224:227], a[96:99], v[128:131]\n"
    "PD\n"
    "ds_read_b64_tr_b16 a[240:241], v10 offset:49152\n"
    "v_mfma_f32_16x16x32_bf16 v[128:131], a[228:231], a[112:115], v[128:131]\n"
    "PD\n"
    "buffer_load_dwordx4 a[192:195], v20, s[40:43], s56 offen offset:0 sc1\n"
    "v_mfma_f32_16x16x32_bf16 v[132:135], a[228:231], a[116:119], v[132:135]\n"
    "PD\n"
    "ds_read_b64_tr_b16 a[242:243], v10 offset:53248\n"
    "v_mfma_f32_16x16x32_bf16 v[132:135], a[224:227], a[100:103], v[132:135]\n"
    "PD\n"
    "v_mfma_f32_16x16x32_bf16 v[136:139], a[224:227], a[104:107], v[136:139]\n"
    "PD\n"
    "ds_read_b64_tr_b16 a[244:245], v10 offset:57344\n"
    "v_mfma_f32_16x16x32_bf16 v[136:139], a[228:231], a[120:123], v[136:139]\n"
    "PD\n"
    "buffer_load_dwordx4 a[208:211], v20, s[40:43], s56 offen offset:1024 sc1\n"
    "v_mfma_f32_16x16x32_bf16 v[140:143], a[228:231], a[124:127], v[140:143]\n"
    "PD\n"
    "ds_read_b64_tr_b16 a[246:247], v10 offset:61440\n"
    "v_xor_b32 v10, 0x10000, v10\n"
    "v_mfma_f32_16x16x32_bf16 v[140:143], a[224:227], a[108:111], v[140:143]\n"
    "PD\n"
    "s_waitcnt lgkmcnt(4)\n"
    "v_mfma_f32_16x16x32_bf16 v[156:159], a[232:235], a[108:111], v[156:159]\n"
    "PD\n"
    "ds_read_b64_tr_b16 a[248:249], v11 offset:49152\n"
    "v_mfma_f32_16x16x32_bf16 v[156:159], a[236:239], a[124:127], v[156:159]\n"
    "PD\n"
    "buffer_load_dwordx4 a[196:199], v21, s[40:43], s56 offen offset:0 sc1\n"
    "v_mfma_f32_16x16x32_bf16 v[152:155], a[236:239], a[120:123], v[152:155]\n"
    "PD\n"
    "ds_read_b64_tr_b16 a[250:251], v11 offset:53248\n"
    "v_mfma_f32_16x16x32_bf16 v[152:155], a[232:235], a[104:107], v[152:155]\n"
    "PD\n"
    "v_mfma_f32_16x16x32_bf16 v[148:151], a[232:235], a[100:103], v[148:151]\n"
    "PD\n"
    "ds_read_b64_tr_b16 a[252:253], v11 offset:57344\n"
    "v_mfma_f32_16x16x32_bf16 v[148:151], a[236:239], a[116:119], v[148:151]\n"
    "PD\n"
    "buffer_load_dwordx4 a[212:215], v21, s[40:43], s56 offen offset:1024 sc1\n"
    "v_mfma_f32_16x16x32_bf16 v[144:147], a[236:239], a[112:115], v[144:147]\n"
    "PD\n"
    "ds_read_b64_tr_b16 a[254:255], v11 offset:61440\n"
    "v_xor_b32 v11, 0x10000, v11\n"
    "v_mfma_f32_16x16x32_bf16 v[144:147], a[232:235], a[96:99], v[144:147]\n"
    "PD\n"
    "s_waitcnt lgkmcnt(4)\n"
    "v_mfma_f32_16x16x32_bf16 v[160:163], a[240:243], a[96:99], v[160:163]\n"
    "PD\n"
    "ds_read_b64_tr_b16 a[224:225], v12 offset:49152\n"
    "v_mfma_f32_16x16x32_bf16 v[160:163], a[244:247], a[112:115], v[160:163]\n"
    "PD\n"
    "buffer_load_dwordx4 a[200:203], v22, s[40:43], s56 offen offset:0 sc1\n"
    "v_mfma_f32_16x16x32_bf16 v[164:167], a[244:247], a[116:119], v[164:167]\n"
    "PD\n"
    "ds_read_b64_tr_b16 a[226:227], v12 offset:53248\n"
    "v_mfma_f32_16x16x32_bf16 v[164:167], a[240:243], a[100:103], v[164:167]\n"
    "PD\n"
    "v_mfma_f32_16x16x32_bf16 v[168:171], a[240:243], a[104:107], v[168:171]\n"
    "PD\n"
    "ds_read_b64_tr_b16 a[228:229], v12 offset:57344\n"
    "v_mfma_f32_16x16x32_bf16 v[168:171], a[244:247], a[120:123], v[168:171]\n"
    "PD\n"
    "buffer_load_dwordx4 a[216:219], v22, s[40:43], s56 offen offset:1024 sc1\n"
    "v_mfma_f32_16x16x32_bf16 v[172:175], a[244:247], a[124:127], v[172:175]\n"
    "PD\n"
    "ds_read_b64_tr_b16 a[230:231], v12 offset:61440\n"
    "v_xor_b32 v12, 0x10000, v12\n"
    "v_mfma_f32_16x16x32_bf16 v[172:175], a[240:243], a[108:111], v[172:175]\n"
    "PD\n"
    "s_waitcnt lgkmcnt(4)\n"
    "v_mfma_f32_16x16x32_bf16 v[188:191], a[248:251], a[108:111], v[188:191]\n"
    "PD\n"
    "ds_read_b64_tr_b16 a[232:233], v13 offset:49152\n"
    "v_mfma_f32_16x16x32_bf16 v[188:191], a[252:255], a[124:127], v[188:191]\n"
    "PD\n"
    "buffer_load_dwordx4 a[204:207], v23, s[40:43], s56 offen offset:0 sc1\n"
    "v_mfma_f32_16x16x32_bf16 v[184:187], a[252:255], a[120:123], v[184:187]\n"
    "PD\n"
    "ds_read_b64_tr_b16 a[234:235], v13 offset:53248\n"
    "v_mfma_f32_16x16x32_bf16 v[184:187], a[248:251], a[104:107], v[184:187]\n"
    "PD\n"
    "v_mfma_f32_16x16x32_bf16 v[180:183], a[248:251], a[100:103], v[180:183]\n"
    "PD\n"
    "ds_read_b64_tr_b16 a[236:237], v13 offset:57344\n"
    "v_mfma_f32_16x16x32_bf16 v[180:183], a[252:255], a[116:119], v[180:183]\n"
    "PD\n"
    "buffer_load_dwordx4 a[220:223], v23, s[40:43], s56 offen offset:1024 sc1\n"
    "v_mfma_f32_16x16x32_bf16 v[176:179], a[252:255], a[112:115], v[176:179]\n"
    "PD\n"
    "ds_read_b64_tr_b16 a[238:239], v13 offset:61440\n"
    "v_xor_b32 v13, 0x10000, v13\n"
    "v_mfma_f32_16x16x32_bf16 v[176:179], a[248:251], a[96:99], v[176:179]\n"
    "PD\n"
    "s_waitcnt lgkmcnt(4)\n"
    "v_mfma_f32_16x16x32_bf16 v[192:195], a[224:227], a[96:99], v[192:195]\n"
    "PD\n"
    "ds_read_b64_tr_b16 a[240:241], v14 offset:49152\n"
    "v_mfma_f32_16x16x32_bf16 v[192:195], a[228:231], a[112:115], v[192:195]\n"
    "PD\n"
    "s_add_u32 s56, s56, s69\n"
    "s_xor_b32 m0, s60, 0x10000\n"
    "v_mfma_f32_16x16x32_bf16 v[196:199], a[228:231], a[116:119], v[196:199]\n"
    "PD\n"
    "ds_read_b64_tr_b16 a[242:243], v14 offset:53248\n"
    "v_mfma_f32_16x16x32_bf16 v[196:199], a[224:227], a[100:103], v[196:199]\n"
    "PD\n"
    "s_add_u32 s37, s37, 1\n"
    "v_mfma_f32_16x16x32_bf16 v[200:203], a[224:227], a[104:107], v[200:203]\n"
    "PD\n"
    "ds_read_b64_tr_b16 a[244:245], v14 offset:57344\n"
    "v_mfma_f32_16x16x32_bf16 v[200:203], a[228:231], a[120:123], v[200:203]\n"
    "PD\n"
    "buffer_load_dwordx4 v24, s[44:47], s57 offen offset:0 lds\n"
    "v_mfma_f32_16x16x32_bf16 v[204:207], a[228:231], a[124:127], v[204:207]\n"
    "PD\n"
    "ds_read_b64_tr_b16 a[246:247], v14 offset:61440\n"
    "v_xor_b32 v14, 0x10000, v14\n"
    "v_mfma_f32_16x16x32_bf16 v[204:207], a[224:227], a[108:111], v[204:207]\n"
    "PD\n"
    "s_waitcnt lgkmcnt(4)\n"
    "v_mfma_f32_16x16x32_bf16 v[220:223], a[232:235], a[108:111], v[220:223]\n"
    "PD\n"
    "ds_read_b64_tr_b16 a[248:249], v15 offset:49152\n"
    "v_mfma_f32_16x16x32_bf16 v[220:223], a[236:239], a[124:127], v[220:223]\n"
    "PD\n"
    "buffer_load_dwordx4 v25, s[44:47], s57 offen offset:1024 lds\n"
    "v_mfma_f32_16x16x32_bf16 v[216:219], a[236:239], a[120:123], v[216:219]\n"
    "PD\n"
    "ds_read_b64_tr_b16 a[250:251], v15 offset:53248\n"
    "v_mfma_f32_16x16x32_bf16 v[216:219], a[232:235], a[104:107], v[216:219]\n"
    "PD\n"
    "v_mfma_f32_16x16x32_bf16 v[212:215], a[232:235], a[100:103], v[212:215]\n"
    "PD\n"
    "ds_read_b64_tr_b16 a[252:253], v15 offset:57344\n"
    "v_mfma_f32_16x16x32_bf16 v[212:215], a[236:239], a[116:119], v[212:215]\n"
    "PD\n"
    "v_mfma_f32_16x16x32_bf16 v[208:211], a[236:239], a[112:115], v[208:211]\n"
    "PD\n"
    "ds_read_b64_tr_b16 a[254:255], v15 offset:61440\n"
    "v_xor_b32 v15, 0x10000, v15\n"
    "v_mfma_f32_16x16x32_bf16 v[208:211], a[232:235], a[96:99], v[208:211]\n"
    "PD\n"
    "s_waitcnt lgkmcnt(4)\n"
    "s_waitcnt vmcnt(34)\n"
    "s_barrier\n"
    "v_mfma_f32_16x16x32_bf16 v[224:227], a[240:243], a[96:99], v[224:227]\n"
    "PD\n"
    "ds_read_b64_tr_b16 a[224:225], v8 offset:0\n"
    "v_mfma_f32_16x16x32_bf16 v[224:227], a[244:247], a[112:115], v[224:227]\n"
    "PD\n"
    "buffer_load_dwordx4 v26, s[44:47], s57 offen offset:2048 lds\n"
    "v_mfma_f32_16x16x32_bf16 v[228:231], a[244:247], a[116:119], v[228:231]\n"
    "PD\n"
    "ds_read_b64_tr_b16 a[226:227], v8 offset:4096\n"
    "v_mfma_f32_16x16x32_bf16 v[228:231], a[240:243], a[100:103], v[228:231]\n"
    "PD\n"
    "s_xor_b32 s60, s60, 0x10000\n"
    "v_mfma_f32_16x16x32_bf16 v[232:235], a[240:243], a[104:107], v[232:235]\n"
    "PD\n"
    "ds_read_b64_tr_b16 a[228:229], v8 offset:8192\n"
    "v_mfma_f32_16x16x32_bf16 v[232:235], a[244:247], a[120:123], v[232:235]\n"
    "PD\n"
    "v_mfma_f32_16x16x32_bf16 v[236:239], a[244:247], a[124:127], v[236:239]\n"
    "PD\n"
    "ds_read_b64_tr_b16 a[230:231], v8 offset:12288\n"
    "v_mfma_f32_16x16x32_bf16 v[236:239], a[240:243], a[108:111], v[236:239]\n"
    "PD\n"
    "s_waitcnt lgkmcnt(4)\n"
    "v_mfma_f32_16x16x32_bf16 v[252:255], a[248:251], a[108:111], v[252:255]\n"
    "PD\n"
    "ds_read_b64_tr_b16 a[232:233], v9 offset:0\n"
    "v_mfma_f32_16x16x32_bf16 v[252:255], a[252:255], a[124:127], v[252:255]\n"
    "PD\n"
    "buffer_load_dwordx4 v27, s[44:47], s57 offen offset:3072 lds\n"
    "v_mfma_f32_16x16x32_bf16 v[248:251], a[252:255], a[120:123], v[248:251]\n"
    "PD\n"
    "ds_read_b64_tr_b16 a[234:235], v9 offset:4096\n"
    "v_mfma_f32_16x16x32_bf16 v[248:251], a[248:251], a[104:107], v[248:251]\n"
    "PD\n"
    "s_add_u32 s57, s57, s59\n"
    "v_mfma_f32_16x16x32_bf16 v[244:247], a[248:251], a[100:103], v[244:247]\n"
    "PD\n"
    "ds_read_b64_tr_b16 a[236:237], v9 offset:8192\n"
    "v_mfma_f32_16x16x32_bf16 v[244:247], a[252:255], a[116:119], v[244:247]\n"
    "PD\n"
    "v_mfma_f32_16x16x32_bf16 v[240:243], a[252:255], a[112:115], v[240:243]\n"
    "PD\n"
    "ds_read_b64_tr_b16 a[238:239], v9 offset:12288\n"
    "s_cmp_lg_u32 s37, s38\n"
    "v_mfma_f32_16x16x32_bf16 v[240:243], a[248:251], a[96:99], v[240:243]\n"
    "PD\n"
    "s_waitcnt vmcnt(28) lgkmcnt(4)\n"
    "s_cbranch_scc1 L_Q\n"
    "PP\n"
    "v_mfma_f32_16x16x32_bf16 v[128:131], a[224:227], a[128:131], v[128:131]\n"
    "PD\n"
    "ds_read_b64_tr_b16 a[240:241], v10 offset:0\n"
    "s_lshl_b32 s62, s25, 19\n"
    "v_mfma_f32_16x16x32_bf16 v[128:131], a[228:231], a[144:147], v[128:131]\n"
    "PD\n"
    "buffer_load_dwordx4 a[96:99], v20, s[40:43], s56 offen offset:0 sc1\n"
    "s_add_u32 s62, s62, 393216\n"
    "s_add_u32 s48, s6, s62\n"
    "v_mfma_f32_16x16x32_bf16 v[132:135], a[228:231], a[148:151], v[132:135]\n"
    "PD\n"
    "ds_read_b64_tr_b16 a[242:243], v10 offset:4096\n"
    "s_addc_u32 s49, s7, 0\n"
    "v_mfma_f32_16x16x32_bf16 v[132:135], a[224:227], a[132:135], v[132:135]\n"
    "PD\n"
    "s_mov_b32 s50, 0x20000\n"
    "s_mov_b32 s58, 0\n"
    "v_lshrrev_b32 v56, 4, v0\n"
    "v_mfma_f32_16x16x32_bf16 v[136:139], a[224:227], a[136:139], v[136:139]\n"
    "PD\n"
    "ds_read_b64_tr_b16 a[244:245], v10 offset:8192\n"
    "v_and_b32 v57, 15, v0\n"
    "v_mfma_f32_16x16x32_bf16 v[136:139], a[228:231], a[152:155], v[136:139]\n"
    "PD\n"
    "buffer_load_dwordx4 a[112:115], v20, s[40:43], s56 offen offset:1024 sc1\n"
    "v_xor_b32 v56, v56, v57\n"
    "v_lshlrev_b32 v56, 4, v56\n"
    "v_mfma_f32_16x16x32_bf16 v[140:143], a[228:231], a[156:159], v[140:143]\n"
    "PD\n"
    "ds_read_b64_tr_b16 a[246:247], v10 offset:12288\n"
    "v_lshl_add_u32 v28, v6, 8, v56\n"
    "v_mfma_f32_16x16x32_bf16 v[140:143], a[224:227], a[140:143], v[140:143]\n"
    "PD\n"
    "s_waitcnt lgkmcnt(4)\n"
    "v_xor_b32 v29, 64, v28\n"
    "v_xor_b32 v30, 128, v28\n"
    "v_mfma_f32_16x16x32_bf16 v[156:159], a[232:235], a[140:143], v[156:159]\n"
    "PD\n"
    "ds_read_b64_tr_b16 a[248:249], v11 offset:0\n"
    "v_xor_b32 v31, 192, v28\n"
    "v_mfma_f32_16x16x32_bf16 v[156:159], a[236:239], a[156:159], v[156:159]\n"
    "PD\n"
    "buffer_load_dwordx4 a[100:103], v21, s[40:43], s56 offen offset:0 sc1\n"
    "s_lshl_b32 s62, s25, 19\n"
    "s_add_u32 s62, s62, 393216\n"
    "v_mfma_f32_16x16x32_bf16 v[152:155], a[236:239], a[152:155], v[152:155]\n"
    "PD\n"
    "ds_read_b64_tr_b16 a[250:251], v11 offset:4096\n"
    "s_add_u32 s44, s8, s62\n"
    "v_mfma_f32_16x16x32_bf16 v[152:155], a[232:235], a[136:139], v[152:155]\n"
    "PD\n"
    "s_addc_u32 s45, s9, 0\n"
    "s_mov_b32 s46, 0x20000\n"
    "s_mov_b32 s57, 0\n"
    "v_mfma_f32_16x16x32_bf16 v[148:151], a[232:235], a[132:135], v[148:151]\n"
    "PD\n"
    "ds_read_b64_tr_b16 a[252:253], v11 offset:8192\n"
    "s_movk_i32 s59, 0x4000\n"
    "v_mfma_f32_16x16x32_bf16 v[148:151], a[236:239], a[148:151], v[148:151]\n"
    "PD\n"
    "buffer_load_dwordx4 a[116:119], v21, s[40:43], s56 offen offset:1024 sc1\n"
    "v_lshl_add_u32 v24, v6, 8, v32\n"
    "v_lshl_add_u32 v25, v6, 8, v33\n"
    "v_mfma_f32_16x16x32_bf16 v[144:147], a[236:239], a[144:147], v[144:147]\n"
    "PD\n"
    "ds_read_b64_tr_b16 a[254:255], v11 offset:12288\n"
    "v_mov_b32 v26, v24\n"
    "v_mfma_f32_16x16x32_bf16 v[144:147], a[232:235], a[128:131], v[144:147]\n"
    "PD\n"
    "s_waitcnt lgkmcnt(4)\n"
    "v_mov_b32 v27, v25\n"
    "s_mov_b32 s68, 1040356334\n"
    "v_mfma_f32_16x16x32_bf16 v[160:163], a[240:243], a[128:131], v[160:163]\n"
    "PD\n"
    "ds_read_b64_tr_b16 a[224:225], v12 offset:0\n"
    "v_mov_b32 v39, 131072\n"
    "v_mfma_f32_16x16x32_bf16 v[160:163], a[244:247], a[144:147], v[160:163]\n"
    "PD\n"
    "buffer_load_dwordx4 a[104:107], v22, s[40:43], s56 offen offset:0 sc1\n"
    "s_and_b32 s62, s25, 1\n"
    "s_lshl_b32 s63, s62, 21\n"
    "v_mfma_f32_16x16x32_bf16 v[164:167], a[244:247], a[148:151], v[164:167]\n"
    "PD\n"
    "ds_read_b64_tr_b16 a[226:227], v12 offset:4096\n"
    "s_mul_i32 s64, s62, 0x1c0000\n"
    "v_mfma_f32_16x16x32_bf16 v[164:167], a[240:243], a[132:135], v[164:167]\n"
    "PD\n"
    "s_add_u32 s70, s12, s63\n"
    "s_addc_u32 s71, s13, 0\n"
    "s_add_u32 s72, s70, 0x400000\n"
    "v_mfma_f32_16x16x32_bf16 v[168:171], a[240:243], a[136:139], v[168:171]\n"
    "PD\n"
    "ds_read_b64_tr_b16 a[228:229], v12 offset:8192\n"
    "s_addc_u32 s73, s71, 0\n"
    "v_mfma_f32_16x16x32_bf16 v[168:171], a[244:247], a[152:155], v[168:171]\n"
    "PD\n"
    "buffer_load_dwordx4 a[120:123], v22, s[40:43], s56 offen offset:1024 sc1\n"
    "s_add_u32 s74, s18, s64\n"
    "s_addc_u32 s75, s19, 0\n"
    "v_mfma_f32_16x16x32_bf16 v[172:175], a[244:247], a[156:159], v[172:175]\n"
    "PD\n"
    "ds_read_b64_tr_b16 a[230:231], v12 offset:12288\n"
    "s_add_u32 s76, s74, 0x380000\n"
    "v_mfma_f32_16x16x32_bf16 v[172:175], a[240:243], a[140:143], v[172:175]\n"
    "PD\n"
    "s_waitcnt lgkmcnt(4)\n"
    "s_addc_u32 s77, s75, 0\n"
    "s_lshr_b32 s62, s2, 1\n"
    "v_mfma_f32_16x16x32_bf16 v[188:191], a[248:251], a[140:143], v[188:191]\n"
    "PD\n"
    "ds_read_b64_tr_b16 a[232:233], v13 offset:0\n"
    "s_lshl2_add_u32 s62, s62, s36\n"
    "v_mfma_f32_16x16x32_bf16 v[188:191], a[252:255], a[156:159], v[188:191]\n"
    "PD\n"
    "buffer_load_dwordx4 a[108:111], v23, s[40:43], s56 offen offset:0 sc1\n"
    "s_lshl_b32 s62, s62, 12\n"
    "v_lshl_add_u32 v1, v0, 6, s62\n"
    "v_mfma_f32_16x16x32_bf16 v[184:187], a[252:255], a[152:155], v[184:187]\n"
    "PD\n"
    "ds_read_b64_tr_b16 a[234:235], v13 offset:4096\n"
    "v_min_u32 v2, 0x1bffc0, v1\n"
    "v_mfma_f32_16x16x32_bf16 v[184:187], a[248:251], a[136:139], v[184:187]\n"
    "PD\n"
    "s_lshl3_add_u32 s62, s26, s24\n"
    "s_mul_i32 s62, s62, 53248\n"
    "s_add_u32 s62, s62, 38797312\n"
    "v_mfma_f32_16x16x32_bf16 v[180:183], a[248:251], a[132:135], v[180:183]\n"
    "PD\n"
    "ds_read_b64_tr_b16 a[236:237], v13 offset:8192\n"
    "s_add_u32 s88, s22, s62\n"
    "v_mfma_f32_16x16x32_bf16 v[180:183], a[252:255], a[148:151], v[180:183]\n"
    "PD\n"
    "buffer_load_dwordx4 a[124:127], v23, s[40:43], s56 offen offset:1024 sc1\n"
    "s_addc_u32 s89, s23, 0\n"
    "s_xor_b32 s98, s29, 1\n"
    "v_mfma_f32_16x16x32_bf16 v[176:179], a[252:255], a[144:147], v[176:179]\n"
    "PD\n"
    "ds_read_b64_tr_b16 a[238:239], v13 offset:12288\n"
    "s_mul_i32 s98, s98, 13312\n"
    "v_mfma_f32_16x16x32_bf16 v[176:179], a[248:251], a[128:131], v[176:179]\n"
    "PD\n"
    "s_waitcnt lgkmcnt(4)\n"
    "s_xor_b32 s0, s29, 2\n"
    "s_mul_i32 s0, s0, 13312\n"
    "v_mfma_f32_16x16x32_bf16 v[192:195], a[224:227], a[128:131], v[192:195]\n"
    "PD\n"
    "ds_read_b64_tr_b16 a[240:241], v14 offset:0\n"
    "s_add_u32 s0, s0, 4096\n"
    "v_mfma_f32_16x16x32_bf16 v[192:195], a[228:231], a[144:147], v[192:195]\n"
    "PD\n"
    "s_xor_b32 s1, s29, 3\n"
    "s_mul_i32 s1, s1, 13312\n"
    "s_add_u32 s1, s1, 8192\n"
    "v_mfma_f32_16x16x32_bf16 v[196:199], a[228:231], a[148:151], v[196:199]\n"
    "PD\n"
    "ds_read_b64_tr_b16 a[242:243], v14 offset:4096\n"
    "s_lshr_b32 s62, s30, 7\n"
    "v_mfma_f32_16x16x32_bf16 v[196:199], a[224:227], a[132:135], v[196:199]\n"
    "PD\n"
    "buffer_store_dwordx4 v[96:99], v4, s[52:55], s69 offen offset:0 sc1\n"
    "s_sub_u32 s62, s62, 8\n"
    "s_add_u32 s63, s91, 896\n"
    "v_mfma_f32_16x16x32_bf16 v[200:203], a[224:227], a[136:139], v[200:203]\n"
    "PD\n"
    "ds_read_b64_tr_b16 a[244:245], v14 offset:8192\n"
    "s_lshl_b32 s64, s24, 7\n"
    "v_mfma_f32_16x16x32_bf16 v[200:203], a[228:231], a[152:155], v[200:203]\n"
    "PD\n"
    "s_add_u32 s64, s64, 1152\n"
    "v_min_u32 v58, s62, v0\n"
    "v_lshl_add_u32 v58, v58, 7, s63\n"
    "v_mfma_f32_16x16x32_bf16 v[204:207], a[228:231], a[156:159], v[204:207]\n"
    "PD\n"
    "ds_read_b64_tr_b16 a[246:247], v14 offset:12288\n"
    "v_and_b32 v59, 3, v0\n"
    "v_mfma_f32_16x16x32_bf16 v[204:207], a[224:227], a[140:143], v[204:207]\n"
    "PD\n"
    "s_waitcnt lgkmcnt(4)\n"
    "v_lshl_add_u32 v59, v59, 10, s64\n"
    "s_mov_b64 s[64:65], 0xffff\n"
    "v_mfma_f32_16x16x32_bf16 v[220:223], a[232:235], a[140:143], v[220:223]\n"
    "PD\n"
    "ds_read_b64_tr_b16 a[248:249], v15 offset:0\n"
    "v_cndmask_b32 v3, v59, v58, s[64:65]\n"
    "v_mfma_f32_16x16x32_bf16 v[220:223], a[236:239], a[156:159], v[220:223]\n"
    "PD\n"
    "s_cmp_lt_u32 s29, 2\n"
    "s_cmov_b32 s62, 0\n"
    "v_min_u32 v61, s62, v0\n"
    "v_mfma_f32_16x16x32_bf16 v[216:219], a[236:239], a[152:155], v[216:219]\n"
    "PD\n"
    "ds_read_b64_tr_b16 a[250:251], v15 offset:4096\n"
    "v_lshl_add_u32 v61, v61, 7, s63\n"
    "v_mfma_f32_16x16x32_bf16 v[216:219], a[232:235], a[136:139], v[216:219]\n"
    "PD\n"
    "s_add_u32 m0, s60, 16384\n"
    "v_mfma_f32_16x16x32_bf16 v[212:215], a[232:235], a[132:135], v[212:215]\n"
    "PD\n"
    "ds_read_b64_tr_b16 a[252:253], v15 offset:8192\n"
    "v_mfma_f32_16x16x32_bf16 v[212:215], a[236:239], a[148:151], v[212:215]\n"
    "PD\n"
    "buffer_load_dwordx4 v28, s[48:51], s58 offen offset:0 lds\n"
    "v_mfma_f32_16x16x32_bf16 v[208:211], a[236:239], a[144:147], v[208:211]\n"
    "PD\n"
    "ds_read_b64_tr_b16 a[254:255], v15 offset:12288\n"
    "v_mfma_f32_16x16x32_bf16 v[208:211], a[232:235], a[128:131], v[208:211]\n"
    "PD\n"
    "s_waitcnt lgkmcnt(4)\n"
    "v_mfma_f32_16x16x32_bf16 v[224:227], a[240:243], a[128:131], v[224:227]\n"
    "PD\n"
    "ds_read_b64_tr_b16 a[224:225], v8 offset:16384\n"
    "v_mfma_f32_16x16x32_bf16 v[224:227], a[244:247], a[144:147], v[224:227]\n"
    "PD\n"
    "v_mfma_f32_16x16x32_bf16 v[228:231], a[244:247], a[148:151], v[228:231]\n"
    "PD\n"
    "ds_read_b64_tr_b16 a[226:227], v8 offset:20480\n"
    "v_mfma_f32_16x16x32_bf16 v[228:231], a[240:243], a[132:135], v[228:231]\n"
    "PD\n"
    "buffer_load_dwordx4 v29, s[48:51], s58 offen offset:1024 lds\n"
    "v_mfma_f32_16x16x32_bf16 v[232:235], a[240:243], a[136:139], v[232:235]\n"
    "PD\n"
    "ds_read_b64_tr_b16 a[228:229], v8 offset:24576\n"
    "v_mfma_f32_16x16x32_bf16 v[232:235], a[244:247], a[152:155], v[232:235]\n"
    "PD\n"
    "v_mfma_f32_16x16x32_bf16 v[236:239], a[244:247], a[156:159], v[236:239]\n"
    "PD\n"
    "ds_read_b64_tr_b16 a[230:231], v8 offset:28672\n"
    "v_mfma_f32_16x16x32_bf16 v[236:239], a[240:243], a[140:143], v[236:239]\n"
    "PD\n"
    "s_waitcnt lgkmcnt(4)\n"
    "v_mfma_f32_16x16x32_bf16 v[252:255], a[248:251], a[140:143], v[252:255]\n"
    "PD\n"
    "ds_read_b64_tr_b16 a[232:233], v9 offset:16384\n"
    "v_mfma_f32_16x16x32_bf16 v[252:255], a[252:255], a[156:159], v[252:255]\n"
    "PD\n"
    "buffer_load_dwordx4 v30, s[48:51], s58 offen offset:2048 lds\n"
    "v_mfma_f32_16x16x32_bf16 v[248:251], a[252:255], a[152:155], v[248:251]\n"
    "PD\n"
    "ds_read_b64_tr_b16 a[234:235], v9 offset:20480\n"
    "v_mfma_f32_16x16x32_bf16 v[248:251], a[248:251], a[136:139], v[248:251]\n"
    "PD\n"
    "v_mfma_f32_16x16x32_bf16 v[244:247], a[248:251], a[132:135], v[244:247]\n"
    "PD\n"
    "ds_read_b64_tr_b16 a[236:237], v9 offset:24576\n"
    "v_mfma_f32_16x16x32_bf16 v[244:247], a[252:255], a[148:151], v[244:247]\n"
    "PD\n"
    "v_mfma_f32_16x16x32_bf16 v[240:243], a[252:255], a[144:147], v[240:243]\n"
    "PD\n"
    "ds_read_b64_tr_b16 a[238:239], v9 offset:28672\n"
    "v_mfma_f32_16x16x32_bf16 v[240:243], a[248:251], a[128:131], v[240:243]\n"
    "PD\n"
    "s_waitcnt vmcnt(27) lgkmcnt(4)\n"
    "s_waitcnt vmcnt(23)\n"
    "s_barrier\n"
    "v_mfma_f32_16x16x32_bf16 v[128:131], a[224:227], a[160:163], v[128:131]\n"
    "PD\n"
    "ds_read_b64_tr_b16 a[240:241], v8 offset:32768\n"
    "v_mfma_f32_16x16x32_bf16 v[128:131], a[228:231], a[176:179], v[128:131]\n"
    "PD\n"
    "buffer_store_dwordx4 v[112:115], v4, s[52:55], s69 offen offset:1024 sc1\n"
    "v_mfma_f32_16x16x32_bf16 v[132:135], a[228:231], a[180:183], v[132:135]\n"
    "PD\n"
    "ds_read_b64_tr_b16 a[242:243], v8 offset:36864\n"
    "v_mfma_f32_16x16x32_bf16 v[132:135], a[224:227], a[164:167], v[132:135]\n"
    "PD\n"
    "buffer_load_dwordx4 v31, s[48:51], s58 offen offset:3072 lds\n"
    "v_mfma_f32_16x16x32_bf16 v[136:139], a[224:227], a[168:171], v[136:139]\n"
    "PD\n"
    "ds_read_b64_tr_b16 a[244:245], v8 offset:40960\n"
    "s_add_u32 s58, s58, s59\n"
    "v_mfma_f32_16x16x32_bf16 v[136:139], a[228:231], a[184:187], v[136:139]\n"
    "PD\n"
    "s_add_u32 m0, s60, 0\n"
    "v_mfma_f32_16x16x32_bf16 v[140:143], a[228:231], a[188:191], v[140:143]\n"
    "PD\n"
    "ds_read_b64_tr_b16 a[246:247], v8 offset:45056\n"
    "v_mfma_f32_16x16x32_bf16 v[140:143], a[224:227], a[172:175], v[140:143]\n"
    "PD\n"
    "s_waitcnt lgkmcnt(4)\n"
    "v_mfma_f32_16x16x32_bf16 v[156:159], a[232:235], a[172:175], v[156:159]\n"
    "PD\n"
    "ds_read_b64_tr_b16 a[248:249], v9 offset:32768\n"
    "v_mfma_f32_16x16x32_bf16 v[156:159], a[236:239], a[188:191], v[156:159]\n"
    "PD\n"
    "buffer_load_dwordx4 v24, s[44:47], s57 offen offset:0 lds\n"
    "v_mfma_f32_16x16x32_bf16 v[152:155], a[236:239], a[184:187], v[152:155]\n"
    "PD\n"
    "ds_read_b64_tr_b16 a[250:251], v9 offset:36864\n"
    "v_mfma_f32_16x16x32_bf16 v[152:155], a[232:235], a[168:171], v[152:155]\n"
    "PD\n"
    "v_mfma_f32_16x16x32_bf16 v[148:151], a[232:235], a[164:167], v[148:151]\n"
    "PD\n"
    "ds_read_b64_tr_b16 a[252:253], v9 offset:40960\n"
    "v_mfma_f32_16x16x32_bf16 v[148:151], a[236:239], a[180:183], v[148:151]\n"
    "PD\n"
    "v_mfma_f32_16x16x32_bf16 v[144:147], a[236:239], a[176:179], v[144:147]\n"
    "PD\n"
    "ds_read_b64_tr_b16 a[254:255], v9 offset:45056\n"
    "v_mfma_f32_16x16x32_bf16 v[144:147], a[232:235], a[160:163], v[144:147]\n"
    "PD\n"
    "s_waitcnt vmcnt(17) lgkmcnt(4)\n"
    "v_mfma_f32_16x16x32_bf16 v[128:131], a[240:243], a[192:195], v[128:131]\n"
    "PD\n"
    "ds_read_b64_tr_b16 a[224:225], v8 offset:49152\n"
    "v_mfma_f32_16x16x32_bf16 v[128:131], a[244:247], a[208:211], v[128:131]\n"
    "PD\n"
    "buffer_load_dwordx4 v25, s[44:47], s57 offen offset:1024 lds\n"
    "v_mfma_f32_16x16x32_bf16 v[132:135], a[244:247], a[212:215], v[132:135]\n"
    "PD\n"
    "ds_read_b64_tr_b16 a[226:227], v8 offset:53248\n"
    "v_mfma_f32_16x16x32_bf16 v[132:135], a[240:243], a[196:199], v[132:135]\n"
    "PD\n"
    "v_mfma_f32_16x16x32_bf16 v[136:139], a[240:243], a[200:203], v[136:139]\n"
    "PD\n"
    "ds_read_b64_tr_b16 a[228:229], v8 offset:57344\n"
    "v_mfma_f32_16x16x32_bf16 v[136:139], a[244:247], a[216:219], v[136:139]\n"
    "PD\n"
    "v_mfma_f32_16x16x32_bf16 v[140:143], a[244:247], a[220:223], v[140:143]\n"
    "PD\n"
    "ds_read_b64_tr_b16 a[230:231], v8 offset:61440\n"
    "v_xor_b32 v8, 0x10000, v8\n"
    "v_mfma_f32_16x16x32_bf16 v[140:143], a[240:243], a[204:207], v[140:143]\n"
    "PD\n"
    "s_waitcnt lgkmcnt(4)\n"
    "v_mfma_f32_16x16x32_bf16 v[156:159], a[248:251], a[204:207], v[156:159]\n"
    "PD\n"
    "ds_read_b64_tr_b16 a[232:233], v9 offset:49152\n"
    "v_mfma_f32_16x16x32_bf16 v[156:159], a[252:255], a[220:223], v[156:159]\n"
    "PD\n"
    "buffer_load_dwordx4 v26, s[44:47], s57 offen offset:2048 lds\n"
    "v_mfma_f32_16x16x32_bf16 v[152:155], a[252:255], a[216:219], v[152:155]\n"
    "PD\n"
    "ds_read_b64_tr_b16 a[234:235], v9 offset:53248\n"
    "v_mfma_f32_16x16x32_bf16 v[152:155], a[248:251], a[200:203], v[152:155]\n"
    "PD\n"
    "buffer_store_dwordx4 v[100:103], v4, s[52:55], s69 offen offset:2048 sc1\n"
    "v_mfma_f32_16x16x32_bf16 v[148:151], a[248:251], a[196:199], v[148:151]\n"
    "PD\n"
    "ds_read_b64_tr_b16 a[236:237], v9 offset:57344\n"
    "v_mfma_f32_16x16x32_bf16 v[148:151], a[252:255], a[212:215], v[148:151]\n"
    "PD\n"
    "v_mfma_f32_16x16x32_bf16 v[144:147], a[252:255], a[208:211], v[144:147]\n"
    "PD\n"
    "ds_read_b64_tr_b16 a[238:239], v9 offset:61440\n"
    "v_xor_b32 v9, 0x10000, v9\n"
    "v_mfma_f32_16x16x32_bf16 v[144:147], a[248:251], a[192:195], v[144:147]\n"
    "PD\n"
    "s_waitcnt vmcnt(7) lgkmcnt(4)\n"
    "v_mfma_f32_16x16x32_bf16 v[128:131], a[224:227], a[96:99], v[128:131]\n"
    "PD\n"
    "ds_read_b64_tr_b16 a[240:241], v10 offset:16384\n"
    "v_mfma_f32_16x16x32_bf16 v[128:131], a[228:231], a[112:115], v[128:131]\n"
    "PD\n"
    "buffer_load_dwordx4 v27, s[44:47], s57 offen offset:3072 lds\n"
    "v_mfma_f32_16x16x32_bf16 v[132:135], a[228:231], a[116:119], v[132:135]\n"
    "PD\n"
    "ds_read_b64_tr_b16 a[242:243], v10 offset:20480\n"
    "s_add_u32 s57, s57, s59\n"
    "v_mfma_f32_16x16x32_bf16 v[132:135], a[224:227], a[100:103], v[132:135]\n"
    "PD\n"
    "s_add_u32 m0, s60, 49152\n"
    "v_mfma_f32_16x16x32_bf16 v[136:139], a[224:227], a[104:107], v[136:139]\n"
    "PD\n"
    "ds_read_b64_tr_b16 a[244:245], v10 offset:24576\n"
    "v_mfma_f32_16x16x32_bf16 v[136:139], a[228:231], a[120:123], v[136:139]\n"
    "PD\n"
    "v_mfma_f32_16x16x32_bf16 v[140:143], a[228:231], a[124:127], v[140:143]\n"
    "PD\n"
    "ds_read_b64_tr_b16 a[246:247], v10 offset:28672\n"
    "v_mfma_f32_16x16x32_bf16 v[140:143], a[224:227], a[108:111], v[140:143]\n"
    "PD\n"
    "s_waitcnt lgkmcnt(4)\n"
    "v_mfma_f32_16x16x32_bf16 v[156:159], a[232:235], a[108:111], v[156:159]\n"
    "PD\n"
    "ds_read_b64_tr_b16 a[248:249], v11 offset:16384\n"
    "v_mfma_f32_16x16x32_bf16 v[156:159], a[236:239], a[124:127], v[156:159]\n"
    "PD\n"
    "buffer_load_dwordx4 v28, s[48:51], s58 offen offset:0 lds\n"
    "v_mfma_f32_16x16x32_bf16 v[152:155], a[236:239], a[120:123], v[152:155]\n"
    "PD\n"
    "ds_read_b64_tr_b16 a[250:251], v11 offset:20480\n"
    "v_mfma_f32_16x16x32_bf16 v[152:155], a[232:235], a[104:107], v[152:155]\n"
    "PD\n"
    "v_mfma_f32_16x16x32_bf16 v[148:151], a[232:235], a[100:103], v[148:151]\n"
    "PD\n"
    "ds_read_b64_tr_b16 a[252:253], v11 offset:24576\n"
    "v_mfma_f32_16x16x32_bf16 v[148:151], a[236:239], a[116:119], v[148:151]\n"
    "PD\n"
    "v_mfma_f32_16x16x32_bf16 v[144:147], a[236:239], a[112:115], v[144:147]\n"
    "PD\n"
    "ds_read_b64_tr_b16 a[254:255], v11 offset:28672\n"
    "v_mfma_f32_16x16x32_bf16 v[144:147], a[232:235], a[96:99], v[144:147]\n"
    "PD\n"
    "s_waitcnt lgkmcnt(4)\n"
    "v_mfma_f32_16x16x32_bf16 v[160:163], a[240:243], a[160:163], v[160:163]\n"
    "PD\n"
    "ds_read_b64_tr_b16 a[224:225], v10 offset:32768\n"
    "v_mfma_f32_16x16x32_bf16 v[160:163], a[244:247], a[176:179], v[160:163]\n"
    "PD\n"
    "buffer_load_dwordx4 v29, s[48:51], s58 offen offset:1024 lds\n"
    "v_mfma_f32_16x16x32_bf16 v[164:167], a[244:247], a[180:183], v[164:167]\n"
    "PD\n"
    "ds_read_b64_tr_b16 a[226:227], v10 offset:36864\n"
    "v_mfma_f32_16x16x32_bf16 v[164:167], a[240:243], a[164:167], v[164:167]\n"
    "PD\n"
    "v_mfma_f32_16x16x32_bf16 v[168:171], a[240:243], a[168:171], v[168:171]\n"
    "PD\n"
    "ds_read_b64_tr_b16 a[228:229], v10 offset:40960\n"
    "v_mul_f32 v128, s68, v128\n"
    "v_mfma_f32_16x16x32_bf16 v[168:171], a[244:247], a[184:187], v[168:171]\n"
    "PD\n"
    "v_mul_f32 v129, s68, v129\n"
    "v_mul_f32 v130, s68, v130\n"
    "v_mfma_f32_16x16x32_bf16 v[172:175], a[244:247], a[188:191], v[172:175]\n"
    "PD\n"
    "ds_read_b64_tr_b16 a[230:231], v10 offset:45056\n"
    "v_mul_f32 v131, s68, v131\n"
    "v_mfma_f32_16x16x32_bf16 v[172:175], a[240:243], a[172:175], v[172:175]\n"
    "PD\n"
    "s_waitcnt lgkmcnt(4)\n"
    "v_mul_f32 v144, s68, v144\n"
    "v_mul_f32 v145, s68, v145\n"
    "v_mfma_f32_16x16x32_bf16 v[188:191], a[248:251], a[172:175], v[188:191]\n"
    "PD\n"
    "ds_read_b64_tr_b16 a[232:233], v11 offset:32768\n"
    "v_mul_f32 v146, s68, v146\n"
    "v_mfma_f32_16x16x32_bf16 v[188:191], a[252:255], a[188:191], v[188:191]\n"
    "PD\n"
    "buffer_store_dwordx4 v[116:119], v4, s[52:55], s69 offen offset:3072 sc1\n"
    "v_mul_f32 v147, s68, v147\n"
    "v_cvt_pk_bf16_f32 v64, v128, v129\n"
    "v_mfma_f32_16x16x32_bf16 v[184:187], a[252:255], a[184:187], v[184:187]\n"
    "PD\n"
    "ds_read_b64_tr_b16 a[234:235], v11 offset:36864\n"
    "v_cvt_pk_bf16_f32 v65, v130, v131\n"
    "v_mfma_f32_16x16x32_bf16 v[184:187], a[248:251], a[168:171], v[184:187]\n"
    "PD\n"
    "buffer_load_dwordx4 v30, s[48:51], s58 offen offset:2048 lds\n"
    "v_cvt_pk_bf16_f32 v66, v144, v145\n"
    "v_cvt_pk_bf16_f32 v67, v146, v147\n"
    "v_mfma_f32_16x16x32_bf16 v[180:183], a[248:251], a[164:167], v[180:183]\n"
    "PD\n"
    "ds_read_b64_tr_b16 a[236:237], v11 offset:40960\n"
    "v_mul_f32 v132, s68, v132\n"
    "v_mfma_f32_16x16x32_bf16 v[180:183], a[252:255], a[180:183], v[180:183]\n"
    "PD\n"
    "v_mul_f32 v133, s68, v133\n"
    "v_mul_f32 v134, s68, v134\n"
    "v_mfma_f32_16x16x32_bf16 v[176:179], a[252:255], a[176:179], v[176:179]\n"
    "PD\n"
    "ds_read_b64_tr_b16 a[238:239], v11 offset:45056\n"
    "v_mul_f32 v135, s68, v135\n"
    "v_mfma_f32_16x16x32_bf16 v[176:179], a[248:251], a[160:163], v[176:179]\n"
    "PD\n"
    "s_waitcnt lgkmcnt(4)\n"
    "v_mul_f32 v148, s68, v148\n"
    "v_mul_f32 v149, s68, v149\n"
    "v_mfma_f32_16x16x32_bf16 v[160:163], a[224:227], a[192:195], v[160:163]\n"
    "PD\n"
    "ds_read_b64_tr_b16 a[240:241], v10 offset:49152\n"
    "v_mul_f32 v150, s68, v150\n"
    "v_mfma_f32_16x16x32_bf16 v[160:163], a[228:231], a[208:211], v[160:163]\n"
    "PD\n"
    "buffer_load_dwordx4 v31, s[48:51], s58 offen offset:3072 lds\n"
    "v_mul_f32 v151, s68, v151\n"
    "v_cvt_pk_bf16_f32 v80, v132, v133\n"
    "v_mfma_f32_16x16x32_bf16 v[164:167], a[228:231], a[212:215], v[164:167]\n"
    "PD\n"
    "ds_read_b64_tr_b16 a[242:243], v10 offset:53248\n"
    "s_add_u32 s58, s58, s59\n"
    "v_mfma_f32_16x16x32_bf16 v[164:167], a[224:227], a[196:199], v[164:167]\n"
    "PD\n"
    "s_add_u32 m0, s60, 32768\n"
    "v_cvt_pk_bf16_f32 v81, v134, v135\n"
    "v_cvt_pk_bf16_f32 v82, v148, v149\n"
    "v_mfma_f32_16x16x32_bf16 v[168:171], a[224:227], a[200:203], v[168:171]\n"
    "PD\n"
    "ds_read_b64_tr_b16 a[244:245], v10 offset:57344\n"
    "v_cvt_pk_bf16_f32 v83, v150, v151\n"
    "v_mfma_f32_16x16x32_bf16 v[168:171], a[228:231], a[216:219], v[168:171]\n"
    "PD\n"
    "v_mul_f32 v136, s68, v136\n"
    "v_mul_f32 v137, s68, v137\n"
    "v_mfma_f32_16x16x32_bf16 v[172:175], a[228:231], a[220:223], v[172:175]\n"
    "PD\n"
    "ds_read_b64_tr_b16 a[246:247], v10 offset:61440\n"
    "v_xor_b32 v10, 0x10000, v10\n"
    "v_mfma_f32_16x16x32_bf16 v[172:175], a[224:227], a[204:207], v[172:175]\n"
    "PD\n"
    "s_waitcnt lgkmcnt(4)\n"
    "v_mul_f32 v138, s68, v138\n"
    "v_mul_f32 v139, s68, v139\n"
    "v_mfma_f32_16x16x32_bf16 v[188:191], a[232:235], a[204:207], v[188:191]\n"
    "PD\n"
    "ds_read_b64_tr_b16 a[248:249], v11 offset:49152\n"
    "v_mul_f32 v152, s68, v152\n"
    "v_mfma_f32_16x16x32_bf16 v[188:191], a[236:239], a[220:223], v[188:191]\n"
    "PD\n"
    "buffer_load_dwordx4 v24, s[44:47], s57 offen offset:0 lds\n"
    "v_mul_f32 v153, s68, v153\n"
    "v_mul_f32 v154, s68, v154\n"
    "v_mfma_f32_16x16x32_bf16 v[184:187], a[236:239], a[216:219], v[184:187]\n"
    "PD\n"
    "ds_read_b64_tr_b16 a[250:251], v11 offset:53248\n"
    "v_mul_f32 v155, s68, v155\n"
    "v_mfma_f32_16x16x32_bf16 v[184:187], a[232:235], a[200:203], v[184:187]\n"
    "PD\n"
    "v_cvt_pk_bf16_f32 v96, v136, v137\n"
    "v_cvt_pk_bf16_f32 v97, v138, v139\n"
    "v_mfma_f32_16x16x32_bf16 v[180:183], a[232:235], a[196:199], v[180:183]\n"
    "PD\n"
    "ds_read_b64_tr_b16 a[252:253], v11 offset:57344\n"
    "v_cvt_pk_bf16_f32 v98, v152, v153\n"
    "v_mfma_f32_16x16x32_bf16 v[180:183], a[236:239], a[212:215], v[180:183]\n"
    "PD\n"
    "v_cvt_pk_bf16_f32 v99, v154, v155\n"
    "v_mul_f32 v140, s68, v140\n"
    "v_mfma_f32_16x16x32_bf16 v[176:179], a[236:239], a[208:211], v[176:179]\n"
    "PD\n"
    "ds_read_b64_tr_b16 a[254:255], v11 offset:61440\n"
    "v_xor_b32 v11, 0x10000, v11\n"
    "v_mfma_f32_16x16x32_bf16 v[176:179], a[232:235], a[192:195], v[176:179]\n"
    "PD\n"
    "s_waitcnt lgkmcnt(4)\n"
    "v_mul_f32 v141, s68, v141\n"
    "v_mul_f32 v142, s68, v142\n"
    "v_mfma_f32_16x16x32_bf16 v[160:163], a[240:243], a[96:99], v[160:163]\n"
    "PD\n"
    "ds_read_b64_tr_b16 a[224:225], v12 offset:16384\n"
    "v_mul_f32 v143, s68, v143\n"
    "v_mfma_f32_16x16x32_bf16 v[160:163], a[244:247], a[112:115], v[160:163]\n"
    "PD\n"
    "buffer_load_dwordx4 v25, s[44:47], s57 offen offset:1024 lds\n"
    "v_mul_f32 v156, s68, v156\n"
    "v_mul_f32 v157, s68, v157\n"
    "v_mfma_f32_16x16x32_bf16 v[164:167], a[244:247], a[116:119], v[164:167]\n"
    "PD\n"
    "ds_read_b64_tr_b16 a[226:227], v12 offset:20480\n"
    "v_mul_f32 v158, s68, v158\n"
    "v_mfma_f32_16x16x32_bf16 v[164:167], a[240:243], a[100:103], v[164:167]\n"
    "PD\n"
    "buffer_store_dwordx4 v[104:107], v5, s[52:55], s69 offen offset:0 sc1\n"
    "v_mul_f32 v159, s68, v159\n"
    "v_cvt_pk_bf16_f32 v112, v140, v141\n"
    "v_mfma_f32_16x16x32_bf16 v[168:171], a[240:243], a[104:107], v[168:171]\n"
    "PD\n"
    "ds_read_b64_tr_b16 a[228:229], v12 offset:24576\n"
    "v_cvt_pk_bf16_f32 v113, v142, v143\n"
    "v_mfma_f32_16x16x32_bf16 v[168:171], a[244:247], a[120:123], v[168:171]\n"
    "PD\n"
    "v_cvt_pk_bf16_f32 v114, v156, v157\n"
    "v_cvt_pk_bf16_f32 v115, v158, v159\n"
    "v_mfma_f32_16x16x32_bf16 v[172:175], a[244:247], a[124:127], v[172:175]\n"
    "PD\n"
    "ds_read_b64_tr_b16 a[230:231], v12 offset:28672\n"
    "v_mfma_f32_16x16x32_bf16 v[172:175], a[240:243], a[108:111], v[172:175]\n"
    "PD\n"
    "s_waitcnt lgkmcnt(4)\n"
    "v_permlane16_swap_b32 v64, v66\n"
    "v_mfma_f32_16x16x32_bf16 v[188:191], a[248:251], a[108:111], v[188:191]\n"
    "PD\n"
    "ds_read_b64_tr_b16 a[232:233], v13 offset:16384\n"
    "v_mfma_f32_16x16x32_bf16 v[188:191], a[252:255], a[124:127], v[188:191]\n"
    "PD\n"
    "buffer_load_dwordx4 v26, s[44:47], s57 offen offset:2048 lds\n"
    "v_permlane16_swap_b32 v65, v67\n"
    "v_mfma_f32_16x16x32_bf16 v[184:187], a[252:255], a[120:123], v[184:187]\n"
    "PD\n"
    "ds_read_b64_tr_b16 a[234:235], v13 offset:20480\n"
    "v_mfma_f32_16x16x32_bf16 v[184:187], a[248:251], a[104:107], v[184:187]\n"
    "PD\n"
    "v_permlane16_swap_b32 v80, v82\n"
    "v_mfma_f32_16x16x32_bf16 v[180:183], a[248:251], a[100:103], v[180:183]\n"
    "PD\n"
    "ds_read_b64_tr_b16 a[236:237], v13 offset:24576\n"
    "v_mfma_f32_16x16x32_bf16 v[180:183], a[252:255], a[116:119], v[180:183]\n"
    "PD\n"
    "v_permlane16_swap_b32 v81, v83\n"
    "v_mfma_f32_16x16x32_bf16 v[176:179], a[252:255], a[112:115], v[176:179]\n"
    "PD\n"
    "ds_read_b64_tr_b16 a[238:239], v13 offset:28672\n"
    "v_mfma_f32_16x16x32_bf16 v[176:179], a[248:251], a[96:99], v[176:179]\n"
    "PD\n"
    "s_waitcnt lgkmcnt(4)\n"
    "v_permlane16_swap_b32 v96, v98\n"
    "v_mfma_f32_16x16x32_bf16 v[192:195], a[224:227], a[160:163], v[192:195]\n"
    "PD\n"
    "ds_read_b64_tr_b16 a[240:241], v12 offset:32768\n"
    "v_mfma_f32_16x16x32_bf16 v[192:195], a[228:231], a[176:179], v[192:195]\n"
    "PD\n"
    "buffer_load_dwordx4 v27, s[44:47], s57 offen offset:3072 lds\n"
    "v_permlane16_swap_b32 v97, v99\n"
    "v_mfma_f32_16x16x32_bf16 v[196:199], a[228:231], a[180:183], v[196:199]\n"
    "PD\n"
    "ds_read_b64_tr_b16 a[242:243], v12 offset:36864\n"
    "s_add_u32 s57, s57, s59\n"
    "v_mfma_f32_16x16x32_bf16 v[196:199], a[224:227], a[164:167], v[196:199]\n"
    "PD\n"
    "s_xor_b32 m0, s60, 65536\n"
    "v_permlane16_swap_b32 v112, v114\n"
    "v_mfma_f32_16x16x32_bf16 v[200:203], a[224:227], a[168:171], v[200:203]\n"
    "PD\n"
    "ds_read_b64_tr_b16 a[244:245], v12 offset:40960\n"
    "v_mfma_f32_16x16x32_bf16 v[200:203], a[228:231], a[184:187], v[200:203]\n"
    "PD\n"
    "v_permlane16_swap_b32 v113, v115\n"
    "v_mfma_f32_16x16x32_bf16 v[204:207], a[228:231], a[188:191], v[204:207]\n"
    "PD\n"
    "ds_read_b64_tr_b16 a[246:247], v12 offset:45056\n"
    "v_mul_f32 v160, s68, v160\n"
    "v_mfma_f32_16x16x32_bf16 v[204:207], a[224:227], a[172:175], v[204:207]\n"
    "PD\n"
    "s_waitcnt lgkmcnt(4)\n"
    "v_mul_f32 v161, s68, v161\n"
    "v_mul_f32 v162, s68, v162\n"
    "v_mfma_f32_16x16x32_bf16 v[220:223], a[232:235], a[172:175], v[220:223]\n"
    "PD\n"
    "ds_read_b64_tr_b16 a[248:249], v13 offset:32768\n"
    "v_mul_f32 v163, s68, v163\n"
    "v_mfma_f32_16x16x32_bf16 v[220:223], a[236:239], a[188:191], v[220:223]\n"
    "PD\n"
    "buffer_load_dwordx4 v24, s[44:47], s57 offen offset:0 lds\n"
    "v_mul_f32 v176, s68, v176\n"
    "v_mul_f32 v177, s68, v177\n"
    "v_mfma_f32_16x16x32_bf16 v[216:219], a[236:239], a[184:187], v[216:219]\n"
    "PD\n"
    "ds_read_b64_tr_b16 a[250:251], v13 offset:36864\n"
    "v_mul_f32 v178, s68, v178\n"
    "v_mfma_f32_16x16x32_bf16 v[216:219], a[232:235], a[168:171], v[216:219]\n"
    "PD\n"
    "v_mul_f32 v179, s68, v179\n"
    "v_cvt_pk_bf16_f32 v68, v160, v161\n"
    "v_mfma_f32_16x16x32_bf16 v[212:215], a[232:235], a[164:167], v[212:215]\n"
    "PD\n"
    "ds_read_b64_tr_b16 a[252:253], v13 offset:40960\n"
    "v_cvt_pk_bf16_f32 v69, v162, v163\n"
    "v_mfma_f32_16x16x32_bf16 v[212:215], a[236:239], a[180:183], v[212:215]\n"
    "PD\n"
    "v_cvt_pk_bf16_f32 v70, v176, v177\n"
    "v_cvt_pk_bf16_f32 v71, v178, v179\n"
    "v_mfma_f32_16x16x32_bf16 v[208:211], a[236:239], a[176:179], v[208:211]\n"
    "PD\n"
    "ds_read_b64_tr_b16 a[254:255], v13 offset:45056\n"
    "v_mul_f32 v164, s68, v164\n"
    "v_mfma_f32_16x16x32_bf16 v[208:211], a[232:235], a[160:163], v[208:211]\n"
    "PD\n"
    "s_waitcnt lgkmcnt(4)\n"
    "v_mul_f32 v165, s68, v165\n"
    "v_mul_f32 v166, s68, v166\n"
    "v_mfma_f32_16x16x32_bf16 v[192:195], a[240:243], a[192:195], v[192:195]\n"
    "PD\n"
    "ds_read_b64_tr_b16 a[224:225], v12 offset:49152\n"
    "v_mul_f32 v167, s68, v167\n"
    "v_mfma_f32_16x16x32_bf16 v[192:195], a[244:247], a[208:211], v[192:195]\n"
    "PD\n"
    "buffer_store_dwordx4 v[120:123], v5, s[52:55], s69 offen offset:1024 sc1\n"
    "v_mul_f32 v180, s68, v180\n"
    "v_mul_f32 v181, s68, v181\n"
    "v_mfma_f32_16x16x32_bf16 v[196:199], a[244:247], a[212:215], v[196:199]\n"
    "PD\n"
    "ds_read_b64_tr_b16 a[226:227], v12 offset:53248\n"
    "v_mul_f32 v182, s68, v182\n"
    "v_mfma_f32_16x16x32_bf16 v[196:199], a[240:243], a[196:199], v[196:199]\n"
    "PD\n"
    "buffer_load_dwordx4 v25, s[44:47], s57 offen offset:1024 lds\n"
    "v_mul_f32 v183, s68, v183\n"
    "v_cvt_pk_bf16_f32 v84, v164, v165\n"
    "v_mfma_f32_16x16x32_bf16 v[200:203], a[240:243], a[200:203], v[200:203]\n"
    "PD\n"
    "ds_read_b64_tr_b16 a[228:229], v12 offset:57344\n"
    "v_cvt_pk_bf16_f32 v85, v166, v167\n"
    "v_mfma_f32_16x16x32_bf16 v[200:203], a[244:247], a[216:219], v[200:203]\n"
    "PD\n"
    "v_cvt_pk_bf16_f32 v86, v180, v181\n"
    "v_cvt_pk_bf16_f32 v87, v182, v183\n"
    "v_mfma_f32_16x16x32_bf16 v[204:207], a[244:247], a[220:223], v[204:207]\n"
    "PD\n"
    "ds_read_b64_tr_b16 a[230:231], v12 offset:61440\n"
    "v_xor_b32 v12, 0x10000, v12\n"
    "v_mfma_f32_16x16x32_bf16 v[204:207], a[240:243], a[204:207], v[204:207]\n"
    "PD\n"
    "s_waitcnt lgkmcnt(4)\n"
    "v_mul_f32 v168, s68, v168\n"
    "v_mul_f32 v169, s68, v169\n"
    "v_mfma_f32_16x16x32_bf16 v[220:223], a[248:251], a[204:207], v[220:223]\n"
    "PD\n"
    "ds_read_b64_tr_b16 a[232:233], v13 offset:49152\n"
    "v_mul_f32 v170, s68, v170\n"
    "v_mfma_f32_16x16x32_bf16 v[220:223], a[252:255], a[220:223], v[220:223]\n"
    "PD\n"
    "buffer_load_dwordx4 v26, s[44:47], s57 offen offset:2048 lds\n"
    "v_mul_f32 v171, s68, v171\n"
    "v_mul_f32 v184, s68, v184\n"
    "v_mfma_f32_16x16x32_bf16 v[216:219], a[252:255], a[216:219], v[216:219]\n"
    "PD\n"
    "ds_read_b64_tr_b16 a[234:235], v13 offset:53248\n"
    "v_mul_f32 v185, s68, v185\n"
    "v_mfma_f32_16x16x32_bf16 v[216:219], a[248:251], a[200:203], v[216:219]\n"
    "PD\n"
    "v_mul_f32 v186, s68, v186\n"
    "v_mul_f32 v187, s68, v187\n"
    "v_mfma_f32_16x16x32_bf16 v[212:215], a[248:251], a[196:199], v[212:215]\n"
    "PD\n"
    "ds_read_b64_tr_b16 a[236:237], v13 offset:57344\n"
    "v_cvt_pk_bf16_f32 v100, v168, v169\n"
    "v_mfma_f32_16x16x32_bf16 v[212:215], a[252:255], a[212:215], v[212:215]\n"
    "PD\n"
    "v_cvt_pk_bf16_f32 v101, v170, v171\n"
    "v_cvt_pk_bf16_f32 v102, v184, v185\n"
    "v_mfma_f32_16x16x32_bf16 v[208:211], a[252:255], a[208:211], v[208:211]\n"
    "PD\n"
    "ds_read_b64_tr_b16 a[238:239], v13 offset:61440\n"
    "v_xor_b32 v13, 0x10000, v13\n"
    "v_mfma_f32_16x16x32_bf16 v[208:211], a[248:251], a[192:195], v[208:211]\n"
    "PD\n"
    "s_waitcnt lgkmcnt(4)\n"
    "v_cvt_pk_bf16_f32 v103, v186, v187\n"
    "v_mul_f32 v172, s68, v172\n"
    "v_mfma_f32_16x16x32_bf16 v[192:195], a[224:227], a[96:99], v[192:195]\n"
    "PD\n"
    "ds_read_b64_tr_b16 a[240:241], v14 offset:16384\n"
    "v_mul_f32 v173, s68, v173\n"
    "v_mfma_f32_16x16x32_bf16 v[192:195], a[228:231], a[112:115], v[192:195]\n"
    "PD\n"
    "buffer_load_dwordx4 v27, s[44:47], s57 offen offset:3072 lds\n"
    "v_mul_f32 v174, s68, v174\n"
    "v_mul_f32 v175, s68, v175\n"
    "v_mfma_f32_16x16x32_bf16 v[196:199], a[228:231], a[116:119], v[196:199]\n"
    "PD\n"
    "ds_read_b64_tr_b16 a[242:243], v14 offset:20480\n"
    "s_add_u32 s57, s57, s59\n"
    "v_mfma_f32_16x16x32_bf16 v[196:199], a[224:227], a[100:103], v[196:199]\n"
    "PD\n"
    "v_mul_f32 v188, s68, v188\n"
    "v_mul_f32 v189, s68, v189\n"
    "v_mfma_f32_16x16x32_bf16 v[200:203], a[224:227], a[104:107], v[200:203]\n"
    "PD\n"
    "ds_read_b64_tr_b16 a[244:245], v14 offset:24576\n"
    "v_mul_f32 v190, s68, v190\n"
    "v_mfma_f32_16x16x32_bf16 v[200:203], a[228:231], a[120:123], v[200:203]\n"
    "PD\n"
    "v_mul_f32 v191, s68, v191\n"
    "v_cvt_pk_bf16_f32 v116, v172, v173\n"
    "v_mfma_f32_16x16x32_bf16 v[204:207], a[228:231], a[124:127], v[204:207]\n"
    "PD\n"
    "ds_read_b64_tr_b16 a[246:247], v14 offset:28672\n"
    "v_cvt_pk_bf16_f32 v117, v174, v175\n"
    "v_mfma_f32_16x16x32_bf16 v[204:207], a[224:227], a[108:111], v[204:207]\n"
    "PD\n"
    "s_waitcnt lgkmcnt(4)\n"
    "v_cvt_pk_bf16_f32 v118, v188, v189\n"
    "v_cvt_pk_bf16_f32 v119, v190, v191\n"
    "v_mfma_f32_16x16x32_bf16 v[220:223], a[232:235], a[108:111], v[220:223]\n"
    "PD\n"
    "ds_read_b64_tr_b16 a[248:249], v15 offset:16384\n"
    "v_mfma_f32_16x16x32_bf16 v[220:223], a[236:239], a[124:127], v[220:223]\n"
    "PD\n"
    "v_permlane16_swap_b32 v68, v70\n"
    "v_mfma_f32_16x16x32_bf16 v[216:219], a[236:239], a[120:123], v[216:219]\n"
    "PD\n"
    "ds_read_b64_tr_b16 a[250:251], v15 offset:20480\n"
    "v_mfma_f32_16x16x32_bf16 v[216:219], a[232:235], a[104:107], v[216:219]\n"
    "PD\n"
    "buffer_store_dwordx4 v[108:111], v5, s[52:55], s69 offen offset:2048 sc1\n"
    "v_permlane16_swap_b32 v69, v71\n"
    "v_mfma_f32_16x16x32_bf16 v[212:215], a[232:235], a[100:103], v[212:215]\n"
    "PD\n"
    "ds_read_b64_tr_b16 a[252:253], v15 offset:24576\n"
    "v_mfma_f32_16x16x32_bf16 v[212:215], a[236:239], a[116:119], v[212:215]\n"
    "PD\n"
    "v_permlane16_swap_b32 v84, v86\n"
    "v_mfma_f32_16x16x32_bf16 v[208:211], a[236:239], a[112:115], v[208:211]\n"
    "PD\n"
    "ds_read_b64_tr_b16 a[254:255], v15 offset:28672\n"
    "v_mfma_f32_16x16x32_bf16 v[208:211], a[232:235], a[96:99], v[208:211]\n"
    "PD\n"
    "s_waitcnt lgkmcnt(4)\n"
    "v_permlane16_swap_b32 v85, v87\n"
    "v_mfma_f32_16x16x32_bf16 v[224:227], a[240:243], a[160:163], v[224:227]\n"
    "PD\n"
    "ds_read_b64_tr_b16 a[224:225], v14 offset:32768\n"
    "v_mfma_f32_16x16x32_bf16 v[224:227], a[244:247], a[176:179], v[224:227]\n"
    "PD\n"
    "v_permlane16_swap_b32 v100, v102\n"
    "v_mfma_f32_16x16x32_bf16 v[228:231], a[244:247], a[180:183], v[228:231]\n"
    "PD\n"
    "ds_read_b64_tr_b16 a[226:227], v14 offset:36864\n"
    "v_mfma_f32_16x16x32_bf16 v[228:231], a[240:243], a[164:167], v[228:231]\n"
    "PD\n"
    "v_permlane16_swap_b32 v101, v103\n"
    "v_mfma_f32_16x16x32_bf16 v[232:235], a[240:243], a[168:171], v[232:235]\n"
    "PD\n"
    "ds_read_b64_tr_b16 a[228:229], v14 offset:40960\n"
    "v_mfma_f32_16x16x32_bf16 v[232:235], a[244:247], a[184:187], v[232:235]\n"
    "PD\n"
    "v_permlane16_swap_b32 v116, v118\n"
    "v_mfma_f32_16x16x32_bf16 v[236:239], a[244:247], a[188:191], v[236:239]\n"
    "PD\n"
    "ds_read_b64_tr_b16 a[230:231], v14 offset:45056\n"
    "v_mfma_f32_16x16x32_bf16 v[236:239], a[240:243], a[172:175], v[236:239]\n"
    "PD\n"
    "s_waitcnt lgkmcnt(4)\n"
    "v_permlane16_swap_b32 v117, v119\n"
    "v_mfma_f32_16x16x32_bf16 v[252:255], a[248:251], a[172:175], v[252:255]\n"
    "PD\n"
    "ds_read_b64_tr_b16 a[232:233], v15 offset:32768\n"
    "v_mul_f32 v192, s68, v192\n"
    "v_mfma_f32_16x16x32_bf16 v[252:255], a[252:255], a[188:191], v[252:255]\n"
    "PD\n"
    "v_mul_f32 v193, s68, v193\n"
    "v_mul_f32 v194, s68, v194\n"
    "v_mfma_f32_16x16x32_bf16 v[248:251], a[252:255], a[184:187], v[248:251]\n"
    "PD\n"
    "ds_read_b64_tr_b16 a[234:235], v15 offset:36864\n"
    "v_mul_f32 v195, s68, v195\n"
    "v_mfma_f32_16x16x32_bf16 v[248:251], a[248:251], a[168:171], v[248:251]\n"
    "PD\n"
    "v_mul_f32 v208, s68, v208\n"
    "v_mul_f32 v209, s68, v209\n"
    "v_mfma_f32_16x16x32_bf16 v[244:247], a[248:251], a[164:167], v[244:247]\n"
    "PD\n"
    "ds_read_b64_tr_b16 a[236:237], v15 offset:40960\n"
    "v_mul_f32 v210, s68, v210\n"
    "v_mfma_f32_16x16x32_bf16 v[244:247], a[252:255], a[180:183], v[244:247]\n"
    "PD\n"
    "v_mul_f32 v211, s68, v211\n"
    "v_cvt_pk_bf16_f32 v72, v192, v193\n"
    "v_mfma_f32_16x16x32_bf16 v[240:243], a[252:255], a[176:179], v[240:243]\n"
    "PD\n"
    "ds_read_b64_tr_b16 a[238:239], v15 offset:45056\n"
    "v_cvt_pk_bf16_f32 v73, v194, v195\n"
    "v_mfma_f32_16x16x32_bf16 v[240:243], a[248:251], a[160:163], v[240:243]\n"
    "PD\n"
    "s_waitcnt lgkmcnt(4)\n"
    "v_cvt_pk_bf16_f32 v74, v208, v209\n"
    "v_cvt_pk_bf16_f32 v75, v210, v211\n"
    "v_mfma_f32_16x16x32_bf16 v[224:227], a[224:227], a[192:195], v[224:227]\n"
    "PD\n"
    "ds_read_b64_tr_b16 a[240:241], v14 offset:49152\n"
    "v_mul_f32 v196, s68, v196\n"
    "v_mfma_f32_16x16x32_bf16 v[224:227], a[228:231], a[208:211], v[224:227]\n"
    "PD\n"
    "v_mul_f32 v197, s68, v197\n"
    "v_mul_f32 v198, s68, v198\n"
    "v_mfma_f32_16x16x32_bf16 v[228:231], a[228:231], a[212:215], v[228:231]\n"
    "PD\n"
    "ds_read_b64_tr_b16 a[242:243], v14 offset:53248\n"
    "v_mul_f32 v199, s68, v199\n"
    "v_mfma_f32_16x16x32_bf16 v[228:231], a[224:227], a[196:199], v[228:231]\n"
    "PD\n"
    "v_mul_f32 v212, s68, v212\n"
    "v_mul_f32 v213, s68, v213\n"
    "v_mfma_f32_16x16x32_bf16 v[232:235], a[224:227], a[200:203], v[232:235]\n"
    "PD\n"
    "ds_read_b64_tr_b16 a[244:245], v14 offset:57344\n"
    "v_mul_f32 v214, s68, v214\n"
    "v_mfma_f32_16x16x32_bf16 v[232:235], a[228:231], a[216:219], v[232:235]\n"
    "PD\n"
    "v_mul_f32 v215, s68, v215\n"
    "v_cvt_pk_bf16_f32 v88, v196, v197\n"
    "v_mfma_f32_16x16x32_bf16 v[236:239], a[228:231], a[220:223], v[236:239]\n"
    "PD\n"
    "ds_read_b64_tr_b16 a[246:247], v14 offset:61440\n"
    "v_xor_b32 v14, 0x10000, v14\n"
    "v_mfma_f32_16x16x32_bf16 v[236:239], a[224:227], a[204:207], v[236:239]\n"
    "PD\n"
    "s_waitcnt lgkmcnt(4)\n"
    "v_cvt_pk_bf16_f32 v89, v198, v199\n"
    "v_cvt_pk_bf16_f32 v90, v212, v213\n"
    "v_mfma_f32_16x16x32_bf16 v[252:255], a[232:235], a[204:207], v[252:255]\n"
    "PD\n"
    "ds_read_b64_tr_b16 a[248:249], v15 offset:49152\n"
    "v_cvt_pk_bf16_f32 v91, v214, v215\n"
    "v_mfma_f32_16x16x32_bf16 v[252:255], a[236:239], a[220:223], v[252:255]\n"
    "PD\n"
    "buffer_store_dwordx4 v[124:127], v5, s[52:55], s69 offen offset:3072 sc1\n"
    "v_mul_f32 v200, s68, v200\n"
    "v_mul_f32 v201, s68, v201\n"
    "v_mfma_f32_16x16x32_bf16 v[248:251], a[236:239], a[216:219], v[248:251]\n"
    "PD\n"
    "ds_read_b64_tr_b16 a[250:251], v15 offset:53248\n"
    "v_mul_f32 v202, s68, v202\n"
    "v_mfma_f32_16x16x32_bf16 v[248:251], a[232:235], a[200:203], v[248:251]\n"
    "PD\n"
    "v_mul_f32 v203, s68, v203\n"
    "v_mul_f32 v216, s68, v216\n"
    "v_mfma_f32_16x16x32_bf16 v[244:247], a[232:235], a[196:199], v[244:247]\n"
    "PD\n"
    "ds_read_b64_tr_b16 a[252:253], v15 offset:57344\n"
    "v_mul_f32 v217, s68, v217\n"
    "v_mfma_f32_16x16x32_bf16 v[244:247], a[236:239], a[212:215], v[244:247]\n"
    "PD\n"
    "v_mul_f32 v218, s68, v218\n"
    "v_mul_f32 v219, s68, v219\n"
    "v_mfma_f32_16x16x32_bf16 v[240:243], a[236:239], a[208:211], v[240:243]\n"
    "PD\n"
    "ds_read_b64_tr_b16 a[254:255], v15 offset:61440\n"
    "v_xor_b32 v15, 0x10000, v15\n"
    "v_mfma_f32_16x16x32_bf16 v[240:243], a[232:235], a[192:195], v[240:243]\n"
    "PD\n"
    "s_waitcnt lgkmcnt(4)\n"
    "s_waitcnt vmcnt(12)\n"
    "s_barrier\n"
    "v_cvt_pk_bf16_f32 v104, v200, v201\n"
    "v_mfma_f32_16x16x32_bf16 v[224:227], a[240:243], a[96:99], v[224:227]\n"
    "PD\n"
    "s_xor_b32 m0, s60, 81920\n"
    "v_cvt_pk_bf16_f32 v105, v202, v203\n"
    "v_cvt_pk_bf16_f32 v106, v216, v217\n"
    "v_mfma_f32_16x16x32_bf16 v[224:227], a[244:247], a[112:115], v[224:227]\n"
    "PD\n"
    "buffer_load_dwordx4 v28, s[48:51], s58 offen offset:0 lds\n"
    "v_cvt_pk_bf16_f32 v107, v218, v219\n"
    "v_mul_f32 v204, s68, v204\n"
    "v_mfma_f32_16x16x32_bf16 v[228:231], a[244:247], a[116:119], v[228:231]\n"
    "PD\n"
    "v_mul_f32 v205, s68, v205\n"
    "v_mul_f32 v206, s68, v206\n"
    "v_mfma_f32_16x16x32_bf16 v[228:231], a[240:243], a[100:103], v[228:231]\n"
    "PD\n"
    "buffer_load_dwordx4 v29, s[48:51], s58 offen offset:1024 lds\n"
    "v_mul_f32 v207, s68, v207\n"
    "v_mul_f32 v220, s68, v220\n"
    "v_mfma_f32_16x16x32_bf16 v[232:235], a[240:243], a[104:107], v[232:235]\n"
    "PD\n"
    "v_mul_f32 v221, s68, v221\n"
    "v_mul_f32 v222, s68, v222\n"
    "v_mfma_f32_16x16x32_bf16 v[232:235], a[244:247], a[120:123], v[232:235]\n"
    "PD\n"
    "buffer_load_dwordx4 v30, s[48:51], s58 offen offset:2048 lds\n"
    "v_mul_f32 v223, s68, v223\n"
    "v_cvt_pk_bf16_f32 v120, v204, v205\n"
    "v_mfma_f32_16x16x32_bf16 v[236:239], a[244:247], a[124:127], v[236:239]\n"
    "PD\n"
    "v_cvt_pk_bf16_f32 v121, v206, v207\n"
    "v_cvt_pk_bf16_f32 v122, v220, v221\n"
    "v_mfma_f32_16x16x32_bf16 v[236:239], a[240:243], a[108:111], v[236:239]\n"
    "PD\n"
    "s_waitcnt lgkmcnt(0)\n"
    "v_cvt_pk_bf16_f32 v123, v222, v223\n"
    "v_mfma_f32_16x16x32_bf16 v[252:255], a[248:251], a[108:111], v[252:255]\n"
    "PD\n"
    "ds_read_b128 a[128:131], v16 offset:16384\n"
    "v_mfma_f32_16x16x32_bf16 v[252:255], a[252:255], a[124:127], v[252:255]\n"
    "PD\n"
    "buffer_load_dwordx4 v31, s[48:51], s58 offen offset:3072 lds\n"
    "v_permlane16_swap_b32 v72, v74\n"
    "v_mfma_f32_16x16x32_bf16 v[248:251], a[252:255], a[120:123], v[248:251]\n"
    "PD\n"
    "ds_read_b128 a[132:135], v17 offset:16384\n"
    "v_mfma_f32_16x16x32_bf16 v[248:251], a[248:251], a[104:107], v[248:251]\n"
    "PD\n"
    "s_add_u32 s58, s58, s59\n"
    "v_permlane16_swap_b32 v73, v75\n"
    "v_mfma_f32_16x16x32_bf16 v[244:247], a[248:251], a[100:103], v[244:247]\n"
    "PD\n"
    "ds_read_b128 a[136:139], v18 offset:16384\n"
    "v_mfma_f32_16x16x32_bf16 v[244:247], a[252:255], a[116:119], v[244:247]\n"
    "PD\n"
    "v_permlane16_swap_b32 v88, v90\n"
    "v_mfma_f32_16x16x32_bf16 v[240:243], a[252:255], a[112:115], v[240:243]\n"
    "PD\n"
    "ds_read_b128 a[140:143], v19 offset:16384\n"
    "v_mfma_f32_16x16x32_bf16 v[240:243], a[248:251], a[96:99], v[240:243]\n"
    "PD\n"
    "v_permlane16_swap_b32 v89, v91\n"
    "v_permlane16_swap_b32 v104, v106\n"
    "v_permlane16_swap_b32 v105, v107\n"
    "v_permlane16_swap_b32 v120, v122\n"
    "v_permlane16_swap_b32 v121, v123\n"
    "v_mov_b32 v56, 0x3f803f80\n"
    "v_accvgpr_write_b32 a208, v56\n"
    "PD\n"
    "v_accvgpr_write_b32 a209, v56\n"
    "PD\n"
    "v_accvgpr_write_b32 a210, v56\n"
    "PD\n"
    "v_accvgpr_write_b32 a211, v56\n"
    "PD\n"
    "v_accvgpr_write_b32 a212, -1\n"
    "PD\n"
    "v_accvgpr_write_b32 a213, -1\n"
    "PD\n"
    "v_accvgpr_write_b32 a214, -1\n"
    "PD\n"
    "v_accvgpr_write_b32 a215, -1\n"
    "PD\n"
    "s_nop 2\n"
    "PD\n"
    "v_pk_mul_f32 v[224:225], v[224:225], s[68:69] op_sel_hi:[1,0]\n"
    "PD\n"
    "v_pk_mul_f32 v[226:227], v[226:227], s[68:69] op_sel_hi:[1,0]\n"
    "PD\n"
    "v_pk_mul_f32 v[240:241], v[240:241], s[68:69] op_sel_hi:[1,0]\n"
    "PD\n"
    "v_pk_mul_f32 v[242:243], v[242:243], s[68:69] op_sel_hi:[1,0]\n"
    "PD\n"
    "v_cvt_pk_bf16_f32 v76, v224, v225\n"
    "PD\n"
    "v_cvt_pk_bf16_f32 v77, v226, v227\n"
    "PD\n"
    "v_cvt_pk_bf16_f32 v78, v240, v241\n"
    "PD\n"
    "v_cvt_pk_bf16_f32 v79, v242, v243\n"
    "PD\n"
    "v_pk_mul_f32 v[228:229], v[228:229], s[68:69] op_sel_hi:[1,0]\n"
    "PD\n"
    "v_pk_mul_f32 v[230:231], v[230:231], s[68:69] op_sel_hi:[1,0]\n"
    "PD\n"
    "v_pk_mul_f32 v[244:245], v[244:245], s[68:69] op_sel_hi:[1,0]\n"
    "PD\n"
    "v_pk_mul_f32 v[246:247], v[246:247], s[68:69] op_sel_hi:[1,0]\n"
    "PD\n"
    "v_cvt_pk_bf16_f32 v92, v228, v229\n"
    "PD\n"
    "v_cvt_pk_bf16_f32 v93, v230, v231\n"
    "PD\n"
    "v_cvt_pk_bf16_f32 v94, v244, v245\n"
    "PD\n"
    "v_cvt_pk_bf16_f32 v95, v246, v247\n"
    "PD\n"
    "v_pk_mul_f32 v[232:233], v[232:233], s[68:69] op_sel_hi:[1,0]\n"
    "PD\n"
    "v_pk_mul_f32 v[234:235], v[234:235], s[68:69] op_sel_hi:[1,0]\n"
    "PD\n"
    "v_pk_mul_f32 v[248:249], v[248:249], s[68:69] op_sel_hi:[1,0]\n"
    "PD\n"
    "v_pk_mul_f32 v[250:251], v[250:251], s[68:69] op_sel_hi:[1,0]\n"
    "PD\n"
    "v_cvt_pk_bf16_f32 v108, v232, v233\n"
    "PD\n"
    "v_cvt_pk_bf16_f32 v109, v234, v235\n"
    "PD\n"
    "v_cvt_pk_bf16_f32 v110, v248, v249\n"
    "PD\n"
    "v_cvt_pk_bf16_f32 v111, v250, v251\n"
    "PD\n"
    "v_pk_mul_f32 v[236:237], v[236:237], s[68:69] op_sel_hi:[1,0]\n"
    "PD\n"
    "v_pk_mul_f32 v[238:239], v[238:239], s[68:69] op_sel_hi:[1,0]\n"
    "PD\n"
    "v_pk_mul_f32 v[252:253], v[252:253], s[68:69] op_sel_hi:[1,0]\n"
    "PD\n"
    "v_pk_mul_f32 v[254:255], v[254:255], s[68:69] op_sel_hi:[1,0]\n"
    "PD\n"
    "v_cvt_pk_bf16_f32 v124, v236, v237\n"
    "PD\n"
    "v_cvt_pk_bf16_f32 v125, v238, v239\n"
    "PD\n"
    "v_cvt_pk_bf16_f32 v126, v252, v253\n"
    "PD\n"
    "v_cvt_pk_bf16_f32 v127, v254, v255\n"
    "PD\n"
    "v_permlane16_swap_b32 v76, v78\n"
    "PD\n"
    "v_permlane16_swap_b32 v77, v79\n"
    "PD\n"
    "v_permlane16_swap_b32 v92, v94\n"
    "PD\n"
    "v_permlane16_swap_b32 v93, v95\n"
    "PD\n"
    "v_permlane16_swap_b32 v108, v110\n"
    "PD\n"
    "v_permlane16_swap_b32 v109, v111\n"
    "PD\n"
    "v_permlane16_swap_b32 v124, v126\n"
    "PD\n"
    "v_permlane16_swap_b32 v125, v127\n"
    "PD\n"
    "s_waitcnt lgkmcnt(3)\n"
    "v_mfma_f32_16x16x32_bf16 v[192:195], a[128:131], v[64:67], 0\n"
    "PD\n"
    "s_waitcnt lgkmcnt(2)\n"
    "v_mfma_f32_16x16x32_bf16 v[192:195], a[132:135], v[68:71], v[192:195]\n"
    "PD\n"
    "s_waitcnt lgkmcnt(1)\n"
    "v_mfma_f32_16x16x32_bf16 v[192:195], a[136:139], v[72:75], v[192:195]\n"
    "PD\n"
    "s_waitcnt lgkmcnt(0)\n"
    "v_mfma_f32_16x16x32_bf16 v[192:195], a[140:143], v[76:79], v[192:195]\n"
    "PD\n"
    "v_mfma_f32_16x16x32_bf16 v[196:199], a[140:143], v[92:95], 0\n"
    "PD\n"
    "v_mfma_f32_16x16x32_bf16 v[196:199], a[136:139], v[88:91], v[196:199]\n"
    "PD\n"
    "v_mfma_f32_16x16x32_bf16 v[196:199], a[132:135], v[84:87], v[196:199]\n"
    "PD\n"
    "v_mfma_f32_16x16x32_bf16 v[196:199], a[128:131], v[80:83], v[196:199]\n"
    "PD\n"
    "v_mfma_f32_16x16x32_bf16 v[200:203], a[128:131], v[96:99], 0\n"
    "PD\n"
    "v_mfma_f32_16x16x32_bf16 v[200:203], a[132:135], v[100:103], v[200:203]\n"
    "PD\n"
    "v_mfma_f32_16x16x32_bf16 v[200:203], a[136:139], v[104:107], v[200:203]\n"
    "PD\n"
    "v_mfma_f32_16x16x32_bf16 v[200:203], a[140:143], v[108:111], v[200:203]\n"
    "PD\n"
    "v_mfma_f32_16x16x32_bf16 v[204:207], a[140:143], v[124:127], 0\n"
    "PD\n"
    "v_mfma_f32_16x16x32_bf16 v[204:207], a[136:139], v[120:123], v[204:207]\n"
    "PD\n"
    "v_mfma_f32_16x16x32_bf16 v[204:207], a[132:135], v[116:119], v[204:207]\n"
    "PD\n"
    "v_mfma_f32_16x16x32_bf16 v[204:207], a[128:131], v[112:115], v[204:207]\n"
    "PD\n"
    "s_nop 4\n"
    "v_max3_f32 v224, v192, v193, v194\n"
    "v_max3_f32 v225, v196, v197, v198\n"
    "v_max3_f32 v226, v200, v201, v202\n"
    "v_max3_f32 v227, v204, v205, v206\n"
    "v_max_f32 v224, v224, v195\n"
    "v_max_f32 v225, v225, v199\n"
    "PD\n"
    "v_max_f32 v226, v226, v203\n"
    "PD\n"
    "v_max_f32 v227, v227, v207\n"
    "PD\n"
    "L_red:\n"
    "PD\n"
    "v_mov_b32 v228, v224\n"
    "PD\n"
    "v_mov_b32 v229, v225\n"
    "PD\n"
    "v_mov_b32 v230, v226\n"
    "PD\n"
    "v_mov_b32 v231, v227\n"
    "PD\n"
    "v_permlane16_swap_b32 v224, v228\n"
    "PD\n"
    "v_permlane16_swap_b32 v225, v229\n"
    "PD\n"
    "v_permlane16_swap_b32 v226, v230\n"
    "PD\n"
    "v_permlane16_swap_b32 v227, v231\n"
    "PD\n"
    "v_max_f32 v224, v224, v228\n"
    "PD\n"
    "v_max_f32 v225, v225, v229\n"
    "PD\n"
    "v_max_f32 v226, v226, v230\n"
    "PD\n"
    "v_max_f32 v227, v227, v231\n"
    "PD\n"
    "v_mov_b32 v228, v224\n"
    "PD\n"
    "v_mov_b32 v229, v225\n"
    "PD\n"
    "v_mov_b32 v230, v226\n"
    "PD\n"
    "v_mov_b32 v231, v227\n"
    "PD\n"
    "v_permlane32_swap_b32 v224, v228\n"
    "PD\n"
    "v_permlane32_swap_b32 v225, v229\n"
    "PD\n"
    "v_permlane32_swap_b32 v226, v230\n"
    "PD\n"
    "v_permlane32_swap_b32 v227, v231\n"
    "PD\n"
    "v_max_f32 v224, v224, v228\n"
    "PD\n"
    "v_max_f32 v225, v225, v229\n"
    "PD\n"
    "v_max_f32 v226, v226, v230\n"
    "PD\n"
    "v_max_f32 v227, v227, v231\n"
    "PD\n"
    "v_xor_b32 v40, 0x80000000, v224\n"
    "PD\n"
    "v_xor_b32 v41, 0x80000000, v224\n"
    "PD\n"
    "v_xor_b32 v42, 0x80000000, v224\n"
    "PD\n"
    "v_xor_b32 v43, 0x80000000, v224\n"
    "PD\n"
    "v_xor_b32 v44, 0x80000000, v225\n"
    "PD\n"
    "v_xor_b32 v45, 0x80000000, v225\n"
    "PD\n"
    "v_xor_b32 v46, 0x80000000, v225\n"
    "PD\n"
    "v_xor_b32 v47, 0x80000000, v225\n"
    "PD\n"
    "v_xor_b32 v48, 0x80000000, v226\n"
    "PD\n"
    "v_xor_b32 v49, 0x80000000, v226\n"
    "PD\n"
    "v_xor_b32 v50, 0x80000000, v226\n"
    "PD\n"
    "v_xor_b32 v51, 0x80000000, v226\n"
    "PD\n"
    "v_xor_b32 v52, 0x80000000, v227\n"
    "PD\n"
    "v_xor_b32 v53, 0x80000000, v227\n"
    "PD\n"
    "v_xor_b32 v54, 0x80000000, v227\n"
    "PD\n"
    "v_xor_b32 v55, 0x80000000, v227\n"
    "PD\n"
    "v_mfma_f32_16x16x32_bf16 v[128:131], a[128:131], v[64:67], v[40:43]\n"
    "PD\n"
    "v_mfma_f32_16x16x32_bf16 v[128:131], a[132:135], v[68:71], v[128:131]\n"
    "PD\n"
    "ds_read_b128 a[144:147], v16 offset:20480\n"
    "v_mfma_f32_16x16x32_bf16 v[128:131], a[136:139], v[72:75], v[128:131]\n"
    "PD\n"
    "v_mfma_f32_16x16x32_bf16 v[128:131], a[140:143], v[76:79], v[128:131]\n"
    "PD\n"
    "ds_read_b128 a[148:151], v17 offset:20480\n"
    "v_mfma_f32_16x16x32_bf16 v[132:135], a[140:143], v[92:95], v[44:47]\n"
    "PD\n"
    "v_mfma_f32_16x16x32_bf16 v[132:135], a[136:139], v[88:91], v[132:135]\n"
    "PD\n"
    "ds_read_b128 a[152:155], v18 offset:20480\n"
    "v_mfma_f32_16x16x32_bf16 v[132:135], a[132:135], v[84:87], v[132:135]\n"
    "PD\n"
    "v_mfma_f32_16x16x32_bf16 v[132:135], a[128:131], v[80:83], v[132:135]\n"
    "PD\n"
    "ds_read_b128 a[156:159], v19 offset:20480\n"
    "v_mfma_f32_16x16x32_bf16 v[136:139], a[128:131], v[96:99], v[48:51]\n"
    "PD\n"
    "v_mfma_f32_16x16x32_bf16 v[136:139], a[132:135], v[100:103], v[136:139]\n"
    "PD\n"
    "v_mfma_f32_16x16x32_bf16 v[136:139], a[136:139], v[104:107], v[136:139]\n"
    "PD\n"
    "v_mfma_f32_16x16x32_bf16 v[136:139], a[140:143], v[108:111], v[136:139]\n"
    "PD\n"
    "v_exp_f32 v128, v128\n"
    "v_mfma_f32_16x16x32_bf16 v[140:143], a[140:143], v[124:127], v[52:55]\n"
    "PD\n"
    "v_exp_f32 v129, v129\n"
    "v_mfma_f32_16x16x32_bf16 v[140:143], a[136:139], v[120:123], v[140:143]\n"
    "PD\n"
    "v_exp_f32 v130, v130\n"
    "v_mfma_f32_16x16x32_bf16 v[140:143], a[132:135], v[116:119], v[140:143]\n"
    "PD\n"
    "v_exp_f32 v131, v131\n"
    "v_mfma_f32_16x16x32_bf16 v[140:143], a[128:131], v[112:115], v[140:143]\n"
    "PD\n"
    "s_waitcnt lgkmcnt(0)\n"
    "v_exp_f32 v132, v132\n"
    "v_mfma_f32_16x16x32_bf16 v[156:159], a[144:147], v[112:115], v[52:55]\n"
    "PD\n"
    "v_exp_f32 v133, v133\n"
    "v_mfma_f32_16x16x32_bf16 v[156:159], a[148:151], v[116:119], v[156:159]\n"
    "PD\n"
    "ds_read_b128 a[128:131], v16 offset:24576\n"
    "v_cvt_pk_bf16_f32 v128, v128, v129\n"
    "v_mfma_f32_16x16x32_bf16 v[156:159], a[152:155], v[120:123], v[156:159]\n"
    "PD\n"
    "v_exp_f32 v134, v134\n"
    "v_mfma_f32_16x16x32_bf16 v[156:159], a[156:159], v[124:127], v[156:159]\n"
    "PD\n"
    "ds_read_b128 a[132:135], v17 offset:24576\n"
    "v_cvt_pk_bf16_f32 v129, v130, v131\n"
    "v_mfma_f32_16x16x32_bf16 v[152:155], a[156:159], v[108:111], v[48:51]\n"
    "PD\n"
    "v_exp_f32 v135, v135\n"
    "v_mfma_f32_16x16x32_bf16 v[152:155], a[152:155], v[104:107], v[152:155]\n"
    "PD\n"
    "ds_read_b128 a[136:139], v18 offset:24576\n"
    "v_cvt_pk_bf16_f32 v132, v132, v133\n"
    "v_mfma_f32_16x16x32_bf16 v[152:155], a[148:151], v[100:103], v[152:155]\n"
    "PD\n"
    "v_exp_f32 v136, v136\n"
    "v_mfma_f32_16x16x32_bf16 v[152:155], a[144:147], v[96:99], v[152:155]\n"
    "PD\n"
    "ds_read_b128 a[140:143], v19 offset:24576\n"
    "v_cvt_pk_bf16_f32 v133, v134, v135\n"
    "v_mfma_f32_16x16x32_bf16 v[148:151], a[144:147], v[80:83], v[44:47]\n"
    "PD\n"
    "v_exp_f32 v137, v137\n"
    "v_mfma_f32_16x16x32_bf16 v[148:151], a[148:151], v[84:87], v[148:151]\n"
    "PD\n"
    "v_exp_f32 v138, v138\n"
    "v_mfma_f32_16x16x32_bf16 v[148:151], a[152:155], v[88:91], v[148:151]\n"
    "PD\n"
    "v_exp_f32 v139, v139\n"
    "v_mfma_f32_16x16x32_bf16 v[148:151], a[156:159], v[92:95], v[148:151]\n"
    "PD\n"
    "v_exp_f32 v140, v140\n"
    "v_mfma_f32_16x16x32_bf16 v[144:147], a[156:159], v[76:79], v[40:43]\n"
    "PD\n"
    "v_exp_f32 v141, v141\n"
    "v_mfma_f32_16x16x32_bf16 v[144:147], a[152:155], v[72:75], v[144:147]\n"
    "PD\n"
    "v_cvt_pk_bf16_f32 v136, v136, v137\n"
    "v_cvt_pk_bf16_f32 v137, v138, v139\n"
    "v_mfma_f32_16x16x32_bf16 v[144:147], a[148:151], v[68:71], v[144:147]\n"
    "PD\n"
    "v_exp_f32 v142, v142\n"
    "v_mfma_f32_16x16x32_bf16 v[144:147], a[144:147], v[64:67], v[144:147]\n"
    "PD\n"
    "s_waitcnt lgkmcnt(0)\n"
    "v_exp_f32 v143, v143\n"
    "v_mfma_f32_16x16x32_bf16 v[160:163], a[128:131], v[64:67], v[40:43]\n"
    "PD\n"
    "v_exp_f32 v156, v156\n"
    "v_mfma_f32_16x16x32_bf16 v[160:163], a[132:135], v[68:71], v[160:163]\n"
    "PD\n"
    "ds_read_b128 a[144:147], v16 offset:28672\n"
    "v_cvt_pk_bf16_f32 v140, v140, v141\n"
    "v_mfma_f32_16x16x32_bf16 v[160:163], a[136:139], v[72:75], v[160:163]\n"
    "PD\n"
    "v_exp_f32 v157, v157\n"
    "v_mfma_f32_16x16x32_bf16 v[160:163], a[140:143], v[76:79], v[160:163]\n"
    "PD\n"
    "ds_read_b128 a[148:151], v17 offset:28672\n"
    "v_cvt_pk_bf16_f32 v141, v142, v143\n"
    "v_mfma_f32_16x16x32_bf16 v[164:167], a[140:143], v[92:95], v[44:47]\n"
    "PD\n"
    "v_exp_f32 v158, v158\n"
    "v_mfma_f32_16x16x32_bf16 v[164:167], a[136:139], v[88:91], v[164:167]\n"
    "PD\n"
    "ds_read_b128 a[152:155], v18 offset:28672\n"
    "v_cvt_pk_bf16_f32 v142, v156, v157\n"
    "v_mfma_f32_16x16x32_bf16 v[164:167], a[132:135], v[84:87], v[164:167]\n"
    "PD\n"
    "v_exp_f32 v159, v159\n"
    "v_mfma_f32_16x16x32_bf16 v[164:167], a[128:131], v[80:83], v[164:167]\n"
    "PD\n"
    "ds_read_b128 a[156:159], v19 offset:28672\n"
    "v_cvt_pk_bf16_f32 v143, v158, v159\n"
    "v_mfma_f32_16x16x32_bf16 v[168:171], a[128:131], v[96:99], v[48:51]\n"
    "PD\n"
    "v_exp_f32 v152, v152\n"
    "v_mfma_f32_16x16x32_bf16 v[168:171], a[132:135], v[100:103], v[168:171]\n"
    "PD\n"
    "v_exp_f32 v153, v153\n"
    "v_mfma_f32_16x16x32_bf16 v[168:171], a[136:139], v[104:107], v[168:171]\n"
    "PD\n"
    "v_exp_f32 v154, v154\n"
    "v_mfma_f32_16x16x32_bf16 v[168:171], a[140:143], v[108:111], v[168:171]\n"
    "PD\n"
    "v_exp_f32 v155, v155\n"
    "v_mfma_f32_16x16x32_bf16 v[172:175], a[140:143], v[124:127], v[52:55]\n"
    "PD\n"
    "v_exp_f32 v148, v148\n"
    "v_mfma_f32_16x16x32_bf16 v[172:175], a[136:139], v[120:123], v[172:175]\n"
    "PD\n"
    "v_exp_f32 v149, v149\n"
    "v_mfma_f32_16x16x32_bf16 v[172:175], a[132:135], v[116:119], v[172:175]\n"
    "PD\n"
    "v_cvt_pk_bf16_f32 v138, v152, v153\n"
    "v_cvt_pk_bf16_f32 v139, v154, v155\n"
    "v_mfma_f32_16x16x32_bf16 v[172:175], a[128:131], v[112:115], v[172:175]\n"
    "PD\n"
    "s_waitcnt lgkmcnt(0)\n"
    "s_waitcnt vmcnt(8)\n"
    "s_barrier\n"
    "v_cvt_pk_bf16_f32 v134, v148, v149\n"
    "v_mfma_f32_16x16x32_bf16 v[188:191], a[144:147], v[112:115], v[52:55]\n"
    "PD\n"
    "v_exp_f32 v150, v150\n"
    "v_mfma_f32_16x16x32_bf16 v[188:191], a[148:151], v[116:119], v[188:191]\n"
    "PD\n"
    "ds_read_b128 a[128:131], v16 offset:49152\n"
    "v_mfma_f32_16x16x32_bf16 v[188:191], a[152:155], v[120:123], v[188:191]\n"
    "PD\n"
    "v_exp_f32 v151, v151\n"
    "v_mfma_f32_16x16x32_bf16 v[188:191], a[156:159], v[124:127], v[188:191]\n"
    "PD\n"
    "ds_read_b128 a[132:135], v17 offset:49152\n"
    "v_cvt_pk_bf16_f32 v135, v150, v151\n"
    "v_mfma_f32_16x16x32_bf16 v[184:187], a[156:159], v[108:111], v[48:51]\n"
    "PD\n"
    "v_exp_f32 v144, v144\n"
    "v_mfma_f32_16x16x32_bf16 v[184:187], a[152:155], v[104:107], v[184:187]\n"
    "PD\n"
    "ds_read_b128 a[136:139], v18 offset:49152\n"
    "v_mfma_f32_16x16x32_bf16 v[184:187], a[148:151], v[100:103], v[184:187]\n"
    "PD\n"
    "v_exp_f32 v145, v145\n"
    "v_mfma_f32_16x16x32_bf16 v[184:187], a[144:147], v[96:99], v[184:187]\n"
    "PD\n"
    "ds_read_b128 a[140:143], v19 offset:49152\n"
    "v_cvt_pk_bf16_f32 v130, v144, v145\n"
    "v_mfma_f32_16x16x32_bf16 v[180:183], a[144:147], v[80:83], v[44:47]\n"
    "PD\n"
    "s_add_u32 m0, s61, 114688\n"
    "v_exp_f32 v146, v146\n"
    "v_mfma_f32_16x16x32_bf16 v[180:183], a[148:151], v[84:87], v[180:183]\n"
    "PD\n"
    "buffer_load_dwordx4 v28, s[48:51], s58 offen offset:0 lds\n"
    "v_exp_f32 v147, v147\n"
    "v_mfma_f32_16x16x32_bf16 v[180:183], a[152:155], v[88:91], v[180:183]\n"
    "PD\n"
    "v_exp_f32 v160, v160\n"
    "v_mfma_f32_16x16x32_bf16 v[180:183], a[156:159], v[92:95], v[180:183]\n"
    "PD\n"
    "buffer_load_dwordx4 v29, s[48:51], s58 offen offset:1024 lds\n"
    "v_exp_f32 v161, v161\n"
    "v_mfma_f32_16x16x32_bf16 v[176:179], a[156:159], v[76:79], v[40:43]\n"
    "PD\n"
    "v_exp_f32 v162, v162\n"
    "v_mfma_f32_16x16x32_bf16 v[176:179], a[152:155], v[72:75], v[176:179]\n"
    "PD\n"
    "buffer_load_dwordx4 v30, s[48:51], s58 offen offset:2048 lds\n"
    "v_exp_f32 v163, v163\n"
    "v_mfma_f32_16x16x32_bf16 v[176:179], a[148:151], v[68:71], v[176:179]\n"
    "PD\n"
    "v_cvt_pk_bf16_f32 v131, v146, v147\n"
    "v_cvt_pk_bf16_f32 v160, v160, v161\n"
    "v_mfma_f32_16x16x32_bf16 v[176:179], a[144:147], v[64:67], v[176:179]\n"
    "PD\n"
    "s_waitcnt lgkmcnt(0)\n"
    "v_exp_f32 v164, v164\n"
    "v_mfma_f32_16x16x32_bf16 v[192:195], a[128:131], v[64:67], v[40:43]\n"
    "PD\n"
    "buffer_load_dwordx4 v31, s[48:51], s58 offen offset:3072 lds\n"
    "v_exp_f32 v165, v165\n"
    "v_mfma_f32_16x16x32_bf16 v[192:195], a[132:135], v[68:71], v[192:195]\n"
    "PD\n"
    "ds_read_b128 a[144:147], v16 offset:53248\n"
    "v_cvt_pk_bf16_f32 v161, v162, v163\n"
    "v_mfma_f32_16x16x32_bf16 v[192:195], a[136:139], v[72:75], v[192:195]\n"
    "PD\n"
    "s_add_u32 s58, s58, s59\n"
    "v_exp_f32 v166, v166\n"
    "v_mfma_f32_16x16x32_bf16 v[192:195], a[140:143], v[76:79], v[192:195]\n"
    "PD\n"
    "ds_read_b128 a[148:151], v17 offset:53248\n"
    "v_cvt_pk_bf16_f32 v164, v164, v165\n"
    "v_mfma_f32_16x16x32_bf16 v[196:199], a[140:143], v[92:95], v[44:47]\n"
    "PD\n"
    "s_add_u32 m0, s61, 98304\n"
    "v_exp_f32 v167, v167\n"
    "v_mfma_f32_16x16x32_bf16 v[196:199], a[136:139], v[88:91], v[196:199]\n"
    "PD\n"
    "ds_read_b128 a[152:155], v18 offset:53248\n"
    "v_cvt_pk_bf16_f32 v165, v166, v167\n"
    "v_mfma_f32_16x16x32_bf16 v[196:199], a[132:135], v[84:87], v[196:199]\n"
    "PD\n"
    "buffer_load_dwordx4 v24, s[44:47], s57 offen offset:0 lds\n"
    "v_exp_f32 v168, v168\n"
    "v_mfma_f32_16x16x32_bf16 v[196:199], a[128:131], v[80:83], v[196:199]\n"
    "PD\n"
    "ds_read_b128 a[156:159], v19 offset:53248\n"
    "v_mfma_f32_16x16x32_bf16 v[200:203], a[128:131], v[96:99], v[48:51]\n"
    "PD\n"
    "buffer_load_dwordx4 v25, s[44:47], s57 offen offset:1024 lds\n"
    "v_exp_f32 v169, v169\n"
    "v_mfma_f32_16x16x32_bf16 v[200:203], a[132:135], v[100:103], v[200:203]\n"
    "PD\n"
    "v_exp_f32 v170, v170\n"
    "v_mfma_f32_16x16x32_bf16 v[200:203], a[136:139], v[104:107], v[200:203]\n"
    "PD\n"
    "buffer_load_dwordx4 v26, s[44:47], s57 offen offset:2048 lds\n"
    "v_exp_f32 v171, v171\n"
    "v_mfma_f32_16x16x32_bf16 v[200:203], a[140:143], v[108:111], v[200:203]\n"
    "PD\n"
    "v_exp_f32 v172, v172\n"
    "v_mfma_f32_16x16x32_bf16 v[204:207], a[140:143], v[124:127], v[52:55]\n"
    "PD\n"
    "buffer_load_dwordx4 v27, s[44:47], s57 offen offset:3072 lds\n"
    "v_exp_f32 v173, v173\n"
    "v_mfma_f32_16x16x32_bf16 v[204:207], a[136:139], v[120:123], v[204:207]\n"
    "PD\n"
    "v_cvt_pk_bf16_f32 v168, v168, v169\n"
    "v_cvt_pk_bf16_f32 v169, v170, v171\n"
    "v_mfma_f32_16x16x32_bf16 v[204:207], a[132:135], v[116:119], v[204:207]\n"
    "PD\n"
    "s_add_u32 s57, s57, s59\n"
    "v_exp_f32 v174, v174\n"
    "v_mfma_f32_16x16x32_bf16 v[204:207], a[128:131], v[112:115], v[204:207]\n"
    "PD\n"
    "s_waitcnt lgkmcnt(0)\n"
    "v_exp_f32 v175, v175\n"
    "v_mfma_f32_16x16x32_bf16 v[220:223], a[144:147], v[112:115], v[52:55]\n"
    "PD\n"
    "global_load_dword v7, v1, s[70:71]\n"
    "v_exp_f32 v188, v188\n"
    "v_mfma_f32_16x16x32_bf16 v[220:223], a[148:151], v[116:119], v[220:223]\n"
    "PD\n"
    "ds_read_b128 a[128:131], v16 offset:57344\n"
    "v_cvt_pk_bf16_f32 v172, v172, v173\n"
    "v_mfma_f32_16x16x32_bf16 v[220:223], a[152:155], v[120:123], v[220:223]\n"
    "PD\n"
    "v_exp_f32 v189, v189\n"
    "v_mfma_f32_16x16x32_bf16 v[220:223], a[156:159], v[124:127], v[220:223]\n"
    "PD\n"
    "ds_read_b128 a[132:135], v17 offset:57344\n"
    "v_cvt_pk_bf16_f32 v173, v174, v175\n"
    "v_mfma_f32_16x16x32_bf16 v[216:219], a[156:159], v[108:111], v[48:51]\n"
    "PD\n"
    "v_exp_f32 v190, v190\n"
    "v_mfma_f32_16x16x32_bf16 v[216:219], a[152:155], v[104:107], v[216:219]\n"
    "PD\n"
    "ds_read_b128 a[136:139], v18 offset:57344\n"
    "v_cvt_pk_bf16_f32 v174, v188, v189\n"
    "v_mfma_f32_16x16x32_bf16 v[216:219], a[148:151], v[100:103], v[216:219]\n"
    "PD\n"
    "v_exp_f32 v191, v191\n"
    "v_mfma_f32_16x16x32_bf16 v[216:219], a[144:147], v[96:99], v[216:219]\n"
    "PD\n"
    "ds_read_b128 a[140:143], v19 offset:57344\n"
    "v_cvt_pk_bf16_f32 v175, v190, v191\n"
    "v_mfma_f32_16x16x32_bf16 v[212:215], a[144:147], v[80:83], v[44:47]\n"
    "PD\n"
    "v_exp_f32 v184, v184\n"
    "v_mfma_f32_16x16x32_bf16 v[212:215], a[148:151], v[84:87], v[212:215]\n"
    "PD\n"
    "v_exp_f32 v185, v185\n"
    "v_mfma_f32_16x16x32_bf16 v[212:215], a[152:155], v[88:91], v[212:215]\n"
    "PD\n"
    "v_exp_f32 v186, v186\n"
    "v_mfma_f32_16x16x32_bf16 v[212:215], a[156:159], v[92:95], v[212:215]\n"
    "PD\n"
    "v_exp_f32 v187, v187\n"
    "v_mfma_f32_16x16x32_bf16 v[208:211], a[156:159], v[76:79], v[40:43]\n"
    "PD\n"
    "v_exp_f32 v180, v180\n"
    "v_mfma_f32_16x16x32_bf16 v[208:211], a[152:155], v[72:75], v[208:211]\n"
    "PD\n"
    "v_exp_f32 v181, v181\n"
    "v_mfma_f32_16x16x32_bf16 v[208:211], a[148:151], v[68:71], v[208:211]\n"
    "PD\n"
    "v_cvt_pk_bf16_f32 v170, v184, v185\n"
    "v_cvt_pk_bf16_f32 v171, v186, v187\n"
    "v_mfma_f32_16x16x32_bf16 v[208:211], a[144:147], v[64:67], v[208:211]\n"
    "PD\n"
    "s_waitcnt lgkmcnt(0)\n"
    "v_exp_f32 v182, v182\n"
    "v_mfma_f32_16x16x32_bf16 v[224:227], a[128:131], v[64:67], v[40:43]\n"
    "PD\n"
    "v_exp_f32 v183, v183\n"
    "v_mfma_f32_16x16x32_bf16 v[224:227], a[132:135], v[68:71], v[224:227]\n"
    "PD\n"
    "ds_read_b128 a[144:147], v16 offset:61440\n"
    "v_xor_b32 v16, 0x10000, v16\n"
    "v_mfma_f32_16x16x32_bf16 v[224:227], a[136:139], v[72:75], v[224:227]\n"
    "PD\n"
    "v_exp_f32 v176, v176\n"
    "v_mfma_f32_16x16x32_bf16 v[224:227], a[140:143], v[76:79], v[224:227]\n"
    "PD\n"
    "ds_read_b128 a[148:151], v17 offset:61440\n"
    "v_xor_b32 v17, 0x10000, v17\n"
    "v_mfma_f32_16x16x32_bf16 v[228:231], a[140:143], v[92:95], v[44:47]\n"
    "PD\n"
    "v_exp_f32 v177, v177\n"
    "v_mfma_f32_16x16x32_bf16 v[228:231], a[136:139], v[88:91], v[228:231]\n"
    "PD\n"
    "ds_read_b128 a[152:155], v18 offset:61440\n"
    "v_xor_b32 v18, 0x10000, v18\n"
    "v_mfma_f32_16x16x32_bf16 v[228:231], a[132:135], v[84:87], v[228:231]\n"
    "PD\n"
    "v_cvt_pk_bf16_f32 v166, v180, v181\n"
    "v_cvt_pk_bf16_f32 v167, v182, v183\n"
    "v_mfma_f32_16x16x32_bf16 v[228:231], a[128:131], v[80:83], v[228:231]\n"
    "PD\n"
    "ds_read_b128 a[156:159], v19 offset:61440\n"
    "v_xor_b32 v19, 0x10000, v19\n"
    "v_mfma_f32_16x16x32_bf16 v[232:235], a[128:131], v[96:99], v[48:51]\n"
    "PD\n"
    "v_exp_f32 v178, v178\n"
    "v_mfma_f32_16x16x32_bf16 v[232:235], a[132:135], v[100:103], v[232:235]\n"
    "PD\n"
    "v_exp_f32 v179, v179\n"
    "v_mfma_f32_16x16x32_bf16 v[232:235], a[136:139], v[104:107], v[232:235]\n"
    "PD\n"
    "v_exp_f32 v192, v192\n"
    "v_mfma_f32_16x16x32_bf16 v[232:235], a[140:143], v[108:111], v[232:235]\n"
    "PD\n"
    "v_exp_f32 v193, v193\n"
    "v_mfma_f32_16x16x32_bf16 v[236:239], a[140:143], v[124:127], v[52:55]\n"
    "PD\n"
    "v_cvt_pk_bf16_f32 v162, v176, v177\n"
    "v_cvt_pk_bf16_f32 v163, v178, v179\n"
    "v_mfma_f32_16x16x32_bf16 v[236:239], a[136:139], v[120:123], v[236:239]\n"
    "PD\n"
    "v_exp_f32 v194, v194\n"
    "v_mfma_f32_16x16x32_bf16 v[236:239], a[132:135], v[116:119], v[236:239]\n"
    "PD\n"
    "ds_read_b64_tr_b16 a[224:225], v8 offset:0\n"
    "v_cvt_pk_bf16_f32 v192, v192, v193\n"
    "v_mfma_f32_16x16x32_bf16 v[236:239], a[128:131], v[112:115], v[236:239]\n"
    "PD\n"
    "s_waitcnt lgkmcnt(1)\n"
    "v_exp_f32 v195, v195\n"
    "v_mfma_f32_16x16x32_bf16 v[252:255], a[144:147], v[112:115], v[52:55]\n"
    "PD\n"
    "ds_read_b64_tr_b16 a[226:227], v8 offset:4096\n"
    "v_cvt_pk_bf16_f32 v193, v194, v195\n"
    "v_mfma_f32_16x16x32_bf16 v[252:255], a[148:151], v[116:119], v[252:255]\n"
    "PD\n"
    "v_exp_f32 v196, v196\n"
    "v_mfma_f32_16x16x32_bf16 v[252:255], a[152:155], v[120:123], v[252:255]\n"
    "PD\n"
    "ds_read_b64_tr_b16 a[228:229], v8 offset:8192\n"
    "v_mfma_f32_16x16x32_bf16 v[252:255], a[156:159], v[124:127], v[252:255]\n"
    "PD\n"
    "v_exp_f32 v197, v197\n"
    "v_mfma_f32_16x16x32_bf16 v[248:251], a[156:159], v[108:111], v[48:51]\n"
    "PD\n"
    "ds_read_b64_tr_b16 a[230:231], v8 offset:12288\n"
    "v_cvt_pk_bf16_f32 v196, v196, v197\n"
    "v_mfma_f32_16x16x32_bf16 v[248:251], a[152:155], v[104:107], v[248:251]\n"
    "PD\n"
    "v_exp_f32 v198, v198\n"
    "v_mfma_f32_16x16x32_bf16 v[248:251], a[148:151], v[100:103], v[248:251]\n"
    "PD\n"
    "ds_read_b64_tr_b16 a[232:233], v9 offset:0\n"
    "v_mfma_f32_16x16x32_bf16 v[248:251], a[144:147], v[96:99], v[248:251]\n"
    "PD\n"
    "v_exp_f32 v199, v199\n"
    "v_mfma_f32_16x16x32_bf16 v[244:247], a[144:147], v[80:83], v[44:47]\n"
    "PD\n"
    "ds_read_b64_tr_b16 a[234:235], v9 offset:4096\n"
    "v_cvt_pk_bf16_f32 v197, v198, v199\n"
    "v_mfma_f32_16x16x32_bf16 v[244:247], a[148:151], v[84:87], v[244:247]\n"
    "PD\n"
    "v_exp_f32 v200, v200\n"
    "v_mfma_f32_16x16x32_bf16 v[244:247], a[152:155], v[88:91], v[244:247]\n"
    "PD\n"
    "ds_read_b64_tr_b16 a[236:237], v9 offset:8192\n"
    "v_mfma_f32_16x16x32_bf16 v[244:247], a[156:159], v[92:95], v[244:247]\n"
    "PD\n"
    "v_exp_f32 v201, v201\n"
    "v_mfma_f32_16x16x32_bf16 v[240:243], a[156:159], v[76:79], v[40:43]\n"
    "PD\n"
    "ds_read_b64_tr_b16 a[238:239], v9 offset:12288\n"
    "v_cvt_pk_bf16_f32 v200, v200, v201\n"
    "v_mfma_f32_16x16x32_bf16 v[240:243], a[152:155], v[72:75], v[240:243]\n"
    "PD\n"
    "v_exp_f32 v202, v202\n"
    "v_mfma_f32_16x16x32_bf16 v[240:243], a[148:151], v[68:71], v[240:243]\n"
    "PD\n"
    "v_exp_f32 v203, v203\n"
    "v_mfma_f32_16x16x32_bf16 v[240:243], a[144:147], v[64:67], v[240:243]\n"
    "PD\n"
    "s_waitcnt lgkmcnt(4)\n"
    "v_exp_f32 v204, v204\n"
    "v_mfma_f32_16x16x32_bf16 a[0:3], a[224:227], v[128:131], 0\n"
    "PD\n"
    "ds_read_b64_tr_b16 a[240:241], v10 offset:0\n"
    "v_cvt_pk_bf16_f32 v201, v202, v203\n"
    "v_mfma_f32_16x16x32_bf16 a[0:3], a[228:231], v[160:163], a[0:3]\n"
    "PD\n"
    "v_exp_f32 v205, v205\n"
    "v_mfma_f32_16x16x32_bf16 a[4:7], a[228:231], v[164:167], 0\n"
    "PD\n"
    "ds_read_b64_tr_b16 a[242:243], v10 offset:4096\n"
    "v_cvt_pk_bf16_f32 v204, v204, v205\n"
    "v_mfma_f32_16x16x32_bf16 a[4:7], a[224:227], v[132:135], a[4:7]\n"
    "PD\n"
    "v_exp_f32 v206, v206\n"
    "v_mfma_f32_16x16x32_bf16 a[8:11], a[224:227], v[136:139], 0\n"
    "PD\n"
    "ds_read_b64_tr_b16 a[244:245], v10 offset:8192\n"
    "v_mfma_f32_16x16x32_bf16 a[8:11], a[228:231], v[168:171], a[8:11]\n"
    "PD\n"
    "v_exp_f32 v207, v207\n"
    "v_mfma_f32_16x16x32_bf16 a[12:15], a[228:231], v[172:175], 0\n"
    "PD\n"
    "ds_read_b64_tr_b16 a[246:247], v10 offset:12288\n"
    "v_cvt_pk_bf16_f32 v205, v206, v207\n"
    "v_mfma_f32_16x16x32_bf16 a[12:15], a[224:227], v[140:143], a[12:15]\n"
    "PD\n"
    "s_waitcnt lgkmcnt(4)\n"
    "v_exp_f32 v220, v220\n"
    "v_mfma_f32_16x16x32_bf16 a[28:31], a[232:235], v[140:143], 0\n"
    "PD\n"
    "ds_read_b64_tr_b16 a[248:249], v11 offset:0\n"
    "v_mfma_f32_16x16x32_bf16 a[28:31], a[236:239], v[172:175], a[28:31]\n"
    "PD\n"
    "v_exp_f32 v221, v221\n"
    "v_mfma_f32_16x16x32_bf16 a[24:27], a[236:239], v[168:171], 0\n"
    "PD\n"
    "ds_read_b64_tr_b16 a[250:251], v11 offset:4096\n"
    "v_cvt_pk_bf16_f32 v206, v220, v221\n"
    "v_mfma_f32_16x16x32_bf16 a[24:27], a[232:235], v[136:139], a[24:27]\n"
    "PD\n"
    "v_exp_f32 v222, v222\n"
    "v_mfma_f32_16x16x32_bf16 a[20:23], a[232:235], v[132:135], 0\n"
    "PD\n"
    "ds_read_b64_tr_b16 a[252:253], v11 offset:8192\n"
    "v_mfma_f32_16x16x32_bf16 a[20:23], a[236:239], v[164:167], a[20:23]\n"
    "PD\n"
    "v_exp_f32 v223, v223\n"
    "v_mfma_f32_16x16x32_bf16 a[16:19], a[236:239], v[160:163], 0\n"
    "PD\n"
    "ds_read_b64_tr_b16 a[254:255], v11 offset:12288\n"
    "v_cvt_pk_bf16_f32 v207, v222, v223\n"
    "v_mfma_f32_16x16x32_bf16 a[16:19], a[232:235], v[128:131], a[16:19]\n"
    "PD\n"
    "s_waitcnt lgkmcnt(4)\n"
    "v_exp_f32 v216, v216\n"
    "v_mfma_f32_16x16x32_bf16 a[32:35], a[240:243], v[128:131], 0\n"
    "PD\n"
    "ds_read_b64_tr_b16 a[224:225], v12 offset:0\n"
    "v_mfma_f32_16x16x32_bf16 a[32:35], a[244:247], v[160:163], a[32:35]\n"
    "PD\n"
    "v_exp_f32 v217, v217\n"
    "v_mfma_f32_16x16x32_bf16 a[36:39], a[244:247], v[164:167], 0\n"
    "PD\n"
    "ds_read_b64_tr_b16 a[226:227], v12 offset:4096\n"
    "v_cvt_pk_bf16_f32 v202, v216, v217\n"
    "v_mfma_f32_16x16x32_bf16 a[36:39], a[240:243], v[132:135], a[36:39]\n"
    "PD\n"
    "v_exp_f32 v218, v218\n"
    "v_mfma_f32_16x16x32_bf16 a[40:43], a[240:243], v[136:139], 0\n"
    "PD\n"
    "ds_read_b64_tr_b16 a[228:229], v12 offset:8192\n"
    "v_mfma_f32_16x16x32_bf16 a[40:43], a[244:247], v[168:171], a[40:43]\n"
    "PD\n"
    "v_exp_f32 v219, v219\n"
    "v_mfma_f32_16x16x32_bf16 a[44:47], a[244:247], v[172:175], 0\n"
    "PD\n"
    "ds_read_b64_tr_b16 a[230:231], v12 offset:12288\n"
    "v_cvt_pk_bf16_f32 v203, v218, v219\n"
    "v_mfma_f32_16x16x32_bf16 a[44:47], a[240:243], v[140:143], a[44:47]\n"
    "PD\n"
    "s_waitcnt lgkmcnt(4)\n"
    "v_exp_f32 v212, v212\n"
    "v_mfma_f32_16x16x32_bf16 a[60:63], a[248:251], v[140:143], 0\n"
    "PD\n"
    "ds_read_b64_tr_b16 a[232:233], v13 offset:0\n"
    "v_mfma_f32_16x16x32_bf16 a[60:63], a[252:255], v[172:175], a[60:63]\n"
    "PD\n"
    "v_exp_f32 v213, v213\n"
    "v_mfma_f32_16x16x32_bf16 a[56:59], a[252:255], v[168:171], 0\n"
    "PD\n"
    "ds_read_b64_tr_b16 a[234:235], v13 offset:4096\n"
    "v_cvt_pk_bf16_f32 v198, v212, v213\n"
    "v_mfma_f32_16x16x32_bf16 a[56:59], a[248:251], v[136:139], a[56:59]\n"
    "PD\n"
    "v_exp_f32 v214, v214\n"
    "v_mfma_f32_16x16x32_bf16 a[52:55], a[248:251], v[132:135], 0\n"
    "PD\n"
    "ds_read_b64_tr_b16 a[236:237], v13 offset:8192\n"
    "v_mfma_f32_16x16x32_bf16 a[52:55], a[252:255], v[164:167], a[52:55]\n"
    "PD\n"
    "v_exp_f32 v215, v215\n"
    "v_mfma_f32_16x16x32_bf16 a[48:51], a[252:255], v[160:163], 0\n"
    "PD\n"
    "ds_read_b64_tr_b16 a[238:239], v13 offset:12288\n"
    "v_cvt_pk_bf16_f32 v199, v214, v215\n"
    "v_mfma_f32_16x16x32_bf16 a[48:51], a[248:251], v[128:131], a[48:51]\n"
    "PD\n"
    "s_waitcnt lgkmcnt(4)\n"
    "v_exp_f32 v208, v208\n"
    "v_mfma_f32_16x16x32_bf16 a[64:67], a[224:227], v[128:131], 0\n"
    "PD\n"
    "ds_read_b64_tr_b16 a[240:241], v14 offset:0\n"
    "v_mfma_f32_16x16x32_bf16 a[64:67], a[228:231], v[160:163], a[64:67]\n"
    "PD\n"
    "v_exp_f32 v209, v209\n"
    "v_mfma_f32_16x16x32_bf16 a[68:71], a[228:231], v[164:167], 0\n"
    "PD\n"
    "ds_read_b64_tr_b16 a[242:243], v14 offset:4096\n"
    "v_cvt_pk_bf16_f32 v194, v208, v209\n"
    "v_mfma_f32_16x16x32_bf16 a[68:71], a[224:227], v[132:135], a[68:71]\n"
    "PD\n"
    "v_exp_f32 v210, v210\n"
    "v_mfma_f32_16x16x32_bf16 a[72:75], a[224:227], v[136:139], 0\n"
    "PD\n"
    "ds_read_b64_tr_b16 a[244:245], v14 offset:8192\n"
    "v_mfma_f32_16x16x32_bf16 a[72:75], a[228:231], v[168:171], a[72:75]\n"
    "PD\n"
    "v_exp_f32 v211, v211\n"
    "v_mfma_f32_16x16x32_bf16 a[76:79], a[228:231], v[172:175], 0\n"
    "PD\n"
    "ds_read_b64_tr_b16 a[246:247], v14 offset:12288\n"
    "v_cvt_pk_bf16_f32 v195, v210, v211\n"
    "v_mfma_f32_16x16x32_bf16 a[76:79], a[224:227], v[140:143], a[76:79]\n"
    "PD\n"
    "s_waitcnt lgkmcnt(4)\n"
    "v_exp_f32 v224, v224\n"
    "v_mfma_f32_16x16x32_bf16 a[92:95], a[232:235], v[140:143], 0\n"
    "PD\n"
    "ds_read_b64_tr_b16 a[248:249], v15 offset:0\n"
    "v_mfma_f32_16x16x32_bf16 a[92:95], a[236:239], v[172:175], a[92:95]\n"
    "PD\n"
    "v_exp_f32 v225, v225\n"
    "v_mfma_f32_16x16x32_bf16 a[88:91], a[236:239], v[168:171], 0\n"
    "PD\n"
    "ds_read_b64_tr_b16 a[250:251], v15 offset:4096\n"
    "v_cvt_pk_bf16_f32 v224, v224, v225\n"
    "v_mfma_f32_16x16x32_bf16 a[88:91], a[232:235], v[136:139], a[88:91]\n"
    "PD\n"
    "v_exp_f32 v226, v226\n"
    "v_mfma_f32_16x16x32_bf16 a[84:87], a[232:235], v[132:135], 0\n"
    "PD\n"
    "ds_read_b64_tr_b16 a[252:253], v15 offset:8192\n"
    "v_mfma_f32_16x16x32_bf16 a[84:87], a[236:239], v[164:167], a[84:87]\n"
    "PD\n"
    "v_exp_f32 v227, v227\n"
    "v_mfma_f32_16x16x32_bf16 a[80:83], a[236:239], v[160:163], 0\n"
    "PD\n"
    "ds_read_b64_tr_b16 a[254:255], v15 offset:12288\n"
    "v_cvt_pk_bf16_f32 v225, v226, v227\n"
    "v_mfma_f32_16x16x32_bf16 a[80:83], a[232:235], v[128:131], a[80:83]\n"
    "PD\n"
    "s_waitcnt lgkmcnt(4)\n"
    "v_exp_f32 v228, v228\n"
    "v_mfma_f32_16x16x32_bf16 a[96:99], a[240:243], v[128:131], 0\n"
    "PD\n"
    "v_exp_f32 v229, v229\n"
    "v_mfma_f32_16x16x32_bf16 a[96:99], a[244:247], v[160:163], a[96:99]\n"
    "PD\n"
    "v_exp_f32 v230, v230\n"
    "v_mfma_f32_16x16x32_bf16 a[100:103], a[244:247], v[164:167], 0\n"
    "PD\n"
    "v_exp_f32 v231, v231\n"
    "v_mfma_f32_16x16x32_bf16 a[100:103], a[240:243], v[132:135], a[100:103]\n"
    "PD\n"
    "v_exp_f32 v232, v232\n"
    "v_mfma_f32_16x16x32_bf16 a[104:107], a[240:243], v[136:139], 0\n"
    "PD\n"
    "v_exp_f32 v233, v233\n"
    "v_mfma_f32_16x16x32_bf16 a[104:107], a[244:247], v[168:171], a[104:107]\n"
    "PD\n"
    "v_cvt_pk_bf16_f32 v228, v228, v229\n"
    "v_cvt_pk_bf16_f32 v229, v230, v231\n"
    "v_mfma_f32_16x16x32_bf16 a[108:111], a[244:247], v[172:175], 0\n"
    "PD\n"
    "v_exp_f32 v234, v234\n"
    "v_mfma_f32_16x16x32_bf16 a[108:111], a[240:243], v[140:143], a[108:111]\n"
    "PD\n"
    "s_waitcnt lgkmcnt(0)\n"
    "s_waitcnt vmcnt(9)\n"
    "s_barrier\n"
    "v_cvt_pk_bf16_f32 v232, v232, v233\n"
    "v_mfma_f32_16x16x32_bf16 a[124:127], a[248:251], v[140:143], 0\n"
    "PD\n"
    "v_exp_f32 v235, v235\n"
    "v_mfma_f32_16x16x32_bf16 a[124:127], a[252:255], v[172:175], a[124:127]\n"
    "PD\n"
    "ds_read_b128 a[128:131], v16 offset:16384\n"
    "v_cvt_pk_bf16_f32 v233, v234, v235\n"
    "v_mfma_f32_16x16x32_bf16 a[120:123], a[252:255], v[168:171], 0\n"
    "PD\n"
    "s_add_u32 m0, s61, 16384\n"
    "v_exp_f32 v236, v236\n"
    "v_mfma_f32_16x16x32_bf16 a[120:123], a[248:251], v[136:139], a[120:123]\n"
    "PD\n"
    "ds_read_b128 a[132:135], v17 offset:16384\n"
    "v_mfma_f32_16x16x32_bf16 a[116:119], a[248:251], v[132:135], 0\n"
    "PD\n"
    "buffer_load_dwordx4 v28, s[48:51], s58 offen offset:0 lds\n"
    "v_exp_f32 v237, v237\n"
    "v_mfma_f32_16x16x32_bf16 a[116:119], a[252:255], v[164:167], a[116:119]\n"
    "PD\n"
    "ds_read_b128 a[136:139], v18 offset:16384\n"
    "v_cvt_pk_bf16_f32 v236, v236, v237\n"
    "v_mfma_f32_16x16x32_bf16 a[112:115], a[252:255], v[160:163], 0\n"
    "PD\n"
    "buffer_load_dwordx4 v29, s[48:51], s58 offen offset:1024 lds\n"
    "v_exp_f32 v238, v238\n"
    "v_mfma_f32_16x16x32_bf16 a[112:115], a[248:251], v[128:131], a[112:115]\n"
    "PD\n"
    "ds_read_b128 a[140:143], v19 offset:16384\n"
    "v_mfma_f32_16x16x32_bf16 a[192:195], a[208:211], v[128:131], 0\n"
    "PD\n"
    "buffer_load_dwordx4 v30, s[48:51], s58 offen offset:2048 lds\n"
    "v_exp_f32 v239, v239\n"
    "v_mfma_f32_16x16x32_bf16 a[192:195], a[208:211], v[160:163], a[192:195]\n"
    "PD\n"
    "v_exp_f32 v252, v252\n"
    "v_mfma_f32_16x16x32_bf16 a[196:199], a[208:211], v[132:135], 0\n"
    "PD\n"
    "buffer_load_dwordx4 v31, s[48:51], s58 offen offset:3072 lds\n"
    "v_exp_f32 v253, v253\n"
    "v_mfma_f32_16x16x32_bf16 a[196:199], a[208:211], v[164:167], a[196:199]\n"
    "PD\n"
    "v_exp_f32 v254, v254\n"
    "v_mfma_f32_16x16x32_bf16 a[200:203], a[208:211], v[136:139], 0\n"
    "PD\n"
    "s_add_u32 s58, s58, s59\n"
    "v_exp_f32 v255, v255\n"
    "v_mfma_f32_16x16x32_bf16 a[200:203], a[208:211], v[168:171], a[200:203]\n"
    "PD\n"
    "s_add_u32 m0, s61, 0\n"
    "v_cvt_pk_bf16_f32 v237, v238, v239\n"
    "v_cvt_pk_bf16_f32 v238, v252, v253\n"
    "v_mfma_f32_16x16x32_bf16 a[204:207], a[208:211], v[140:143], 0\n"
    "PD\n"
    "buffer_load_dwordx4 v24, s[44:47], s57 offen offset:0 lds\n"
    "v_exp_f32 v248, v248\n"
    "v_mfma_f32_16x16x32_bf16 a[204:207], a[208:211], v[172:175], a[204:207]\n"
    "PD\n"
    "s_waitcnt lgkmcnt(0)\n"
    "v_exp_f32 v249, v249\n"
    "v_mfma_f32_16x16x32_bf16 v[128:131], a[128:131], v[64:67], v[40:43]\n"
    "PD\n"
    "buffer_load_dwordx4 v25, s[44:47], s57 offen offset:1024 lds\n"
    "v_exp_f32 v250, v250\n"
    "v_mfma_f32_16x16x32_bf16 v[128:131], a[132:135], v[68:71], v[128:131]\n"
    "PD\n"
    "ds_read_b128 a[144:147], v16 offset:20480\n"
    "v_cvt_pk_bf16_f32 v239, v254, v255\n"
    "v_mfma_f32_16x16x32_bf16 v[128:131], a[136:139], v[72:75], v[128:131]\n"
    "PD\n"
    "buffer_load_dwordx4 v26, s[44:47], s57 offen offset:2048 lds\n"
    "v_exp_f32 v251, v251\n"
    "v_mfma_f32_16x16x32_bf16 v[128:131], a[140:143], v[76:79], v[128:131]\n"
    "PD\n"
    "ds_read_b128 a[148:151], v17 offset:20480\n"
    "v_cvt_pk_bf16_f32 v234, v248, v249\n"
    "v_mfma_f32_16x16x32_bf16 v[132:135], a[140:143], v[92:95], v[44:47]\n"
    "PD\n"
    "buffer_load_dwordx4 v27, s[44:47], s57 offen offset:3072 lds\n"
    "v_exp_f32 v244, v244\n"
    "v_mfma_f32_16x16x32_bf16 v[132:135], a[136:139], v[88:91], v[132:135]\n"
    "PD\n"
    "ds_read_b128 a[152:155], v18 offset:20480\n"
    "v_cvt_pk_bf16_f32 v235, v250, v251\n"
    "v_mfma_f32_16x16x32_bf16 v[132:135], a[132:135], v[84:87], v[132:135]\n"
    "PD\n"
    "s_add_u32 s57, s57, s59\n"
    "v_exp_f32 v245, v245\n"
    "v_mfma_f32_16x16x32_bf16 v[132:135], a[128:131], v[80:83], v[132:135]\n"
    "PD\n"
    "ds_read_b128 a[156:159], v19 offset:20480\n"
    "v_add_u32 v60, s98, v4\n"
    "v_mfma_f32_16x16x32_bf16 v[136:139], a[128:131], v[96:99], v[48:51]\n"
    "PD\n"
    "global_store_dwordx4 v60, a[212:215], s[88:89] offset:0 sc1\n"
    "v_exp_f32 v246, v246\n"
    "v_mfma_f32_16x16x32_bf16 v[136:139], a[132:135], v[100:103], v[136:139]\n"
    "PD\n"
    "v_exp_f32 v247, v247\n"
    "v_mfma_f32_16x16x32_bf16 v[136:139], a[136:139], v[104:107], v[136:139]\n"
    "PD\n"
    "v_exp_f32 v240, v240\n"
    "v_mfma_f32_16x16x32_bf16 v[136:139], a[140:143], v[108:111], v[136:139]\n"
    "PD\n"
    "v_exp_f32 v241, v241\n"
    "v_mfma_f32_16x16x32_bf16 v[140:143], a[140:143], v[124:127], v[52:55]\n"
    "PD\n"
    "v_cvt_pk_bf16_f32 v230, v244, v245\n"
    "v_cvt_pk_bf16_f32 v231, v246, v247\n"
    "v_mfma_f32_16x16x32_bf16 v[140:143], a[136:139], v[120:123], v[140:143]\n"
    "PD\n"
    "v_exp_f32 v242, v242\n"
    "v_mfma_f32_16x16x32_bf16 v[140:143], a[132:135], v[116:119], v[140:143]\n"
    "PD\n"
    "v_exp_f32 v243, v243\n"
    "v_mfma_f32_16x16x32_bf16 v[140:143], a[128:131], v[112:115], v[140:143]\n"
    "PD\n"
    "s_waitcnt lgkmcnt(0)\n"
    "v_exp_f32 v128, v128\n"
    "v_mfma_f32_16x16x32_bf16 v[156:159], a[144:147], v[112:115], v[52:55]\n"
    "PD\n"
    "v_exp_f32 v129, v129\n"
    "v_mfma_f32_16x16x32_bf16 v[156:159], a[148:151], v[116:119], v[156:159]\n"
    "PD\n"
    "ds_read_b128 a[128:131], v16 offset:24576\n"
    "v_cvt_pk_bf16_f32 v226, v240, v241\n"
    "v_mfma_f32_16x16x32_bf16 v[156:159], a[152:155], v[120:123], v[156:159]\n"
    "PD\n"
    "global_store_dwordx4 v60, a[212:215], s[88:89] offset:1024 sc1\n"
    "v_exp_f32 v130, v130\n"
    "v_mfma_f32_16x16x32_bf16 v[156:159], a[156:159], v[124:127], v[156:159]\n"
    "PD\n"
    "ds_read_b128 a[132:135], v17 offset:24576\n"
    "v_cvt_pk_bf16_f32 v227, v242, v243\n"
    "v_mfma_f32_16x16x32_bf16 v[152:155], a[156:159], v[108:111], v[48:51]\n"
    "PD\n"
    "v_exp_f32 v131, v131\n"
    "v_mfma_f32_16x16x32_bf16 v[152:155], a[152:155], v[104:107], v[152:155]\n"
    "PD\n"
    "ds_read_b128 a[136:139], v18 offset:24576\n"
    "v_cvt_pk_bf16_f32 v128, v128, v129\n"
    "v_mfma_f32_16x16x32_bf16 v[152:155], a[148:151], v[100:103], v[152:155]\n"
    "PD\n"
    "v_exp_f32 v132, v132\n"
    "v_mfma_f32_16x16x32_bf16 v[152:155], a[144:147], v[96:99], v[152:155]\n"
    "PD\n"
    "ds_read_b128 a[140:143], v19 offset:24576\n"
    "v_cvt_pk_bf16_f32 v129, v130, v131\n"
    "v_mfma_f32_16x16x32_bf16 v[148:151], a[144:147], v[80:83], v[44:47]\n"
    "PD\n"
    "v_exp_f32 v133, v133\n"
    "v_mfma_f32_16x16x32_bf16 v[148:151], a[148:151], v[84:87], v[148:151]\n"
    "PD\n"
    "v_exp_f32 v134, v134\n"
    "v_mfma_f32_16x16x32_bf16 v[148:151], a[152:155], v[88:91], v[148:151]\n"
    "PD\n"
    "v_exp_f32 v135, v135\n"
    "v_mfma_f32_16x16x32_bf16 v[148:151], a[156:159], v[92:95], v[148:151]\n"
    "PD\n"
    "v_exp_f32 v136, v136\n"
    "v_mfma_f32_16x16x32_bf16 v[144:147], a[156:159], v[76:79], v[40:43]\n"
    "PD\n"
    "global_store_dwordx4 v60, a[212:215], s[88:89] offset:2048 sc1\n"
    "v_exp_f32 v137, v137\n"
    "v_mfma_f32_16x16x32_bf16 v[144:147], a[152:155], v[72:75], v[144:147]\n"
    "PD\n"
    "v_cvt_pk_bf16_f32 v132, v132, v133\n"
    "v_cvt_pk_bf16_f32 v133, v134, v135\n"
    "v_mfma_f32_16x16x32_bf16 v[144:147], a[148:151], v[68:71], v[144:147]\n"
    "PD\n"
    "v_exp_f32 v138, v138\n"
    "v_mfma_f32_16x16x32_bf16 v[144:147], a[144:147], v[64:67], v[144:147]\n"
    "PD\n"
    "s_waitcnt lgkmcnt(0)\n"
    "v_exp_f32 v139, v139\n"
    "v_mfma_f32_16x16x32_bf16 v[160:163], a[128:131], v[64:67], v[40:43]\n"
    "PD\n"
    "v_exp_f32 v140, v140\n"
    "v_mfma_f32_16x16x32_bf16 v[160:163], a[132:135], v[68:71], v[160:163]\n"
    "PD\n"
    "ds_read_b128 a[144:147], v16 offset:28672\n"
    "v_cvt_pk_bf16_f32 v136, v136, v137\n"
    "v_mfma_f32_16x16x32_bf16 v[160:163], a[136:139], v[72:75], v[160:163]\n"
    "PD\n"
    "v_exp_f32 v141, v141\n"
    "v_mfma_f32_16x16x32_bf16 v[160:163], a[140:143], v[76:79], v[160:163]\n"
    "PD\n"
    "ds_read_b128 a[148:151], v17 offset:28672\n"
    "v_cvt_pk_bf16_f32 v137, v138, v139\n"
    "v_mfma_f32_16x16x32_bf16 v[164:167], a[140:143], v[92:95], v[44:47]\n"
    "PD\n"
    "v_exp_f32 v142, v142\n"
    "v_mfma_f32_16x16x32_bf16 v[164:167], a[136:139], v[88:91], v[164:167]\n"
    "PD\n"
    "ds_read_b128 a[152:155], v18 offset:28672\n"
    "v_cvt_pk_bf16_f32 v140, v140, v141\n"
    "v_mfma_f32_16x16x32_bf16 v[164:167], a[132:135], v[84:87], v[164:167]\n"
    "PD\n"
    "global_store_dwordx4 v60, a[212:215], s[88:89] offset:3072 sc1\n"
    "v_exp_f32 v143, v143\n"
    "v_mfma_f32_16x16x32_bf16 v[164:167], a[128:131], v[80:83], v[164:167]\n"
    "PD\n"
    "ds_read_b128 a[156:159], v19 offset:28672\n"
    "v_cvt_pk_bf16_f32 v141, v142, v143\n"
    "v_mfma_f32_16x16x32_bf16 v[168:171], a[128:131], v[96:99], v[48:51]\n"
    "PD\n"
    "v_exp_f32 v156, v156\n"
    "v_mfma_f32_16x16x32_bf16 v[168:171], a[132:135], v[100:103], v[168:171]\n"
    "PD\n"
    "v_exp_f32 v157, v157\n"
    "v_mfma_f32_16x16x32_bf16 v[168:171], a[136:139], v[104:107], v[168:171]\n"
    "PD\n"
    "v_exp_f32 v158, v158\n"
    "v_mfma_f32_16x16x32_bf16 v[168:171], a[140:143], v[108:111], v[168:171]\n"
    "PD\n"
    "v_exp_f32 v159, v159\n"
    "v_mfma_f32_16x16x32_bf16 v[172:175], a[140:143], v[124:127], v[52:55]\n"
    "PD\n"
    "v_exp_f32 v152, v152\n"
    "v_mfma_f32_16x16x32_bf16 v[172:175], a[136:139], v[120:123], v[172:175]\n"
    "PD\n"
    "v_exp_f32 v153, v153\n"
    "v_mfma_f32_16x16x32_bf16 v[172:175], a[132:135], v[116:119], v[172:175]\n"
    "PD\n"
    "ds_read_b64_tr_b16 a[224:225], v8 offset:32768\n"
    "v_cvt_pk_bf16_f32 v142, v156, v157\n"
    "v_mfma_f32_16x16x32_bf16 v[172:175], a[128:131], v[112:115], v[172:175]\n"
    "PD\n"
    "s_waitcnt lgkmcnt(1)\n"
    "v_exp_f32 v154, v154\n"
    "v_mfma_f32_16x16x32_bf16 v[188:191], a[144:147], v[112:115], v[52:55]\n"
    "PD\n"
    "ds_read_b64_tr_b16 a[226:227], v8 offset:36864\n"
    "v_cvt_pk_bf16_f32 v143, v158, v159\n"
    "v_mfma_f32_16x16x32_bf16 v[188:191], a[148:151], v[116:119], v[188:191]\n"
    "PD\n"
    "v_exp_f32 v155, v155\n"
    "v_mfma_f32_16x16x32_bf16 v[188:191], a[152:155], v[120:123], v[188:191]\n"
    "PD\n"
    "ds_read_b64_tr_b16 a[228:229], v8 offset:40960\n"
    "v_cvt_pk_bf16_f32 v138, v152, v153\n"
    "v_mfma_f32_16x16x32_bf16 v[188:191], a[156:159], v[124:127], v[188:191]\n"
    "PD\n"
    "v_exp_f32 v148, v148\n"
    "v_mfma_f32_16x16x32_bf16 v[184:187], a[156:159], v[108:111], v[48:51]\n"
    "PD\n"
    "ds_read_b64_tr_b16 a[230:231], v8 offset:45056\n"
    "v_xor_b32 v8, 0x10000, v8\n"
    "v_mfma_f32_16x16x32_bf16 v[184:187], a[152:155], v[104:107], v[184:187]\n"
    "PD\n"
    "v_exp_f32 v149, v149\n"
    "v_mfma_f32_16x16x32_bf16 v[184:187], a[148:151], v[100:103], v[184:187]\n"
    "PD\n"
    "ds_read_b64_tr_b16 a[232:233], v9 offset:32768\n"
    "v_cvt_pk_bf16_f32 v139, v154, v155\n"
    "v_mfma_f32_16x16x32_bf16 v[184:187], a[144:147], v[96:99], v[184:187]\n"
    "PD\n"
    "v_exp_f32 v150, v150\n"
    "v_mfma_f32_16x16x32_bf16 v[180:183], a[144:147], v[80:83], v[44:47]\n"
    "PD\n"
    "ds_read_b64_tr_b16 a[234:235], v9 offset:36864\n"
    "v_cvt_pk_bf16_f32 v134, v148, v149\n"
    "v_mfma_f32_16x16x32_bf16 v[180:183], a[148:151], v[84:87], v[180:183]\n"
    "PD\n"
    "v_exp_f32 v151, v151\n"
    "v_mfma_f32_16x16x32_bf16 v[180:183], a[152:155], v[88:91], v[180:183]\n"
    "PD\n"
    "ds_read_b64_tr_b16 a[236:237], v9 offset:40960\n"
    "v_cvt_pk_bf16_f32 v135, v150, v151\n"
    "v_mfma_f32_16x16x32_bf16 v[180:183], a[156:159], v[92:95], v[180:183]\n"
    "PD\n"
    "v_exp_f32 v144, v144\n"
    "v_mfma_f32_16x16x32_bf16 v[176:179], a[156:159], v[76:79], v[40:43]\n"
    "PD\n"
    "ds_read_b64_tr_b16 a[238:239], v9 offset:45056\n"
    "v_xor_b32 v9, 0x10000, v9\n"
    "v_mfma_f32_16x16x32_bf16 v[176:179], a[152:155], v[72:75], v[176:179]\n"
    "PD\n"
    "v_exp_f32 v145, v145\n"
    "v_mfma_f32_16x16x32_bf16 v[176:179], a[148:151], v[68:71], v[176:179]\n"
    "PD\n"
    "v_exp_f32 v146, v146\n"
    "v_mfma_f32_16x16x32_bf16 v[176:179], a[144:147], v[64:67], v[176:179]\n"
    "PD\n"
    "s_waitcnt lgkmcnt(4)\n"
    "v_exp_f32 v147, v147\n"
    "v_mfma_f32_16x16x32_bf16 a[0:3], a[224:227], v[192:195], a[0:3]\n"
    "PD\n"
    "ds_read_b64_tr_b16 a[240:241], v10 offset:32768\n"
    "v_cvt_pk_bf16_f32 v130, v144, v145\n"
    "v_mfma_f32_16x16x32_bf16 a[0:3], a[228:231], v[224:227], a[0:3]\n"
    "PD\n"
    "s_waitcnt vmcnt(16)\n"
    "buffer_store_dwordx2 v[34:35], off, s[80:83], s84 sc1\n"
    "v_cvt_pk_bf16_f32 v131, v146, v147\n"
    "v_mfma_f32_16x16x32_bf16 a[4:7], a[228:231], v[228:231], a[4:7]\n"
    "PD\n"
    "ds_read_b64_tr_b16 a[242:243], v10 offset:36864\n"
    "v_mfma_f32_16x16x32_bf16 a[4:7], a[224:227], v[196:199], a[4:7]\n"
    "PD\n"
    "v_exp_f32 v160, v160\n"
    "v_mfma_f32_16x16x32_bf16 a[8:11], a[224:227], v[200:203], a[8:11]\n"
    "PD\n"
    "ds_read_b64_tr_b16 a[244:245], v10 offset:40960\n"
    "v_mfma_f32_16x16x32_bf16 a[8:11], a[228:231], v[232:235], a[8:11]\n"
    "PD\n"
    "v_exp_f32 v161, v161\n"
    "v_mfma_f32_16x16x32_bf16 a[12:15], a[228:231], v[236:239], a[12:15]\n"
    "PD\n"
    "ds_read_b64_tr_b16 a[246:247], v10 offset:45056\n"
    "v_xor_b32 v10, 0x10000, v10\n"
    "v_mfma_f32_16x16x32_bf16 a[12:15], a[224:227], v[204:207], a[12:15]\n"
    "PD\n"
    "s_waitcnt lgkmcnt(4)\n"
    "v_exp_f32 v162, v162\n"
    "v_mfma_f32_16x16x32_bf16 a[28:31], a[232:235], v[204:207], a[28:31]\n"
    "PD\n"
    "ds_read_b64_tr_b16 a[248:249], v11 offset:32768\n"
    "v_cvt_pk_bf16_f32 v160, v160, v161\n"
    "v_mfma_f32_16x16x32_bf16 a[28:31], a[236:239], v[236:239], a[28:31]\n"
    "PD\n"
    "v_exp_f32 v163, v163\n"
    "v_mfma_f32_16x16x32_bf16 a[24:27], a[236:239], v[232:235], a[24:27]\n"
    "PD\n"
    "ds_read_b64_tr_b16 a[250:251], v11 offset:36864\n"
    "v_cvt_pk_bf16_f32 v161, v162, v163\n"
    "v_mfma_f32_16x16x32_bf16 a[24:27], a[232:235], v[200:203], a[24:27]\n"
    "PD\n"
    "v_exp_f32 v164, v164\n"
    "v_mfma_f32_16x16x32_bf16 a[20:23], a[232:235], v[196:199], a[20:23]\n"
    "PD\n"
    "ds_read_b64_tr_b16 a[252:253], v11 offset:40960\n"
    "v_mfma_f32_16x16x32_bf16 a[20:23], a[236:239], v[228:231], a[20:23]\n"
    "PD\n"
    "v_exp_f32 v165, v165\n"
    "v_mfma_f32_16x16x32_bf16 a[16:19], a[236:239], v[224:227], a[16:19]\n"
    "PD\n"
    "ds_read_b64_tr_b16 a[254:255], v11 offset:45056\n"
    "v_xor_b32 v11, 0x10000, v11\n"
    "v_mfma_f32_16x16x32_bf16 a[16:19], a[232:235], v[192:195], a[16:19]\n"
    "PD\n"
    "s_waitcnt lgkmcnt(4)\n"
    "v_exp_f32 v166, v166\n"
    "v_mfma_f32_16x16x32_bf16 a[32:35], a[240:243], v[192:195], a[32:35]\n"
    "PD\n"
    "ds_read_b64_tr_b16 a[224:225], v12 offset:32768\n"
    "v_cvt_pk_bf16_f32 v164, v164, v165\n"
    "v_mfma_f32_16x16x32_bf16 a[32:35], a[244:247], v[224:227], a[32:35]\n"
    "PD\n"
    "v_exp_f32 v167, v167\n"
    "v_mfma_f32_16x16x32_bf16 a[36:39], a[244:247], v[228:231], a[36:39]\n"
    "PD\n"
    "ds_read_b64_tr_b16 a[226:227], v12 offset:36864\n"
    "v_cvt_pk_bf16_f32 v165, v166, v167\n"
    "v_mfma_f32_16x16x32_bf16 a[36:39], a[240:243], v[196:199], a[36:39]\n"
    "PD\n"
    "v_exp_f32 v168, v168\n"
    "v_mfma_f32_16x16x32_bf16 a[40:43], a[240:243], v[200:203], a[40:43]\n"
    "PD\n"
    "ds_read_b64_tr_b16 a[228:229], v12 offset:40960\n"
    "v_mfma_f32_16x16x32_bf16 a[40:43], a[244:247], v[232:235], a[40:43]\n"
    "PD\n"
    "v_exp_f32 v169, v169\n"
    "v_mfma_f32_16x16x32_bf16 a[44:47], a[244:247], v[236:239], a[44:47]\n"
    "PD\n"
    "ds_read_b64_tr_b16 a[230:231], v12 offset:45056\n"
    "v_xor_b32 v12, 0x10000, v12\n"
    "v_mfma_f32_16x16x32_bf16 a[44:47], a[240:243], v[204:207], a[44:47]\n"
    "PD\n"
    "s_waitcnt lgkmcnt(4)\n"
    "v_exp_f32 v170, v170\n"
    "v_mfma_f32_16x16x32_bf16 a[60:63], a[248:251], v[204:207], a[60:63]\n"
    "PD\n"
    "ds_read_b64_tr_b16 a[232:233], v13 offset:32768\n"
    "v_cvt_pk_bf16_f32 v168, v168, v169\n"
    "v_mfma_f32_16x16x32_bf16 a[60:63], a[252:255], v[236:239], a[60:63]\n"
    "PD\n"
    "v_exp_f32 v171, v171\n"
    "v_mfma_f32_16x16x32_bf16 a[56:59], a[252:255], v[232:235], a[56:59]\n"
    "PD\n"
    "ds_read_b64_tr_b16 a[234:235], v13 offset:36864\n"
    "v_cvt_pk_bf16_f32 v169, v170, v171\n"
    "v_mfma_f32_16x16x32_bf16 a[56:59], a[248:251], v[200:203], a[56:59]\n"
    "PD\n"
    "v_exp_f32 v172, v172\n"
    "v_mfma_f32_16x16x32_bf16 a[52:55], a[248:251], v[196:199], a[52:55]\n"
    "PD\n"
    "ds_read_b64_tr_b16 a[236:237], v13 offset:40960\n"
    "v_mfma_f32_16x16x32_bf16 a[52:55], a[252:255], v[228:231], a[52:55]\n"
    "PD\n"
    "v_exp_f32 v173, v173\n"
    "v_mfma_f32_16x16x32_bf16 a[48:51], a[252:255], v[224:227], a[48:51]\n"
    "PD\n"
    "ds_read_b64_tr_b16 a[238:239], v13 offset:45056\n"
    "v_xor_b32 v13, 0x10000, v13\n"
    "v_mfma_f32_16x16x32_bf16 a[48:51], a[248:251], v[192:195], a[48:51]\n"
    "PD\n"
    "s_waitcnt lgkmcnt(4)\n"
    "v_exp_f32 v174, v174\n"
    "v_mfma_f32_16x16x32_bf16 a[64:67], a[224:227], v[192:195], a[64:67]\n"
    "PD\n"
    "ds_read_b64_tr_b16 a[240:241], v14 offset:32768\n"
    "v_cvt_pk_bf16_f32 v172, v172, v173\n"
    "v_mfma_f32_16x16x32_bf16 a[64:67], a[228:231], v[224:227], a[64:67]\n"
    "PD\n"
    "v_exp_f32 v175, v175\n"
    "v_mfma_f32_16x16x32_bf16 a[68:71], a[228:231], v[228:231], a[68:71]\n"
    "PD\n"
    "ds_read_b64_tr_b16 a[242:243], v14 offset:36864\n"
    "v_cvt_pk_bf16_f32 v173, v174, v175\n"
    "v_mfma_f32_16x16x32_bf16 a[68:71], a[224:227], v[196:199], a[68:71]\n"
    "PD\n"
    "v_exp_f32 v188, v188\n"
    "v_mfma_f32_16x16x32_bf16 a[72:75], a[224:227], v[200:203], a[72:75]\n"
    "PD\n"
    "ds_read_b64_tr_b16 a[244:245], v14 offset:40960\n"
    "v_mfma_f32_16x16x32_bf16 a[72:75], a[228:231], v[232:235], a[72:75]\n"
    "PD\n"
    "v_exp_f32 v189, v189\n"
    "v_mfma_f32_16x16x32_bf16 a[76:79], a[228:231], v[236:239], a[76:79]\n"
    "PD\n"
    "ds_read_b64_tr_b16 a[246:247], v14 offset:45056\n"
    "v_xor_b32 v14, 0x10000, v14\n"
    "v_mfma_f32_16x16x32_bf16 a[76:79], a[224:227], v[204:207], a[76:79]\n"
    "PD\n"
    "s_waitcnt lgkmcnt(4)\n"
    "v_exp_f32 v190, v190\n"
    "v_mfma_f32_16x16x32_bf16 a[92:95], a[232:235], v[204:207], a[92:95]\n"
    "PD\n"
    "ds_read_b64_tr_b16 a[248:249], v15 offset:32768\n"
    "v_cvt_pk_bf16_f32 v174, v188, v189\n"
    "v_mfma_f32_16x16x32_bf16 a[92:95], a[236:239], v[236:239], a[92:95]\n"
    "PD\n"
    "v_exp_f32 v191, v191\n"
    "v_mfma_f32_16x16x32_bf16 a[88:91], a[236:239], v[232:235], a[88:91]\n"
    "PD\n"
    "ds_read_b64_tr_b16 a[250:251], v15 offset:36864\n"
    "v_cvt_pk_bf16_f32 v175, v190, v191\n"
    "v_mfma_f32_16x16x32_bf16 a[88:91], a[232:235], v[200:203], a[88:91]\n"
    "PD\n"
    "v_exp_f32 v184, v184\n"
    "v_mfma_f32_16x16x32_bf16 a[84:87], a[232:235], v[196:199], a[84:87]\n"
    "PD\n"
    "ds_read_b64_tr_b16 a[252:253], v15 offset:40960\n"
    "v_mfma_f32_16x16x32_bf16 a[84:87], a[236:239], v[228:231], a[84:87]\n"
    "PD\n"
    "v_exp_f32 v185, v185\n"
    "v_mfma_f32_16x16x32_bf16 a[80:83], a[236:239], v[224:227], a[80:83]\n"
    "PD\n"
    "ds_read_b64_tr_b16 a[254:255], v15 offset:45056\n"
    "v_xor_b32 v15, 0x10000, v15\n"
    "v_mfma_f32_16x16x32_bf16 a[80:83], a[232:235], v[192:195], a[80:83]\n"
    "PD\n"
    "s_waitcnt lgkmcnt(4)\n"
    "v_exp_f32 v186, v186\n"
    "v_mfma_f32_16x16x32_bf16 a[96:99], a[240:243], v[192:195], a[96:99]\n"
    "PD\n"
    "v_exp_f32 v187, v187\n"
    "v_mfma_f32_16x16x32_bf16 a[96:99], a[244:247], v[224:227], a[96:99]\n"
    "PD\n"
    "v_exp_f32 v180, v180\n"
    "v_mfma_f32_16x16x32_bf16 a[100:103], a[244:247], v[228:231], a[100:103]\n"
    "PD\n"
    "v_exp_f32 v181, v181\n"
    "v_mfma_f32_16x16x32_bf16 a[100:103], a[240:243], v[196:199], a[100:103]\n"
    "PD\n"
    "v_cvt_pk_bf16_f32 v170, v184, v185\n"
    "v_cvt_pk_bf16_f32 v171, v186, v187\n"
    "v_mfma_f32_16x16x32_bf16 a[104:107], a[240:243], v[200:203], a[104:107]\n"
    "PD\n"
    "v_exp_f32 v182, v182\n"
    "v_mfma_f32_16x16x32_bf16 a[104:107], a[244:247], v[232:235], a[104:107]\n"
    "PD\n"
    "v_exp_f32 v183, v183\n"
    "v_mfma_f32_16x16x32_bf16 a[108:111], a[244:247], v[236:239], a[108:111]\n"
    "PD\n"
    "v_exp_f32 v176, v176\n"
    "v_mfma_f32_16x16x32_bf16 a[108:111], a[240:243], v[204:207], a[108:111]\n"
    "PD\n"
    "s_waitcnt lgkmcnt(0)\n"
    "s_waitcnt vmcnt(14)\n"
    "s_barrier\n"
    "v_cvt_pk_bf16_f32 v166, v180, v181\n"
    "v_mfma_f32_16x16x32_bf16 a[124:127], a[248:251], v[204:207], a[124:127]\n"
    "PD\n"
    "v_exp_f32 v177, v177\n"
    "v_mfma_f32_16x16x32_bf16 a[124:127], a[252:255], v[236:239], a[124:127]\n"
    "PD\n"
    "ds_read_b128 a[128:131], v16 offset:49152\n"
    "v_cvt_pk_bf16_f32 v167, v182, v183\n"
    "v_mfma_f32_16x16x32_bf16 a[120:123], a[252:255], v[232:235], a[120:123]\n"
    "PD\n"
    "s_add_u32 m0, s61, 49152\n"
    "v_exp_f32 v178, v178\n"
    "v_mfma_f32_16x16x32_bf16 a[120:123], a[248:251], v[200:203], a[120:123]\n"
    "PD\n"
    "ds_read_b128 a[132:135], v17 offset:49152\n"
    "v_cvt_pk_bf16_f32 v162, v176, v177\n"
    "v_mfma_f32_16x16x32_bf16 a[116:119], a[248:251], v[196:199], a[116:119]\n"
    "PD\n"
    "buffer_load_dwordx4 v28, s[48:51], s58 offen offset:0 lds\n"
    "v_exp_f32 v179, v179\n"
    "v_mfma_f32_16x16x32_bf16 a[116:119], a[252:255], v[228:231], a[116:119]\n"
    "PD\n"
    "ds_read_b128 a[136:139], v18 offset:49152\n"
    "v_cvt_pk_bf16_f32 v163, v178, v179\n"
    "v_mfma_f32_16x16x32_bf16 a[112:115], a[252:255], v[224:227], a[112:115]\n"
    "PD\n"
    "buffer_load_dwordx4 v29, s[48:51], s58 offen offset:1024 lds\n"
    "v_mfma_f32_16x16x32_bf16 a[112:115], a[248:251], v[192:195], a[112:115]\n"
    "PD\n"
    "ds_read_b128 a[140:143], v19 offset:49152\n"
    "v_mfma_f32_16x16x32_bf16 a[192:195], a[208:211], v[192:195], a[192:195]\n"
    "PD\n"
    "buffer_load_dwordx4 v30, s[48:51], s58 offen offset:2048 lds\n"
    "v_mfma_f32_16x16x32_bf16 a[192:195], a[208:211], v[224:227], a[192:195]\n"
    "PD\n"
    "v_mfma_f32_16x16x32_bf16 a[196:199], a[208:211], v[196:199], a[196:199]\n"
    "PD\n"
    "buffer_load_dwordx4 v31, s[48:51], s58 offen offset:3072 lds\n"
    "v_mfma_f32_16x16x32_bf16 a[196:199], a[208:211], v[228:231], a[196:199]\n"
    "PD\n"
    "v_mfma_f32_16x16x32_bf16 a[200:203], a[208:211], v[200:203], a[200:203]\n"
    "PD\n"
    "s_add_u32 s58, s58, s59\n"
    "v_mfma_f32_16x16x32_bf16 a[200:203], a[208:211], v[232:235], a[200:203]\n"
    "PD\n"
    "s_add_u32 m0, s61, 32768\n"
    "v_mfma_f32_16x16x32_bf16 a[204:207], a[208:211], v[204:207], a[204:207]\n"
    "PD\n"
    "buffer_load_dwordx4 v24, s[44:47], s57 offen offset:0 lds\n"
    "v_mfma_f32_16x16x32_bf16 a[204:207], a[208:211], v[236:239], a[204:207]\n"
    "PD\n"
    "s_waitcnt lgkmcnt(0)\n"
    "v_mfma_f32_16x16x32_bf16 v[192:195], a[128:131], v[64:67], v[40:43]\n"
    "PD\n"
    "buffer_load_dwordx4 v25, s[44:47], s57 offen offset:1024 lds\n"
    "v_mfma_f32_16x16x32_bf16 v[192:195], a[132:135], v[68:71], v[192:195]\n"
    "PD\n"
    "ds_read_b128 a[144:147], v16 offset:53248\n"
    "v_mfma_f32_16x16x32_bf16 v[192:195], a[136:139], v[72:75], v[192:195]\n"
    "PD\n"
    "buffer_load_dwordx4 v26, s[44:47], s57 offen offset:2048 lds\n"
    "v_mfma_f32_16x16x32_bf16 v[192:195], a[140:143], v[76:79], v[192:195]\n"
    "PD\n"
    "ds_read_b128 a[148:151], v17 offset:53248\n"
    "v_mfma_f32_16x16x32_bf16 v[196:199], a[140:143], v[92:95], v[44:47]\n"
    "PD\n"
    "buffer_load_dwordx4 v27, s[44:47], s57 offen offset:3072 lds\n"
    "v_mfma_f32_16x16x32_bf16 v[196:199], a[136:139], v[88:91], v[196:199]\n"
    "PD\n"
    "ds_read_b128 a[152:155], v18 offset:53248\n"
    "v_mfma_f32_16x16x32_bf16 v[196:199], a[132:135], v[84:87], v[196:199]\n"
    "PD\n"
    "s_add_u32 s57, s57, s59\n"
    "v_mfma_f32_16x16x32_bf16 v[196:199], a[128:131], v[80:83], v[196:199]\n"
    "PD\n"
    "ds_read_b128 a[156:159], v19 offset:53248\n"
    "v_mfma_f32_16x16x32_bf16 v[200:203], a[128:131], v[96:99], v[48:51]\n"
    "PD\n"
    "global_load_dword v7, v1, s[72:73]\n"
    "v_mfma_f32_16x16x32_bf16 v[200:203], a[132:135], v[100:103], v[200:203]\n"
    "PD\n"
    "v_mfma_f32_16x16x32_bf16 v[200:203], a[136:139], v[104:107], v[200:203]\n"
    "PD\n"
    "v_mfma_f32_16x16x32_bf16 v[200:203], a[140:143], v[108:111], v[200:203]\n"
    "PD\n"
    "v_exp_f32 v192, v192\n"
    "v_mfma_f32_16x16x32_bf16 v[204:207], a[140:143], v[124:127], v[52:55]\n"
    "PD\n"
    "v_exp_f32 v193, v193\n"
    "v_mfma_f32_16x16x32_bf16 v[204:207], a[136:139], v[120:123], v[204:207]\n"
    "PD\n"
    "v_exp_f32 v194, v194\n"
    "v_mfma_f32_16x16x32_bf16 v[204:207], a[132:135], v[116:119], v[204:207]\n"
    "PD\n"
    "v_exp_f32 v195, v195\n"
    "v_mfma_f32_16x16x32_bf16 v[204:207], a[128:131], v[112:115], v[204:207]\n"
    "PD\n"
    "s_waitcnt lgkmcnt(0)\n"
    "v_exp_f32 v196, v196\n"
    "v_mfma_f32_16x16x32_bf16 v[220:223], a[144:147], v[112:115], v[52:55]\n"
    "PD\n"
    "v_exp_f32 v197, v197\n"
    "v_mfma_f32_16x16x32_bf16 v[220:223], a[148:151], v[116:119], v[220:223]\n"
    "PD\n"
    "ds_read_b128 a[128:131], v16 offset:57344\n"
    "v_add_u32 v60, s0, v4\n"
    "v_mfma_f32_16x16x32_bf16 v[220:223], a[152:155], v[120:123], v[220:223]\n"
    "PD\n"
    "global_store_dwordx4 v60, a[212:215], s[88:89] offset:0 sc1\n"
    "v_cvt_pk_bf16_f32 v192, v192, v193\n"
    "v_cvt_pk_bf16_f32 v193, v194, v195\n"
    "v_mfma_f32_16x16x32_bf16 v[220:223], a[156:159], v[124:127], v[220:223]\n"
    "PD\n"
    "ds_read_b128 a[132:135], v17 offset:57344\n"
    "v_cvt_pk_bf16_f32 v196, v196, v197\n"
    "v_mfma_f32_16x16x32_bf16 v[216:219], a[156:159], v[108:111], v[48:51]\n"
    "PD\n"
    "v_exp_f32 v198, v198\n"
    "v_mfma_f32_16x16x32_bf16 v[216:219], a[152:155], v[104:107], v[216:219]\n"
    "PD\n"
    "ds_read_b128 a[136:139], v18 offset:57344\n"
    "v_mfma_f32_16x16x32_bf16 v[216:219], a[148:151], v[100:103], v[216:219]\n"
    "PD\n"
    "v_exp_f32 v199, v199\n"
    "v_mfma_f32_16x16x32_bf16 v[216:219], a[144:147], v[96:99], v[216:219]\n"
    "PD\n"
    "ds_read_b128 a[140:143], v19 offset:57344\n"
    "v_cvt_pk_bf16_f32 v197, v198, v199\n"
    "v_mfma_f32_16x16x32_bf16 v[212:215], a[144:147], v[80:83], v[44:47]\n"
    "PD\n"
    "v_exp_f32 v200, v200\n"
    "v_mfma_f32_16x16x32_bf16 v[212:215], a[148:151], v[84:87], v[212:215]\n"
    "PD\n"
    "v_exp_f32 v201, v201\n"
    "v_mfma_f32_16x16x32_bf16 v[212:215], a[152:155], v[88:91], v[212:215]\n"
    "PD\n"
    "v_exp_f32 v202, v202\n"
    "v_mfma_f32_16x16x32_bf16 v[212:215], a[156:159], v[92:95], v[212:215]\n"
    "PD\n"
    "v_exp_f32 v203, v203\n"
    "v_mfma_f32_16x16x32_bf16 v[208:211], a[156:159], v[76:79], v[40:43]\n"
    "PD\n"
    "global_store_dwordx4 v60, a[212:215], s[88:89] offset:1024 sc1\n"
    "v_exp_f32 v204, v204\n"
    "v_mfma_f32_16x16x32_bf16 v[208:211], a[152:155], v[72:75], v[208:211]\n"
    "PD\n"
    "v_exp_f32 v205, v205\n"
    "v_mfma_f32_16x16x32_bf16 v[208:211], a[148:151], v[68:71], v[208:211]\n"
    "PD\n"
    "v_cvt_pk_bf16_f32 v200, v200, v201\n"
    "v_cvt_pk_bf16_f32 v201, v202, v203\n"
    "v_mfma_f32_16x16x32_bf16 v[208:211], a[144:147], v[64:67], v[208:211]\n"
    "PD\n"
    "s_waitcnt lgkmcnt(0)\n"
    "v_exp_f32 v206, v206\n"
    "v_mfma_f32_16x16x32_bf16 v[224:227], a[128:131], v[64:67], v[40:43]\n"
    "PD\n"
    "v_exp_f32 v207, v207\n"
    "v_mfma_f32_16x16x32_bf16 v[224:227], a[132:135], v[68:71], v[224:227]\n"
    "PD\n"
    "ds_read_b128 a[144:147], v16 offset:61440\n"
    "v_xor_b32 v16, 0x10000, v16\n"
    "v_mfma_f32_16x16x32_bf16 v[224:227], a[136:139], v[72:75], v[224:227]\n"
    "PD\n"
    "v_exp_f32 v220, v220\n"
    "v_mfma_f32_16x16x32_bf16 v[224:227], a[140:143], v[76:79], v[224:227]\n"
    "PD\n"
    "ds_read_b128 a[148:151], v17 offset:61440\n"
    "v_xor_b32 v17, 0x10000, v17\n"
    "v_mfma_f32_16x16x32_bf16 v[228:231], a[140:143], v[92:95], v[44:47]\n"
    "PD\n"
    "v_exp_f32 v221, v221\n"
    "v_mfma_f32_16x16x32_bf16 v[228:231], a[136:139], v[88:91], v[228:231]\n"
    "PD\n"
    "ds_read_b128 a[152:155], v18 offset:61440\n"
    "v_xor_b32 v18, 0x10000, v18\n"
    "v_mfma_f32_16x16x32_bf16 v[228:231], a[132:135], v[84:87], v[228:231]\n"
    "PD\n"
    "global_store_dwordx4 v60, a[212:215], s[88:89] offset:2048 sc1\n"
    "v_cvt_pk_bf16_f32 v204, v204, v205\n"
    "v_cvt_pk_bf16_f32 v205, v206, v207\n"
    "v_mfma_f32_16x16x32_bf16 v[228:231], a[128:131], v[80:83], v[228:231]\n"
    "PD\n"
    "ds_read_b128 a[156:159], v19 offset:61440\n"
    "v_xor_b32 v19, 0x10000, v19\n"
    "v_mfma_f32_16x16x32_bf16 v[232:235], a[128:131], v[96:99], v[48:51]\n"
    "PD\n"
    "v_exp_f32 v222, v222\n"
    "v_mfma_f32_16x16x32_bf16 v[232:235], a[132:135], v[100:103], v[232:235]\n"
    "PD\n"
    "v_exp_f32 v223, v223\n"
    "v_mfma_f32_16x16x32_bf16 v[232:235], a[136:139], v[104:107], v[232:235]\n"
    "PD\n"
    "v_exp_f32 v216, v216\n"
    "v_mfma_f32_16x16x32_bf16 v[232:235], a[140:143], v[108:111], v[232:235]\n"
    "PD\n"
    "v_exp_f32 v217, v217\n"
    "v_mfma_f32_16x16x32_bf16 v[236:239], a[140:143], v[124:127], v[52:55]\n"
    "PD\n"
    "v_cvt_pk_bf16_f32 v206, v220, v221\n"
    "v_cvt_pk_bf16_f32 v207, v222, v223\n"
    "v_mfma_f32_16x16x32_bf16 v[236:239], a[136:139], v[120:123], v[236:239]\n"
    "PD\n"
    "v_exp_f32 v218, v218\n"
    "v_mfma_f32_16x16x32_bf16 v[236:239], a[132:135], v[116:119], v[236:239]\n"
    "PD\n"
    "ds_read_b64_tr_b16 a[224:225], v8 offset:0\n"
    "v_cvt_pk_bf16_f32 v202, v216, v217\n"
    "v_mfma_f32_16x16x32_bf16 v[236:239], a[128:131], v[112:115], v[236:239]\n"
    "PD\n"
    "s_waitcnt lgkmcnt(1)\n"
    "v_exp_f32 v219, v219\n"
    "v_mfma_f32_16x16x32_bf16 v[252:255], a[144:147], v[112:115], v[52:55]\n"
    "PD\n"
    "ds_read_b64_tr_b16 a[226:227], v8 offset:4096\n"
    "v_cvt_pk_bf16_f32 v203, v218, v219\n"
    "v_mfma_f32_16x16x32_bf16 v[252:255], a[148:151], v[116:119], v[252:255]\n"
    "PD\n"
    "global_store_dwordx4 v60, a[212:215], s[88:89] offset:3072 sc1\n"
    "v_exp_f32 v212, v212\n"
    "v_mfma_f32_16x16x32_bf16 v[252:255], a[152:155], v[120:123], v[252:255]\n"
    "PD\n"
    "ds_read_b64_tr_b16 a[228:229], v8 offset:8192\n"
    "v_mfma_f32_16x16x32_bf16 v[252:255], a[156:159], v[124:127], v[252:255]\n"
    "PD\n"
    "v_exp_f32 v213, v213\n"
    "v_mfma_f32_16x16x32_bf16 v[248:251], a[156:159], v[108:111], v[48:51]\n"
    "PD\n"
    "ds_read_b64_tr_b16 a[230:231], v8 offset:12288\n"
    "v_cvt_pk_bf16_f32 v198, v212, v213\n"
    "v_mfma_f32_16x16x32_bf16 v[248:251], a[152:155], v[104:107], v[248:251]\n"
    "PD\n"
    "v_exp_f32 v214, v214\n"
    "v_mfma_f32_16x16x32_bf16 v[248:251], a[148:151], v[100:103], v[248:251]\n"
    "PD\n"
    "ds_read_b64_tr_b16 a[232:233], v9 offset:0\n"
    "v_mfma_f32_16x16x32_bf16 v[248:251], a[144:147], v[96:99], v[248:251]\n"
    "PD\n"
    "v_exp_f32 v215, v215\n"
    "v_mfma_f32_16x16x32_bf16 v[244:247], a[144:147], v[80:83], v[44:47]\n"
    "PD\n"
    "ds_read_b64_tr_b16 a[234:235], v9 offset:4096\n"
    "v_cvt_pk_bf16_f32 v199, v214, v215\n"
    "v_mfma_f32_16x16x32_bf16 v[244:247], a[148:151], v[84:87], v[244:247]\n"
    "PD\n"
    "v_exp_f32 v208, v208\n"
    "v_mfma_f32_16x16x32_bf16 v[244:247], a[152:155], v[88:91], v[244:247]\n"
    "PD\n"
    "ds_read_b64_tr_b16 a[236:237], v9 offset:8192\n"
    "v_mfma_f32_16x16x32_bf16 v[244:247], a[156:159], v[92:95], v[244:247]\n"
    "PD\n"
    "v_exp_f32 v209, v209\n"
    "v_mfma_f32_16x16x32_bf16 v[240:243], a[156:159], v[76:79], v[40:43]\n"
    "PD\n"
    "ds_read_b64_tr_b16 a[238:239], v9 offset:12288\n"
    "v_cvt_pk_bf16_f32 v194, v208, v209\n"
    "v_mfma_f32_16x16x32_bf16 v[240:243], a[152:155], v[72:75], v[240:243]\n"
    "PD\n"
    "v_exp_f32 v210, v210\n"
    "v_mfma_f32_16x16x32_bf16 v[240:243], a[148:151], v[68:71], v[240:243]\n"
    "PD\n"
    "v_exp_f32 v211, v211\n"
    "v_mfma_f32_16x16x32_bf16 v[240:243], a[144:147], v[64:67], v[240:243]\n"
    "PD\n"
    "s_waitcnt lgkmcnt(4)\n"
    "v_exp_f32 v224, v224\n"
    "v_mfma_f32_16x16x32_bf16 a[0:3], a[224:227], v[128:131], a[0:3]\n"
    "PD\n"
    "ds_read_b64_tr_b16 a[240:241], v10 offset:0\n"
    "v_cvt_pk_bf16_f32 v195, v210, v211\n"
    "v_mfma_f32_16x16x32_bf16 a[0:3], a[228:231], v[160:163], a[0:3]\n"
    "PD\n"
    "v_exp_f32 v225, v225\n"
    "v_mfma_f32_16x16x32_bf16 a[4:7], a[228:231], v[164:167], a[4:7]\n"
    "PD\n"
    "ds_read_b64_tr_b16 a[242:243], v10 offset:4096\n"
    "v_cvt_pk_bf16_f32 v224, v224, v225\n"
    "v_mfma_f32_16x16x32_bf16 a[4:7], a[224:227], v[132:135], a[4:7]\n"
    "PD\n"
    "v_exp_f32 v226, v226\n"
    "v_mfma_f32_16x16x32_bf16 a[8:11], a[224:227], v[136:139], a[8:11]\n"
    "PD\n"
    "ds_read_b64_tr_b16 a[244:245], v10 offset:8192\n"
    "v_mfma_f32_16x16x32_bf16 a[8:11], a[228:231], v[168:171], a[8:11]\n"
    "PD\n"
    "v_exp_f32 v227, v227\n"
    "v_mfma_f32_16x16x32_bf16 a[12:15], a[228:231], v[172:175], a[12:15]\n"
    "PD\n"
    "ds_read_b64_tr_b16 a[246:247], v10 offset:12288\n"
    "v_cvt_pk_bf16_f32 v225, v226, v227\n"
    "v_mfma_f32_16x16x32_bf16 a[12:15], a[224:227], v[140:143], a[12:15]\n"
    "PD\n"
    "s_waitcnt lgkmcnt(4)\n"
    "v_exp_f32 v228, v228\n"
    "v_mfma_f32_16x16x32_bf16 a[28:31], a[232:235], v[140:143], a[28:31]\n"
    "PD\n"
    "ds_read_b64_tr_b16 a[248:249], v11 offset:0\n"
    "v_mfma_f32_16x16x32_bf16 a[28:31], a[236:239], v[172:175], a[28:31]\n"
    "PD\n"
    "v_exp_f32 v229, v229\n"
    "v_mfma_f32_16x16x32_bf16 a[24:27], a[236:239], v[168:171], a[24:27]\n"
    "PD\n"
    "ds_read_b64_tr_b16 a[250:251], v11 offset:4096\n"
    "v_cvt_pk_bf16_f32 v228, v228, v229\n"
    "v_mfma_f32_16x16x32_bf16 a[24:27], a[232:235], v[136:139], a[24:27]\n"
    "PD\n"
    "v_exp_f32 v230, v230\n"
    "v_mfma_f32_16x16x32_bf16 a[20:23], a[232:235], v[132:135], a[20:23]\n"
    "PD\n"
    "ds_read_b64_tr_b16 a[252:253], v11 offset:8192\n"
    "v_mfma_f32_16x16x32_bf16 a[20:23], a[236:239], v[164:167], a[20:23]\n"
    "PD\n"
    "v_exp_f32 v231, v231\n"
    "v_mfma_f32_16x16x32_bf16 a[16:19], a[236:239], v[160:163], a[16:19]\n"
    "PD\n"
    "ds_read_b64_tr_b16 a[254:255], v11 offset:12288\n"
    "v_cvt_pk_bf16_f32 v229, v230, v231\n"
    "v_mfma_f32_16x16x32_bf16 a[16:19], a[232:235], v[128:131], a[16:19]\n"
    "PD\n"
    "s_waitcnt lgkmcnt(4)\n"
    "v_exp_f32 v232, v232\n"
    "v_mfma_f32_16x16x32_bf16 a[32:35], a[240:243], v[128:131], a[32:35]\n"
    "PD\n"
    "ds_read_b64_tr_b16 a[224:225], v12 offset:0\n"
    "v_mfma_f32_16x16x32_bf16 a[32:35], a[244:247], v[160:163], a[32:35]\n"
    "PD\n"
    "v_exp_f32 v233, v233\n"
    "v_mfma_f32_16x16x32_bf16 a[36:39], a[244:247], v[164:167], a[36:39]\n"
    "PD\n"
    "ds_read_b64_tr_b16 a[226:227], v12 offset:4096\n"
    "v_cvt_pk_bf16_f32 v232, v232, v233\n"
    "v_mfma_f32_16x16x32_bf16 a[36:39], a[240:243], v[132:135], a[36:39]\n"
    "PD\n"
    "v_exp_f32 v234, v234\n"
    "v_mfma_f32_16x16x32_bf16 a[40:43], a[240:243], v[136:139], a[40:43]\n"
    "PD\n"
    "ds_read_b64_tr_b16 a[228:229], v12 offset:8192\n"
    "v_mfma_f32_16x16x32_bf16 a[40:43], a[244:247], v[168:171], a[40:43]\n"
    "PD\n"
    "v_exp_f32 v235, v235\n"
    "v_mfma_f32_16x16x32_bf16 a[44:47], a[244:247], v[172:175], a[44:47]\n"
    "PD\n"
    "ds_read_b64_tr_b16 a[230:231], v12 offset:12288\n"
    "v_cvt_pk_bf16_f32 v233, v234, v235\n"
    "v_mfma_f32_16x16x32_bf16 a[44:47], a[240:243], v[140:143], a[44:47]\n"
    "PD\n"
    "s_waitcnt lgkmcnt(4)\n"
    "v_exp_f32 v236, v236\n"
    "v_mfma_f32_16x16x32_bf16 a[60:63], a[248:251], v[140:143], a[60:63]\n"
    "PD\n"
    "ds_read_b64_tr_b16 a[232:233], v13 offset:0\n"
    "v_mfma_f32_16x16x32_bf16 a[60:63], a[252:255], v[172:175], a[60:63]\n"
    "PD\n"
    "v_exp_f32 v237, v237\n"
    "v_mfma_f32_16x16x32_bf16 a[56:59], a[252:255], v[168:171], a[56:59]\n"
    "PD\n"
    "ds_read_b64_tr_b16 a[234:235], v13 offset:4096\n"
    "v_cvt_pk_bf16_f32 v236, v236, v237\n"
    "v_mfma_f32_16x16x32_bf16 a[56:59], a[248:251], v[136:139], a[56:59]\n"
    "PD\n"
    "v_exp_f32 v238, v238\n"
    "v_mfma_f32_16x16x32_bf16 a[52:55], a[248:251], v[132:135], a[52:55]\n"
    "PD\n"
    "ds_read_b64_tr_b16 a[236:237], v13 offset:8192\n"
    "v_mfma_f32_16x16x32_bf16 a[52:55], a[252:255], v[164:167], a[52:55]\n"
    "PD\n"
    "v_exp_f32 v239, v239\n"
    "v_mfma_f32_16x16x32_bf16 a[48:51], a[252:255], v[160:163], a[48:51]\n"
    "PD\n"
    "ds_read_b64_tr_b16 a[238:239], v13 offset:12288\n"
    "v_cvt_pk_bf16_f32 v237, v238, v239\n"
    "v_mfma_f32_16x16x32_bf16 a[48:51], a[248:251], v[128:131], a[48:51]\n"
    "PD\n"
    "s_waitcnt lgkmcnt(4)\n"
    "v_exp_f32 v252, v252\n"
    "v_mfma_f32_16x16x32_bf16 a[64:67], a[224:227], v[128:131], a[64:67]\n"
    "PD\n"
    "ds_read_b64_tr_b16 a[240:241], v14 offset:0\n"
    "v_mfma_f32_16x16x32_bf16 a[64:67], a[228:231], v[160:163], a[64:67]\n"
    "PD\n"
    "v_exp_f32 v253, v253\n"
    "v_mfma_f32_16x16x32_bf16 a[68:71], a[228:231], v[164:167], a[68:71]\n"
    "PD\n"
    "ds_read_b64_tr_b16 a[242:243], v14 offset:4096\n"
    "v_cvt_pk_bf16_f32 v238, v252, v253\n"
    "v_mfma_f32_16x16x32_bf16 a[68:71], a[224:227], v[132:135], a[68:71]\n"
    "PD\n"
    "v_exp_f32 v254, v254\n"
    "v_mfma_f32_16x16x32_bf16 a[72:75], a[224:227], v[136:139], a[72:75]\n"
    "PD\n"
    "ds_read_b64_tr_b16 a[244:245], v14 offset:8192\n"
    "v_mfma_f32_16x16x32_bf16 a[72:75], a[228:231], v[168:171], a[72:75]\n"
    "PD\n"
    "v_exp_f32 v255, v255\n"
    "v_mfma_f32_16x16x32_bf16 a[76:79], a[228:231], v[172:175], a[76:79]\n"
    "PD\n"
    "ds_read_b64_tr_b16 a[246:247], v14 offset:12288\n"
    "v_cvt_pk_bf16_f32 v239, v254, v255\n"
    "v_mfma_f32_16x16x32_bf16 a[76:79], a[224:227], v[140:143], a[76:79]\n"
    "PD\n"
    "s_waitcnt lgkmcnt(4)\n"
    "v_exp_f32 v248, v248\n"
    "v_mfma_f32_16x16x32_bf16 a[92:95], a[232:235], v[140:143], a[92:95]\n"
    "PD\n"
    "ds_read_b64_tr_b16 a[248:249], v15 offset:0\n"
    "v_mfma_f32_16x16x32_bf16 a[92:95], a[236:239], v[172:175], a[92:95]\n"
    "PD\n"
    "v_exp_f32 v249, v249\n"
    "v_mfma_f32_16x16x32_bf16 a[88:91], a[236:239], v[168:171], a[88:91]\n"
    "PD\n"
    "ds_read_b64_tr_b16 a[250:251], v15 offset:4096\n"
    "v_cvt_pk_bf16_f32 v234, v248, v249\n"
    "v_mfma_f32_16x16x32_bf16 a[88:91], a[232:235], v[136:139], a[88:91]\n"
    "PD\n"
    "v_exp_f32 v250, v250\n"
    "v_mfma_f32_16x16x32_bf16 a[84:87], a[232:235], v[132:135], a[84:87]\n"
    "PD\n"
    "ds_read_b64_tr_b16 a[252:253], v15 offset:8192\n"
    "v_mfma_f32_16x16x32_bf16 a[84:87], a[236:239], v[164:167], a[84:87]\n"
    "PD\n"
    "v_exp_f32 v251, v251\n"
    "v_mfma_f32_16x16x32_bf16 a[80:83], a[236:239], v[160:163], a[80:83]\n"
    "PD\n"
    "ds_read_b64_tr_b16 a[254:255], v15 offset:12288\n"
    "v_cvt_pk_bf16_f32 v235, v250, v251\n"
    "v_mfma_f32_16x16x32_bf16 a[80:83], a[232:235], v[128:131], a[80:83]\n"
    "PD\n"
    "s_waitcnt lgkmcnt(4)\n"
    "v_exp_f32 v244, v244\n"
    "v_mfma_f32_16x16x32_bf16 a[96:99], a[240:243], v[128:131], a[96:99]\n"
    "PD\n"
    "v_exp_f32 v245, v245\n"
    "v_mfma_f32_16x16x32_bf16 a[96:99], a[244:247], v[160:163], a[96:99]\n"
    "PD\n"
    "v_exp_f32 v246, v246\n"
    "v_mfma_f32_16x16x32_bf16 a[100:103], a[244:247], v[164:167], a[100:103]\n"
    "PD\n"
    "v_exp_f32 v247, v247\n"
    "v_mfma_f32_16x16x32_bf16 a[100:103], a[240:243], v[132:135], a[100:103]\n"
    "PD\n"
    "v_exp_f32 v240, v240\n"
    "v_mfma_f32_16x16x32_bf16 a[104:107], a[240:243], v[136:139], a[104:107]\n"
    "PD\n"
    "v_exp_f32 v241, v241\n"
    "v_mfma_f32_16x16x32_bf16 a[104:107], a[244:247], v[168:171], a[104:107]\n"
    "PD\n"
    "v_cvt_pk_bf16_f32 v230, v244, v245\n"
    "v_cvt_pk_bf16_f32 v231, v246, v247\n"
    "v_mfma_f32_16x16x32_bf16 a[108:111], a[244:247], v[172:175], a[108:111]\n"
    "PD\n"
    "v_exp_f32 v242, v242\n"
    "v_mfma_f32_16x16x32_bf16 a[108:111], a[240:243], v[140:143], a[108:111]\n"
    "PD\n"
    "s_waitcnt lgkmcnt(0)\n"
    "s_waitcnt vmcnt(18)\n"
    "s_barrier\n"
    "v_cvt_pk_bf16_f32 v226, v240, v241\n"
    "v_mfma_f32_16x16x32_bf16 a[124:127], a[248:251], v[140:143], a[124:127]\n"
    "PD\n"
    "v_exp_f32 v243, v243\n"
    "v_mfma_f32_16x16x32_bf16 a[124:127], a[252:255], v[172:175], a[124:127]\n"
    "PD\n"
    "ds_read_b128 a[128:131], v16 offset:16384\n"
    "v_cvt_pk_bf16_f32 v227, v242, v243\n"
    "v_mfma_f32_16x16x32_bf16 a[120:123], a[252:255], v[168:171], a[120:123]\n"
    "PD\n"
    "s_add_u32 m0, s61, 81920\n"
    "v_mfma_f32_16x16x32_bf16 a[120:123], a[248:251], v[136:139], a[120:123]\n"
    "PD\n"
    "ds_read_b128 a[132:135], v17 offset:16384\n"
    "v_mfma_f32_16x16x32_bf16 a[116:119], a[248:251], v[132:135], a[116:119]\n"
    "PD\n"
    "buffer_load_dwordx4 v28, s[48:51], s58 offen offset:0 lds\n"
    "v_mfma_f32_16x16x32_bf16 a[116:119], a[252:255], v[164:167], a[116:119]\n"
    "PD\n"
    "ds_read_b128 a[136:139], v18 offset:16384\n"
    "v_mfma_f32_16x16x32_bf16 a[112:115], a[252:255], v[160:163], a[112:115]\n"
    "PD\n"
    "buffer_load_dwordx4 v29, s[48:51], s58 offen offset:1024 lds\n"
    "v_mfma_f32_16x16x32_bf16 a[112:115], a[248:251], v[128:131], a[112:115]\n"
    "PD\n"
    "ds_read_b128 a[140:143], v19 offset:16384\n"
    "v_mfma_f32_16x16x32_bf16 a[192:195], a[208:211], v[128:131], a[192:195]\n"
    "PD\n"
    "buffer_load_dwordx4 v30, s[48:51], s58 offen offset:2048 lds\n"
    "v_mfma_f32_16x16x32_bf16 a[192:195], a[208:211], v[160:163], a[192:195]\n"
    "PD\n"
    "v_mfma_f32_16x16x32_bf16 a[196:199], a[208:211], v[132:135], a[196:199]\n"
    "PD\n"
    "buffer_load_dwordx4 v31, s[48:51], s58 offen offset:3072 lds\n"
    "v_mfma_f32_16x16x32_bf16 a[196:199], a[208:211], v[164:167], a[196:199]\n"
    "PD\n"
    "v_mfma_f32_16x16x32_bf16 a[200:203], a[208:211], v[136:139], a[200:203]\n"
    "PD\n"
    "s_add_u32 s58, s58, s59\n"
    "v_mfma_f32_16x16x32_bf16 a[200:203], a[208:211], v[168:171], a[200:203]\n"
    "PD\n"
    "s_add_u32 m0, s61, 65536\n"
    "v_mfma_f32_16x16x32_bf16 a[204:207], a[208:211], v[140:143], a[204:207]\n"
    "PD\n"
    "buffer_load_dwordx4 v24, s[44:47], s57 offen offset:0 lds\n"
    "v_mfma_f32_16x16x32_bf16 a[204:207], a[208:211], v[172:175], a[204:207]\n"
    "PD\n"
    "s_waitcnt lgkmcnt(0)\n"
    "v_mfma_f32_16x16x32_bf16 v[128:131], a[128:131], v[64:67], v[40:43]\n"
    "PD\n"
    "buffer_load_dwordx4 v25, s[44:47], s57 offen offset:1024 lds\n"
    "v_mfma_f32_16x16x32_bf16 v[128:131], a[132:135], v[68:71], v[128:131]\n"
    "PD\n"
    "ds_read_b128 a[144:147], v16 offset:20480\n"
    "v_mfma_f32_16x16x32_bf16 v[128:131], a[136:139], v[72:75], v[128:131]\n"
    "PD\n"
    "buffer_load_dwordx4 v26, s[44:47], s57 offen offset:2048 lds\n"
    "v_mfma_f32_16x16x32_bf16 v[128:131], a[140:143], v[76:79], v[128:131]\n"
    "PD\n"
    "ds_read_b128 a[148:151], v17 offset:20480\n"
    "v_mfma_f32_16x16x32_bf16 v[132:135], a[140:143], v[92:95], v[44:47]\n"
    "PD\n"
    "buffer_load_dwordx4 v27, s[44:47], s57 offen offset:3072 lds\n"
    "v_mfma_f32_16x16x32_bf16 v[132:135], a[136:139], v[88:91], v[132:135]\n"
    "PD\n"
    "ds_read_b128 a[152:155], v18 offset:20480\n"
    "v_mfma_f32_16x16x32_bf16 v[132:135], a[132:135], v[84:87], v[132:135]\n"
    "PD\n"
    "s_add_u32 s57, s57, s59\n"
    "v_mfma_f32_16x16x32_bf16 v[132:135], a[128:131], v[80:83], v[132:135]\n"
    "PD\n"
    "ds_read_b128 a[156:159], v19 offset:20480\n"
    "v_add_u32 v60, s1, v4\n"
    "v_mfma_f32_16x16x32_bf16 v[136:139], a[128:131], v[96:99], v[48:51]\n"
    "PD\n"
    "global_store_dwordx4 v60, a[212:215], s[88:89] offset:0 sc1\n"
    "v_mfma_f32_16x16x32_bf16 v[136:139], a[132:135], v[100:103], v[136:139]\n"
    "PD\n"
    "v_mfma_f32_16x16x32_bf16 v[136:139], a[136:139], v[104:107], v[136:139]\n"
    "PD\n"
    "v_mfma_f32_16x16x32_bf16 v[136:139], a[140:143], v[108:111], v[136:139]\n"
    "PD\n"
    "v_exp_f32 v128, v128\n"
    "v_mfma_f32_16x16x32_bf16 v[140:143], a[140:143], v[124:127], v[52:55]\n"
    "PD\n"
    "v_exp_f32 v129, v129\n"
    "v_mfma_f32_16x16x32_bf16 v[140:143], a[136:139], v[120:123], v[140:143]\n"
    "PD\n"
    "v_exp_f32 v130, v130\n"
    "v_mfma_f32_16x16x32_bf16 v[140:143], a[132:135], v[116:119], v[140:143]\n"
    "PD\n"
    "v_exp_f32 v131, v131\n"
    "v_mfma_f32_16x16x32_bf16 v[140:143], a[128:131], v[112:115], v[140:143]\n"
    "PD\n"
    "s_waitcnt lgkmcnt(0)\n"
    "v_exp_f32 v132, v132\n"
    "v_mfma_f32_16x16x32_bf16 v[156:159], a[144:147], v[112:115], v[52:55]\n"
    "PD\n"
    "v_exp_f32 v133, v133\n"
    "v_mfma_f32_16x16x32_bf16 v[156:159], a[148:151], v[116:119], v[156:159]\n"
    "PD\n"
    "ds_read_b128 a[128:131], v16 offset:24576\n"
    "v_cvt_pk_bf16_f32 v128, v128, v129\n"
    "v_mfma_f32_16x16x32_bf16 v[156:159], a[152:155], v[120:123], v[156:159]\n"
    "PD\n"
    "global_store_dwordx4 v60, a[212:215], s[88:89] offset:1024 sc1\n"
    "v_exp_f32 v134, v134\n"
    "v_mfma_f32_16x16x32_bf16 v[156:159], a[156:159], v[124:127], v[156:159]\n"
    "PD\n"
    "ds_read_b128 a[132:135], v17 offset:24576\n"
    "v_cvt_pk_bf16_f32 v129, v130, v131\n"
    "v_mfma_f32_16x16x32_bf16 v[152:155], a[156:159], v[108:111], v[48:51]\n"
    "PD\n"
    "v_exp_f32 v135, v135\n"
    "v_mfma_f32_16x16x32_bf16 v[152:155], a[152:155], v[104:107], v[152:155]\n"
    "PD\n"
    "ds_read_b128 a[136:139], v18 offset:24576\n"
    "v_cvt_pk_bf16_f32 v132, v132, v133\n"
    "v_mfma_f32_16x16x32_bf16 v[152:155], a[148:151], v[100:103], v[152:155]\n"
    "PD\n"
    "v_exp_f32 v136, v136\n"
    "v_mfma_f32_16x16x32_bf16 v[152:155], a[144:147], v[96:99], v[152:155]\n"
    "PD\n"
    "ds_read_b128 a[140:143], v19 offset:24576\n"
    "v_cvt_pk_bf16_f32 v133, v134, v135\n"
    "v_mfma_f32_16x16x32_bf16 v[148:151], a[144:147], v[80:83], v[44:47]\n"
    "PD\n"
    "v_exp_f32 v137, v137\n"
    "v_mfma_f32_16x16x32_bf16 v[148:151], a[148:151], v[84:87], v[148:151]\n"
    "PD\n"
    "v_exp_f32 v138, v138\n"
    "v_mfma_f32_16x16x32_bf16 v[148:151], a[152:155], v[88:91], v[148:151]\n"
    "PD\n"
    "v_exp_f32 v139, v139\n"
    "v_mfma_f32_16x16x32_bf16 v[148:151], a[156:159], v[92:95], v[148:151]\n"
    "PD\n"
    "v_exp_f32 v140, v140\n"
    "v_mfma_f32_16x16x32_bf16 v[144:147], a[156:159], v[76:79], v[40:43]\n"
    "PD\n"
    "global_store_dwordx4 v60, a[212:215], s[88:89] offset:2048 sc1\n"
    "v_exp_f32 v141, v141\n"
    "v_mfma_f32_16x16x32_bf16 v[144:147], a[152:155], v[72:75], v[144:147]\n"
    "PD\n"
    "v_cvt_pk_bf16_f32 v136, v136, v137\n"
    "v_cvt_pk_bf16_f32 v137, v138, v139\n"
    "v_mfma_f32_16x16x32_bf16 v[144:147], a[148:151], v[68:71], v[144:147]\n"
    "PD\n"
    "v_exp_f32 v142, v142\n"
    "v_mfma_f32_16x16x32_bf16 v[144:147], a[144:147], v[64:67], v[144:147]\n"
    "PD\n"
    "s_waitcnt lgkmcnt(0)\n"
    "v_exp_f32 v143, v143\n"
    "v_mfma_f32_16x16x32_bf16 v[160:163], a[128:131], v[64:67], v[40:43]\n"
    "PD\n"
    "v_exp_f32 v156, v156\n"
    "v_mfma_f32_16x16x32_bf16 v[160:163], a[132:135], v[68:71], v[160:163]\n"
    "PD\n"
    "ds_read_b128 a[144:147], v16 offset:28672\n"
    "v_cvt_pk_bf16_f32 v140, v140, v141\n"
    "v_mfma_f32_16x16x32_bf16 v[160:163], a[136:139], v[72:75], v[160:163]\n"
    "PD\n"
    "v_exp_f32 v157, v157\n"
    "v_mfma_f32_16x16x32_bf16 v[160:163], a[140:143], v[76:79], v[160:163]\n"
    "PD\n"
    "ds_read_b128 a[148:151], v17 offset:28672\n"
    "v_cvt_pk_bf16_f32 v141, v142, v143\n"
    "v_mfma_f32_16x16x32_bf16 v[164:167], a[140:143], v[92:95], v[44:47]\n"
    "PD\n"
    "v_exp_f32 v158, v158\n"
    "v_mfma_f32_16x16x32_bf16 v[164:167], a[136:139], v[88:91], v[164:167]\n"
    "PD\n"
    "ds_read_b128 a[152:155], v18 offset:28672\n"
    "v_cvt_pk_bf16_f32 v142, v156, v157\n"
    "v_mfma_f32_16x16x32_bf16 v[164:167], a[132:135], v[84:87], v[164:167]\n"
    "PD\n"
    "global_store_dwordx4 v60, a[212:215], s[88:89] offset:3072 sc1\n"
    "v_exp_f32 v159, v159\n"
    "v_mfma_f32_16x16x32_bf16 v[164:167], a[128:131], v[80:83], v[164:167]\n"
    "PD\n"
    "ds_read_b128 a[156:159], v19 offset:28672\n"
    "v_cvt_pk_bf16_f32 v143, v158, v159\n"
    "v_mfma_f32_16x16x32_bf16 v[168:171], a[128:131], v[96:99], v[48:51]\n"
    "PD\n"
    "v_exp_f32 v152, v152\n"
    "v_mfma_f32_16x16x32_bf16 v[168:171], a[132:135], v[100:103], v[168:171]\n"
    "PD\n"
    "v_exp_f32 v153, v153\n"
    "v_mfma_f32_16x16x32_bf16 v[168:171], a[136:139], v[104:107], v[168:171]\n"
    "PD\n"
    "v_exp_f32 v154, v154\n"
    "v_mfma_f32_16x16x32_bf16 v[168:171], a[140:143], v[108:111], v[168:171]\n"
    "PD\n"
    "v_exp_f32 v155, v155\n"
    "v_mfma_f32_16x16x32_bf16 v[172:175], a[140:143], v[124:127], v[52:55]\n"
    "PD\n"
    "v_exp_f32 v148, v148\n"
    "v_mfma_f32_16x16x32_bf16 v[172:175], a[136:139], v[120:123], v[172:175]\n"
    "PD\n"
    "v_exp_f32 v149, v149\n"
    "v_mfma_f32_16x16x32_bf16 v[172:175], a[132:135], v[116:119], v[172:175]\n"
    "PD\n"
    "ds_read_b64_tr_b16 a[224:225], v8 offset:32768\n"
    "v_cvt_pk_bf16_f32 v138, v152, v153\n"
    "v_mfma_f32_16x16x32_bf16 v[172:175], a[128:131], v[112:115], v[172:175]\n"
    "PD\n"
    "s_waitcnt lgkmcnt(1)\n"
    "v_exp_f32 v150, v150\n"
    "v_mfma_f32_16x16x32_bf16 v[188:191], a[144:147], v[112:115], v[52:55]\n"
    "PD\n"
    "ds_read_b64_tr_b16 a[226:227], v8 offset:36864\n"
    "v_cvt_pk_bf16_f32 v139, v154, v155\n"
    "v_mfma_f32_16x16x32_bf16 v[188:191], a[148:151], v[116:119], v[188:191]\n"
    "PD\n"
    "v_exp_f32 v151, v151\n"
    "v_mfma_f32_16x16x32_bf16 v[188:191], a[152:155], v[120:123], v[188:191]\n"
    "PD\n"
    "ds_read_b64_tr_b16 a[228:229], v8 offset:40960\n"
    "v_cvt_pk_bf16_f32 v134, v148, v149\n"
    "v_mfma_f32_16x16x32_bf16 v[188:191], a[156:159], v[124:127], v[188:191]\n"
    "PD\n"
    "v_exp_f32 v144, v144\n"
    "v_mfma_f32_16x16x32_bf16 v[184:187], a[156:159], v[108:111], v[48:51]\n"
    "PD\n"
    "ds_read_b64_tr_b16 a[230:231], v8 offset:45056\n"
    "v_xor_b32 v8, 0x10000, v8\n"
    "v_mfma_f32_16x16x32_bf16 v[184:187], a[152:155], v[104:107], v[184:187]\n"
    "PD\n"
    "v_exp_f32 v145, v145\n"
    "v_mfma_f32_16x16x32_bf16 v[184:187], a[148:151], v[100:103], v[184:187]\n"
    "PD\n"
    "ds_read_b64_tr_b16 a[232:233], v9 offset:32768\n"
    "v_cvt_pk_bf16_f32 v135, v150, v151\n"
    "v_mfma_f32_16x16x32_bf16 v[184:187], a[144:147], v[96:99], v[184:187]\n"
    "PD\n"
    "v_exp_f32 v146, v146\n"
    "v_mfma_f32_16x16x32_bf16 v[180:183], a[144:147], v[80:83], v[44:47]\n"
    "PD\n"
    "ds_read_b64_tr_b16 a[234:235], v9 offset:36864\n"
    "v_cvt_pk_bf16_f32 v130, v144, v145\n"
    "v_mfma_f32_16x16x32_bf16 v[180:183], a[148:151], v[84:87], v[180:183]\n"
    "PD\n"
    "v_exp_f32 v147, v147\n"
    "v_mfma_f32_16x16x32_bf16 v[180:183], a[152:155], v[88:91], v[180:183]\n"
    "PD\n"
    "ds_read_b64_tr_b16 a[236:237], v9 offset:40960\n"
    "v_cvt_pk_bf16_f32 v131, v146, v147\n"
    "v_mfma_f32_16x16x32_bf16 v[180:183], a[156:159], v[92:95], v[180:183]\n"
    "PD\n"
    "v_exp_f32 v160, v160\n"
    "v_mfma_f32_16x16x32_bf16 v[176:179], a[156:159], v[76:79], v[40:43]\n"
    "PD\n"
    "ds_read_b64_tr_b16 a[238:239], v9 offset:45056\n"
    "v_xor_b32 v9, 0x10000, v9\n"
    "v_mfma_f32_16x16x32_bf16 v[176:179], a[152:155], v[72:75], v[176:179]\n"
    "PD\n"
    "v_exp_f32 v161, v161\n"
    "v_mfma_f32_16x16x32_bf16 v[176:179], a[148:151], v[68:71], v[176:179]\n"
    "PD\n"
    "v_exp_f32 v162, v162\n"
    "v_mfma_f32_16x16x32_bf16 v[176:179], a[144:147], v[64:67], v[176:179]\n"
    "PD\n"
    "s_waitcnt lgkmcnt(4)\n"
    "v_exp_f32 v163, v163\n"
    "v_mfma_f32_16x16x32_bf16 a[0:3], a[224:227], v[192:195], a[0:3]\n"
    "PD\n"
    "ds_read_b64_tr_b16 a[240:241], v10 offset:32768\n"
    "v_cvt_pk_bf16_f32 v160, v160, v161\n"
    "v_mfma_f32_16x16x32_bf16 a[0:3], a[228:231], v[224:227], a[0:3]\n"
    "PD\n"
    "v_exp_f32 v164, v164\n"
    "v_mfma_f32_16x16x32_bf16 a[4:7], a[228:231], v[228:231], a[4:7]\n"
    "PD\n"
    "ds_read_b64_tr_b16 a[242:243], v10 offset:36864\n"
    "v_cvt_pk_bf16_f32 v161, v162, v163\n"
    "v_mfma_f32_16x16x32_bf16 a[4:7], a[224:227], v[196:199], a[4:7]\n"
    "PD\n"
    "v_exp_f32 v165, v165\n"
    "v_mfma_f32_16x16x32_bf16 a[8:11], a[224:227], v[200:203], a[8:11]\n"
    "PD\n"
    "ds_read_b64_tr_b16 a[244:245], v10 offset:40960\n"
    "v_cvt_pk_bf16_f32 v164, v164, v165\n"
    "v_mfma_f32_16x16x32_bf16 a[8:11], a[228:231], v[232:235], a[8:11]\n"
    "PD\n"
    "v_exp_f32 v166, v166\n"
    "v_mfma_f32_16x16x32_bf16 a[12:15], a[228:231], v[236:239], a[12:15]\n"
    "PD\n"
    "ds_read_b64_tr_b16 a[246:247], v10 offset:45056\n"
    "v_xor_b32 v10, 0x10000, v10\n"
    "v_mfma_f32_16x16x32_bf16 a[12:15], a[224:227], v[204:207], a[12:15]\n"
    "PD\n"
    "s_waitcnt lgkmcnt(4)\n"
    "v_exp_f32 v167, v167\n"
    "v_mfma_f32_16x16x32_bf16 a[28:31], a[232:235], v[204:207], a[28:31]\n"
    "PD\n"
    "ds_read_b64_tr_b16 a[248:249], v11 offset:32768\n"
    "v_cvt_pk_bf16_f32 v165, v166, v167\n"
    "v_mfma_f32_16x16x32_bf16 a[28:31], a[236:239], v[236:239], a[28:31]\n"
    "PD\n"
    "v_exp_f32 v168, v168\n"
    "v_mfma_f32_16x16x32_bf16 a[24:27], a[236:239], v[232:235], a[24:27]\n"
    "PD\n"
    "ds_read_b64_tr_b16 a[250:251], v11 offset:36864\n"
    "v_mfma_f32_16x16x32_bf16 a[24:27], a[232:235], v[200:203], a[24:27]\n"
    "PD\n"
    "v_exp_f32 v169, v169\n"
    "v_mfma_f32_16x16x32_bf16 a[20:23], a[232:235], v[196:199], a[20:23]\n"
    "PD\n"
    "ds_read_b64_tr_b16 a[252:253], v11 offset:40960\n"
    "v_cvt_pk_bf16_f32 v168, v168, v169\n"
    "v_mfma_f32_16x16x32_bf16 a[20:23], a[236:239], v[228:231], a[20:23]\n"
    "PD\n"
    "v_exp_f32 v170, v170\n"
    "v_mfma_f32_16x16x32_bf16 a[16:19], a[236:239], v[224:227], a[16:19]\n"
    "PD\n"
    "ds_read_b64_tr_b16 a[254:255], v11 offset:45056\n"
    "v_xor_b32 v11, 0x10000, v11\n"
    "v_mfma_f32_16x16x32_bf16 a[16:19], a[232:235], v[192:195], a[16:19]\n"
    "PD\n"
    "s_waitcnt lgkmcnt(4)\n"
    "v_exp_f32 v171, v171\n"
    "v_mfma_f32_16x16x32_bf16 a[32:35], a[240:243], v[192:195], a[32:35]\n"
    "PD\n"
    "ds_read_b64_tr_b16 a[224:225], v12 offset:32768\n"
    "v_cvt_pk_bf16_f32 v169, v170, v171\n"
    "v_mfma_f32_16x16x32_bf16 a[32:35], a[244:247], v[224:227], a[32:35]\n"
    "PD\n"
    "v_exp_f32 v172, v172\n"
    "v_mfma_f32_16x16x32_bf16 a[36:39], a[244:247], v[228:231], a[36:39]\n"
    "PD\n"
    "ds_read_b64_tr_b16 a[226:227], v12 offset:36864\n"
    "v_mfma_f32_16x16x32_bf16 a[36:39], a[240:243], v[196:199], a[36:39]\n"
    "PD\n"
    "v_exp_f32 v173, v173\n"
    "v_mfma_f32_16x16x32_bf16 a[40:43], a[240:243], v[200:203], a[40:43]\n"
    "PD\n"
    "ds_read_b64_tr_b16 a[228:229], v12 offset:40960\n"
    "v_cvt_pk_bf16_f32 v172, v172, v173\n"
    "v_mfma_f32_16x16x32_bf16 a[40:43], a[244:247], v[232:235], a[40:43]\n"
    "PD\n"
    "v_exp_f32 v174, v174\n"
    "v_mfma_f32_16x16x32_bf16 a[44:47], a[244:247], v[236:239], a[44:47]\n"
    "PD\n"
    "ds_read_b64_tr_b16 a[230:231], v12 offset:45056\n"
    "v_xor_b32 v12, 0x10000, v12\n"
    "v_mfma_f32_16x16x32_bf16 a[44:47], a[240:243], v[204:207], a[44:47]\n"
    "PD\n"
    "s_waitcnt lgkmcnt(4)\n"
    "v_exp_f32 v175, v175\n"
    "v_mfma_f32_16x16x32_bf16 a[60:63], a[248:251], v[204:207], a[60:63]\n"
    "PD\n"
    "ds_read_b64_tr_b16 a[232:233], v13 offset:32768\n"
    "v_cvt_pk_bf16_f32 v173, v174, v175\n"
    "v_mfma_f32_16x16x32_bf16 a[60:63], a[252:255], v[236:239], a[60:63]\n"
    "PD\n"
    "v_exp_f32 v188, v188\n"
    "v_mfma_f32_16x16x32_bf16 a[56:59], a[252:255], v[232:235], a[56:59]\n"
    "PD\n"
    "ds_read_b64_tr_b16 a[234:235], v13 offset:36864\n"
    "v_mfma_f32_16x16x32_bf16 a[56:59], a[248:251], v[200:203], a[56:59]\n"
    "PD\n"
    "v_exp_f32 v189, v189\n"
    "v_mfma_f32_16x16x32_bf16 a[52:55], a[248:251], v[196:199], a[52:55]\n"
    "PD\n"
    "ds_read_b64_tr_b16 a[236:237], v13 offset:40960\n"
    "v_cvt_pk_bf16_f32 v174, v188, v189\n"
    "v_mfma_f32_16x16x32_bf16 a[52:55], a[252:255], v[228:231], a[52:55]\n"
    "PD\n"
    "v_exp_f32 v190, v190\n"
    "v_mfma_f32_16x16x32_bf16 a[48:51], a[252:255], v[224:227], a[48:51]\n"
    "PD\n"
    "ds_read_b64_tr_b16 a[238:239], v13 offset:45056\n"
    "v_xor_b32 v13, 0x10000, v13\n"
    "v_mfma_f32_16x16x32_bf16 a[48:51], a[248:251], v[192:195], a[48:51]\n"
    "PD\n"
    "s_waitcnt lgkmcnt(4)\n"
    "v_exp_f32 v191, v191\n"
    "v_mfma_f32_16x16x32_bf16 a[64:67], a[224:227], v[192:195], a[64:67]\n"
    "PD\n"
    "ds_read_b64_tr_b16 a[240:241], v14 offset:32768\n"
    "v_cvt_pk_bf16_f32 v175, v190, v191\n"
    "v_mfma_f32_16x16x32_bf16 a[64:67], a[228:231], v[224:227], a[64:67]\n"
    "PD\n"
    "v_exp_f32 v184, v184\n"
    "v_mfma_f32_16x16x32_bf16 a[68:71], a[228:231], v[228:231], a[68:71]\n"
    "PD\n"
    "ds_read_b64_tr_b16 a[242:243], v14 offset:36864\n"
    "v_mfma_f32_16x16x32_bf16 a[68:71], a[224:227], v[196:199], a[68:71]\n"
    "PD\n"
    "v_exp_f32 v185, v185\n"
    "v_mfma_f32_16x16x32_bf16 a[72:75], a[224:227], v[200:203], a[72:75]\n"
    "PD\n"
    "ds_read_b64_tr_b16 a[244:245], v14 offset:40960\n"
    "v_cvt_pk_bf16_f32 v170, v184, v185\n"
    "v_mfma_f32_16x16x32_bf16 a[72:75], a[228:231], v[232:235], a[72:75]\n"
    "PD\n"
    "v_exp_f32 v186, v186\n"
    "v_mfma_f32_16x16x32_bf16 a[76:79], a[228:231], v[236:239], a[76:79]\n"
    "PD\n"
    "ds_read_b64_tr_b16 a[246:247], v14 offset:45056\n"
    "v_xor_b32 v14, 0x10000, v14\n"
    "v_mfma_f32_16x16x32_bf16 a[76:79], a[224:227], v[204:207], a[76:79]\n"
    "PD\n"
    "s_waitcnt lgkmcnt(4)\n"
    "v_exp_f32 v187, v187\n"
    "v_mfma_f32_16x16x32_bf16 a[92:95], a[232:235], v[204:207], a[92:95]\n"
    "PD\n"
    "ds_read_b64_tr_b16 a[248:249], v15 offset:32768\n"
    "v_cvt_pk_bf16_f32 v171, v186, v187\n"
    "v_mfma_f32_16x16x32_bf16 a[92:95], a[236:239], v[236:239], a[92:95]\n"
    "PD\n"
    "v_exp_f32 v180, v180\n"
    "v_mfma_f32_16x16x32_bf16 a[88:91], a[236:239], v[232:235], a[88:91]\n"
    "PD\n"
    "ds_read_b64_tr_b16 a[250:251], v15 offset:36864\n"
    "v_mfma_f32_16x16x32_bf16 a[88:91], a[232:235], v[200:203], a[88:91]\n"
    "PD\n"
    "v_exp_f32 v181, v181\n"
    "v_mfma_f32_16x16x32_bf16 a[84:87], a[232:235], v[196:199], a[84:87]\n"
    "PD\n"
    "ds_read_b64_tr_b16 a[252:253], v15 offset:40960\n"
    "v_cvt_pk_bf16_f32 v166, v180, v181\n"
    "v_mfma_f32_16x16x32_bf16 a[84:87], a[236:239], v[228:231], a[84:87]\n"
    "PD\n"
    "v_exp_f32 v182, v182\n"
    "v_mfma_f32_16x16x32_bf16 a[80:83], a[236:239], v[224:227], a[80:83]\n"
    "PD\n"
    "ds_read_b64_tr_b16 a[254:255], v15 offset:45056\n"
    "v_xor_b32 v15, 0x10000, v15\n"
    "v_mfma_f32_16x16x32_bf16 a[80:83], a[232:235], v[192:195], a[80:83]\n"
    "PD\n"
    "s_waitcnt lgkmcnt(4)\n"
    "v_exp_f32 v183, v183\n"
    "v_mfma_f32_16x16x32_bf16 a[96:99], a[240:243], v[192:195], a[96:99]\n"
    "PD\n"
    "v_exp_f32 v176, v176\n"
    "v_mfma_f32_16x16x32_bf16 a[96:99], a[244:247], v[224:227], a[96:99]\n"
    "PD\n"
    "v_exp_f32 v177, v177\n"
    "v_mfma_f32_16x16x32_bf16 a[100:103], a[244:247], v[228:231], a[100:103]\n"
    "PD\n"
    "v_exp_f32 v178, v178\n"
    "v_mfma_f32_16x16x32_bf16 a[100:103], a[240:243], v[196:199], a[100:103]\n"
    "PD\n"
    "v_exp_f32 v179, v179\n"
    "v_mfma_f32_16x16x32_bf16 a[104:107], a[240:243], v[200:203], a[104:107]\n"
    "PD\n"
    "v_cvt_pk_bf16_f32 v167, v182, v183\n"
    "v_cvt_pk_bf16_f32 v162, v176, v177\n"
    "v_mfma_f32_16x16x32_bf16 a[104:107], a[244:247], v[232:235], a[104:107]\n"
    "PD\n"
    "v_cvt_pk_bf16_f32 v163, v178, v179\n"
    "v_mfma_f32_16x16x32_bf16 a[108:111], a[244:247], v[236:239], a[108:111]\n"
    "PD\n"
    "v_mfma_f32_16x16x32_bf16 a[108:111], a[240:243], v[204:207], a[108:111]\n"
    "PD\n"
    "s_waitcnt lgkmcnt(0)\n"
    "s_waitcnt vmcnt(17)\n"
    "s_barrier\n"
    "v_mfma_f32_16x16x32_bf16 a[124:127], a[248:251], v[204:207], a[124:127]\n"
    "PD\n"
    "v_mfma_f32_16x16x32_bf16 a[124:127], a[252:255], v[236:239], a[124:127]\n"
    "PD\n"
    "ds_read_b128 a[128:131], v16 offset:49152\n"
    "v_mfma_f32_16x16x32_bf16 a[120:123], a[252:255], v[232:235], a[120:123]\n"
    "PD\n"
    "s_add_u32 m0, s61, 114688\n"
    "v_mfma_f32_16x16x32_bf16 a[120:123], a[248:251], v[200:203], a[120:123]\n"
    "PD\n"
    "ds_read_b128 a[132:135], v17 offset:49152\n"
    "v_mfma_f32_16x16x32_bf16 a[116:119], a[248:251], v[196:199], a[116:119]\n"
    "PD\n"
    "buffer_load_dwordx4 v28, s[48:51], s58 offen offset:0 lds\n"
    "v_mfma_f32_16x16x32_bf16 a[116:119], a[252:255], v[228:231], a[116:119]\n"
    "PD\n"
    "ds_read_b128 a[136:139], v18 offset:49152\n"
    "v_mfma_f32_16x16x32_bf16 a[112:115], a[252:255], v[224:227], a[112:115]\n"
    "PD\n"
    "buffer_load_dwordx4 v29, s[48:51], s58 offen offset:1024 lds\n"
    "v_mfma_f32_16x16x32_bf16 a[112:115], a[248:251], v[192:195], a[112:115]\n"
    "PD\n"
    "ds_read_b128 a[140:143], v19 offset:49152\n"
    "v_mfma_f32_16x16x32_bf16 a[192:195], a[208:211], v[192:195], a[192:195]\n"
    "PD\n"
    "buffer_load_dwordx4 v30, s[48:51], s58 offen offset:2048 lds\n"
    "v_mfma_f32_16x16x32_bf16 a[192:195], a[208:211], v[224:227], a[192:195]\n"
    "PD\n"
    "v_mfma_f32_16x16x32_bf16 a[196:199], a[208:211], v[196:199], a[196:199]\n"
    "PD\n"
    "buffer_load_dwordx4 v31, s[48:51], s58 offen offset:3072 lds\n"
    "v_mfma_f32_16x16x32_bf16 a[196:199], a[208:211], v[228:231], a[196:199]\n"
    "PD\n"
    "v_mfma_f32_16x16x32_bf16 a[200:203], a[208:211], v[200:203], a[200:203]\n"
    "PD\n"
    "s_add_u32 s58, s58, s59\n"
    "v_mfma_f32_16x16x32_bf16 a[200:203], a[208:211], v[232:235], a[200:203]\n"
    "PD\n"
    "s_add_u32 m0, s61, 98304\n"
    "v_mfma_f32_16x16x32_bf16 a[204:207], a[208:211], v[204:207], a[204:207]\n"
    "PD\n"
    "buffer_load_dwordx4 v24, s[44:47], s57 offen offset:0 lds\n"
    "v_mfma_f32_16x16x32_bf16 a[204:207], a[208:211], v[236:239], a[204:207]\n"
    "PD\n"
    "s_waitcnt lgkmcnt(0)\n"
    "v_mfma_f32_16x16x32_bf16 v[192:195], a[128:131], v[64:67], v[40:43]\n"
    "PD\n"
    "buffer_load_dwordx4 v25, s[44:47], s57 offen offset:1024 lds\n"
    "v_mfma_f32_16x16x32_bf16 v[192:195], a[132:135], v[68:71], v[192:195]\n"
    "PD\n"
    "ds_read_b128 a[144:147], v16 offset:53248\n"
    "v_mfma_f32_16x16x32_bf16 v[192:195], a[136:139], v[72:75], v[192:195]\n"
    "PD\n"
    "buffer_load_dwordx4 v26, s[44:47], s57 offen offset:2048 lds\n"
    "v_mfma_f32_16x16x32_bf16 v[192:195], a[140:143], v[76:79], v[192:195]\n"
    "PD\n"
    "ds_read_b128 a[148:151], v17 offset:53248\n"
    "v_mfma_f32_16x16x32_bf16 v[196:199], a[140:143], v[92:95], v[44:47]\n"
    "PD\n"
    "buffer_load_dwordx4 v27, s[44:47], s57 offen offset:3072 lds\n"
    "v_mfma_f32_16x16x32_bf16 v[196:199], a[136:139], v[88:91], v[196:199]\n"
    "PD\n"
    "ds_read_b128 a[152:155], v18 offset:53248\n"
    "v_mfma_f32_16x16x32_bf16 v[196:199], a[132:135], v[84:87], v[196:199]\n"
    "PD\n"
    "s_add_u32 s57, s57, s59\n"
    "v_mfma_f32_16x16x32_bf16 v[196:199], a[128:131], v[80:83], v[196:199]\n"
    "PD\n"
    "ds_read_b128 a[156:159], v19 offset:53248\n"
    "v_mfma_f32_16x16x32_bf16 v[200:203], a[128:131], v[96:99], v[48:51]\n"
    "PD\n"
    "global_load_dword v7, v2, s[74:75]\n"
    "v_mfma_f32_16x16x32_bf16 v[200:203], a[132:135], v[100:103], v[200:203]\n"
    "PD\n"
    "v_mfma_f32_16x16x32_bf16 v[200:203], a[136:139], v[104:107], v[200:203]\n"
    "PD\n"
    "s_lshl_b32 s62, s28, 8\n"
    "v_mfma_f32_16x16x32_bf16 v[200:203], a[140:143], v[108:111], v[200:203]\n"
    "PD\n"
    "s_add_u32 s44, s14, s62\n"
    "v_exp_f32 v192, v192\n"
    "v_mfma_f32_16x16x32_bf16 v[204:207], a[140:143], v[124:127], v[52:55]\n"
    "PD\n"
    "s_addc_u32 s45, s15, 0\n"
    "v_exp_f32 v193, v193\n"
    "v_mfma_f32_16x16x32_bf16 v[204:207], a[136:139], v[120:123], v[204:207]\n"
    "PD\n"
    "s_add_u32 s48, s16, s62\n"
    "v_exp_f32 v194, v194\n"
    "v_mfma_f32_16x16x32_bf16 v[204:207], a[132:135], v[116:119], v[204:207]\n"
    "PD\n"
    "s_addc_u32 s49, s17, 0\n"
    "v_exp_f32 v195, v195\n"
    "v_mfma_f32_16x16x32_bf16 v[204:207], a[128:131], v[112:115], v[204:207]\n"
    "PD\n"
    "s_waitcnt lgkmcnt(0)\n"
    "v_exp_f32 v196, v196\n"
    "v_mfma_f32_16x16x32_bf16 v[220:223], a[144:147], v[112:115], v[52:55]\n"
    "PD\n"
    "s_mov_b32 s59, 458752\n"
    "v_exp_f32 v197, v197\n"
    "v_mfma_f32_16x16x32_bf16 v[220:223], a[148:151], v[116:119], v[220:223]\n"
    "PD\n"
    "ds_read_b128 a[128:131], v16 offset:57344\n"
    "v_cvt_pk_bf16_f32 v192, v192, v193\n"
    "v_mfma_f32_16x16x32_bf16 v[220:223], a[152:155], v[120:123], v[220:223]\n"
    "PD\n"
    "s_mov_b32 s58, 0\n"
    "v_exp_f32 v198, v198\n"
    "v_mfma_f32_16x16x32_bf16 v[220:223], a[156:159], v[124:127], v[220:223]\n"
    "PD\n"
    "ds_read_b128 a[132:135], v17 offset:57344\n"
    "v_cvt_pk_bf16_f32 v193, v194, v195\n"
    "v_mfma_f32_16x16x32_bf16 v[216:219], a[156:159], v[108:111], v[48:51]\n"
    "PD\n"
    "s_sub_u32 s46, 7340032, s62\n"
    "v_exp_f32 v199, v199\n"
    "v_mfma_f32_16x16x32_bf16 v[216:219], a[152:155], v[104:107], v[216:219]\n"
    "PD\n"
    "ds_read_b128 a[136:139], v18 offset:57344\n"
    "v_cvt_pk_bf16_f32 v196, v196, v197\n"
    "v_mfma_f32_16x16x32_bf16 v[216:219], a[148:151], v[100:103], v[216:219]\n"
    "PD\n"
    "s_movk_i32 s68, 7168\n"
    "v_exp_f32 v200, v200\n"
    "v_mfma_f32_16x16x32_bf16 v[216:219], a[144:147], v[96:99], v[216:219]\n"
    "PD\n"
    "ds_read_b128 a[140:143], v19 offset:57344\n"
    "v_cvt_pk_bf16_f32 v197, v198, v199\n"
    "v_mfma_f32_16x16x32_bf16 v[212:215], a[144:147], v[80:83], v[44:47]\n"
    "PD\n"
    "s_cmp_eq_u32 s31, 2\n"
    "v_exp_f32 v201, v201\n"
    "v_mfma_f32_16x16x32_bf16 v[212:215], a[148:151], v[84:87], v[212:215]\n"
    "PD\n"
    "s_cmov_b64 s[44:45], s[34:35]\n"
    "v_exp_f32 v202, v202\n"
    "v_mfma_f32_16x16x32_bf16 v[212:215], a[152:155], v[88:91], v[212:215]\n"
    "PD\n"
    "s_cmov_b64 s[48:49], s[34:35]\n"
    "v_exp_f32 v203, v203\n"
    "v_mfma_f32_16x16x32_bf16 v[212:215], a[156:159], v[92:95], v[212:215]\n"
    "PD\n"
    "s_cmov_b32 s46, s90\n"
    "v_exp_f32 v204, v204\n"
    "v_mfma_f32_16x16x32_bf16 v[208:211], a[156:159], v[76:79], v[40:43]\n"
    "PD\n"
    "s_cmovk_i32 s68, 2048\n"
    "v_exp_f32 v205, v205\n"
    "v_mfma_f32_16x16x32_bf16 v[208:211], a[152:155], v[72:75], v[208:211]\n"
    "PD\n"
    "s_cmov_b32 s59, 262144\n"
    "v_cvt_pk_bf16_f32 v200, v200, v201\n"
    "v_cvt_pk_bf16_f32 v201, v202, v203\n"
    "v_mfma_f32_16x16x32_bf16 v[208:211], a[148:151], v[68:71], v[208:211]\n"
    "PD\n"
    "s_cmov_b32 s58, 131072\n"
    "v_exp_f32 v206, v206\n"
    "v_mfma_f32_16x16x32_bf16 v[208:211], a[144:147], v[64:67], v[208:211]\n"
    "PD\n"
    "s_waitcnt lgkmcnt(0)\n"
    "v_exp_f32 v207, v207\n"
    "v_mfma_f32_16x16x32_bf16 v[224:227], a[128:131], v[64:67], v[40:43]\n"
    "PD\n"
    "s_mov_b32 s50, s46\n"
    "v_exp_f32 v220, v220\n"
    "v_mfma_f32_16x16x32_bf16 v[224:227], a[132:135], v[68:71], v[224:227]\n"
    "PD\n"
    "ds_read_b128 a[144:147], v16 offset:61440\n"
    "v_xor_b32 v16, 0x10000, v16\n"
    "v_mfma_f32_16x16x32_bf16 v[224:227], a[136:139], v[72:75], v[224:227]\n"
    "PD\n"
    "s_mov_b32 s57, 0\n"
    "v_exp_f32 v221, v221\n"
    "v_mfma_f32_16x16x32_bf16 v[224:227], a[140:143], v[76:79], v[224:227]\n"
    "PD\n"
    "ds_read_b128 a[148:151], v17 offset:61440\n"
    "v_xor_b32 v17, 0x10000, v17\n"
    "v_mfma_f32_16x16x32_bf16 v[228:231], a[140:143], v[92:95], v[44:47]\n"
    "PD\n"
    "s_lshl_b32 s62, s68, 2\n"
    "v_cvt_pk_bf16_f32 v204, v204, v205\n"
    "v_cvt_pk_bf16_f32 v205, v206, v207\n"
    "v_mfma_f32_16x16x32_bf16 v[228:231], a[136:139], v[88:91], v[228:231]\n"
    "PD\n"
    "ds_read_b128 a[152:155], v18 offset:61440\n"
    "v_xor_b32 v18, 0x10000, v18\n"
    "v_mfma_f32_16x16x32_bf16 v[228:231], a[132:135], v[84:87], v[228:231]\n"
    "PD\n"
    "s_sub_u32 s62, s62, 0x400\n"
    "v_exp_f32 v222, v222\n"
    "v_mfma_f32_16x16x32_bf16 v[228:231], a[128:131], v[80:83], v[228:231]\n"
    "PD\n"
    "ds_read_b128 a[156:159], v19 offset:61440\n"
    "v_xor_b32 v19, 0x10000, v19\n"
    "v_mfma_f32_16x16x32_bf16 v[232:235], a[128:131], v[96:99], v[48:51]\n"
    "PD\n"
    "s_lshl_b32 s63, s62, 1\n"
    "v_exp_f32 v223, v223\n"
    "v_mfma_f32_16x16x32_bf16 v[232:235], a[132:135], v[100:103], v[232:235]\n"
    "PD\n"
    "v_exp_f32 v216, v216\n"
    "v_mfma_f32_16x16x32_bf16 v[232:235], a[136:139], v[104:107], v[232:235]\n"
    "PD\n"
    "v_exp_f32 v217, v217\n"
    "v_mfma_f32_16x16x32_bf16 v[232:235], a[140:143], v[108:111], v[232:235]\n"
    "PD\n"
    "v_cvt_pk_bf16_f32 v206, v220, v221\n"
    "v_cvt_pk_bf16_f32 v207, v222, v223\n"
    "v_mfma_f32_16x16x32_bf16 v[236:239], a[140:143], v[124:127], v[52:55]\n"
    "PD\n"
    "v_exp_f32 v218, v218\n"
    "v_mfma_f32_16x16x32_bf16 v[236:239], a[136:139], v[120:123], v[236:239]\n"
    "PD\n"
    "v_exp_f32 v219, v219\n"
    "v_mfma_f32_16x16x32_bf16 v[236:239], a[132:135], v[116:119], v[236:239]\n"
    "PD\n"
    "ds_read_b64_tr_b16 a[224:225], v8 offset:0\n"
    "v_mad_u32_u24 v24, v6, s68, v32\n"
    "v_mfma_f32_16x16x32_bf16 v[236:239], a[128:131], v[112:115], v[236:239]\n"
    "PD\n"
    "s_waitcnt lgkmcnt(1)\n"
    "v_exp_f32 v212, v212\n"
    "v_mfma_f32_16x16x32_bf16 v[252:255], a[144:147], v[112:115], v[52:55]\n"
    "PD\n"
    "ds_read_b64_tr_b16 a[226:227], v8 offset:4096\n"
    "v_mad_u32_u24 v25, v6, s68, v33\n"
    "v_mfma_f32_16x16x32_bf16 v[252:255], a[148:151], v[116:119], v[252:255]\n"
    "PD\n"
    "v_exp_f32 v213, v213\n"
    "v_mfma_f32_16x16x32_bf16 v[252:255], a[152:155], v[120:123], v[252:255]\n"
    "PD\n"
    "ds_read_b64_tr_b16 a[228:229], v8 offset:8192\n"
    "v_add_u32 v25, s62, v25\n"
    "v_mfma_f32_16x16x32_bf16 v[252:255], a[156:159], v[124:127], v[252:255]\n"
    "PD\n"
    "v_cvt_pk_bf16_f32 v202, v216, v217\n"
    "v_cvt_pk_bf16_f32 v203, v218, v219\n"
    "v_mfma_f32_16x16x32_bf16 v[248:251], a[156:159], v[108:111], v[48:51]\n"
    "PD\n"
    "ds_read_b64_tr_b16 a[230:231], v8 offset:12288\n"
    "v_add_u32 v26, s63, v24\n"
    "v_mfma_f32_16x16x32_bf16 v[248:251], a[152:155], v[104:107], v[248:251]\n"
    "PD\n"
    "v_exp_f32 v214, v214\n"
    "v_mfma_f32_16x16x32_bf16 v[248:251], a[148:151], v[100:103], v[248:251]\n"
    "PD\n"
    "ds_read_b64_tr_b16 a[232:233], v9 offset:0\n"
    "v_add_u32 v27, s63, v25\n"
    "v_mfma_f32_16x16x32_bf16 v[248:251], a[144:147], v[96:99], v[248:251]\n"
    "PD\n"
    "v_exp_f32 v215, v215\n"
    "v_mfma_f32_16x16x32_bf16 v[244:247], a[144:147], v[80:83], v[44:47]\n"
    "PD\n"
    "ds_read_b64_tr_b16 a[234:235], v9 offset:4096\n"
    "v_mov_b32 v28, v24\n"
    "v_mfma_f32_16x16x32_bf16 v[244:247], a[148:151], v[84:87], v[244:247]\n"
    "PD\n"
    "v_exp_f32 v208, v208\n"
    "v_mfma_f32_16x16x32_bf16 v[244:247], a[152:155], v[88:91], v[244:247]\n"
    "PD\n"
    "ds_read_b64_tr_b16 a[236:237], v9 offset:8192\n"
    "v_mov_b32 v29, v25\n"
    "v_mfma_f32_16x16x32_bf16 v[244:247], a[156:159], v[92:95], v[244:247]\n"
    "PD\n"
    "v_exp_f32 v209, v209\n"
    "v_mfma_f32_16x16x32_bf16 v[240:243], a[156:159], v[76:79], v[40:43]\n"
    "PD\n"
    "ds_read_b64_tr_b16 a[238:239], v9 offset:12288\n"
    "v_mov_b32 v30, v26\n"
    "v_mfma_f32_16x16x32_bf16 v[240:243], a[152:155], v[72:75], v[240:243]\n"
    "PD\n"
    "v_cvt_pk_bf16_f32 v198, v212, v213\n"
    "v_cvt_pk_bf16_f32 v199, v214, v215\n"
    "v_mfma_f32_16x16x32_bf16 v[240:243], a[148:151], v[68:71], v[240:243]\n"
    "PD\n"
    "v_exp_f32 v210, v210\n"
    "v_mfma_f32_16x16x32_bf16 v[240:243], a[144:147], v[64:67], v[240:243]\n"
    "PD\n"
    "s_waitcnt lgkmcnt(4)\n"
    "v_exp_f32 v211, v211\n"
    "v_mfma_f32_16x16x32_bf16 a[0:3], a[224:227], v[128:131], a[0:3]\n"
    "PD\n"
    "ds_read_b64_tr_b16 a[240:241], v10 offset:0\n"
    "v_mov_b32 v31, v27\n"
    "v_mfma_f32_16x16x32_bf16 a[0:3], a[228:231], v[160:163], a[0:3]\n"
    "PD\n"
    "s_mov_b32 s86, s91\n"
    "v_exp_f32 v224, v224\n"
    "v_mfma_f32_16x16x32_bf16 a[4:7], a[228:231], v[164:167], a[4:7]\n"
    "PD\n"
    "ds_read_b64_tr_b16 a[242:243], v10 offset:4096\n"
    "v_min_u32 v38, 6, v0\n"
    "v_mfma_f32_16x16x32_bf16 a[4:7], a[224:227], v[132:135], a[4:7]\n"
    "PD\n"
    "v_exp_f32 v225, v225\n"
    "v_mfma_f32_16x16x32_bf16 a[8:11], a[224:227], v[136:139], a[8:11]\n"
    "PD\n"
    "ds_read_b64_tr_b16 a[244:245], v10 offset:8192\n"
    "v_lshlrev_b32 v38, 7, v38\n"
    "v_mfma_f32_16x16x32_bf16 a[8:11], a[228:231], v[168:171], a[8:11]\n"
    "PD\n"
    "buffer_load_dwordx2 v[36:37], v38, s[80:83], s86 offen sc1\n"
    "v_cvt_pk_bf16_f32 v194, v208, v209\n"
    "v_cvt_pk_bf16_f32 v195, v210, v211\n"
    "v_mfma_f32_16x16x32_bf16 a[12:15], a[228:231], v[172:175], a[12:15]\n"
    "PD\n"
    "ds_read_b64_tr_b16 a[246:247], v10 offset:12288\n"
    "v_cvt_pk_bf16_f32 v224, v224, v225\n"
    "v_mfma_f32_16x16x32_bf16 a[12:15], a[224:227], v[140:143], a[12:15]\n"
    "PD\n"
    "s_waitcnt lgkmcnt(4)\n"
    "v_exp_f32 v226, v226\n"
    "v_mfma_f32_16x16x32_bf16 a[28:31], a[232:235], v[140:143], a[28:31]\n"
    "PD\n"
    "ds_read_b64_tr_b16 a[248:249], v11 offset:0\n"
    "v_mfma_f32_16x16x32_bf16 a[28:31], a[236:239], v[172:175], a[28:31]\n"
    "PD\n"
    "v_exp_f32 v227, v227\n"
    "v_mfma_f32_16x16x32_bf16 a[24:27], a[236:239], v[168:171], a[24:27]\n"
    "PD\n"
    "ds_read_b64_tr_b16 a[250:251], v11 offset:4096\n"
    "v_cvt_pk_bf16_f32 v225, v226, v227\n"
    "v_mfma_f32_16x16x32_bf16 a[24:27], a[232:235], v[136:139], a[24:27]\n"
    "PD\n"
    "v_exp_f32 v228, v228\n"
    "v_mfma_f32_16x16x32_bf16 a[20:23], a[232:235], v[132:135], a[20:23]\n"
    "PD\n"
    "ds_read_b64_tr_b16 a[252:253], v11 offset:8192\n"
    "v_mfma_f32_16x16x32_bf16 a[20:23], a[236:239], v[164:167], a[20:23]\n"
    "PD\n"
    "v_exp_f32 v229, v229\n"
    "v_mfma_f32_16x16x32_bf16 a[16:19], a[236:239], v[160:163], a[16:19]\n"
    "PD\n"
    "ds_read_b64_tr_b16 a[254:255], v11 offset:12288\n"
    "v_cvt_pk_bf16_f32 v228, v228, v229\n"
    "v_mfma_f32_16x16x32_bf16 a[16:19], a[232:235], v[128:131], a[16:19]\n"
    "PD\n"
    "s_waitcnt lgkmcnt(4)\n"
    "v_exp_f32 v230, v230\n"
    "v_mfma_f32_16x16x32_bf16 a[32:35], a[240:243], v[128:131], a[32:35]\n"
    "PD\n"
    "ds_read_b64_tr_b16 a[224:225], v12 offset:0\n"
    "v_mfma_f32_16x16x32_bf16 a[32:35], a[244:247], v[160:163], a[32:35]\n"
    "PD\n"
    "v_exp_f32 v231, v231\n"
    "v_mfma_f32_16x16x32_bf16 a[36:39], a[244:247], v[164:167], a[36:39]\n"
    "PD\n"
    "ds_read_b64_tr_b16 a[226:227], v12 offset:4096\n"
    "v_cvt_pk_bf16_f32 v229, v230, v231\n"
    "v_mfma_f32_16x16x32_bf16 a[36:39], a[240:243], v[132:135], a[36:39]\n"
    "PD\n"
    "v_exp_f32 v232, v232\n"
    "v_mfma_f32_16x16x32_bf16 a[40:43], a[240:243], v[136:139], a[40:43]\n"
    "PD\n"
    "ds_read_b64_tr_b16 a[228:229], v12 offset:8192\n"
    "v_mfma_f32_16x16x32_bf16 a[40:43], a[244:247], v[168:171], a[40:43]\n"
    "PD\n"
    "v_exp_f32 v233, v233\n"
    "v_mfma_f32_16x16x32_bf16 a[44:47], a[244:247], v[172:175], a[44:47]\n"
    "PD\n"
    "ds_read_b64_tr_b16 a[230:231], v12 offset:12288\n"
    "v_cvt_pk_bf16_f32 v232, v232, v233\n"
    "v_mfma_f32_16x16x32_bf16 a[44:47], a[240:243], v[140:143], a[44:47]\n"
    "PD\n"
    "s_waitcnt lgkmcnt(4)\n"
    "v_exp_f32 v234, v234\n"
    "v_mfma_f32_16x16x32_bf16 a[60:63], a[248:251], v[140:143], a[60:63]\n"
    "PD\n"
    "ds_read_b64_tr_b16 a[232:233], v13 offset:0\n"
    "v_mfma_f32_16x16x32_bf16 a[60:63], a[252:255], v[172:175], a[60:63]\n"
    "PD\n"
    "v_exp_f32 v235, v235\n"
    "v_mfma_f32_16x16x32_bf16 a[56:59], a[252:255], v[168:171], a[56:59]\n"
    "PD\n"
    "ds_read_b64_tr_b16 a[234:235], v13 offset:4096\n"
    "v_cvt_pk_bf16_f32 v233, v234, v235\n"
    "v_mfma_f32_16x16x32_bf16 a[56:59], a[248:251], v[136:139], a[56:59]\n"
    "PD\n"
    "v_exp_f32 v236, v236\n"
    "v_mfma_f32_16x16x32_bf16 a[52:55], a[248:251], v[132:135], a[52:55]\n"
    "PD\n"
    "ds_read_b64_tr_b16 a[236:237], v13 offset:8192\n"
    "v_mfma_f32_16x16x32_bf16 a[52:55], a[252:255], v[164:167], a[52:55]\n"
    "PD\n"
    "v_exp_f32 v237, v237\n"
    "v_mfma_f32_16x16x32_bf16 a[48:51], a[252:255], v[160:163], a[48:51]\n"
    "PD\n"
    "ds_read_b64_tr_b16 a[238:239], v13 offset:12288\n"
    "v_cvt_pk_bf16_f32 v236, v236, v237\n"
    "v_mfma_f32_16x16x32_bf16 a[48:51], a[248:251], v[128:131], a[48:51]\n"
    "PD\n"
    "s_waitcnt lgkmcnt(4)\n"
    "v_exp_f32 v238, v238\n"
    "v_mfma_f32_16x16x32_bf16 a[64:67], a[224:227], v[128:131], a[64:67]\n"
    "PD\n"
    "ds_read_b64_tr_b16 a[240:241], v14 offset:0\n"
    "v_mfma_f32_16x16x32_bf16 a[64:67], a[228:231], v[160:163], a[64:67]\n"
    "PD\n"
    "v_exp_f32 v239, v239\n"
    "v_mfma_f32_16x16x32_bf16 a[68:71], a[228:231], v[164:167], a[68:71]\n"
    "PD\n"
    "ds_read_b64_tr_b16 a[242:243], v14 offset:4096\n"
    "v_cvt_pk_bf16_f32 v237, v238, v239\n"
    "v_mfma_f32_16x16x32_bf16 a[68:71], a[224:227], v[132:135], a[68:71]\n"
    "PD\n"
    "v_exp_f32 v252, v252\n"
    "v_mfma_f32_16x16x32_bf16 a[72:75], a[224:227], v[136:139], a[72:75]\n"
    "PD\n"
    "ds_read_b64_tr_b16 a[244:245], v14 offset:8192\n"
    "v_mfma_f32_16x16x32_bf16 a[72:75], a[228:231], v[168:171], a[72:75]\n"
    "PD\n"
    "v_exp_f32 v253, v253\n"
    "v_mfma_f32_16x16x32_bf16 a[76:79], a[228:231], v[172:175], a[76:79]\n"
    "PD\n"
    "ds_read_b64_tr_b16 a[246:247], v14 offset:12288\n"
    "v_cvt_pk_bf16_f32 v238, v252, v253\n"
    "v_mfma_f32_16x16x32_bf16 a[76:79], a[224:227], v[140:143], a[76:79]\n"
    "PD\n"
    "s_waitcnt lgkmcnt(4)\n"
    "v_exp_f32 v254, v254\n"
    "v_mfma_f32_16x16x32_bf16 a[92:95], a[232:235], v[140:143], a[92:95]\n"
    "PD\n"
    "ds_read_b64_tr_b16 a[248:249], v15 offset:0\n"
    "v_mfma_f32_16x16x32_bf16 a[92:95], a[236:239], v[172:175], a[92:95]\n"
    "PD\n"
    "v_exp_f32 v255, v255\n"
    "v_mfma_f32_16x16x32_bf16 a[88:91], a[236:239], v[168:171], a[88:91]\n"
    "PD\n"
    "ds_read_b64_tr_b16 a[250:251], v15 offset:4096\n"
    "v_cvt_pk_bf16_f32 v239, v254, v255\n"
    "v_mfma_f32_16x16x32_bf16 a[88:91], a[232:235], v[136:139], a[88:91]\n"
    "PD\n"
    "v_exp_f32 v248, v248\n"
    "v_mfma_f32_16x16x32_bf16 a[84:87], a[232:235], v[132:135], a[84:87]\n"
    "PD\n"
    "ds_read_b64_tr_b16 a[252:253], v15 offset:8192\n"
    "v_mfma_f32_16x16x32_bf16 a[84:87], a[236:239], v[164:167], a[84:87]\n"
    "PD\n"
    "v_exp_f32 v249, v249\n"
    "v_mfma_f32_16x16x32_bf16 a[80:83], a[236:239], v[160:163], a[80:83]\n"
    "PD\n"
    "ds_read_b64_tr_b16 a[254:255], v15 offset:12288\n"
    "v_cvt_pk_bf16_f32 v234, v248, v249\n"
    "v_mfma_f32_16x16x32_bf16 a[80:83], a[232:235], v[128:131], a[80:83]\n"
    "PD\n"
    "s_waitcnt lgkmcnt(4)\n"
    "v_exp_f32 v250, v250\n"
    "v_mfma_f32_16x16x32_bf16 a[96:99], a[240:243], v[128:131], a[96:99]\n"
    "PD\n"
    "v_exp_f32 v251, v251\n"
    "v_mfma_f32_16x16x32_bf16 a[96:99], a[244:247], v[160:163], a[96:99]\n"
    "PD\n"
    "v_exp_f32 v244, v244\n"
    "v_mfma_f32_16x16x32_bf16 a[100:103], a[244:247], v[164:167], a[100:103]\n"
    "PD\n"
    "v_exp_f32 v245, v245\n"
    "v_mfma_f32_16x16x32_bf16 a[100:103], a[240:243], v[132:135], a[100:103]\n"
    "PD\n"
    "v_exp_f32 v246, v246\n"
    "v_mfma_f32_16x16x32_bf16 a[104:107], a[240:243], v[136:139], a[104:107]\n"
    "PD\n"
    "v_exp_f32 v247, v247\n"
    "v_mfma_f32_16x16x32_bf16 a[104:107], a[244:247], v[168:171], a[104:107]\n"
    "PD\n"
    "v_cvt_pk_bf16_f32 v235, v250, v251\n"
    "v_cvt_pk_bf16_f32 v230, v244, v245\n"
    "v_mfma_f32_16x16x32_bf16 a[108:111], a[244:247], v[172:175], a[108:111]\n"
    "PD\n"
    "v_exp_f32 v240, v240\n"
    "v_mfma_f32_16x16x32_bf16 a[108:111], a[240:243], v[140:143], a[108:111]\n"
    "PD\n"
    "s_waitcnt lgkmcnt(0)\n"
    "s_waitcnt vmcnt(14)\n"
    "s_barrier\n"
    "v_cvt_pk_bf16_f32 v231, v246, v247\n"
    "v_mfma_f32_16x16x32_bf16 a[124:127], a[248:251], v[140:143], a[124:127]\n"
    "PD\n"
    "v_exp_f32 v241, v241\n"
    "v_mfma_f32_16x16x32_bf16 a[124:127], a[252:255], v[172:175], a[124:127]\n"
    "PD\n"
    "ds_read_b128 a[128:131], v16 offset:16384\n"
    "v_cvt_pk_bf16_f32 v226, v240, v241\n"
    "v_mfma_f32_16x16x32_bf16 a[120:123], a[252:255], v[168:171], a[120:123]\n"
    "PD\n"
    "s_add_u32 m0, s61, 0\n"
    "v_exp_f32 v242, v242\n"
    "v_mfma_f32_16x16x32_bf16 a[120:123], a[248:251], v[136:139], a[120:123]\n"
    "PD\n"
    "ds_read_b128 a[132:135], v17 offset:16384\n"
    "v_mfma_f32_16x16x32_bf16 a[116:119], a[248:251], v[132:135], a[116:119]\n"
    "PD\n"
    "buffer_load_dwordx4 v24, s[44:47], s57 offen offset:0 lds\n"
    "v_exp_f32 v243, v243\n"
    "v_mfma_f32_16x16x32_bf16 a[116:119], a[252:255], v[164:167], a[116:119]\n"
    "PD\n"
    "ds_read_b128 a[136:139], v18 offset:16384\n"
    "v_cvt_pk_bf16_f32 v227, v242, v243\n"
    "v_mfma_f32_16x16x32_bf16 a[112:115], a[252:255], v[160:163], a[112:115]\n"
    "PD\n"
    "buffer_load_dwordx4 v25, s[44:47], s57 offen offset:1024 lds\n"
    "v_mfma_f32_16x16x32_bf16 a[112:115], a[248:251], v[128:131], a[112:115]\n"
    "PD\n"
    "ds_read_b128 a[140:143], v19 offset:16384\n"
    "v_mfma_f32_16x16x32_bf16 a[192:195], a[208:211], v[128:131], a[192:195]\n"
    "PD\n"
    "buffer_load_dwordx4 v26, s[44:47], s57 offen offset:2048 lds\n"
    "v_mfma_f32_16x16x32_bf16 a[192:195], a[208:211], v[160:163], a[192:195]\n"
    "PD\n"
    "v_mfma_f32_16x16x32_bf16 a[196:199], a[208:211], v[132:135], a[196:199]\n"
    "PD\n"
    "buffer_load_dwordx4 v27, s[44:47], s57 offen offset:3072 lds\n"
    "v_mfma_f32_16x16x32_bf16 a[196:199], a[208:211], v[164:167], a[196:199]\n"
    "PD\n"
    "v_mfma_f32_16x16x32_bf16 a[200:203], a[208:211], v[136:139], a[200:203]\n"
    "PD\n"
    "s_add_u32 s57, s57, s59\n"
    "v_mfma_f32_16x16x32_bf16 a[200:203], a[208:211], v[168:171], a[200:203]\n"
    "PD\n"
    "s_add_u32 m0, s61, 16384\n"
    "v_mfma_f32_16x16x32_bf16 a[204:207], a[208:211], v[140:143], a[204:207]\n"
    "PD\n"
    "buffer_load_dwordx4 v28, s[48:51], s58 offen offset:0 lds\n"
    "v_mfma_f32_16x16x32_bf16 a[204:207], a[208:211], v[172:175], a[204:207]\n"
    "PD\n"
    "s_waitcnt lgkmcnt(0)\n"
    "v_mfma_f32_16x16x32_bf16 v[128:131], a[128:131], v[64:67], v[40:43]\n"
    "PD\n"
    "buffer_load_dwordx4 v29, s[48:51], s58 offen offset:1024 lds\n"
    "v_mfma_f32_16x16x32_bf16 v[128:131], a[132:135], v[68:71], v[128:131]\n"
    "PD\n"
    "ds_read_b128 a[144:147], v16 offset:20480\n"
    "v_mfma_f32_16x16x32_bf16 v[128:131], a[136:139], v[72:75], v[128:131]\n"
    "PD\n"
    "buffer_load_dwordx4 v30, s[48:51], s58 offen offset:2048 lds\n"
    "v_mfma_f32_16x16x32_bf16 v[128:131], a[140:143], v[76:79], v[128:131]\n"
    "PD\n"
    "ds_read_b128 a[148:151], v17 offset:20480\n"
    "v_mfma_f32_16x16x32_bf16 v[132:135], a[140:143], v[92:95], v[44:47]\n"
    "PD\n"
    "buffer_load_dwordx4 v31, s[48:51], s58 offen offset:3072 lds\n"
    "v_mfma_f32_16x16x32_bf16 v[132:135], a[136:139], v[88:91], v[132:135]\n"
    "PD\n"
    "ds_read_b128 a[152:155], v18 offset:20480\n"
    "v_mfma_f32_16x16x32_bf16 v[132:135], a[132:135], v[84:87], v[132:135]\n"
    "PD\n"
    "s_add_u32 s58, s58, s59\n"
    "v_mfma_f32_16x16x32_bf16 v[132:135], a[128:131], v[80:83], v[132:135]\n"
    "PD\n"
    "ds_read_b128 a[156:159], v19 offset:20480\n"
    "v_mfma_f32_16x16x32_bf16 v[136:139], a[128:131], v[96:99], v[48:51]\n"
    "PD\n"
    "global_load_dword v7, v2, s[76:77]\n"
    "v_mfma_f32_16x16x32_bf16 v[136:139], a[132:135], v[100:103], v[136:139]\n"
    "PD\n"
    "v_mfma_f32_16x16x32_bf16 v[136:139], a[136:139], v[104:107], v[136:139]\n"
    "PD\n"
    "s_mul_i32 s62, s26, 525312\n"
    "v_mfma_f32_16x16x32_bf16 v[136:139], a[140:143], v[108:111], v[136:139]\n"
    "PD\n"
    "s_lshl_b32 s63, s27, 14\n"
    "v_exp_f32 v128, v128\n"
    "v_mfma_f32_16x16x32_bf16 v[140:143], a[140:143], v[124:127], v[52:55]\n"
    "PD\n"
    "s_add_u32 s62, s62, s63\n"
    "v_exp_f32 v129, v129\n"
    "v_mfma_f32_16x16x32_bf16 v[140:143], a[136:139], v[120:123], v[140:143]\n"
    "PD\n"
    "s_add_u32 s62, s62, 5242880\n"
    "v_exp_f32 v130, v130\n"
    "v_mfma_f32_16x16x32_bf16 v[140:143], a[132:135], v[116:119], v[140:143]\n"
    "PD\n"
    "s_lshl_b32 s84, s27, 7\n"
    "v_exp_f32 v131, v131\n"
    "v_mfma_f32_16x16x32_bf16 v[140:143], a[128:131], v[112:115], v[140:143]\n"
    "PD\n"
    "s_waitcnt lgkmcnt(0)\n"
    "v_exp_f32 v132, v132\n"
    "v_mfma_f32_16x16x32_bf16 v[156:159], a[144:147], v[112:115], v[52:55]\n"
    "PD\n"
    "s_add_u32 s84, s84, 1152\n"
    "v_exp_f32 v133, v133\n"
    "v_mfma_f32_16x16x32_bf16 v[156:159], a[148:151], v[116:119], v[156:159]\n"
    "PD\n"
    "ds_read_b128 a[128:131], v16 offset:24576\n"
    "v_cvt_pk_bf16_f32 v128, v128, v129\n"
    "v_mfma_f32_16x16x32_bf16 v[156:159], a[152:155], v[120:123], v[156:159]\n"
    "PD\n"
    "s_add_u32 s52, s22, s62\n"
    "v_exp_f32 v134, v134\n"
    "v_mfma_f32_16x16x32_bf16 v[156:159], a[156:159], v[124:127], v[156:159]\n"
    "PD\n"
    "ds_read_b128 a[132:135], v17 offset:24576\n"
    "v_cvt_pk_bf16_f32 v129, v130, v131\n"
    "v_mfma_f32_16x16x32_bf16 v[152:155], a[156:159], v[108:111], v[48:51]\n"
    "PD\n"
    "s_addc_u32 s53, s23, 0\n"
    "v_exp_f32 v135, v135\n"
    "v_mfma_f32_16x16x32_bf16 v[152:155], a[152:155], v[104:107], v[152:155]\n"
    "PD\n"
    "ds_read_b128 a[136:139], v18 offset:24576\n"
    "v_cvt_pk_bf16_f32 v132, v132, v133\n"
    "v_mfma_f32_16x16x32_bf16 v[152:155], a[148:151], v[100:103], v[152:155]\n"
    "PD\n"
    "s_movk_i32 s54, 0x4000\n"
    "v_exp_f32 v136, v136\n"
    "v_mfma_f32_16x16x32_bf16 v[152:155], a[144:147], v[96:99], v[152:155]\n"
    "PD\n"
    "ds_read_b128 a[140:143], v19 offset:24576\n"
    "v_cvt_pk_bf16_f32 v133, v134, v135\n"
    "v_mfma_f32_16x16x32_bf16 v[148:151], a[144:147], v[80:83], v[44:47]\n"
    "PD\n"
    "v_exp_f32 v137, v137\n"
    "v_mfma_f32_16x16x32_bf16 v[148:151], a[148:151], v[84:87], v[148:151]\n"
    "PD\n"
    "v_exp_f32 v138, v138\n"
    "v_mfma_f32_16x16x32_bf16 v[148:151], a[152:155], v[88:91], v[148:151]\n"
    "PD\n"
    "v_exp_f32 v139, v139\n"
    "v_mfma_f32_16x16x32_bf16 v[148:151], a[156:159], v[92:95], v[148:151]\n"
    "PD\n"
    "v_exp_f32 v140, v140\n"
    "v_mfma_f32_16x16x32_bf16 v[144:147], a[156:159], v[76:79], v[40:43]\n"
    "PD\n"
    "v_exp_f32 v141, v141\n"
    "v_mfma_f32_16x16x32_bf16 v[144:147], a[152:155], v[72:75], v[144:147]\n"
    "PD\n"
    "v_cvt_pk_bf16_f32 v136, v136, v137\n"
    "v_cvt_pk_bf16_f32 v137, v138, v139\n"
    "v_mfma_f32_16x16x32_bf16 v[144:147], a[148:151], v[68:71], v[144:147]\n"
    "PD\n"
    "v_exp_f32 v142, v142\n"
    "v_mfma_f32_16x16x32_bf16 v[144:147], a[144:147], v[64:67], v[144:147]\n"
    "PD\n"
    "s_waitcnt lgkmcnt(0)\n"
    "v_exp_f32 v143, v143\n"
    "v_mfma_f32_16x16x32_bf16 v[160:163], a[128:131], v[64:67], v[40:43]\n"
    "PD\n"
    "v_exp_f32 v156, v156\n"
    "v_mfma_f32_16x16x32_bf16 v[160:163], a[132:135], v[68:71], v[160:163]\n"
    "PD\n"
    "ds_read_b128 a[144:147], v16 offset:28672\n"
    "v_cvt_pk_bf16_f32 v140, v140, v141\n"
    "v_mfma_f32_16x16x32_bf16 v[160:163], a[136:139], v[72:75], v[160:163]\n"
    "PD\n"
    "v_exp_f32 v157, v157\n"
    "v_mfma_f32_16x16x32_bf16 v[160:163], a[140:143], v[76:79], v[160:163]\n"
    "PD\n"
    "ds_read_b128 a[148:151], v17 offset:28672\n"
    "v_cvt_pk_bf16_f32 v141, v142, v143\n"
    "v_mfma_f32_16x16x32_bf16 v[164:167], a[140:143], v[92:95], v[44:47]\n"
    "PD\n"
    "v_exp_f32 v158, v158\n"
    "v_mfma_f32_16x16x32_bf16 v[164:167], a[136:139], v[88:91], v[164:167]\n"
    "PD\n"
    "ds_read_b128 a[152:155], v18 offset:28672\n"
    "v_cvt_pk_bf16_f32 v142, v156, v157\n"
    "v_mfma_f32_16x16x32_bf16 v[164:167], a[132:135], v[84:87], v[164:167]\n"
    "PD\n"
    "v_exp_f32 v159, v159\n"
    "v_mfma_f32_16x16x32_bf16 v[164:167], a[128:131], v[80:83], v[164:167]\n"
    "PD\n"
    "ds_read_b128 a[156:159], v19 offset:28672\n"
    "v_cvt_pk_bf16_f32 v143, v158, v159\n"
    "v_mfma_f32_16x16x32_bf16 v[168:171], a[128:131], v[96:99], v[48:51]\n"
    "PD\n"
    "v_exp_f32 v152, v152\n"
    "v_mfma_f32_16x16x32_bf16 v[168:171], a[132:135], v[100:103], v[168:171]\n"
    "PD\n"
    "v_exp_f32 v153, v153\n"
    "v_mfma_f32_16x16x32_bf16 v[168:171], a[136:139], v[104:107], v[168:171]\n"
    "PD\n"
    "v_exp_f32 v154, v154\n"
    "v_mfma_f32_16x16x32_bf16 v[168:171], a[140:143], v[108:111], v[168:171]\n"
    "PD\n"
    "v_exp_f32 v155, v155\n"
    "v_mfma_f32_16x16x32_bf16 v[172:175], a[140:143], v[124:127], v[52:55]\n"
    "PD\n"
    "v_exp_f32 v148, v148\n"
    "v_mfma_f32_16x16x32_bf16 v[172:175], a[136:139], v[120:123], v[172:175]\n"
    "PD\n"
    "v_exp_f32 v149, v149\n"
    "v_mfma_f32_16x16x32_bf16 v[172:175], a[132:135], v[116:119], v[172:175]\n"
    "PD\n"
    "ds_read_b64_tr_b16 a[224:225], v8 offset:32768\n"
    "v_cvt_pk_bf16_f32 v138, v152, v153\n"
    "v_mfma_f32_16x16x32_bf16 v[172:175], a[128:131], v[112:115], v[172:175]\n"
    "PD\n"
    "s_waitcnt lgkmcnt(1)\n"
    "v_exp_f32 v150, v150\n"
    "v_mfma_f32_16x16x32_bf16 v[188:191], a[144:147], v[112:115], v[52:55]\n"
    "PD\n"
    "ds_read_b64_tr_b16 a[226:227], v8 offset:36864\n"
    "v_cvt_pk_bf16_f32 v139, v154, v155\n"
    "v_mfma_f32_16x16x32_bf16 v[188:191], a[148:151], v[116:119], v[188:191]\n"
    "PD\n"
    "v_exp_f32 v151, v151\n"
    "v_mfma_f32_16x16x32_bf16 v[188:191], a[152:155], v[120:123], v[188:191]\n"
    "PD\n"
    "ds_read_b64_tr_b16 a[228:229], v8 offset:40960\n"
    "v_cvt_pk_bf16_f32 v134, v148, v149\n"
    "v_mfma_f32_16x16x32_bf16 v[188:191], a[156:159], v[124:127], v[188:191]\n"
    "PD\n"
    "v_exp_f32 v144, v144\n"
    "v_mfma_f32_16x16x32_bf16 v[184:187], a[156:159], v[108:111], v[48:51]\n"
    "PD\n"
    "ds_read_b64_tr_b16 a[230:231], v8 offset:45056\n"
    "v_xor_b32 v8, 0x10000, v8\n"
    "v_mfma_f32_16x16x32_bf16 v[184:187], a[152:155], v[104:107], v[184:187]\n"
    "PD\n"
    "v_exp_f32 v145, v145\n"
    "v_mfma_f32_16x16x32_bf16 v[184:187], a[148:151], v[100:103], v[184:187]\n"
    "PD\n"
    "ds_read_b64_tr_b16 a[232:233], v9 offset:32768\n"
    "v_cvt_pk_bf16_f32 v135, v150, v151\n"
    "v_mfma_f32_16x16x32_bf16 v[184:187], a[144:147], v[96:99], v[184:187]\n"
    "PD\n"
    "v_exp_f32 v146, v146\n"
    "v_mfma_f32_16x16x32_bf16 v[180:183], a[144:147], v[80:83], v[44:47]\n"
    "PD\n"
    "ds_read_b64_tr_b16 a[234:235], v9 offset:36864\n"
    "v_cvt_pk_bf16_f32 v130, v144, v145\n"
    "v_mfma_f32_16x16x32_bf16 v[180:183], a[148:151], v[84:87], v[180:183]\n"
    "PD\n"
    "v_exp_f32 v147, v147\n"
    "v_mfma_f32_16x16x32_bf16 v[180:183], a[152:155], v[88:91], v[180:183]\n"
    "PD\n"
    "ds_read_b64_tr_b16 a[236:237], v9 offset:40960\n"
    "v_cvt_pk_bf16_f32 v131, v146, v147\n"
    "v_mfma_f32_16x16x32_bf16 v[180:183], a[156:159], v[92:95], v[180:183]\n"
    "PD\n"
    "v_exp_f32 v160, v160\n"
    "v_mfma_f32_16x16x32_bf16 v[176:179], a[156:159], v[76:79], v[40:43]\n"
    "PD\n"
    "ds_read_b64_tr_b16 a[238:239], v9 offset:45056\n"
    "v_xor_b32 v9, 0x10000, v9\n"
    "v_mfma_f32_16x16x32_bf16 v[176:179], a[152:155], v[72:75], v[176:179]\n"
    "PD\n"
    "v_exp_f32 v161, v161\n"
    "v_mfma_f32_16x16x32_bf16 v[176:179], a[148:151], v[68:71], v[176:179]\n"
    "PD\n"
    "v_exp_f32 v162, v162\n"
    "v_mfma_f32_16x16x32_bf16 v[176:179], a[144:147], v[64:67], v[176:179]\n"
    "PD\n"
    "s_waitcnt lgkmcnt(4)\n"
    "v_exp_f32 v163, v163\n"
    "v_mfma_f32_16x16x32_bf16 a[0:3], a[224:227], v[192:195], a[0:3]\n"
    "PD\n"
    "ds_read_b64_tr_b16 a[240:241], v10 offset:32768\n"
    "v_cvt_pk_bf16_f32 v160, v160, v161\n"
    "v_mfma_f32_16x16x32_bf16 a[0:3], a[228:231], v[224:227], a[0:3]\n"
    "PD\n"
    "v_exp_f32 v164, v164\n"
    "v_mfma_f32_16x16x32_bf16 a[4:7], a[228:231], v[228:231], a[4:7]\n"
    "PD\n"
    "ds_read_b64_tr_b16 a[242:243], v10 offset:36864\n"
    "v_cvt_pk_bf16_f32 v161, v162, v163\n"
    "v_mfma_f32_16x16x32_bf16 a[4:7], a[224:227], v[196:199], a[4:7]\n"
    "PD\n"
    "v_exp_f32 v165, v165\n"
    "v_mfma_f32_16x16x32_bf16 a[8:11], a[224:227], v[200:203], a[8:11]\n"
    "PD\n"
    "ds_read_b64_tr_b16 a[244:245], v10 offset:40960\n"
    "v_cvt_pk_bf16_f32 v164, v164, v165\n"
    "v_mfma_f32_16x16x32_bf16 a[8:11], a[228:231], v[232:235], a[8:11]\n"
    "PD\n"
    "v_exp_f32 v166, v166\n"
    "v_mfma_f32_16x16x32_bf16 a[12:15], a[228:231], v[236:239], a[12:15]\n"
    "PD\n"
    "ds_read_b64_tr_b16 a[246:247], v10 offset:45056\n"
    "v_xor_b32 v10, 0x10000, v10\n"
    "v_mfma_f32_16x16x32_bf16 a[12:15], a[224:227], v[204:207], a[12:15]\n"
    "PD\n"
    "s_waitcnt lgkmcnt(4)\n"
    "v_exp_f32 v167, v167\n"
    "v_mfma_f32_16x16x32_bf16 a[28:31], a[232:235], v[204:207], a[28:31]\n"
    "PD\n"
    "ds_read_b64_tr_b16 a[248:249], v11 offset:32768\n"
    "v_cvt_pk_bf16_f32 v165, v166, v167\n"
    "v_mfma_f32_16x16x32_bf16 a[28:31], a[236:239], v[236:239], a[28:31]\n"
    "PD\n"
    "v_exp_f32 v168, v168\n"
    "v_mfma_f32_16x16x32_bf16 a[24:27], a[236:239], v[232:235], a[24:27]\n"
    "PD\n"
    "ds_read_b64_tr_b16 a[250:251], v11 offset:36864\n"
    "v_mfma_f32_16x16x32_bf16 a[24:27], a[232:235], v[200:203], a[24:27]\n"
    "PD\n"
    "v_exp_f32 v169, v169\n"
    "v_mfma_f32_16x16x32_bf16 a[20:23], a[232:235], v[196:199], a[20:23]\n"
    "PD\n"
    "ds_read_b64_tr_b16 a[252:253], v11 offset:40960\n"
    "v_cvt_pk_bf16_f32 v168, v168, v169\n"
    "v_mfma_f32_16x16x32_bf16 a[20:23], a[236:239], v[228:231], a[20:23]\n"
    "PD\n"
    "v_exp_f32 v170, v170\n"
    "v_mfma_f32_16x16x32_bf16 a[16:19], a[236:239], v[224:227], a[16:19]\n"
    "PD\n"
    "ds_read_b64_tr_b16 a[254:255], v11 offset:45056\n"
    "v_xor_b32 v11, 0x10000, v11\n"
    "v_mfma_f32_16x16x32_bf16 a[16:19], a[232:235], v[192:195], a[16:19]\n"
    "PD\n"
    "s_waitcnt lgkmcnt(4)\n"
    "v_exp_f32 v171, v171\n"
    "v_mfma_f32_16x16x32_bf16 a[32:35], a[240:243], v[192:195], a[32:35]\n"
    "PD\n"
    "ds_read_b64_tr_b16 a[224:225], v12 offset:32768\n"
    "v_cvt_pk_bf16_f32 v169, v170, v171\n"
    "v_mfma_f32_16x16x32_bf16 a[32:35], a[244:247], v[224:227], a[32:35]\n"
    "PD\n"
    "v_exp_f32 v172, v172\n"
    "v_mfma_f32_16x16x32_bf16 a[36:39], a[244:247], v[228:231], a[36:39]\n"
    "PD\n"
    "ds_read_b64_tr_b16 a[226:227], v12 offset:36864\n"
    "v_mfma_f32_16x16x32_bf16 a[36:39], a[240:243], v[196:199], a[36:39]\n"
    "PD\n"
    "v_exp_f32 v173, v173\n"
    "v_mfma_f32_16x16x32_bf16 a[40:43], a[240:243], v[200:203], a[40:43]\n"
    "PD\n"
    "ds_read_b64_tr_b16 a[228:229], v12 offset:40960\n"
    "v_cvt_pk_bf16_f32 v172, v172, v173\n"
    "v_mfma_f32_16x16x32_bf16 a[40:43], a[244:247], v[232:235], a[40:43]\n"
    "PD\n"
    "v_exp_f32 v174, v174\n"
    "v_mfma_f32_16x16x32_bf16 a[44:47], a[244:247], v[236:239], a[44:47]\n"
    "PD\n"
    "ds_read_b64_tr_b16 a[230:231], v12 offset:45056\n"
    "v_xor_b32 v12, 0x10000, v12\n"
    "v_mfma_f32_16x16x32_bf16 a[44:47], a[240:243], v[204:207], a[44:47]\n"
    "PD\n"
    "s_waitcnt lgkmcnt(4)\n"
    "v_exp_f32 v175, v175\n"
    "v_mfma_f32_16x16x32_bf16 a[60:63], a[248:251], v[204:207], a[60:63]\n"
    "PD\n"
    "ds_read_b64_tr_b16 a[232:233], v13 offset:32768\n"
    "v_cvt_pk_bf16_f32 v173, v174, v175\n"
    "v_mfma_f32_16x16x32_bf16 a[60:63], a[252:255], v[236:239], a[60:63]\n"
    "PD\n"
    "v_exp_f32 v188, v188\n"
    "v_mfma_f32_16x16x32_bf16 a[56:59], a[252:255], v[232:235], a[56:59]\n"
    "PD\n"
    "ds_read_b64_tr_b16 a[234:235], v13 offset:36864\n"
    "v_mfma_f32_16x16x32_bf16 a[56:59], a[248:251], v[200:203], a[56:59]\n"
    "PD\n"
    "v_exp_f32 v189, v189\n"
    "v_mfma_f32_16x16x32_bf16 a[52:55], a[248:251], v[196:199], a[52:55]\n"
    "PD\n"
    "ds_read_b64_tr_b16 a[236:237], v13 offset:40960\n"
    "v_cvt_pk_bf16_f32 v174, v188, v189\n"
    "v_mfma_f32_16x16x32_bf16 a[52:55], a[252:255], v[228:231], a[52:55]\n"
    "PD\n"
    "v_exp_f32 v190, v190\n"
    "v_mfma_f32_16x16x32_bf16 a[48:51], a[252:255], v[224:227], a[48:51]\n"
    "PD\n"
    "ds_read_b64_tr_b16 a[238:239], v13 offset:45056\n"
    "v_xor_b32 v13, 0x10000, v13\n"
    "v_mfma_f32_16x16x32_bf16 a[48:51], a[248:251], v[192:195], a[48:51]\n"
    "PD\n"
    "s_waitcnt lgkmcnt(4)\n"
    "v_exp_f32 v191, v191\n"
    "v_mfma_f32_16x16x32_bf16 a[64:67], a[224:227], v[192:195], a[64:67]\n"
    "PD\n"
    "ds_read_b64_tr_b16 a[240:241], v14 offset:32768\n"
    "v_cvt_pk_bf16_f32 v175, v190, v191\n"
    "v_mfma_f32_16x16x32_bf16 a[64:67], a[228:231], v[224:227], a[64:67]\n"
    "PD\n"
    "v_exp_f32 v184, v184\n"
    "v_mfma_f32_16x16x32_bf16 a[68:71], a[228:231], v[228:231], a[68:71]\n"
    "PD\n"
    "ds_read_b64_tr_b16 a[242:243], v14 offset:36864\n"
    "v_mfma_f32_16x16x32_bf16 a[68:71], a[224:227], v[196:199], a[68:71]\n"
    "PD\n"
    "v_exp_f32 v185, v185\n"
    "v_mfma_f32_16x16x32_bf16 a[72:75], a[224:227], v[200:203], a[72:75]\n"
    "PD\n"
    "ds_read_b64_tr_b16 a[244:245], v14 offset:40960\n"
    "v_cvt_pk_bf16_f32 v170, v184, v185\n"
    "v_mfma_f32_16x16x32_bf16 a[72:75], a[228:231], v[232:235], a[72:75]\n"
    "PD\n"
    "v_exp_f32 v186, v186\n"
    "v_mfma_f32_16x16x32_bf16 a[76:79], a[228:231], v[236:239], a[76:79]\n"
    "PD\n"
    "ds_read_b64_tr_b16 a[246:247], v14 offset:45056\n"
    "v_xor_b32 v14, 0x10000, v14\n"
    "v_mfma_f32_16x16x32_bf16 a[76:79], a[224:227], v[204:207], a[76:79]\n"
    "PD\n"
    "s_waitcnt lgkmcnt(4)\n"
    "v_exp_f32 v187, v187\n"
    "v_mfma_f32_16x16x32_bf16 a[92:95], a[232:235], v[204:207], a[92:95]\n"
    "PD\n"
    "ds_read_b64_tr_b16 a[248:249], v15 offset:32768\n"
    "v_cvt_pk_bf16_f32 v171, v186, v187\n"
    "v_mfma_f32_16x16x32_bf16 a[92:95], a[236:239], v[236:239], a[92:95]\n"
    "PD\n"
    "v_exp_f32 v180, v180\n"
    "v_mfma_f32_16x16x32_bf16 a[88:91], a[236:239], v[232:235], a[88:91]\n"
    "PD\n"
    "ds_read_b64_tr_b16 a[250:251], v15 offset:36864\n"
    "v_mfma_f32_16x16x32_bf16 a[88:91], a[232:235], v[200:203], a[88:91]\n"
    "PD\n"
    "v_exp_f32 v181, v181\n"
    "v_mfma_f32_16x16x32_bf16 a[84:87], a[232:235], v[196:199], a[84:87]\n"
    "PD\n"
    "ds_read_b64_tr_b16 a[252:253], v15 offset:40960\n"
    "v_cvt_pk_bf16_f32 v166, v180, v181\n"
    "v_mfma_f32_16x16x32_bf16 a[84:87], a[236:239], v[228:231], a[84:87]\n"
    "PD\n"
    "v_exp_f32 v182, v182\n"
    "v_mfma_f32_16x16x32_bf16 a[80:83], a[236:239], v[224:227], a[80:83]\n"
    "PD\n"
    "ds_read_b64_tr_b16 a[254:255], v15 offset:45056\n"
    "v_xor_b32 v15, 0x10000, v15\n"
    "v_mfma_f32_16x16x32_bf16 a[80:83], a[232:235], v[192:195], a[80:83]\n"
    "PD\n"
    "s_waitcnt lgkmcnt(4)\n"
    "v_exp_f32 v183, v183\n"
    "v_mfma_f32_16x16x32_bf16 a[96:99], a[240:243], v[192:195], a[96:99]\n"
    "PD\n"
    "v_exp_f32 v176, v176\n"
    "v_mfma_f32_16x16x32_bf16 a[96:99], a[244:247], v[224:227], a[96:99]\n"
    "PD\n"
    "v_exp_f32 v177, v177\n"
    "v_mfma_f32_16x16x32_bf16 a[100:103], a[244:247], v[228:231], a[100:103]\n"
    "PD\n"
    "v_exp_f32 v178, v178\n"
    "v_mfma_f32_16x16x32_bf16 a[100:103], a[240:243], v[196:199], a[100:103]\n"
    "PD\n"
    "v_exp_f32 v179, v179\n"
    "v_mfma_f32_16x16x32_bf16 a[104:107], a[240:243], v[200:203], a[104:107]\n"
    "PD\n"
    "v_cvt_pk_bf16_f32 v167, v182, v183\n"
    "v_cvt_pk_bf16_f32 v162, v176, v177\n"
    "v_mfma_f32_16x16x32_bf16 a[104:107], a[244:247], v[232:235], a[104:107]\n"
    "PD\n"
    "v_cvt_pk_bf16_f32 v163, v178, v179\n"
    "v_mfma_f32_16x16x32_bf16 a[108:111], a[244:247], v[236:239], a[108:111]\n"
    "PD\n"
    "v_mfma_f32_16x16x32_bf16 a[108:111], a[240:243], v[204:207], a[108:111]\n"
    "PD\n"
    "s_waitcnt lgkmcnt(0)\n"
    "s_waitcnt vmcnt(11)\n"
    "s_barrier\n"
    "v_mfma_f32_16x16x32_bf16 a[124:127], a[248:251], v[204:207], a[124:127]\n"
    "PD\n"
    "v_mfma_f32_16x16x32_bf16 a[124:127], a[252:255], v[236:239], a[124:127]\n"
    "PD\n"
    "ds_read_b128 a[128:131], v16 offset:49152\n"
    "v_mfma_f32_16x16x32_bf16 a[120:123], a[252:255], v[232:235], a[120:123]\n"
    "PD\n"
    "s_add_u32 m0, s61, 32768\n"
    "v_mfma_f32_16x16x32_bf16 a[120:123], a[248:251], v[200:203], a[120:123]\n"
    "PD\n"
    "ds_read_b128 a[132:135], v17 offset:49152\n"
    "v_mfma_f32_16x16x32_bf16 a[116:119], a[248:251], v[196:199], a[116:119]\n"
    "PD\n"
    "buffer_load_dwordx4 v24, s[44:47], s57 offen offset:0 lds\n"
    "v_mfma_f32_16x16x32_bf16 a[116:119], a[252:255], v[228:231], a[116:119]\n"
    "PD\n"
    "ds_read_b128 a[136:139], v18 offset:49152\n"
    "v_mfma_f32_16x16x32_bf16 a[112:115], a[252:255], v[224:227], a[112:115]\n"
    "PD\n"
    "buffer_load_dwordx4 v25, s[44:47], s57 offen offset:1024 lds\n"
    "v_mfma_f32_16x16x32_bf16 a[112:115], a[248:251], v[192:195], a[112:115]\n"
    "PD\n"
    "ds_read_b128 a[140:143], v19 offset:49152\n"
    "v_mfma_f32_16x16x32_bf16 a[192:195], a[208:211], v[192:195], a[192:195]\n"
    "PD\n"
    "buffer_load_dwordx4 v26, s[44:47], s57 offen offset:2048 lds\n"
    "v_mfma_f32_16x16x32_bf16 a[192:195], a[208:211], v[224:227], a[192:195]\n"
    "PD\n"
    "v_mfma_f32_16x16x32_bf16 a[196:199], a[208:211], v[196:199], a[196:199]\n"
    "PD\n"
    "buffer_load_dwordx4 v27, s[44:47], s57 offen offset:3072 lds\n"
    "v_mfma_f32_16x16x32_bf16 a[196:199], a[208:211], v[228:231], a[196:199]\n"
    "PD\n"
    "v_mfma_f32_16x16x32_bf16 a[200:203], a[208:211], v[200:203], a[200:203]\n"
    "PD\n"
    "s_add_u32 s57, s57, s59\n"
    "v_mfma_f32_16x16x32_bf16 a[200:203], a[208:211], v[232:235], a[200:203]\n"
    "PD\n"
    "s_add_u32 m0, s61, 49152\n"
    "v_mfma_f32_16x16x32_bf16 a[204:207], a[208:211], v[204:207], a[204:207]\n"
    "PD\n"
    "buffer_load_dwordx4 v28, s[48:51], s58 offen offset:0 lds\n"
    "v_mfma_f32_16x16x32_bf16 a[204:207], a[208:211], v[236:239], a[204:207]\n"
    "PD\n"
    "s_waitcnt lgkmcnt(0)\n"
    "v_mfma_f32_16x16x32_bf16 v[192:195], a[128:131], v[64:67], v[40:43]\n"
    "PD\n"
    "buffer_load_dwordx4 v29, s[48:51], s58 offen offset:1024 lds\n"
    "v_mfma_f32_16x16x32_bf16 v[192:195], a[132:135], v[68:71], v[192:195]\n"
    "PD\n"
    "ds_read_b128 a[144:147], v16 offset:53248\n"
    "v_mfma_f32_16x16x32_bf16 v[192:195], a[136:139], v[72:75], v[192:195]\n"
    "PD\n"
    "buffer_load_dwordx4 v30, s[48:51], s58 offen offset:2048 lds\n"
    "v_mfma_f32_16x16x32_bf16 v[192:195], a[140:143], v[76:79], v[192:195]\n"
    "PD\n"
    "ds_read_b128 a[148:151], v17 offset:53248\n"
    "v_mfma_f32_16x16x32_bf16 v[196:199], a[140:143], v[92:95], v[44:47]\n"
    "PD\n"
    "buffer_load_dwordx4 v31, s[48:51], s58 offen offset:3072 lds\n"
    "v_mfma_f32_16x16x32_bf16 v[196:199], a[136:139], v[88:91], v[196:199]\n"
    "PD\n"
    "ds_read_b128 a[152:155], v18 offset:53248\n"
    "v_mfma_f32_16x16x32_bf16 v[196:199], a[132:135], v[84:87], v[196:199]\n"
    "PD\n"
    "s_add_u32 s58, s58, s59\n"
    "v_mfma_f32_16x16x32_bf16 v[196:199], a[128:131], v[80:83], v[196:199]\n"
    "PD\n"
    "ds_read_b128 a[156:159], v19 offset:53248\n"
    "s_waitcnt vmcnt(17)\n"
    "v_cmp_ne_u64 vcc, v[36:37], v[34:35]\n"
    "v_mfma_f32_16x16x32_bf16 v[200:203], a[128:131], v[96:99], v[48:51]\n"
    "PD\n"
    "s_lshl_b32 s62, s29, 11\n"
    "v_mfma_f32_16x16x32_bf16 v[200:203], a[132:135], v[100:103], v[200:203]\n"
    "PD\n"
    "s_lshl_b32 s63, s30, 7\n"
    "v_mfma_f32_16x16x32_bf16 v[200:203], a[136:139], v[104:107], v[200:203]\n"
    "PD\n"
    "s_cmp_lg_u32 s31, 2\n"
    "v_mfma_f32_16x16x32_bf16 v[200:203], a[140:143], v[108:111], v[200:203]\n"
    "PD\n"
    "s_cmov_b32 s62, 0\n"
    "v_exp_f32 v192, v192\n"
    "v_mfma_f32_16x16x32_bf16 v[204:207], a[140:143], v[124:127], v[52:55]\n"
    "PD\n"
    "s_cmov_b64 vcc, 0\n"
    "v_exp_f32 v193, v193\n"
    "v_mfma_f32_16x16x32_bf16 v[204:207], a[136:139], v[120:123], v[204:207]\n"
    "PD\n"
    "s_mov_b32 s56, 0\n"
    "v_exp_f32 v194, v194\n"
    "v_mfma_f32_16x16x32_bf16 v[204:207], a[132:135], v[116:119], v[204:207]\n"
    "PD\n"
    "s_cbranch_vccnz H_Ax\n"
    "R_Ax:\n"
    "v_exp_f32 v195, v195\n"
    "v_mfma_f32_16x16x32_bf16 v[204:207], a[128:131], v[112:115], v[204:207]\n"
    "PD\n"
    "s_waitcnt lgkmcnt(0)\n"
    "v_exp_f32 v196, v196\n"
    "v_mfma_f32_16x16x32_bf16 v[220:223], a[144:147], v[112:115], v[52:55]\n"
    "PD\n"
    "s_cmp_eq_u32 s31, 2\n"
    "v_exp_f32 v197, v197\n"
    "v_mfma_f32_16x16x32_bf16 v[220:223], a[148:151], v[116:119], v[220:223]\n"
    "PD\n"
    "ds_read_b128 a[128:131], v16 offset:57344\n"
    "v_cvt_pk_bf16_f32 v192, v192, v193\n"
    "v_mfma_f32_16x16x32_bf16 v[220:223], a[152:155], v[120:123], v[220:223]\n"
    "PD\n"
    "s_cmov_b64 s[40:41], s[78:79]\n"
    "v_exp_f32 v198, v198\n"
    "v_mfma_f32_16x16x32_bf16 v[220:223], a[156:159], v[124:127], v[220:223]\n"
    "PD\n"
    "ds_read_b128 a[132:135], v17 offset:57344\n"
    "v_cvt_pk_bf16_f32 v193, v194, v195\n"
    "v_mfma_f32_16x16x32_bf16 v[216:219], a[156:159], v[108:111], v[48:51]\n"
    "PD\n"
    "s_cmov_b32 s42, s63\n"
    "v_exp_f32 v199, v199\n"
    "v_mfma_f32_16x16x32_bf16 v[216:219], a[152:155], v[104:107], v[216:219]\n"
    "PD\n"
    "ds_read_b128 a[136:139], v18 offset:57344\n"
    "v_xor_b32 v20, s62, v4\n"
    "v_mfma_f32_16x16x32_bf16 v[216:219], a[148:151], v[100:103], v[216:219]\n"
    "PD\n"
    "v_exp_f32 v200, v200\n"
    "v_mfma_f32_16x16x32_bf16 v[216:219], a[144:147], v[96:99], v[216:219]\n"
    "PD\n"
    "ds_read_b128 a[140:143], v19 offset:57344\n"
    "v_xor_b32 v21, 2048, v20\n"
    "v_mfma_f32_16x16x32_bf16 v[212:215], a[144:147], v[80:83], v[44:47]\n"
    "PD\n"
    "v_exp_f32 v201, v201\n"
    "v_mfma_f32_16x16x32_bf16 v[212:215], a[148:151], v[84:87], v[212:215]\n"
    "PD\n"
    "v_cvt_pk_bf16_f32 v196, v196, v197\n"
    "v_cvt_pk_bf16_f32 v197, v198, v199\n"
    "v_mfma_f32_16x16x32_bf16 v[212:215], a[152:155], v[88:91], v[212:215]\n"
    "PD\n"
    "v_exp_f32 v202, v202\n"
    "v_mfma_f32_16x16x32_bf16 v[212:215], a[156:159], v[92:95], v[212:215]\n"
    "PD\n"
    "v_exp_f32 v203, v203\n"
    "v_mfma_f32_16x16x32_bf16 v[208:211], a[156:159], v[76:79], v[40:43]\n"
    "PD\n"
    "v_exp_f32 v204, v204\n"
    "v_mfma_f32_16x16x32_bf16 v[208:211], a[152:155], v[72:75], v[208:211]\n"
    "PD\n"
    "v_exp_f32 v205, v205\n"
    "v_mfma_f32_16x16x32_bf16 v[208:211], a[148:151], v[68:71], v[208:211]\n"
    "PD\n"
    "v_cvt_pk_bf16_f32 v200, v200, v201\n"
    "v_cvt_pk_bf16_f32 v201, v202, v203\n"
    "v_mfma_f32_16x16x32_bf16 v[208:211], a[144:147], v[64:67], v[208:211]\n"
    "PD\n"
    "s_waitcnt lgkmcnt(0)\n"
    "v_exp_f32 v206, v206\n"
    "v_mfma_f32_16x16x32_bf16 v[224:227], a[128:131], v[64:67], v[40:43]\n"
    "PD\n"
    "v_exp_f32 v207, v207\n"
    "v_mfma_f32_16x16x32_bf16 v[224:227], a[132:135], v[68:71], v[224:227]\n"
    "PD\n"
    "ds_read_b128 a[144:147], v16 offset:61440\n"
    "v_xor_b32 v16, 0x10000, v16\n"
    "v_mfma_f32_16x16x32_bf16 v[224:227], a[136:139], v[72:75], v[224:227]\n"
    "PD\n"
    "v_exp_f32 v220, v220\n"
    "v_mfma_f32_16x16x32_bf16 v[224:227], a[140:143], v[76:79], v[224:227]\n"
    "PD\n"
    "ds_read_b128 a[148:151], v17 offset:61440\n"
    "v_xor_b32 v17, 0x10000, v17\n"
    "v_mfma_f32_16x16x32_bf16 v[228:231], a[140:143], v[92:95], v[44:47]\n"
    "PD\n"
    "v_exp_f32 v221, v221\n"
    "v_mfma_f32_16x16x32_bf16 v[228:231], a[136:139], v[88:91], v[228:231]\n"
    "PD\n"
    "ds_read_b128 a[152:155], v18 offset:61440\n"
    "v_xor_b32 v18, 0x10000, v18\n"
    "v_mfma_f32_16x16x32_bf16 v[228:231], a[132:135], v[84:87], v[228:231]\n"
    "PD\n"
    "v_cvt_pk_bf16_f32 v204, v204, v205\n"
    "v_cvt_pk_bf16_f32 v205, v206, v207\n"
    "v_mfma_f32_16x16x32_bf16 v[228:231], a[128:131], v[80:83], v[228:231]\n"
    "PD\n"
    "ds_read_b128 a[156:159], v19 offset:61440\n"
    "v_xor_b32 v19, 0x10000, v19\n"
    "v_mfma_f32_16x16x32_bf16 v[232:235], a[128:131], v[96:99], v[48:51]\n"
    "PD\n"
    "v_exp_f32 v222, v222\n"
    "v_mfma_f32_16x16x32_bf16 v[232:235], a[132:135], v[100:103], v[232:235]\n"
    "PD\n"
    "v_exp_f32 v223, v223\n"
    "v_mfma_f32_16x16x32_bf16 v[232:235], a[136:139], v[104:107], v[232:235]\n"
    "PD\n"
    "v_exp_f32 v216, v216\n"
    "v_mfma_f32_16x16x32_bf16 v[232:235], a[140:143], v[108:111], v[232:235]\n"
    "PD\n"
    "v_exp_f32 v217, v217\n"
    "v_mfma_f32_16x16x32_bf16 v[236:239], a[140:143], v[124:127], v[52:55]\n"
    "PD\n"
    "v_cvt_pk_bf16_f32 v206, v220, v221\n"
    "v_cvt_pk_bf16_f32 v207, v222, v223\n"
    "v_mfma_f32_16x16x32_bf16 v[236:239], a[136:139], v[120:123], v[236:239]\n"
    "PD\n"
    "v_exp_f32 v218, v218\n"
    "v_mfma_f32_16x16x32_bf16 v[236:239], a[132:135], v[116:119], v[236:239]\n"
    "PD\n"
    "ds_read_b64_tr_b16 a[224:225], v8 offset:0\n"
    "v_xor_b32 v22, 4096, v20\n"
    "v_mfma_f32_16x16x32_bf16 v[236:239], a[128:131], v[112:115], v[236:239]\n"
    "PD\n"
    "s_waitcnt lgkmcnt(1)\n"
    "v_exp_f32 v219, v219\n"
    "v_mfma_f32_16x16x32_bf16 v[252:255], a[144:147], v[112:115], v[52:55]\n"
    "PD\n"
    "ds_read_b64_tr_b16 a[226:227], v8 offset:4096\n"
    "v_xor_b32 v23, 6144, v20\n"
    "v_mfma_f32_16x16x32_bf16 v[252:255], a[148:151], v[116:119], v[252:255]\n"
    "PD\n"
    "v_exp_f32 v212, v212\n"
    "v_mfma_f32_16x16x32_bf16 v[252:255], a[152:155], v[120:123], v[252:255]\n"
    "PD\n"
    "ds_read_b64_tr_b16 a[228:229], v8 offset:8192\n"
    "v_cvt_pk_bf16_f32 v202, v216, v217\n"
    "v_mfma_f32_16x16x32_bf16 v[252:255], a[156:159], v[124:127], v[252:255]\n"
    "PD\n"
    "v_exp_f32 v213, v213\n"
    "v_mfma_f32_16x16x32_bf16 v[248:251], a[156:159], v[108:111], v[48:51]\n"
    "PD\n"
    "ds_read_b64_tr_b16 a[230:231], v8 offset:12288\n"
    "v_cvt_pk_bf16_f32 v203, v218, v219\n"
    "v_mfma_f32_16x16x32_bf16 v[248:251], a[152:155], v[104:107], v[248:251]\n"
    "PD\n"
    "v_exp_f32 v214, v214\n"
    "v_mfma_f32_16x16x32_bf16 v[248:251], a[148:151], v[100:103], v[248:251]\n"
    "PD\n"
    "ds_read_b64_tr_b16 a[232:233], v9 offset:0\n"
    "v_cvt_pk_bf16_f32 v198, v212, v213\n"
    "v_mfma_f32_16x16x32_bf16 v[248:251], a[144:147], v[96:99], v[248:251]\n"
    "PD\n"
    "v_exp_f32 v215, v215\n"
    "v_mfma_f32_16x16x32_bf16 v[244:247], a[144:147], v[80:83], v[44:47]\n"
    "PD\n"
    "ds_read_b64_tr_b16 a[234:235], v9 offset:4096\n"
    "v_cvt_pk_bf16_f32 v199, v214, v215\n"
    "v_mfma_f32_16x16x32_bf16 v[244:247], a[148:151], v[84:87], v[244:247]\n"
    "PD\n"
    "v_exp_f32 v208, v208\n"
    "v_mfma_f32_16x16x32_bf16 v[244:247], a[152:155], v[88:91], v[244:247]\n"
    "PD\n"
    "ds_read_b64_tr_b16 a[236:237], v9 offset:8192\n"
    "v_mfma_f32_16x16x32_bf16 v[244:247], a[156:159], v[92:95], v[244:247]\n"
    "PD\n"
    "v_exp_f32 v209, v209\n"
    "v_mfma_f32_16x16x32_bf16 v[240:243], a[156:159], v[76:79], v[40:43]\n"
    "PD\n"
    "ds_read_b64_tr_b16 a[238:239], v9 offset:12288\n"
    "v_cvt_pk_bf16_f32 v194, v208, v209\n"
    "v_mfma_f32_16x16x32_bf16 v[240:243], a[152:155], v[72:75], v[240:243]\n"
    "PD\n"
    "v_exp_f32 v210, v210\n"
    "v_mfma_f32_16x16x32_bf16 v[240:243], a[148:151], v[68:71], v[240:243]\n"
    "PD\n"
    "v_exp_f32 v211, v211\n"
    "v_mfma_f32_16x16x32_bf16 v[240:243], a[144:147], v[64:67], v[240:243]\n"
    "PD\n"
    "s_waitcnt lgkmcnt(4)\n"
    "v_exp_f32 v224, v224\n"
    "v_mfma_f32_16x16x32_bf16 a[0:3], a[224:227], v[128:131], a[0:3]\n"
    "PD\n"
    "ds_read_b64_tr_b16 a[240:241], v10 offset:0\n"
    "v_cvt_pk_bf16_f32 v195, v210, v211\n"
    "v_mfma_f32_16x16x32_bf16 a[0:3], a[228:231], v[160:163], a[0:3]\n"
    "PD\n"
    "buffer_load_dwordx4 a[128:131], v20, s[40:43], s56 offen offset:0 sc1\n"
    "v_exp_f32 v225, v225\n"
    "v_mfma_f32_16x16x32_bf16 a[4:7], a[228:231], v[164:167], a[4:7]\n"
    "PD\n"
    "ds_read_b64_tr_b16 a[242:243], v10 offset:4096\n"
    "v_cvt_pk_bf16_f32 v224, v224, v225\n"
    "v_mfma_f32_16x16x32_bf16 a[4:7], a[224:227], v[132:135], a[4:7]\n"
    "PD\n"
    "buffer_load_dwordx4 a[144:147], v20, s[40:43], s56 offen offset:1024 sc1\n"
    "v_exp_f32 v226, v226\n"
    "v_mfma_f32_16x16x32_bf16 a[8:11], a[224:227], v[136:139], a[8:11]\n"
    "PD\n"
    "ds_read_b64_tr_b16 a[244:245], v10 offset:8192\n"
    "v_mfma_f32_16x16x32_bf16 a[8:11], a[228:231], v[168:171], a[8:11]\n"
    "PD\n"
    "buffer_load_dwordx4 a[132:135], v21, s[40:43], s56 offen offset:0 sc1\n"
    "v_exp_f32 v227, v227\n"
    "v_mfma_f32_16x16x32_bf16 a[12:15], a[228:231], v[172:175], a[12:15]\n"
    "PD\n"
    "ds_read_b64_tr_b16 a[246:247], v10 offset:12288\n"
    "v_cvt_pk_bf16_f32 v225, v226, v227\n"
    "v_mfma_f32_16x16x32_bf16 a[12:15], a[224:227], v[140:143], a[12:15]\n"
    "PD\n"
    "s_waitcnt lgkmcnt(4)\n"
    "v_exp_f32 v228, v228\n"
    "v_mfma_f32_16x16x32_bf16 a[28:31], a[232:235], v[140:143], a[28:31]\n"
    "PD\n"
    "ds_read_b64_tr_b16 a[248:249], v11 offset:0\n"
    "v_mfma_f32_16x16x32_bf16 a[28:31], a[236:239], v[172:175], a[28:31]\n"
    "PD\n"
    "buffer_load_dwordx4 a[148:151], v21, s[40:43], s56 offen offset:1024 sc1\n"
    "v_exp_f32 v229, v229\n"
    "v_mfma_f32_16x16x32_bf16 a[24:27], a[236:239], v[168:171], a[24:27]\n"
    "PD\n"
    "ds_read_b64_tr_b16 a[250:251], v11 offset:4096\n"
    "v_cvt_pk_bf16_f32 v228, v228, v229\n"
    "v_mfma_f32_16x16x32_bf16 a[24:27], a[232:235], v[136:139], a[24:27]\n"
    "PD\n"
    "buffer_load_dwordx4 a[136:139], v22, s[40:43], s56 offen offset:0 sc1\n"
    "v_exp_f32 v230, v230\n"
    "v_mfma_f32_16x16x32_bf16 a[20:23], a[232:235], v[132:135], a[20:23]\n"
    "PD\n"
    "ds_read_b64_tr_b16 a[252:253], v11 offset:8192\n"
    "v_mfma_f32_16x16x32_bf16 a[20:23], a[236:239], v[164:167], a[20:23]\n"
    "PD\n"
    "buffer_load_dwordx4 a[152:155], v22, s[40:43], s56 offen offset:1024 sc1\n"
    "v_exp_f32 v231, v231\n"
    "v_mfma_f32_16x16x32_bf16 a[16:19], a[236:239], v[160:163], a[16:19]\n"
    "PD\n"
    "ds_read_b64_tr_b16 a[254:255], v11 offset:12288\n"
    "v_cvt_pk_bf16_f32 v229, v230, v231\n"
    "v_mfma_f32_16x16x32_bf16 a[16:19], a[232:235], v[128:131], a[16:19]\n"
    "PD\n"
    "s_waitcnt lgkmcnt(4)\n"
    "v_exp_f32 v232, v232\n"
    "v_mfma_f32_16x16x32_bf16 a[32:35], a[240:243], v[128:131], a[32:35]\n"
    "PD\n"
    "ds_read_b64_tr_b16 a[224:225], v12 offset:0\n"
    "v_mfma_f32_16x16x32_bf16 a[32:35], a[244:247], v[160:163], a[32:35]\n"
    "PD\n"
    "buffer_load_dwordx4 a[140:143], v23, s[40:43], s56 offen offset:0 sc1\n"
    "v_exp_f32 v233, v233\n"
    "v_mfma_f32_16x16x32_bf16 a[36:39], a[244:247], v[164:167], a[36:39]\n"
    "PD\n"
    "ds_read_b64_tr_b16 a[226:227], v12 offset:4096\n"
    "v_cvt_pk_bf16_f32 v232, v232, v233\n"
    "v_mfma_f32_16x16x32_bf16 a[36:39], a[240:243], v[132:135], a[36:39]\n"
    "PD\n"
    "buffer_load_dwordx4 a[156:159], v23, s[40:43], s56 offen offset:1024 sc1\n"
    "v_exp_f32 v234, v234\n"
    "v_mfma_f32_16x16x32_bf16 a[40:43], a[240:243], v[136:139], a[40:43]\n"
    "PD\n"
    "ds_read_b64_tr_b16 a[228:229], v12 offset:8192\n"
    "v_mfma_f32_16x16x32_bf16 a[40:43], a[244:247], v[168:171], a[40:43]\n"
    "PD\n"
    "s_cmp_eq_u32 s31, 0\n"
    "v_exp_f32 v235, v235\n"
    "v_mfma_f32_16x16x32_bf16 a[44:47], a[244:247], v[172:175], a[44:47]\n"
    "PD\n"
    "ds_read_b64_tr_b16 a[230:231], v12 offset:12288\n"
    "v_cvt_pk_bf16_f32 v233, v234, v235\n"
    "v_mfma_f32_16x16x32_bf16 a[44:47], a[240:243], v[140:143], a[44:47]\n"
    "PD\n"
    "s_waitcnt lgkmcnt(4)\n"
    "v_exp_f32 v236, v236\n"
    "v_mfma_f32_16x16x32_bf16 a[60:63], a[248:251], v[140:143], a[60:63]\n"
    "PD\n"
    "ds_read_b64_tr_b16 a[232:233], v13 offset:0\n"
    "v_mfma_f32_16x16x32_bf16 a[60:63], a[252:255], v[172:175], a[60:63]\n"
    "PD\n"
    "s_cselect_b32 s56, 0x4000, s69\n"
    "v_exp_f32 v237, v237\n"
    "v_mfma_f32_16x16x32_bf16 a[56:59], a[252:255], v[168:171], a[56:59]\n"
    "PD\n"
    "ds_read_b64_tr_b16 a[234:235], v13 offset:4096\n"
    "v_cvt_pk_bf16_f32 v236, v236, v237\n"
    "v_mfma_f32_16x16x32_bf16 a[56:59], a[248:251], v[136:139], a[56:59]\n"
    "PD\n"
    "buffer_load_dwordx4 a[160:163], v20, s[40:43], s56 offen offset:0 sc1\n"
    "v_exp_f32 v238, v238\n"
    "v_mfma_f32_16x16x32_bf16 a[52:55], a[248:251], v[132:135], a[52:55]\n"
    "PD\n"
    "ds_read_b64_tr_b16 a[236:237], v13 offset:8192\n"
    "v_mfma_f32_16x16x32_bf16 a[52:55], a[252:255], v[164:167], a[52:55]\n"
    "PD\n"
    "buffer_load_dwordx4 a[176:179], v20, s[40:43], s56 offen offset:1024 sc1\n"
    "v_exp_f32 v239, v239\n"
    "v_mfma_f32_16x16x32_bf16 a[48:51], a[252:255], v[160:163], a[48:51]\n"
    "PD\n"
    "ds_read_b64_tr_b16 a[238:239], v13 offset:12288\n"
    "v_cvt_pk_bf16_f32 v237, v238, v239\n"
    "v_mfma_f32_16x16x32_bf16 a[48:51], a[248:251], v[128:131], a[48:51]\n"
    "PD\n"
    "s_waitcnt lgkmcnt(4)\n"
    "v_exp_f32 v252, v252\n"
    "v_mfma_f32_16x16x32_bf16 a[64:67], a[224:227], v[128:131], a[64:67]\n"
    "PD\n"
    "ds_read_b64_tr_b16 a[240:241], v14 offset:0\n"
    "v_mfma_f32_16x16x32_bf16 a[64:67], a[228:231], v[160:163], a[64:67]\n"
    "PD\n"
    "buffer_load_dwordx4 a[164:167], v21, s[40:43], s56 offen offset:0 sc1\n"
    "v_exp_f32 v253, v253\n"
    "v_mfma_f32_16x16x32_bf16 a[68:71], a[228:231], v[164:167], a[68:71]\n"
    "PD\n"
    "ds_read_b64_tr_b16 a[242:243], v14 offset:4096\n"
    "v_cvt_pk_bf16_f32 v238, v252, v253\n"
    "v_mfma_f32_16x16x32_bf16 a[68:71], a[224:227], v[132:135], a[68:71]\n"
    "PD\n"
    "buffer_load_dwordx4 a[180:183], v21, s[40:43], s56 offen offset:1024 sc1\n"
    "v_exp_f32 v254, v254\n"
    "v_mfma_f32_16x16x32_bf16 a[72:75], a[224:227], v[136:139], a[72:75]\n"
    "PD\n"
    "ds_read_b64_tr_b16 a[244:245], v14 offset:8192\n"
    "v_mfma_f32_16x16x32_bf16 a[72:75], a[228:231], v[168:171], a[72:75]\n"
    "PD\n"
    "buffer_load_dwordx4 a[168:171], v22, s[40:43], s56 offen offset:0 sc1\n"
    "v_exp_f32 v255, v255\n"
    "v_mfma_f32_16x16x32_bf16 a[76:79], a[228:231], v[172:175], a[76:79]\n"
    "PD\n"
    "ds_read_b64_tr_b16 a[246:247], v14 offset:12288\n"
    "v_cvt_pk_bf16_f32 v239, v254, v255\n"
    "v_mfma_f32_16x16x32_bf16 a[76:79], a[224:227], v[140:143], a[76:79]\n"
    "PD\n"
    "s_waitcnt lgkmcnt(4)\n"
    "v_exp_f32 v248, v248\n"
    "v_mfma_f32_16x16x32_bf16 a[92:95], a[232:235], v[140:143], a[92:95]\n"
    "PD\n"
    "ds_read_b64_tr_b16 a[248:249], v15 offset:0\n"
    "v_mfma_f32_16x16x32_bf16 a[92:95], a[236:239], v[172:175], a[92:95]\n"
    "PD\n"
    "buffer_load_dwordx4 a[184:187], v22, s[40:43], s56 offen offset:1024 sc1\n"
    "v_exp_f32 v249, v249\n"
    "v_mfma_f32_16x16x32_bf16 a[88:91], a[236:239], v[168:171], a[88:91]\n"
    "PD\n"
    "ds_read_b64_tr_b16 a[250:251], v15 offset:4096\n"
    "v_cvt_pk_bf16_f32 v234, v248, v249\n"
    "v_mfma_f32_16x16x32_bf16 a[88:91], a[232:235], v[136:139], a[88:91]\n"
    "PD\n"
    "buffer_load_dwordx4 a[172:175], v23, s[40:43], s56 offen offset:0 sc1\n"
    "v_exp_f32 v250, v250\n"
    "v_mfma_f32_16x16x32_bf16 a[84:87], a[232:235], v[132:135], a[84:87]\n"
    "PD\n"
    "ds_read_b64_tr_b16 a[252:253], v15 offset:8192\n"
    "v_mfma_f32_16x16x32_bf16 a[84:87], a[236:239], v[164:167], a[84:87]\n"
    "PD\n"
    "buffer_load_dwordx4 a[188:191], v23, s[40:43], s56 offen offset:1024 sc1\n"
    "v_exp_f32 v251, v251\n"
    "v_mfma_f32_16x16x32_bf16 a[80:83], a[236:239], v[160:163], a[80:83]\n"
    "PD\n"
    "ds_read_b64_tr_b16 a[254:255], v15 offset:12288\n"
    "v_cvt_pk_bf16_f32 v235, v250, v251\n"
    "v_mfma_f32_16x16x32_bf16 a[80:83], a[232:235], v[128:131], a[80:83]\n"
    "PD\n"
    "s_waitcnt lgkmcnt(4)\n"
    "v_exp_f32 v244, v244\n"
    "v_mfma_f32_16x16x32_bf16 a[96:99], a[240:243], v[128:131], a[96:99]\n"
    "PD\n"
    "s_xor_b32 s56, s56, 0x6000\n"
    "v_exp_f32 v245, v245\n"
    "v_mfma_f32_16x16x32_bf16 a[96:99], a[244:247], v[160:163], a[96:99]\n"
    "PD\n"
    "v_exp_f32 v246, v246\n"
    "v_mfma_f32_16x16x32_bf16 a[100:103], a[244:247], v[164:167], a[100:103]\n"
    "PD\n"
    "v_exp_f32 v247, v247\n"
    "v_mfma_f32_16x16x32_bf16 a[100:103], a[240:243], v[132:135], a[100:103]\n"
    "PD\n"
    "v_exp_f32 v240, v240\n"
    "v_mfma_f32_16x16x32_bf16 a[104:107], a[240:243], v[136:139], a[104:107]\n"
    "PD\n"
    "v_exp_f32 v241, v241\n"
    "v_mfma_f32_16x16x32_bf16 a[104:107], a[244:247], v[168:171], a[104:107]\n"
    "PD\n"
    "v_cvt_pk_bf16_f32 v230, v244, v245\n"
    "v_cvt_pk_bf16_f32 v231, v246, v247\n"
    "v_mfma_f32_16x16x32_bf16 a[108:111], a[244:247], v[172:175], a[108:111]\n"
    "PD\n"
    "v_exp_f32 v242, v242\n"
    "v_mfma_f32_16x16x32_bf16 a[108:111], a[240:243], v[140:143], a[108:111]\n"
    "PD\n"
    "s_waitcnt lgkmcnt(0)\n"
    "v_exp_f32 v243, v243\n"
    "v_mfma_f32_16x16x32_bf16 a[124:127], a[248:251], v[140:143], a[124:127]\n"
    "PD\n"
    "ds_read_b64_tr_b16 a[224:225], v8 offset:32768\n"
    "v_cvt_pk_bf16_f32 v226, v240, v241\n"
    "v_mfma_f32_16x16x32_bf16 a[124:127], a[252:255], v[172:175], a[124:127]\n"
    "PD\n"
    "v_cvt_pk_bf16_f32 v227, v242, v243\n"
    "v_mfma_f32_16x16x32_bf16 a[120:123], a[252:255], v[168:171], a[120:123]\n"
    "PD\n"
    "ds_read_b64_tr_b16 a[226:227], v8 offset:36864\n"
    "v_mfma_f32_16x16x32_bf16 a[120:123], a[248:251], v[136:139], a[120:123]\n"
    "PD\n"
    "v_mfma_f32_16x16x32_bf16 a[116:119], a[248:251], v[132:135], a[116:119]\n"
    "PD\n"
    "ds_read_b64_tr_b16 a[228:229], v8 offset:40960\n"
    "v_mfma_f32_16x16x32_bf16 a[116:119], a[252:255], v[164:167], a[116:119]\n"
    "PD\n"
    "v_mfma_f32_16x16x32_bf16 a[112:115], a[252:255], v[160:163], a[112:115]\n"
    "PD\n"
    "ds_read_b64_tr_b16 a[230:231], v8 offset:45056\n"
    "v_xor_b32 v8, 0x10000, v8\n"
    "v_mfma_f32_16x16x32_bf16 a[112:115], a[248:251], v[128:131], a[112:115]\n"
    "PD\n"
    "v_mfma_f32_16x16x32_bf16 a[192:195], a[208:211], v[128:131], a[192:195]\n"
    "PD\n"
    "ds_read_b64_tr_b16 a[232:233], v9 offset:32768\n"
    "v_mfma_f32_16x16x32_bf16 a[192:195], a[208:211], v[160:163], a[192:195]\n"
    "PD\n"
    "v_mfma_f32_16x16x32_bf16 a[196:199], a[208:211], v[132:135], a[196:199]\n"
    "PD\n"
    "ds_read_b64_tr_b16 a[234:235], v9 offset:36864\n"
    "v_mfma_f32_16x16x32_bf16 a[196:199], a[208:211], v[164:167], a[196:199]\n"
    "PD\n"
    "v_mfma_f32_16x16x32_bf16 a[200:203], a[208:211], v[136:139], a[200:203]\n"
    "PD\n"
    "ds_read_b64_tr_b16 a[236:237], v9 offset:40960\n"
    "v_mfma_f32_16x16x32_bf16 a[200:203], a[208:211], v[168:171], a[200:203]\n"
    "PD\n"
    "v_mfma_f32_16x16x32_bf16 a[204:207], a[208:211], v[140:143], a[204:207]\n"
    "PD\n"
    "ds_read_b64_tr_b16 a[238:239], v9 offset:45056\n"
    "v_xor_b32 v9, 0x10000, v9\n"
    "v_mfma_f32_16x16x32_bf16 a[204:207], a[208:211], v[172:175], a[204:207]\n"
    "PD\n"
    "v_mfma_f32_16x16x32_bf16 a[192:195], a[208:211], v[192:195], a[192:195]\n"
    "PD\n"
    "v_mfma_f32_16x16x32_bf16 a[192:195], a[208:211], v[224:227], a[192:195]\n"
    "PD\n"
    "v_mfma_f32_16x16x32_bf16 a[196:199], a[208:211], v[196:199], a[196:199]\n"
    "PD\n"
    "v_mfma_f32_16x16x32_bf16 a[196:199], a[208:211], v[228:231], a[196:199]\n"
    "PD\n"
    "v_mfma_f32_16x16x32_bf16 a[200:203], a[208:211], v[200:203], a[200:203]\n"
    "PD\n"
    "v_mfma_f32_16x16x32_bf16 a[200:203], a[208:211], v[232:235], a[200:203]\n"
    "PD\n"
    "v_mfma_f32_16x16x32_bf16 a[204:207], a[208:211], v[204:207], a[204:207]\n"
    "PD\n"
    "v_mfma_f32_16x16x32_bf16 a[204:207], a[208:211], v[236:239], a[204:207]\n"
    "PD\n"
    "s_waitcnt lgkmcnt(4)\n"
    "v_mfma_f32_16x16x32_bf16 a[0:3], a[224:227], v[192:195], a[0:3]\n"
    "PD\n"
    "ds_read_b64_tr_b16 a[240:241], v10 offset:32768\n"
    "v_mfma_f32_16x16x32_bf16 a[0:3], a[228:231], v[224:227], a[0:3]\n"
    "PD\n"
    "v_mfma_f32_16x16x32_bf16 a[4:7], a[228:231], v[228:231], a[4:7]\n"
    "PD\n"
    "ds_read_b64_tr_b16 a[242:243], v10 offset:36864\n"
    "v_mfma_f32_16x16x32_bf16 a[4:7], a[224:227], v[196:199], a[4:7]\n"
    "PD\n"
    "v_mfma_f32_16x16x32_bf16 a[8:11], a[224:227], v[200:203], a[8:11]\n"
    "PD\n"
    "ds_read_b64_tr_b16 a[244:245], v10 offset:40960\n"
    "v_mfma_f32_16x16x32_bf16 a[8:11], a[228:231], v[232:235], a[8:11]\n"
    "PD\n"
    "v_mfma_f32_16x16x32_bf16 a[12:15], a[228:231], v[236:239], a[12:15]\n"
    "PD\n"
    "ds_read_b64_tr_b16 a[246:247], v10 offset:45056\n"
    "v_xor_b32 v10, 0x10000, v10\n"
    "v_mfma_f32_16x16x32_bf16 a[12:15], a[224:227], v[204:207], a[12:15]\n"
    "PD\n"
    "s_waitcnt lgkmcnt(4)\n"
    "v_mfma_f32_16x16x32_bf16 a[28:31], a[232:235], v[204:207], a[28:31]\n"
    "PD\n"
    "ds_read_b64_tr_b16 a[248:249], v11 offset:32768\n"
    "v_mfma_f32_16x16x32_bf16 a[28:31], a[236:239], v[236:239], a[28:31]\n"
    "PD\n"
    "v_accvgpr_read_b32 v56, a192\n"
    "v_mfma_f32_16x16x32_bf16 a[24:27], a[236:239], v[232:235], a[24:27]\n"
    "PD\n"
    "ds_read_b64_tr_b16 a[250:251], v11 offset:36864\n"
    "v_mfma_f32_16x16x32_bf16 a[24:27], a[232:235], v[200:203], a[24:27]\n"
    "PD\n"
    "v_accvgpr_read_b32 v57, a196\n"
    "v_mfma_f32_16x16x32_bf16 a[20:23], a[232:235], v[196:199], a[20:23]\n"
    "PD\n"
    "ds_read_b64_tr_b16 a[252:253], v11 offset:40960\n"
    "v_mfma_f32_16x16x32_bf16 a[20:23], a[236:239], v[228:231], a[20:23]\n"
    "PD\n"
    "v_accvgpr_read_b32 v58, a200\n"
    "v_mfma_f32_16x16x32_bf16 a[16:19], a[236:239], v[224:227], a[16:19]\n"
    "PD\n"
    "ds_read_b64_tr_b16 a[254:255], v11 offset:45056\n"
    "v_xor_b32 v11, 0x10000, v11\n"
    "v_accvgpr_read_b32 v59, a204\n"
    "v_mfma_f32_16x16x32_bf16 a[16:19], a[232:235], v[192:195], a[16:19]\n"
    "PD\n"
    "s_waitcnt lgkmcnt(4)\n"
    "v_mfma_f32_16x16x32_bf16 a[32:35], a[240:243], v[192:195], a[32:35]\n"
    "PD\n"
    "ds_read_b64_tr_b16 a[224:225], v12 offset:32768\n"
    "v_mfma_f32_16x16x32_bf16 a[32:35], a[244:247], v[224:227], a[32:35]\n"
    "PD\n"
    "buffer_load_dwordx4 a[192:195], v20, s[40:43], s56 offen offset:0 sc1\n"
    "v_add_f32 v128, v56, v57\n"
    "v_mfma_f32_16x16x32_bf16 a[36:39], a[244:247], v[228:231], a[36:39]\n"
    "PD\n"
    "ds_read_b64_tr_b16 a[226:227], v12 offset:36864\n"
    "v_mfma_f32_16x16x32_bf16 a[36:39], a[240:243], v[196:199], a[36:39]\n"
    "PD\n"
    "v_add_f32 v129, v58, v59\n"
    "v_mfma_f32_16x16x32_bf16 a[40:43], a[240:243], v[200:203], a[40:43]\n"
    "PD\n"
    "ds_read_b64_tr_b16 a[228:229], v12 offset:40960\n"
    "v_mfma_f32_16x16x32_bf16 a[40:43], a[244:247], v[232:235], a[40:43]\n"
    "PD\n"
    "buffer_load_dwordx4 a[208:211], v20, s[40:43], s56 offen offset:1024 sc1\n"
    "v_add_f32 v128, v128, v129\n"
    "v_mfma_f32_16x16x32_bf16 a[44:47], a[244:247], v[236:239], a[44:47]\n"
    "PD\n"
    "ds_read_b64_tr_b16 a[230:231], v12 offset:45056\n"
    "v_xor_b32 v12, 0x10000, v12\n"
    "v_cmp_nge_f32 vcc, 0x53800000, v128\n"
    "v_mfma_f32_16x16x32_bf16 a[44:47], a[240:243], v[204:207], a[44:47]\n"
    "PD\n"
    "s_waitcnt lgkmcnt(4)\n"
    "v_mfma_f32_16x16x32_bf16 a[60:63], a[248:251], v[204:207], a[60:63]\n"
    "PD\n"
    "ds_read_b64_tr_b16 a[232:233], v13 offset:32768\n"
    "v_mfma_f32_16x16x32_bf16 a[60:63], a[252:255], v[236:239], a[60:63]\n"
    "PD\n"
    "s_cmp_lg_u64 vcc, 0\n"
    "s_cselect_b32 s62, 1, 0\n"
    "v_mfma_f32_16x16x32_bf16 a[56:59], a[252:255], v[232:235], a[56:59]\n"
    "PD\n"
    "ds_read_b64_tr_b16 a[234:235], v13 offset:36864\n"
    "v_mfma_f32_16x16x32_bf16 a[56:59], a[248:251], v[200:203], a[56:59]\n"
    "PD\n"
    "v_mov_b32 v128, s62\n"
    "v_lshl_add_u32 v129, s36, 2, v39\n"
    "v_mfma_f32_16x16x32_bf16 a[52:55], a[248:251], v[196:199], a[52:55]\n"
    "PD\n"
    "ds_read_b64_tr_b16 a[236:237], v13 offset:40960\n"
    "v_mfma_f32_16x16x32_bf16 a[52:55], a[252:255], v[228:231], a[52:55]\n"
    "PD\n"
    "ds_write_b32 v129, v128\n"
    "v_mfma_f32_16x16x32_bf16 a[48:51], a[252:255], v[224:227], a[48:51]\n"
    "PD\n"
    "ds_read_b64_tr_b16 a[238:239], v13 offset:45056\n"
    "v_xor_b32 v13, 0x10000, v13\n"
    "v_mfma_f32_16x16x32_bf16 a[48:51], a[248:251], v[192:195], a[48:51]\n"
    "PD\n"
    "s_waitcnt lgkmcnt(5)\n"
    "v_mfma_f32_16x16x32_bf16 a[64:67], a[224:227], v[192:195], a[64:67]\n"
    "PD\n"
    "ds_read_b64_tr_b16 a[240:241], v14 offset:32768\n"
    "v_mfma_f32_16x16x32_bf16 a[64:67], a[228:231], v[224:227], a[64:67]\n"
    "PD\n"
    "buffer_load_dwordx4 a[196:199], v21, s[40:43], s56 offen offset:0 sc1\n"
    "v_rcp_f32 v56, v56\n"
    "v_mfma_f32_16x16x32_bf16 a[68:71], a[228:231], v[228:231], a[68:71]\n"
    "PD\n"
    "ds_read_b64_tr_b16 a[242:243], v14 offset:36864\n"
    "v_mfma_f32_16x16x32_bf16 a[68:71], a[224:227], v[196:199], a[68:71]\n"
    "PD\n"
    "v_rcp_f32 v57, v57\n"
    "v_mfma_f32_16x16x32_bf16 a[72:75], a[224:227], v[200:203], a[72:75]\n"
    "PD\n"
    "ds_read_b64_tr_b16 a[244:245], v14 offset:40960\n"
    "v_mfma_f32_16x16x32_bf16 a[72:75], a[228:231], v[232:235], a[72:75]\n"
    "PD\n"
    "buffer_load_dwordx4 a[212:215], v21, s[40:43], s56 offen offset:1024 sc1\n"
    "v_rcp_f32 v58, v58\n"
    "v_mfma_f32_16x16x32_bf16 a[76:79], a[228:231], v[236:239], a[76:79]\n"
    "PD\n"
    "ds_read_b64_tr_b16 a[246:247], v14 offset:45056\n"
    "v_xor_b32 v14, 0x10000, v14\n"
    "v_mfma_f32_16x16x32_bf16 a[76:79], a[224:227], v[204:207], a[76:79]\n"
    "PD\n"
    "s_waitcnt lgkmcnt(4)\n"
    "v_mfma_f32_16x16x32_bf16 a[92:95], a[232:235], v[204:207], a[92:95]\n"
    "PD\n"
    "ds_read_b64_tr_b16 a[248:249], v15 offset:32768\n"
    "v_mfma_f32_16x16x32_bf16 a[92:95], a[236:239], v[236:239], a[92:95]\n"
    "PD\n"
    "v_rcp_f32 v59, v59\n"
    "v_mfma_f32_16x16x32_bf16 a[88:91], a[236:239], v[232:235], a[88:91]\n"
    "PD\n"
    "ds_read_b64_tr_b16 a[250:251], v15 offset:36864\n"
    "v_mfma_f32_16x16x32_bf16 a[88:91], a[232:235], v[200:203], a[88:91]\n"
    "PD\n"
    "buffer_load_dwordx4 a[200:203], v22, s[40:43], s56 offen offset:0 sc1\n"
    "v_mfma_f32_16x16x32_bf16 a[84:87], a[232:235], v[196:199], a[84:87]\n"
    "PD\n"
    "ds_read_b64_tr_b16 a[252:253], v15 offset:40960\n"
    "v_mfma_f32_16x16x32_bf16 a[84:87], a[236:239], v[228:231], a[84:87]\n"
    "PD\n"
    "buffer_load_dwordx4 a[216:219], v22, s[40:43], s56 offen offset:1024 sc1\n"
    "v_mfma_f32_16x16x32_bf16 a[80:83], a[236:239], v[224:227], a[80:83]\n"
    "PD\n"
    "ds_read_b64_tr_b16 a[254:255], v15 offset:45056\n"
    "v_xor_b32 v15, 0x10000, v15\n"
    "v_mfma_f32_16x16x32_bf16 a[80:83], a[232:235], v[192:195], a[80:83]\n"
    "PD\n"
    "s_waitcnt lgkmcnt(4)\n"
    "v_mfma_f32_16x16x32_bf16 a[96:99], a[240:243], v[192:195], a[96:99]\n"
    "PD\n"
    "v_mfma_f32_16x16x32_bf16 a[96:99], a[244:247], v[224:227], a[96:99]\n"
    "PD\n"
    "buffer_load_dwordx4 a[204:207], v23, s[40:43], s56 offen offset:0 sc1\n"
    "v_mfma_f32_16x16x32_bf16 a[100:103], a[244:247], v[228:231], a[100:103]\n"
    "PD\n"
    "v_mfma_f32_16x16x32_bf16 a[100:103], a[240:243], v[196:199], a[100:103]\n"
    "PD\n"
    "buffer_load_dwordx4 a[220:223], v23, s[40:43], s56 offen offset:1024 sc1\n"
    "v_mfma_f32_16x16x32_bf16 a[104:107], a[240:243], v[200:203], a[104:107]\n"
    "PD\n"
    "v_mfma_f32_16x16x32_bf16 a[104:107], a[244:247], v[232:235], a[104:107]\n"
    "PD\n"
    "v_mfma_f32_16x16x32_bf16 a[108:111], a[244:247], v[236:239], a[108:111]\n"
    "PD\n"
    "v_mfma_f32_16x16x32_bf16 a[108:111], a[240:243], v[204:207], a[108:111]\n"
    "PD\n"
    "s_waitcnt lgkmcnt(0)\n"
    "s_waitcnt vmcnt(33)\n"
    "s_barrier\n"
    "v_mfma_f32_16x16x32_bf16 a[124:127], a[248:251], v[204:207], a[124:127]\n"
    "PD\n"
    "ds_read_b128 v[40:43], v39\n"
    "v_mfma_f32_16x16x32_bf16 a[124:127], a[252:255], v[236:239], a[124:127]\n"
    "PD\n"
    "s_add_u32 m0, s61, 65536\n"
    "ds_read_b64_tr_b16 a[224:225], v8 offset:0\n"
    "v_mfma_f32_16x16x32_bf16 a[120:123], a[252:255], v[232:235], a[120:123]\n"
    "PD\n"
    "buffer_load_dwordx4 v24, s[44:47], s57 offen offset:0 lds\n"
    "ds_read_b64_tr_b16 a[226:227], v8 offset:4096\n"
    "v_mfma_f32_16x16x32_bf16 a[120:123], a[248:251], v[200:203], a[120:123]\n"
    "PD\n"
    "buffer_load_dwordx4 v25, s[44:47], s57 offen offset:1024 lds\n"
    "ds_read_b64_tr_b16 a[228:229], v8 offset:8192\n"
    "v_mfma_f32_16x16x32_bf16 a[116:119], a[248:251], v[196:199], a[116:119]\n"
    "PD\n"
    "buffer_load_dwordx4 v26, s[44:47], s57 offen offset:2048 lds\n"
    "ds_read_b64_tr_b16 a[230:231], v8 offset:12288\n"
    "v_mfma_f32_16x16x32_bf16 a[116:119], a[252:255], v[228:231], a[116:119]\n"
    "PD\n"
    "buffer_load_dwordx4 v27, s[44:47], s57 offen offset:3072 lds\n"
    "ds_read_b64_tr_b16 a[232:233], v9 offset:0\n"
    "v_mfma_f32_16x16x32_bf16 a[112:115], a[252:255], v[224:227], a[112:115]\n"
    "PD\n"
    "s_add_u32 s57, s57, s59\n"
    "ds_read_b64_tr_b16 a[234:235], v9 offset:4096\n"
    "v_mfma_f32_16x16x32_bf16 a[112:115], a[248:251], v[192:195], a[112:115]\n"
    "PD\n"
    "ds_read_b64_tr_b16 a[236:237], v9 offset:8192\n"
    "ds_read_b64_tr_b16 a[238:239], v9 offset:12288\n"
    "s_waitcnt lgkmcnt(8)\n"
    "v_or3_b32 v40, v40, v41, v42\n"
    "v_or_b32 v40, v40, v43\n"
    "s_nop 0\n"
    "v_readfirstlane_b32 s62, v40\n"
    "PD\n"
    "s_mov_b64 vcc, 0\n"
    "PD\n"
    "s_cmp_gt_u32 s62, s39\n"
    "PD\n"
    "s_cbranch_scc1 H_fb\n"
    "PD\n"
    "s_cmp_eq_u32 s31, 2\n"
    "PD\n"
    "s_cbranch_scc1 F_O\n"
    "PD\n"
    "F_U:\n"
    "PD\n"
    "s_waitcnt vmcnt(20) lgkmcnt(4)\n"
    "v_mfma_f32_16x16x32_bf16 v[128:131], a[224:227], a[128:131], 0\n"
    "PD\n"
    "ds_read_b64_tr_b16 a[240:241], v10 offset:0\n"
    "v_mfma_f32_16x16x32_bf16 v[128:131], a[228:231], a[144:147], v[128:131]\n"
    "PD\n"
    "s_mov_b32 s37, 0\n"
    "v_mfma_f32_16x16x32_bf16 v[132:135], a[228:231], a[148:151], 0\n"
    "PD\n"
    "ds_read_b64_tr_b16 a[242:243], v10 offset:4096\n"
    "v_accvgpr_read_b32 v40, a0\n"
    "v_mfma_f32_16x16x32_bf16 v[132:135], a[224:227], a[132:135], v[132:135]\n"
    "PD\n"
    "s_xor_b32 s60, s61, 0x10000\n"
    "v_accvgpr_read_b32 v41, a1\n"
    "v_accvgpr_read_b32 v42, a2\n"
    "v_mfma_f32_16x16x32_bf16 v[136:139], a[224:227], a[136:139], 0\n"
    "PD\n"
    "ds_read_b64_tr_b16 a[244:245], v10 offset:8192\n"
    "v_accvgpr_read_b32 v43, a3\n"
    "v_mfma_f32_16x16x32_bf16 v[136:139], a[228:231], a[152:155], v[136:139]\n"
    "PD\n"
    "s_movk_i32 s56, 8192\n"
    "v_accvgpr_read_b32 v44, a16\n"
    "v_accvgpr_read_b32 v45, a17\n"
    "v_mfma_f32_16x16x32_bf16 v[140:143], a[228:231], a[156:159], 0\n"
    "PD\n"
    "ds_read_b64_tr_b16 a[246:247], v10 offset:12288\n"
    "v_accvgpr_read_b32 v46, a18\n"
    "v_mfma_f32_16x16x32_bf16 v[140:143], a[224:227], a[140:143], v[140:143]\n"
    "PD\n"
    "s_waitcnt lgkmcnt(4)\n"
    "v_accvgpr_read_b32 v47, a19\n"
    "v_mul_f32 v40, v40, v56\n"
    "v_mfma_f32_16x16x32_bf16 v[156:159], a[232:235], a[140:143], 0\n"
    "PD\n"
    "ds_read_b64_tr_b16 a[248:249], v11 offset:0\n"
    "v_mul_f32 v41, v41, v56\n"
    "v_mfma_f32_16x16x32_bf16 v[156:159], a[236:239], a[156:159], v[156:159]\n"
    "PD\n"
    "s_mov_b32 s38, 7\n"
    "v_mul_f32 v42, v42, v56\n"
    "v_mul_f32 v43, v43, v56\n"
    "v_mfma_f32_16x16x32_bf16 v[152:155], a[236:239], a[152:155], 0\n"
    "PD\n"
    "ds_read_b64_tr_b16 a[250:251], v11 offset:4096\n"
    "v_mul_f32 v44, v44, v56\n"
    "v_mfma_f32_16x16x32_bf16 v[152:155], a[232:235], a[136:139], v[152:155]\n"
    "PD\n"
    "s_mov_b32 s31, s99\n"
    "v_mul_f32 v45, v45, v56\n"
    "v_mul_f32 v46, v46, v56\n"
    "v_mfma_f32_16x16x32_bf16 v[148:151], a[232:235], a[132:135], 0\n"
    "PD\n"
    "ds_read_b64_tr_b16 a[252:253], v11 offset:8192\n"
    "v_mul_f32 v47, v47, v56\n"
    "v_mfma_f32_16x16x32_bf16 v[148:151], a[236:239], a[148:151], v[148:151]\n"
    "PD\n"
    "s_mov_b32 s99, 2\n"
    "v_cvt_pk_bf16_f32 v64, v40, v41\n"
    "v_cvt_pk_bf16_f32 v65, v42, v43\n"
    "v_mfma_f32_16x16x32_bf16 v[144:147], a[236:239], a[144:147], 0\n"
    "PD\n"
    "ds_read_b64_tr_b16 a[254:255], v11 offset:12288\n"
    "v_cvt_pk_bf16_f32 v66, v44, v45\n"
    "v_mfma_f32_16x16x32_bf16 v[144:147], a[232:235], a[128:131], v[144:147]\n"
    "PD\n"
    "s_waitcnt lgkmcnt(4)\n"
    "v_cvt_pk_bf16_f32 v67, v46, v47\n"
    "v_accvgpr_read_b32 v48, a4\n"
    "v_mfma_f32_16x16x32_bf16 v[160:163], a[240:243], a[128:131], 0\n"
    "PD\n"
    "buffer_store_dwordx4 v[64:67], v4, s[52:55], 0 offen offset:0 sc1\n"
    "ds_read_b64_tr_b16 a[224:225], v12 offset:0\n"
    "v_mfma_f32_16x16x32_bf16 v[160:163], a[244:247], a[144:147], v[160:163]\n"
    "PD\n"
    "s_movk_i32 s56, 0x6000\n"
    "v_accvgpr_read_b32 v49, a5\n"
    "v_accvgpr_read_b32 v50, a6\n"
    "v_mfma_f32_16x16x32_bf16 v[164:167], a[244:247], a[148:151], 0\n"
    "PD\n"
    "ds_read_b64_tr_b16 a[226:227], v12 offset:4096\n"
    "v_accvgpr_read_b32 v51, a7\n"
    "v_mfma_f32_16x16x32_bf16 v[164:167], a[240:243], a[132:135], v[164:167]\n"
    "PD\n"
    "s_mov_b32 s37, 1\n"
    "v_accvgpr_read_b32 v52, a20\n"
    "v_accvgpr_read_b32 v53, a21\n"
    "v_mfma_f32_16x16x32_bf16 v[168:171], a[240:243], a[136:139], 0\n"
    "PD\n"
    "ds_read_b64_tr_b16 a[228:229], v12 offset:8192\n"
    "v_accvgpr_read_b32 v54, a22\n"
    "v_mfma_f32_16x16x32_bf16 v[168:171], a[244:247], a[152:155], v[168:171]\n"
    "PD\n"
    "s_add_u32 m0, s60, 16384\n"
    "v_accvgpr_read_b32 v55, a23\n"
    "v_mul_f32 v48, v48, v57\n"
    "v_mfma_f32_16x16x32_bf16 v[172:175], a[244:247], a[156:159], 0\n"
    "PD\n"
    "ds_read_b64_tr_b16 a[230:231], v12 offset:12288\n"
    "v_mul_f32 v49, v49, v57\n"
    "v_mfma_f32_16x16x32_bf16 v[172:175], a[240:243], a[140:143], v[172:175]\n"
    "PD\n"
    "s_waitcnt lgkmcnt(4)\n"
    "v_mul_f32 v50, v50, v57\n"
    "v_mul_f32 v51, v51, v57\n"
    "v_mfma_f32_16x16x32_bf16 v[188:191], a[248:251], a[140:143], 0\n"
    "PD\n"
    "ds_read_b64_tr_b16 a[232:233], v13 offset:0\n"
    "v_mul_f32 v52, v52, v57\n"
    "v_mfma_f32_16x16x32_bf16 v[188:191], a[252:255], a[156:159], v[188:191]\n"
    "PD\n"
    "buffer_load_dwordx4 v28, s[48:51], s58 offen offset:0 lds\n"
    "v_mul_f32 v53, v53, v57\n"
    "v_mul_f32 v54, v54, v57\n"
    "v_mfma_f32_16x16x32_bf16 v[184:187], a[252:255], a[152:155], 0\n"
    "PD\n"
    "ds_read_b64_tr_b16 a[234:235], v13 offset:4096\n"
    "v_mul_f32 v55, v55, v57\n"
    "v_mfma_f32_16x16x32_bf16 v[184:187], a[248:251], a[136:139], v[184:187]\n"
    "PD\n"
    "buffer_load_dwordx4 v29, s[48:51], s58 offen offset:1024 lds\n"
    "v_cvt_pk_bf16_f32 v68, v48, v49\n"
    "v_cvt_pk_bf16_f32 v69, v50, v51\n"
    "v_mfma_f32_16x16x32_bf16 v[180:183], a[248:251], a[132:135], 0\n"
    "PD\n"
    "ds_read_b64_tr_b16 a[236:237], v13 offset:8192\n"
    "v_cvt_pk_bf16_f32 v70, v52, v53\n"
    "v_mfma_f32_16x16x32_bf16 v[180:183], a[252:255], a[148:151], v[180:183]\n"
    "PD\n"
    "buffer_load_dwordx4 v30, s[48:51], s58 offen offset:2048 lds\n"
    "v_cvt_pk_bf16_f32 v71, v54, v55\n"
    "v_accvgpr_read_b32 v40, a8\n"
    "v_mfma_f32_16x16x32_bf16 v[176:179], a[252:255], a[144:147], 0\n"
    "PD\n"
    "buffer_store_dwordx4 v[68:71], v4, s[52:55], 0 offen offset:2048 sc1\n"
    "ds_read_b64_tr_b16 a[238:239], v13 offset:12288\n"
    "v_mfma_f32_16x16x32_bf16 v[176:179], a[248:251], a[128:131], v[176:179]\n"
    "PD\n"
    "s_waitcnt lgkmcnt(4)\n"
    "v_accvgpr_read_b32 v41, a9\n"
    "v_accvgpr_read_b32 v42, a10\n"
    "v_mfma_f32_16x16x32_bf16 v[192:195], a[224:227], a[128:131], 0\n"
    "PD\n"
    "ds_read_b64_tr_b16 a[240:241], v14 offset:0\n"
    "v_accvgpr_read_b32 v43, a11\n"
    "v_mfma_f32_16x16x32_bf16 v[192:195], a[228:231], a[144:147], v[192:195]\n"
    "PD\n"
    "buffer_load_dwordx4 v31, s[48:51], s58 offen offset:3072 lds\n"
    "v_accvgpr_read_b32 v44, a24\n"
    "v_accvgpr_read_b32 v45, a25\n"
    "v_mfma_f32_16x16x32_bf16 v[196:199], a[228:231], a[148:151], 0\n"
    "PD\n"
    "ds_read_b64_tr_b16 a[242:243], v14 offset:4096\n"
    "v_accvgpr_read_b32 v46, a26\n"
    "v_mfma_f32_16x16x32_bf16 v[196:199], a[224:227], a[132:135], v[196:199]\n"
    "PD\n"
    "s_add_u32 s58, s58, s59\n"
    "v_accvgpr_read_b32 v47, a27\n"
    "v_mul_f32 v40, v40, v58\n"
    "v_mfma_f32_16x16x32_bf16 v[200:203], a[224:227], a[136:139], 0\n"
    "PD\n"
    "ds_read_b64_tr_b16 a[244:245], v14 offset:8192\n"
    "v_mul_f32 v41, v41, v58\n"
    "v_mfma_f32_16x16x32_bf16 v[200:203], a[228:231], a[152:155], v[200:203]\n"
    "PD\n"
    "v_mul_f32 v42, v42, v58\n"
    "v_mul_f32 v43, v43, v58\n"
    "v_mfma_f32_16x16x32_bf16 v[204:207], a[228:231], a[156:159], 0\n"
    "PD\n"
    "ds_read_b64_tr_b16 a[246:247], v14 offset:12288\n"
    "v_mul_f32 v44, v44, v58\n"
    "v_mfma_f32_16x16x32_bf16 v[204:207], a[224:227], a[140:143], v[204:207]\n"
    "PD\n"
    "s_waitcnt lgkmcnt(4)\n"
    "v_mul_f32 v45, v45, v58\n"
    "v_mul_f32 v46, v46, v58\n"
    "v_mfma_f32_16x16x32_bf16 v[220:223], a[232:235], a[140:143], 0\n"
    "PD\n"
    "ds_read_b64_tr_b16 a[248:249], v15 offset:0\n"
    "v_mul_f32 v47, v47, v58\n"
    "v_mfma_f32_16x16x32_bf16 v[220:223], a[236:239], a[156:159], v[220:223]\n"
    "PD\n"
    "v_cvt_pk_bf16_f32 v72, v40, v41\n"
    "v_cvt_pk_bf16_f32 v73, v42, v43\n"
    "v_mfma_f32_16x16x32_bf16 v[216:219], a[236:239], a[152:155], 0\n"
    "PD\n"
    "ds_read_b64_tr_b16 a[250:251], v15 offset:4096\n"
    "v_cvt_pk_bf16_f32 v74, v44, v45\n"
    "v_mfma_f32_16x16x32_bf16 v[216:219], a[232:235], a[136:139], v[216:219]\n"
    "PD\n"
    "v_cvt_pk_bf16_f32 v75, v46, v47\n"
    "v_accvgpr_read_b32 v48, a12\n"
    "v_mfma_f32_16x16x32_bf16 v[212:215], a[232:235], a[132:135], 0\n"
    "PD\n"
    "buffer_store_dwordx4 v[72:75], v5, s[52:55], 0 offen offset:0 sc1\n"
    "ds_read_b64_tr_b16 a[252:253], v15 offset:8192\n"
    "v_mfma_f32_16x16x32_bf16 v[212:215], a[236:239], a[148:151], v[212:215]\n"
    "PD\n"
    "v_accvgpr_read_b32 v49, a13\n"
    "v_accvgpr_read_b32 v50, a14\n"
    "v_mfma_f32_16x16x32_bf16 v[208:211], a[236:239], a[144:147], 0\n"
    "PD\n"
    "ds_read_b64_tr_b16 a[254:255], v15 offset:12288\n"
    "v_accvgpr_read_b32 v51, a15\n"
    "v_mfma_f32_16x16x32_bf16 v[208:211], a[232:235], a[128:131], v[208:211]\n"
    "PD\n"
    "s_waitcnt lgkmcnt(4)\n"
    "s_barrier\n"
    "v_accvgpr_read_b32 v52, a28\n"
    "v_mfma_f32_16x16x32_bf16 v[224:227], a[240:243], a[128:131], 0\n"
    "PD\n"
    "ds_read_b64_tr_b16 a[224:225], v8 offset:32768\n"
    "v_accvgpr_read_b32 v53, a29\n"
    "v_mfma_f32_16x16x32_bf16 v[224:227], a[244:247], a[144:147], v[224:227]\n"
    "PD\n"
    "v_accvgpr_read_b32 v54, a30\n"
    "v_accvgpr_read_b32 v55, a31\n"
    "v_mfma_f32_16x16x32_bf16 v[228:231], a[244:247], a[148:151], 0\n"
    "PD\n"
    "ds_read_b64_tr_b16 a[226:227], v8 offset:36864\n"
    "v_mul_f32 v48, v48, v59\n"
    "v_mfma_f32_16x16x32_bf16 v[228:231], a[240:243], a[132:135], v[228:231]\n"
    "PD\n"
    "v_mul_f32 v49, v49, v59\n"
    "v_mul_f32 v50, v50, v59\n"
    "v_mfma_f32_16x16x32_bf16 v[232:235], a[240:243], a[136:139], 0\n"
    "PD\n"
    "ds_read_b64_tr_b16 a[228:229], v8 offset:40960\n"
    "v_mul_f32 v51, v51, v59\n"
    "v_mfma_f32_16x16x32_bf16 v[232:235], a[244:247], a[152:155], v[232:235]\n"
    "PD\n"
    "v_mul_f32 v52, v52, v59\n"
    "v_mul_f32 v53, v53, v59\n"
    "v_mfma_f32_16x16x32_bf16 v[236:239], a[244:247], a[156:159], 0\n"
    "PD\n"
    "ds_read_b64_tr_b16 a[230:231], v8 offset:45056\n"
    "v_mul_f32 v54, v54, v59\n"
    "v_mfma_f32_16x16x32_bf16 v[236:239], a[240:243], a[140:143], v[236:239]\n"
    "PD\n"
    "s_waitcnt lgkmcnt(4)\n"
    "v_mul_f32 v55, v55, v59\n"
    "v_cvt_pk_bf16_f32 v76, v48, v49\n"
    "v_mfma_f32_16x16x32_bf16 v[252:255], a[248:251], a[140:143], 0\n"
    "PD\n"
    "ds_read_b64_tr_b16 a[232:233], v9 offset:32768\n"
    "v_cvt_pk_bf16_f32 v77, v50, v51\n"
    "v_mfma_f32_16x16x32_bf16 v[252:255], a[252:255], a[156:159], v[252:255]\n"
    "PD\n"
    "v_cvt_pk_bf16_f32 v78, v52, v53\n"
    "v_cvt_pk_bf16_f32 v79, v54, v55\n"
    "v_mfma_f32_16x16x32_bf16 v[248:251], a[252:255], a[152:155], 0\n"
    "PD\n"
    "buffer_store_dwordx4 v[76:79], v5, s[52:55], 0 offen offset:2048 sc1\n"
    "ds_read_b64_tr_b16 a[234:235], v9 offset:36864\n"
    "v_mfma_f32_16x16x32_bf16 v[248:251], a[248:251], a[136:139], v[248:251]\n"
    "PD\n"
    "v_accvgpr_read_b32 v40, a32\n"
    "v_accvgpr_read_b32 v41, a33\n"
    "v_mfma_f32_16x16x32_bf16 v[244:247], a[248:251], a[132:135], 0\n"
    "PD\n"
    "ds_read_b64_tr_b16 a[236:237], v9 offset:40960\n"
    "v_accvgpr_read_b32 v42, a34\n"
    "v_mfma_f32_16x16x32_bf16 v[244:247], a[252:255], a[148:151], v[244:247]\n"
    "PD\n"
    "v_accvgpr_read_b32 v43, a35\n"
    "v_accvgpr_read_b32 v44, a48\n"
    "v_mfma_f32_16x16x32_bf16 v[240:243], a[252:255], a[144:147], 0\n"
    "PD\n"
    "ds_read_b64_tr_b16 a[238:239], v9 offset:45056\n"
    "v_accvgpr_read_b32 v45, a49\n"
    "v_mfma_f32_16x16x32_bf16 v[240:243], a[248:251], a[128:131], v[240:243]\n"
    "PD\n"
    "s_waitcnt vmcnt(12) lgkmcnt(4)\n"
    "v_accvgpr_read_b32 v46, a50\n"
    "v_accvgpr_read_b32 v47, a51\n"
    "v_mfma_f32_16x16x32_bf16 v[128:131], a[224:227], a[192:195], v[128:131]\n"
    "PD\n"
    "ds_read_b64_tr_b16 a[240:241], v10 offset:32768\n"
    "v_mul_f32 v40, v40, v56\n"
    "v_mfma_f32_16x16x32_bf16 v[128:131], a[228:231], a[208:211], v[128:131]\n"
    "PD\n"
    "v_mul_f32 v41, v41, v56\n"
    "v_mul_f32 v42, v42, v56\n"
    "v_mfma_f32_16x16x32_bf16 v[132:135], a[228:231], a[212:215], v[132:135]\n"
    "PD\n"
    "ds_read_b64_tr_b16 a[242:243], v10 offset:36864\n"
    "v_mul_f32 v43, v43, v56\n"
    "v_mfma_f32_16x16x32_bf16 v[132:135], a[224:227], a[196:199], v[132:135]\n"
    "PD\n"
    "v_mul_f32 v44, v44, v56\n"
    "v_mul_f32 v45, v45, v56\n"
    "v_mfma_f32_16x16x32_bf16 v[136:139], a[224:227], a[200:203], v[136:139]\n"
    "PD\n"
    "ds_read_b64_tr_b16 a[244:245], v10 offset:40960\n"
    "v_mul_f32 v46, v46, v56\n"
    "v_mfma_f32_16x16x32_bf16 v[136:139], a[228:231], a[216:219], v[136:139]\n"
    "PD\n"
    "v_mul_f32 v47, v47, v56\n"
    "v_cvt_pk_bf16_f32 v80, v40, v41\n"
    "v_mfma_f32_16x16x32_bf16 v[140:143], a[228:231], a[220:223], v[140:143]\n"
    "PD\n"
    "ds_read_b64_tr_b16 a[246:247], v10 offset:45056\n"
    "v_cvt_pk_bf16_f32 v81, v42, v43\n"
    "v_mfma_f32_16x16x32_bf16 v[140:143], a[224:227], a[204:207], v[140:143]\n"
    "PD\n"
    "s_waitcnt lgkmcnt(4)\n"
    "v_cvt_pk_bf16_f32 v82, v44, v45\n"
    "v_cvt_pk_bf16_f32 v83, v46, v47\n"
    "v_mfma_f32_16x16x32_bf16 v[156:159], a[232:235], a[204:207], v[156:159]\n"
    "PD\n"
    "buffer_store_dwordx4 v[80:83], v4, s[52:55], 0 offen offset:1024 sc1\n"
    "ds_read_b64_tr_b16 a[248:249], v11 offset:32768\n"
    "v_mfma_f32_16x16x32_bf16 v[156:159], a[236:239], a[220:223], v[156:159]\n"
    "PD\n"
    "s_add_u32 m0, s60, 32768\n"
    "v_accvgpr_read_b32 v48, a36\n"
    "v_accvgpr_read_b32 v49, a37\n"
    "v_mfma_f32_16x16x32_bf16 v[152:155], a[236:239], a[216:219], v[152:155]\n"
    "PD\n"
    "ds_read_b64_tr_b16 a[250:251], v11 offset:36864\n"
    "v_accvgpr_read_b32 v50, a38\n"
    "v_mfma_f32_16x16x32_bf16 v[152:155], a[232:235], a[200:203], v[152:155]\n"
    "PD\n"
    "buffer_load_dwordx4 v24, s[44:47], s57 offen offset:0 lds\n"
    "v_accvgpr_read_b32 v51, a39\n"
    "v_accvgpr_read_b32 v52, a52\n"
    "v_mfma_f32_16x16x32_bf16 v[148:151], a[232:235], a[196:199], v[148:151]\n"
    "PD\n"
    "ds_read_b64_tr_b16 a[252:253], v11 offset:40960\n"
    "v_accvgpr_read_b32 v53, a53\n"
    "v_mfma_f32_16x16x32_bf16 v[148:151], a[236:239], a[212:215], v[148:151]\n"
    "PD\n"
    "buffer_load_dwordx4 v25, s[44:47], s57 offen offset:1024 lds\n"
    "v_accvgpr_read_b32 v54, a54\n"
    "v_accvgpr_read_b32 v55, a55\n"
    "v_mfma_f32_16x16x32_bf16 v[144:147], a[236:239], a[208:211], v[144:147]\n"
    "PD\n"
    "ds_read_b64_tr_b16 a[254:255], v11 offset:45056\n"
    "v_mul_f32 v48, v48, v57\n"
    "v_mfma_f32_16x16x32_bf16 v[144:147], a[232:235], a[192:195], v[144:147]\n"
    "PD\n"
    "s_waitcnt lgkmcnt(4)\n"
    "v_mul_f32 v49, v49, v57\n"
    "v_mul_f32 v50, v50, v57\n"
    "v_mfma_f32_16x16x32_bf16 v[160:163], a[240:243], a[192:195], v[160:163]\n"
    "PD\n"
    "ds_read_b64_tr_b16 a[224:225], v12 offset:32768\n"
    "v_mul_f32 v51, v51, v57\n"
    "v_mfma_f32_16x16x32_bf16 v[160:163], a[244:247], a[208:211], v[160:163]\n"
    "PD\n"
    "buffer_load_dwordx4 v26, s[44:47], s57 offen offset:2048 lds\n"
    "v_mul_f32 v52, v52, v57\n"
    "v_mul_f32 v53, v53, v57\n"
    "v_mfma_f32_16x16x32_bf16 v[164:167], a[244:247], a[212:215], v[164:167]\n"
    "PD\n"
    "ds_read_b64_tr_b16 a[226:227], v12 offset:36864\n"
    "v_mul_f32 v54, v54, v57\n"
    "v_mfma_f32_16x16x32_bf16 v[164:167], a[240:243], a[196:199], v[164:167]\n"
    "PD\n"
    "buffer_load_dwordx4 v27, s[44:47], s57 offen offset:3072 lds\n"
    "v_mul_f32 v55, v55, v57\n"
    "v_cvt_pk_bf16_f32 v84, v48, v49\n"
    "v_mfma_f32_16x16x32_bf16 v[168:171], a[240:243], a[200:203], v[168:171]\n"
    "PD\n"
    "ds_read_b64_tr_b16 a[228:229], v12 offset:40960\n"
    "v_cvt_pk_bf16_f32 v85, v50, v51\n"
    "v_mfma_f32_16x16x32_bf16 v[168:171], a[244:247], a[216:219], v[168:171]\n"
    "PD\n"
    "s_add_u32 s57, s57, s59\n"
    "v_cvt_pk_bf16_f32 v86, v52, v53\n"
    "v_cvt_pk_bf16_f32 v87, v54, v55\n"
    "v_mfma_f32_16x16x32_bf16 v[172:175], a[244:247], a[220:223], v[172:175]\n"
    "PD\n"
    "buffer_store_dwordx4 v[84:87], v4, s[52:55], 0 offen offset:3072 sc1\n"
    "ds_read_b64_tr_b16 a[230:231], v12 offset:45056\n"
    "v_mfma_f32_16x16x32_bf16 v[172:175], a[240:243], a[204:207], v[172:175]\n"
    "PD\n"
    "s_waitcnt lgkmcnt(4)\n"
    "v_accvgpr_read_b32 v40, a40\n"
    "v_accvgpr_read_b32 v41, a41\n"
    "v_mfma_f32_16x16x32_bf16 v[188:191], a[248:251], a[204:207], v[188:191]\n"
    "PD\n"
    "ds_read_b64_tr_b16 a[232:233], v13 offset:32768\n"
    "v_accvgpr_read_b32 v42, a42\n"
    "v_mfma_f32_16x16x32_bf16 v[188:191], a[252:255], a[220:223], v[188:191]\n"
    "PD\n"
    "v_accvgpr_read_b32 v43, a43\n"
    "v_accvgpr_read_b32 v44, a56\n"
    "v_mfma_f32_16x16x32_bf16 v[184:187], a[252:255], a[216:219], v[184:187]\n"
    "PD\n"
    "ds_read_b64_tr_b16 a[234:235], v13 offset:36864\n"
    "v_accvgpr_read_b32 v45, a57\n"
    "v_mfma_f32_16x16x32_bf16 v[184:187], a[248:251], a[200:203], v[184:187]\n"
    "PD\n"
    "v_accvgpr_read_b32 v46, a58\n"
    "v_accvgpr_read_b32 v47, a59\n"
    "v_mfma_f32_16x16x32_bf16 v[180:183], a[248:251], a[196:199], v[180:183]\n"
    "PD\n"
    "ds_read_b64_tr_b16 a[236:237], v13 offset:40960\n"
    "v_mul_f32 v40, v40, v58\n"
    "v_mfma_f32_16x16x32_bf16 v[180:183], a[252:255], a[212:215], v[180:183]\n"
    "PD\n"
    "v_mul_f32 v41, v41, v58\n"
    "v_mul_f32 v42, v42, v58\n"
    "v_mfma_f32_16x16x32_bf16 v[176:179], a[252:255], a[208:211], v[176:179]\n"
    "PD\n"
    "ds_read_b64_tr_b16 a[238:239], v13 offset:45056\n"
    "v_mul_f32 v43, v43, v58\n"
    "v_mfma_f32_16x16x32_bf16 v[176:179], a[248:251], a[192:195], v[176:179]\n"
    "PD\n"
    "s_waitcnt lgkmcnt(4)\n"
    "v_mul_f32 v44, v44, v58\n"
    "v_mul_f32 v45, v45, v58\n"
    "v_mfma_f32_16x16x32_bf16 v[192:195], a[224:227], a[192:195], v[192:195]\n"
    "PD\n"
    "ds_read_b64_tr_b16 a[240:241], v14 offset:32768\n"
    "v_mul_f32 v46, v46, v58\n"
    "v_mfma_f32_16x16x32_bf16 v[192:195], a[228:231], a[208:211], v[192:195]\n"
    "PD\n"
    "v_mul_f32 v47, v47, v58\n"
    "v_cvt_pk_bf16_f32 v88, v40, v41\n"
    "v_mfma_f32_16x16x32_bf16 v[196:199], a[228:231], a[212:215], v[196:199]\n"
    "PD\n"
    "ds_read_b64_tr_b16 a[242:243], v14 offset:36864\n"
    "v_cvt_pk_bf16_f32 v89, v42, v43\n"
    "v_mfma_f32_16x16x32_bf16 v[196:199], a[224:227], a[196:199], v[196:199]\n"
    "PD\n"
    "v_cvt_pk_bf16_f32 v90, v44, v45\n"
    "v_cvt_pk_bf16_f32 v91, v46, v47\n"
    "v_mfma_f32_16x16x32_bf16 v[200:203], a[224:227], a[200:203], v[200:203]\n"
    "PD\n"
    "buffer_store_dwordx4 v[88:91], v5, s[52:55], 0 offen offset:1024 sc1\n"
    "ds_read_b64_tr_b16 a[244:245], v14 offset:40960\n"
    "v_mfma_f32_16x16x32_bf16 v[200:203], a[228:231], a[216:219], v[200:203]\n"
    "PD\n"
    "v_accvgpr_read_b32 v48, a44\n"
    "v_accvgpr_read_b32 v49, a45\n"
    "v_mfma_f32_16x16x32_bf16 v[204:207], a[228:231], a[220:223], v[204:207]\n"
    "PD\n"
    "ds_read_b64_tr_b16 a[246:247], v14 offset:45056\n"
    "v_accvgpr_read_b32 v50, a46\n"
    "v_mfma_f32_16x16x32_bf16 v[204:207], a[224:227], a[204:207], v[204:207]\n"
    "PD\n"
    "s_waitcnt lgkmcnt(4)\n"
    "v_accvgpr_read_b32 v51, a47\n"
    "v_accvgpr_read_b32 v52, a60\n"
    "v_mfma_f32_16x16x32_bf16 v[220:223], a[232:235], a[204:207], v[220:223]\n"
    "PD\n"
    "ds_read_b64_tr_b16 a[248:249], v15 offset:32768\n"
    "v_accvgpr_read_b32 v53, a61\n"
    "v_mfma_f32_16x16x32_bf16 v[220:223], a[236:239], a[220:223], v[220:223]\n"
    "PD\n"
    "v_accvgpr_read_b32 v54, a62\n"
    "v_accvgpr_read_b32 v55, a63\n"
    "v_mfma_f32_16x16x32_bf16 v[216:219], a[236:239], a[216:219], v[216:219]\n"
    "PD\n"
    "ds_read_b64_tr_b16 a[250:251], v15 offset:36864\n"
    "v_mul_f32 v48, v48, v59\n"
    "v_mfma_f32_16x16x32_bf16 v[216:219], a[232:235], a[200:203], v[216:219]\n"
    "PD\n"
    "v_mul_f32 v49, v49, v59\n"
    "v_mul_f32 v50, v50, v59\n"
    "v_mfma_f32_16x16x32_bf16 v[212:215], a[232:235], a[196:199], v[212:215]\n"
    "PD\n"
    "ds_read_b64_tr_b16 a[252:253], v15 offset:40960\n"
    "v_mul_f32 v51, v51, v59\n"
    "v_mfma_f32_16x16x32_bf16 v[212:215], a[236:239], a[212:215], v[212:215]\n"
    "PD\n"
    "v_mul_f32 v52, v52, v59\n"
    "v_mul_f32 v53, v53, v59\n"
    "v_mfma_f32_16x16x32_bf16 v[208:211], a[236:239], a[208:211], v[208:211]\n"
    "PD\n"
    "ds_read_b64_tr_b16 a[254:255], v15 offset:45056\n"
    "v_mul_f32 v54, v54, v59\n"
    "v_mfma_f32_16x16x32_bf16 v[208:211], a[232:235], a[192:195], v[208:211]\n"
    "PD\n"
    "s_waitcnt lgkmcnt(4)\n"
    "v_mul_f32 v55, v55, v59\n"
    "v_cvt_pk_bf16_f32 v92, v48, v49\n"
    "v_mfma_f32_16x16x32_bf16 v[224:227], a[240:243], a[192:195], v[224:227]\n"
    "PD\n"
    "ds_read_b64_tr_b16 a[224:225], v8 offset:16384\n"
    "v_cvt_pk_bf16_f32 v93, v50, v51\n"
    "v_mfma_f32_16x16x32_bf16 v[224:227], a[244:247], a[208:211], v[224:227]\n"
    "PD\n"
    "v_cvt_pk_bf16_f32 v94, v52, v53\n"
    "v_cvt_pk_bf16_f32 v95, v54, v55\n"
    "v_mfma_f32_16x16x32_bf16 v[228:231], a[244:247], a[212:215], v[228:231]\n"
    "PD\n"
    "buffer_store_dwordx4 v[92:95], v5, s[52:55], 0 offen offset:3072 sc1\n"
    "ds_read_b64_tr_b16 a[226:227], v8 offset:20480\n"
    "v_mfma_f32_16x16x32_bf16 v[228:231], a[240:243], a[196:199], v[228:231]\n"
    "PD\n"
    "v_accvgpr_read_b32 v40, a64\n"
    "v_accvgpr_read_b32 v41, a65\n"
    "v_mfma_f32_16x16x32_bf16 v[232:235], a[240:243], a[200:203], v[232:235]\n"
    "PD\n"
    "ds_read_b64_tr_b16 a[228:229], v8 offset:24576\n"
    "v_accvgpr_read_b32 v42, a66\n"
    "v_mfma_f32_16x16x32_bf16 v[232:235], a[244:247], a[216:219], v[232:235]\n"
    "PD\n"
    "v_accvgpr_read_b32 v43, a67\n"
    "v_accvgpr_read_b32 v44, a80\n"
    "v_mfma_f32_16x16x32_bf16 v[236:239], a[244:247], a[220:223], v[236:239]\n"
    "PD\n"
    "ds_read_b64_tr_b16 a[230:231], v8 offset:28672\n"
    "v_accvgpr_read_b32 v45, a81\n"
    "v_mfma_f32_16x16x32_bf16 v[236:239], a[240:243], a[204:207], v[236:239]\n"
    "PD\n"
    "s_waitcnt lgkmcnt(4)\n"
    "v_accvgpr_read_b32 v46, a82\n"
    "v_accvgpr_read_b32 v47, a83\n"
    "v_mfma_f32_16x16x32_bf16 v[252:255], a[248:251], a[204:207], v[252:255]\n"
    "PD\n"
    "ds_read_b64_tr_b16 a[232:233], v9 offset:16384\n"
    "v_mul_f32 v40, v40, v56\n"
    "v_mfma_f32_16x16x32_bf16 v[252:255], a[252:255], a[220:223], v[252:255]\n"
    "PD\n"
    "v_mul_f32 v41, v41, v56\n"
    "v_mul_f32 v42, v42, v56\n"
    "v_mfma_f32_16x16x32_bf16 v[248:251], a[252:255], a[216:219], v[248:251]\n"
    "PD\n"
    "ds_read_b64_tr_b16 a[234:235], v9 offset:20480\n"
    "v_mul_f32 v43, v43, v56\n"
    "v_mfma_f32_16x16x32_bf16 v[248:251], a[248:251], a[200:203], v[248:251]\n"
    "PD\n"
    "v_mul_f32 v44, v44, v56\n"
    "v_mul_f32 v45, v45, v56\n"
    "v_mfma_f32_16x16x32_bf16 v[244:247], a[248:251], a[196:199], v[244:247]\n"
    "PD\n"
    "ds_read_b64_tr_b16 a[236:237], v9 offset:24576\n"
    "v_mul_f32 v46, v46, v56\n"
    "v_mfma_f32_16x16x32_bf16 v[244:247], a[252:255], a[212:215], v[244:247]\n"
    "PD\n"
    "v_mul_f32 v47, v47, v56\n"
    "v_cvt_pk_bf16_f32 v96, v40, v41\n"
    "v_mfma_f32_16x16x32_bf16 v[240:243], a[252:255], a[208:211], v[240:243]\n"
    "PD\n"
    "ds_read_b64_tr_b16 a[238:239], v9 offset:28672\n"
    "v_cvt_pk_bf16_f32 v97, v42, v43\n"
    "v_mfma_f32_16x16x32_bf16 v[240:243], a[248:251], a[192:195], v[240:243]\n"
    "PD\n"
    "s_waitcnt lgkmcnt(4)\n"
    "v_cvt_pk_bf16_f32 v98, v44, v45\n"
    "v_cvt_pk_bf16_f32 v99, v46, v47\n"
    "v_mfma_f32_16x16x32_bf16 a[0:3], a[224:227], a[128:131], 0\n"
    "PD\n"
    "buffer_store_dwordx4 v[96:99], v4, s[52:55], s69 offen offset:0 sc1\n"
    "ds_read_b64_tr_b16 a[240:241], v8 offset:49152\n"
    "v_mfma_f32_16x16x32_bf16 a[0:3], a[228:231], a[144:147], a[0:3]\n"
    "PD\n"
    "v_accvgpr_read_b32 v48, a68\n"
    "v_accvgpr_read_b32 v49, a69\n"
    "v_mfma_f32_16x16x32_bf16 a[4:7], a[228:231], a[148:151], 0\n"
    "PD\n"
    "ds_read_b64_tr_b16 a[242:243], v8 offset:53248\n"
    "v_accvgpr_read_b32 v50, a70\n"
    "v_mfma_f32_16x16x32_bf16 a[4:7], a[224:227], a[132:135], a[4:7]\n"
    "PD\n"
    "v_accvgpr_read_b32 v51, a71\n"
    "v_accvgpr_read_b32 v52, a84\n"
    "v_mfma_f32_16x16x32_bf16 a[8:11], a[224:227], a[136:139], 0\n"
    "PD\n"
    "ds_read_b64_tr_b16 a[244:245], v8 offset:57344\n"
    "v_accvgpr_read_b32 v53, a85\n"
    "v_mfma_f32_16x16x32_bf16 a[8:11], a[228:231], a[152:155], a[8:11]\n"
    "PD\n"
    "v_accvgpr_read_b32 v54, a86\n"
    "v_accvgpr_read_b32 v55, a87\n"
    "v_mfma_f32_16x16x32_bf16 a[12:15], a[228:231], a[156:159], 0\n"
    "PD\n"
    "ds_read_b64_tr_b16 a[246:247], v8 offset:61440\n"
    "v_xor_b32 v8, 0x10000, v8\n"
    "v_mfma_f32_16x16x32_bf16 a[12:15], a[224:227], a[140:143], a[12:15]\n"
    "PD\n"
    "s_waitcnt lgkmcnt(4)\n"
    "v_mul_f32 v48, v48, v57\n"
    "v_mul_f32 v49, v49, v57\n"
    "v_mfma_f32_16x16x32_bf16 a[28:31], a[232:235], a[140:143], 0\n"
    "PD\n"
    "ds_read_b64_tr_b16 a[248:249], v9 offset:49152\n"
    "v_mul_f32 v50, v50, v57\n"
    "v_mfma_f32_16x16x32_bf16 a[28:31], a[236:239], a[156:159], a[28:31]\n"
    "PD\n"
    "v_mul_f32 v51, v51, v57\n"
    "v_mul_f32 v52, v52, v57\n"
    "v_mfma_f32_16x16x32_bf16 a[24:27], a[236:239], a[152:155], 0\n"
    "PD\n"
    "ds_read_b64_tr_b16 a[250:251], v9 offset:53248\n"
    "v_mul_f32 v53, v53, v57\n"
    "v_mfma_f32_16x16x32_bf16 a[24:27], a[232:235], a[136:139], a[24:27]\n"
    "PD\n"
    "v_mul_f32 v54, v54, v57\n"
    "v_mul_f32 v55, v55, v57\n"
    "v_mfma_f32_16x16x32_bf16 a[20:23], a[232:235], a[132:135], 0\n"
    "PD\n"
    "ds_read_b64_tr_b16 a[252:253], v9 offset:57344\n"
    "v_cvt_pk_bf16_f32 v100, v48, v49\n"
    "v_mfma_f32_16x16x32_bf16 a[20:23], a[236:239], a[148:151], a[20:23]\n"
    "PD\n"
    "s_add_u32 m0, s60, 49152\n"
    "v_cvt_pk_bf16_f32 v101, v50, v51\n"
    "v_cvt_pk_bf16_f32 v102, v52, v53\n"
    "v_mfma_f32_16x16x32_bf16 a[16:19], a[236:239], a[144:147], 0\n"
    "PD\n"
    "ds_read_b64_tr_b16 a[254:255], v9 offset:61440\n"
    "v_xor_b32 v9, 0x10000, v9\n"
    "v_mfma_f32_16x16x32_bf16 a[16:19], a[232:235], a[128:131], a[16:19]\n"
    "PD\n"
    "s_waitcnt lgkmcnt(4)\n"
    "v_cvt_pk_bf16_f32 v103, v54, v55\n"
    "v_accvgpr_read_b32 v40, a72\n"
    "v_mfma_f32_16x16x32_bf16 a[0:3], a[240:243], a[192:195], a[0:3]\n"
    "PD\n"
    "buffer_store_dwordx4 v[100:103], v4, s[52:55], s69 offen offset:2048 sc1\n"
    "ds_read_b64_tr_b16 a[224:225], v10 offset:16384\n"
    "v_mfma_f32_16x16x32_bf16 a[0:3], a[244:247], a[208:211], a[0:3]\n"
    "PD\n"
    "buffer_load_dwordx4 v28, s[48:51], s58 offen offset:0 lds\n"
    "v_accvgpr_read_b32 v41, a73\n"
    "v_accvgpr_read_b32 v42, a74\n"
    "v_mfma_f32_16x16x32_bf16 a[4:7], a[244:247], a[212:215], a[4:7]\n"
    "PD\n"
    "ds_read_b64_tr_b16 a[226:227], v10 offset:20480\n"
    "v_accvgpr_read_b32 v43, a75\n"
    "v_mfma_f32_16x16x32_bf16 a[4:7], a[240:243], a[196:199], a[4:7]\n"
    "PD\n"
    "buffer_load_dwordx4 v29, s[48:51], s58 offen offset:1024 lds\n"
    "v_accvgpr_read_b32 v44, a88\n"
    "v_accvgpr_read_b32 v45, a89\n"
    "v_mfma_f32_16x16x32_bf16 a[8:11], a[240:243], a[200:203], a[8:11]\n"
    "PD\n"
    "ds_read_b64_tr_b16 a[228:229], v10 offset:24576\n"
    "v_accvgpr_read_b32 v46, a90\n"
    "v_mfma_f32_16x16x32_bf16 a[8:11], a[244:247], a[216:219], a[8:11]\n"
    "PD\n"
    "buffer_load_dwordx4 v30, s[48:51], s58 offen offset:2048 lds\n"
    "v_accvgpr_read_b32 v47, a91\n"
    "v_mul_f32 v40, v40, v58\n"
    "v_mfma_f32_16x16x32_bf16 a[12:15], a[244:247], a[220:223], a[12:15]\n"
    "PD\n"
    "ds_read_b64_tr_b16 a[230:231], v10 offset:28672\n"
    "v_mul_f32 v41, v41, v58\n"
    "v_mfma_f32_16x16x32_bf16 a[12:15], a[240:243], a[204:207], a[12:15]\n"
    "PD\n"
    "s_waitcnt lgkmcnt(4)\n"
    "v_mul_f32 v42, v42, v58\n"
    "v_mul_f32 v43, v43, v58\n"
    "v_mfma_f32_16x16x32_bf16 a[28:31], a[248:251], a[204:207], a[28:31]\n"
    "PD\n"
    "ds_read_b64_tr_b16 a[232:233], v11 offset:16384\n"
    "v_mul_f32 v44, v44, v58\n"
    "v_mfma_f32_16x16x32_bf16 a[28:31], a[252:255], a[220:223], a[28:31]\n"
    "PD\n"
    "buffer_load_dwordx4 v31, s[48:51], s58 offen offset:3072 lds\n"
    "v_mul_f32 v45, v45, v58\n"
    "v_mul_f32 v46, v46, v58\n"
    "v_mfma_f32_16x16x32_bf16 a[24:27], a[252:255], a[216:219], a[24:27]\n"
    "PD\n"
    "ds_read_b64_tr_b16 a[234:235], v11 offset:20480\n"
    "v_mul_f32 v47, v47, v58\n"
    "v_mfma_f32_16x16x32_bf16 a[24:27], a[248:251], a[200:203], a[24:27]\n"
    "PD\n"
    "s_add_u32 s58, s58, s59\n"
    "v_cvt_pk_bf16_f32 v104, v40, v41\n"
    "v_cvt_pk_bf16_f32 v105, v42, v43\n"
    "v_mfma_f32_16x16x32_bf16 a[20:23], a[248:251], a[196:199], a[20:23]\n"
    "PD\n"
    "ds_read_b64_tr_b16 a[236:237], v11 offset:24576\n"
    "v_cvt_pk_bf16_f32 v106, v44, v45\n"
    "v_mfma_f32_16x16x32_bf16 a[20:23], a[252:255], a[212:215], a[20:23]\n"
    "PD\n"
    "s_xor_b32 m0, s60, 65536\n"
    "s_xor_b32 s60, s60, 0x10000\n"
    "v_cvt_pk_bf16_f32 v107, v46, v47\n"
    "v_mfma_f32_16x16x32_bf16 a[16:19], a[252:255], a[208:211], a[16:19]\n"
    "PD\n"
    "buffer_store_dwordx4 v[104:107], v5, s[52:55], s69 offen offset:0 sc1\n"
    "ds_read_b64_tr_b16 a[238:239], v11 offset:28672\n"
    "v_mfma_f32_16x16x32_bf16 a[16:19], a[248:251], a[192:195], a[16:19]\n"
    "PD\n"
    "s_waitcnt lgkmcnt(4)\n"
    "v_accvgpr_read_b32 v48, a76\n"
    "v_accvgpr_read_b32 v49, a77\n"
    "v_mfma_f32_16x16x32_bf16 a[32:35], a[224:227], a[128:131], 0\n"
    "PD\n"
    "ds_read_b64_tr_b16 a[240:241], v10 offset:49152\n"
    "v_accvgpr_read_b32 v50, a78\n"
    "v_mfma_f32_16x16x32_bf16 a[32:35], a[228:231], a[144:147], a[32:35]\n"
    "PD\n"
    "v_accvgpr_read_b32 v51, a79\n"
    "v_accvgpr_read_b32 v52, a92\n"
    "v_mfma_f32_16x16x32_bf16 a[36:39], a[228:231], a[148:151], 0\n"
    "PD\n"
    "ds_read_b64_tr_b16 a[242:243], v10 offset:53248\n"
    "v_accvgpr_read_b32 v53, a93\n"
    "v_mfma_f32_16x16x32_bf16 a[36:39], a[224:227], a[132:135], a[36:39]\n"
    "PD\n"
    "v_accvgpr_read_b32 v54, a94\n"
    "v_accvgpr_read_b32 v55, a95\n"
    "v_mfma_f32_16x16x32_bf16 a[40:43], a[224:227], a[136:139], 0\n"
    "PD\n"
    "ds_read_b64_tr_b16 a[244:245], v10 offset:57344\n"
    "v_mul_f32 v48, v48, v59\n"
    "v_mfma_f32_16x16x32_bf16 a[40:43], a[228:231], a[152:155], a[40:43]\n"
    "PD\n"
    "v_mul_f32 v49, v49, v59\n"
    "v_mul_f32 v50, v50, v59\n"
    "v_mfma_f32_16x16x32_bf16 a[44:47], a[228:231], a[156:159], 0\n"
    "PD\n"
    "ds_read_b64_tr_b16 a[246:247], v10 offset:61440\n"
    "v_xor_b32 v10, 0x10000, v10\n"
    "v_mfma_f32_16x16x32_bf16 a[44:47], a[224:227], a[140:143], a[44:47]\n"
    "PD\n"
    "s_waitcnt lgkmcnt(4)\n"
    "v_mul_f32 v51, v51, v59\n"
    "v_mul_f32 v52, v52, v59\n"
    "v_mfma_f32_16x16x32_bf16 a[60:63], a[232:235], a[140:143], 0\n"
    "PD\n"
    "ds_read_b64_tr_b16 a[248:249], v11 offset:49152\n"
    "v_mul_f32 v53, v53, v59\n"
    "v_mfma_f32_16x16x32_bf16 a[60:63], a[236:239], a[156:159], a[60:63]\n"
    "PD\n"
    "v_mul_f32 v54, v54, v59\n"
    "v_mul_f32 v55, v55, v59\n"
    "v_mfma_f32_16x16x32_bf16 a[56:59], a[236:239], a[152:155], 0\n"
    "PD\n"
    "ds_read_b64_tr_b16 a[250:251], v11 offset:53248\n"
    "v_cvt_pk_bf16_f32 v108, v48, v49\n"
    "v_mfma_f32_16x16x32_bf16 a[56:59], a[232:235], a[136:139], a[56:59]\n"
    "PD\n"
    "v_cvt_pk_bf16_f32 v109, v50, v51\n"
    "v_cvt_pk_bf16_f32 v110, v52, v53\n"
    "v_mfma_f32_16x16x32_bf16 a[52:55], a[232:235], a[132:135], 0\n"
    "PD\n"
    "ds_read_b64_tr_b16 a[252:253], v11 offset:57344\n"
    "v_cvt_pk_bf16_f32 v111, v54, v55\n"
    "v_mfma_f32_16x16x32_bf16 a[52:55], a[236:239], a[148:151], a[52:55]\n"
    "PD\n"
    "buffer_store_dwordx4 v[108:111], v5, s[52:55], s69 offen offset:2048 sc1\n"
    "v_accvgpr_read_b32 v40, a96\n"
    "v_accvgpr_read_b32 v41, a97\n"
    "v_mfma_f32_16x16x32_bf16 a[48:51], a[236:239], a[144:147], 0\n"
    "PD\n"
    "ds_read_b64_tr_b16 a[254:255], v11 offset:61440\n"
    "v_xor_b32 v11, 0x10000, v11\n"
    "v_mfma_f32_16x16x32_bf16 a[48:51], a[232:235], a[128:131], a[48:51]\n"
    "PD\n"
    "s_waitcnt lgkmcnt(4)\n"
    "v_accvgpr_read_b32 v42, a98\n"
    "v_accvgpr_read_b32 v43, a99\n"
    "v_mfma_f32_16x16x32_bf16 a[32:35], a[240:243], a[192:195], a[32:35]\n"
    "PD\n"
    "ds_read_b64_tr_b16 a[224:225], v12 offset:16384\n"
    "v_accvgpr_read_b32 v44, a112\n"
    "v_mfma_f32_16x16x32_bf16 a[32:35], a[244:247], a[208:211], a[32:35]\n"
    "PD\n"
    "v_accvgpr_read_b32 v45, a113\n"
    "v_accvgpr_read_b32 v46, a114\n"
    "v_mfma_f32_16x16x32_bf16 a[36:39], a[244:247], a[212:215], a[36:39]\n"
    "PD\n"
    "ds_read_b64_tr_b16 a[226:227], v12 offset:20480\n"
    "v_accvgpr_read_b32 v47, a115\n"
    "v_mfma_f32_16x16x32_bf16 a[36:39], a[240:243], a[196:199], a[36:39]\n"
    "PD\n"
    "v_mul_f32 v40, v40, v56\n"
    "v_mul_f32 v41, v41, v56\n"
    "v_mfma_f32_16x16x32_bf16 a[40:43], a[240:243], a[200:203], a[40:43]\n"
    "PD\n"
    "ds_read_b64_tr_b16 a[228:229], v12 offset:24576\n"
    "v_mul_f32 v42, v42, v56\n"
    "v_mfma_f32_16x16x32_bf16 a[40:43], a[244:247], a[216:219], a[40:43]\n"
    "PD\n"
    "v_mul_f32 v43, v43, v56\n"
    "v_mul_f32 v44, v44, v56\n"
    "v_mfma_f32_16x16x32_bf16 a[44:47], a[244:247], a[220:223], a[44:47]\n"
    "PD\n"
    "ds_read_b64_tr_b16 a[230:231], v12 offset:28672\n"
    "v_mul_f32 v45, v45, v56\n"
    "v_mfma_f32_16x16x32_bf16 a[44:47], a[240:243], a[204:207], a[44:47]\n"
    "PD\n"
    "s_waitcnt lgkmcnt(4)\n"
    "v_mul_f32 v46, v46, v56\n"
    "v_mul_f32 v47, v47, v56\n"
    "v_mfma_f32_16x16x32_bf16 a[60:63], a[248:251], a[204:207], a[60:63]\n"
    "PD\n"
    "ds_read_b64_tr_b16 a[232:233], v13 offset:16384\n"
    "v_cvt_pk_bf16_f32 v112, v40, v41\n"
    "v_mfma_f32_16x16x32_bf16 a[60:63], a[252:255], a[220:223], a[60:63]\n"
    "PD\n"
    "v_cvt_pk_bf16_f32 v113, v42, v43\n"
    "v_cvt_pk_bf16_f32 v114, v44, v45\n"
    "v_mfma_f32_16x16x32_bf16 a[56:59], a[252:255], a[216:219], a[56:59]\n"
    "PD\n"
    "ds_read_b64_tr_b16 a[234:235], v13 offset:20480\n"
    "v_cvt_pk_bf16_f32 v115, v46, v47\n"
    "v_mfma_f32_16x16x32_bf16 a[56:59], a[248:251], a[200:203], a[56:59]\n"
    "PD\n"
    "buffer_store_dwordx4 v[112:115], v4, s[52:55], s69 offen offset:1024 sc1\n"
    "v_accvgpr_read_b32 v48, a100\n"
    "v_accvgpr_read_b32 v49, a101\n"
    "v_mfma_f32_16x16x32_bf16 a[52:55], a[248:251], a[196:199], a[52:55]\n"
    "PD\n"
    "ds_read_b64_tr_b16 a[236:237], v13 offset:24576\n"
    "v_accvgpr_read_b32 v50, a102\n"
    "v_mfma_f32_16x16x32_bf16 a[52:55], a[252:255], a[212:215], a[52:55]\n"
    "PD\n"
    "v_accvgpr_read_b32 v51, a103\n"
    "v_accvgpr_read_b32 v52, a116\n"
    "v_mfma_f32_16x16x32_bf16 a[48:51], a[252:255], a[208:211], a[48:51]\n"
    "PD\n"
    "ds_read_b64_tr_b16 a[238:239], v13 offset:28672\n"
    "v_accvgpr_read_b32 v53, a117\n"
    "v_mfma_f32_16x16x32_bf16 a[48:51], a[248:251], a[192:195], a[48:51]\n"
    "PD\n"
    "s_waitcnt lgkmcnt(4)\n"
    "v_accvgpr_read_b32 v54, a118\n"
    "v_accvgpr_read_b32 v55, a119\n"
    "v_mfma_f32_16x16x32_bf16 a[64:67], a[224:227], a[128:131], 0\n"
    "PD\n"
    "ds_read_b64_tr_b16 a[240:241], v12 offset:49152\n"
    "v_mul_f32 v48, v48, v57\n"
    "v_mfma_f32_16x16x32_bf16 a[64:67], a[228:231], a[144:147], a[64:67]\n"
    "PD\n"
    "v_mul_f32 v49, v49, v57\n"
    "v_mul_f32 v50, v50, v57\n"
    "v_mfma_f32_16x16x32_bf16 a[68:71], a[228:231], a[148:151], 0\n"
    "PD\n"
    "ds_read_b64_tr_b16 a[242:243], v12 offset:53248\n"
    "v_mul_f32 v51, v51, v57\n"
    "v_mfma_f32_16x16x32_bf16 a[68:71], a[224:227], a[132:135], a[68:71]\n"
    "PD\n"
    "v_mul_f32 v52, v52, v57\n"
    "v_mul_f32 v53, v53, v57\n"
    "v_mfma_f32_16x16x32_bf16 a[72:75], a[224:227], a[136:139], 0\n"
    "PD\n"
    "ds_read_b64_tr_b16 a[244:245], v12 offset:57344\n"
    "v_mul_f32 v54, v54, v57\n"
    "v_mfma_f32_16x16x32_bf16 a[72:75], a[228:231], a[152:155], a[72:75]\n"
    "PD\n"
    "v_mul_f32 v55, v55, v57\n"
    "v_cvt_pk_bf16_f32 v116, v48, v49\n"
    "v_mfma_f32_16x16x32_bf16 a[76:79], a[228:231], a[156:159], 0\n"
    "PD\n"
    "ds_read_b64_tr_b16 a[246:247], v12 offset:61440\n"
    "v_xor_b32 v12, 0x10000, v12\n"
    "v_mfma_f32_16x16x32_bf16 a[76:79], a[224:227], a[140:143], a[76:79]\n"
    "PD\n"
    "s_waitcnt lgkmcnt(4)\n"
    "v_cvt_pk_bf16_f32 v117, v50, v51\n"
    "v_cvt_pk_bf16_f32 v118, v52, v53\n"
    "v_mfma_f32_16x16x32_bf16 a[92:95], a[232:235], a[140:143], 0\n"
    "PD\n"
    "ds_read_b64_tr_b16 a[248:249], v13 offset:49152\n"
    "v_cvt_pk_bf16_f32 v119, v54, v55\n"
    "v_mfma_f32_16x16x32_bf16 a[92:95], a[236:239], a[156:159], a[92:95]\n"
    "PD\n"
    "buffer_store_dwordx4 v[116:119], v4, s[52:55], s69 offen offset:3072 sc1\n"
    "v_accvgpr_read_b32 v40, a104\n"
    "v_accvgpr_read_b32 v41, a105\n"
    "v_mfma_f32_16x16x32_bf16 a[88:91], a[236:239], a[152:155], 0\n"
    "PD\n"
    "ds_read_b64_tr_b16 a[250:251], v13 offset:53248\n"
    "v_accvgpr_read_b32 v42, a106\n"
    "v_mfma_f32_16x16x32_bf16 a[88:91], a[232:235], a[136:139], a[88:91]\n"
    "PD\n"
    "v_accvgpr_read_b32 v43, a107\n"
    "v_accvgpr_read_b32 v44, a120\n"
    "v_mfma_f32_16x16x32_bf16 a[84:87], a[232:235], a[132:135], 0\n"
    "PD\n"
    "ds_read_b64_tr_b16 a[252:253], v13 offset:57344\n"
    "v_accvgpr_read_b32 v45, a121\n"
    "v_mfma_f32_16x16x32_bf16 a[84:87], a[236:239], a[148:151], a[84:87]\n"
    "PD\n"
    "v_accvgpr_read_b32 v46, a122\n"
    "v_accvgpr_read_b32 v47, a123\n"
    "v_mfma_f32_16x16x32_bf16 a[80:83], a[236:239], a[144:147], 0\n"
    "PD\n"
    "ds_read_b64_tr_b16 a[254:255], v13 offset:61440\n"
    "v_xor_b32 v13, 0x10000, v13\n"
    "v_mfma_f32_16x16x32_bf16 a[80:83], a[232:235], a[128:131], a[80:83]\n"
    "PD\n"
    "s_waitcnt lgkmcnt(4)\n"
    "v_mul_f32 v40, v40, v58\n"
    "v_mul_f32 v41, v41, v58\n"
    "v_mfma_f32_16x16x32_bf16 a[64:67], a[240:243], a[192:195], a[64:67]\n"
    "PD\n"
    "ds_read_b64_tr_b16 a[224:225], v14 offset:16384\n"
    "v_mul_f32 v42, v42, v58\n"
    "v_mfma_f32_16x16x32_bf16 a[64:67], a[244:247], a[208:211], a[64:67]\n"
    "PD\n"
    "v_mul_f32 v43, v43, v58\n"
    "v_mul_f32 v44, v44, v58\n"
    "v_mfma_f32_16x16x32_bf16 a[68:71], a[244:247], a[212:215], a[68:71]\n"
    "PD\n"
    "ds_read_b64_tr_b16 a[226:227], v14 offset:20480\n"
    "v_mul_f32 v45, v45, v58\n"
    "v_mfma_f32_16x16x32_bf16 a[68:71], a[240:243], a[196:199], a[68:71]\n"
    "PD\n"
    "v_mul_f32 v46, v46, v58\n"
    "v_mul_f32 v47, v47, v58\n"
    "v_mfma_f32_16x16x32_bf16 a[72:75], a[240:243], a[200:203], a[72:75]\n"
    "PD\n"
    "ds_read_b64_tr_b16 a[228:229], v14 offset:24576\n"
    "v_cvt_pk_bf16_f32 v120, v40, v41\n"
    "v_mfma_f32_16x16x32_bf16 a[72:75], a[244:247], a[216:219], a[72:75]\n"
    "PD\n"
    "v_cvt_pk_bf16_f32 v121, v42, v43\n"
    "v_cvt_pk_bf16_f32 v122, v44, v45\n"
    "v_mfma_f32_16x16x32_bf16 a[76:79], a[244:247], a[220:223], a[76:79]\n"
    "PD\n"
    "ds_read_b64_tr_b16 a[230:231], v14 offset:28672\n"
    "v_cvt_pk_bf16_f32 v123, v46, v47\n"
    "v_mfma_f32_16x16x32_bf16 a[76:79], a[240:243], a[204:207], a[76:79]\n"
    "PD\n"
    "s_waitcnt lgkmcnt(4)\n"
    "buffer_store_dwordx4 v[120:123], v5, s[52:55], s69 offen offset:1024 sc1\n"
    "v_accvgpr_read_b32 v48, a108\n"
    "v_mfma_f32_16x16x32_bf16 a[92:95], a[248:251], a[204:207], a[92:95]\n"
    "PD\n"
    "ds_read_b64_tr_b16 a[232:233], v15 offset:16384\n"
    "v_accvgpr_read_b32 v49, a109\n"
    "v_mfma_f32_16x16x32_bf16 a[92:95], a[252:255], a[220:223], a[92:95]\n"
    "PD\n"
    "v_accvgpr_read_b32 v50, a110\n"
    "v_accvgpr_read_b32 v51, a111\n"
    "v_mfma_f32_16x16x32_bf16 a[88:91], a[252:255], a[216:219], a[88:91]\n"
    "PD\n"
    "ds_read_b64_tr_b16 a[234:235], v15 offset:20480\n"
    "v_accvgpr_read_b32 v52, a124\n"
    "v_mfma_f32_16x16x32_bf16 a[88:91], a[248:251], a[200:203], a[88:91]\n"
    "PD\n"
    "v_accvgpr_read_b32 v53, a125\n"
    "v_accvgpr_read_b32 v54, a126\n"
    "v_mfma_f32_16x16x32_bf16 a[84:87], a[248:251], a[196:199], a[84:87]\n"
    "PD\n"
    "ds_read_b64_tr_b16 a[236:237], v15 offset:24576\n"
    "v_accvgpr_read_b32 v55, a127\n"
    "v_mfma_f32_16x16x32_bf16 a[84:87], a[252:255], a[212:215], a[84:87]\n"
    "PD\n"
    "v_mul_f32 v48, v48, v59\n"
    "v_mul_f32 v49, v49, v59\n"
    "v_mfma_f32_16x16x32_bf16 a[80:83], a[252:255], a[208:211], a[80:83]\n"
    "PD\n"
    "ds_read_b64_tr_b16 a[238:239], v15 offset:28672\n"
    "v_mul_f32 v50, v50, v59\n"
    "v_mfma_f32_16x16x32_bf16 a[80:83], a[248:251], a[192:195], a[80:83]\n"
    "PD\n"
    "s_waitcnt lgkmcnt(4)\n"
    "v_mul_f32 v51, v51, v59\n"
    "v_mul_f32 v52, v52, v59\n"
    "v_mfma_f32_16x16x32_bf16 a[96:99], a[224:227], a[128:131], 0\n"
    "PD\n"
    "ds_read_b64_tr_b16 a[240:241], v14 offset:49152\n"
    "v_mul_f32 v53, v53, v59\n"
    "v_mfma_f32_16x16x32_bf16 a[96:99], a[228:231], a[144:147], a[96:99]\n"
    "PD\n"
    "v_mul_f32 v54, v54, v59\n"
    "v_mul_f32 v55, v55, v59\n"
    "v_mfma_f32_16x16x32_bf16 a[100:103], a[228:231], a[148:151], 0\n"
    "PD\n"
    "ds_read_b64_tr_b16 a[242:243], v14 offset:53248\n"
    "v_cvt_pk_bf16_f32 v124, v48, v49\n"
    "v_mfma_f32_16x16x32_bf16 a[100:103], a[224:227], a[132:135], a[100:103]\n"
    "PD\n"
    "v_cvt_pk_bf16_f32 v125, v50, v51\n"
    "v_cvt_pk_bf16_f32 v126, v52, v53\n"
    "v_mfma_f32_16x16x32_bf16 a[104:107], a[224:227], a[136:139], 0\n"
    "PD\n"
    "ds_read_b64_tr_b16 a[244:245], v14 offset:57344\n"
    "v_cvt_pk_bf16_f32 v127, v54, v55\n"
    "v_mfma_f32_16x16x32_bf16 a[104:107], a[228:231], a[152:155], a[104:107]\n"
    "PD\n"
    "buffer_store_dwordx4 v[124:127], v5, s[52:55], s69 offen offset:3072 sc1\n"
    "v_mfma_f32_16x16x32_bf16 a[108:111], a[228:231], a[156:159], 0\n"
    "PD\n"
    "ds_read_b64_tr_b16 a[246:247], v14 offset:61440\n"
    "v_xor_b32 v14, 0x10000, v14\n"
    "v_mfma_f32_16x16x32_bf16 a[108:111], a[224:227], a[140:143], a[108:111]\n"
    "PD\n"
    "s_waitcnt lgkmcnt(4)\n"
    "v_mfma_f32_16x16x32_bf16 a[124:127], a[232:235], a[140:143], 0\n"
    "PD\n"
    "ds_read_b64_tr_b16 a[248:249], v15 offset:49152\n"
    "v_mfma_f32_16x16x32_bf16 a[124:127], a[236:239], a[156:159], a[124:127]\n"
    "PD\n"
    "v_mfma_f32_16x16x32_bf16 a[120:123], a[236:239], a[152:155], 0\n"
    "PD\n"
    "ds_read_b64_tr_b16 a[250:251], v15 offset:53248\n"
    "v_mfma_f32_16x16x32_bf16 a[120:123], a[232:235], a[136:139], a[120:123]\n"
    "PD\n"
    "v_mfma_f32_16x16x32_bf16 a[116:119], a[232:235], a[132:135], 0\n"
    "PD\n"
    "ds_read_b64_tr_b16 a[252:253], v15 offset:57344\n"
    "v_mfma_f32_16x16x32_bf16 a[116:119], a[236:239], a[148:151], a[116:119]\n"
    "PD\n"
    "v_mfma_f32_16x16x32_bf16 a[112:115], a[236:239], a[144:147], 0\n"
    "PD\n"
    "ds_read_b64_tr_b16 a[254:255], v15 offset:61440\n"
    "v_xor_b32 v15, 0x10000, v15\n"
    "v_mfma_f32_16x16x32_bf16 a[112:115], a[232:235], a[128:131], a[112:115]\n"
    "PD\n"
    "s_waitcnt lgkmcnt(4)\n"
    "s_waitcnt vmcnt(22)\n"
    "s_barrier\n"
    "v_mfma_f32_16x16x32_bf16 a[96:99], a[240:243], a[192:195], a[96:99]\n"
    "PD\n"
    "ds_read_b64_tr_b16 a[224:225], v8 offset:0\n"
    "v_mfma_f32_16x16x32_bf16 a[96:99], a[244:247], a[208:211], a[96:99]\n"
    "PD\n"
    "buffer_load_dwordx4 v24, s[44:47], s57 offen offset:0 lds\n"
    "v_mfma_f32_16x16x32_bf16 a[100:103], a[244:247], a[212:215], a[100:103]\n"
    "PD\n"
    "ds_read_b64_tr_b16 a[226:227], v8 offset:4096\n"
    "v_mfma_f32_16x16x32_bf16 a[100:103], a[240:243], a[196:199], a[100:103]\n"
    "PD\n"
    "buffer_load_dwordx4 v25, s[44:47], s57 offen offset:1024 lds\n"
    "v_mfma_f32_16x16x32_bf16 a[104:107], a[240:243], a[200:203], a[104:107]\n"
    "PD\n"
    "ds_read_b64_tr_b16 a[228:229], v8 offset:8192\n"
    "v_mfma_f32_16x16x32_bf16 a[104:107], a[244:247], a[216:219], a[104:107]\n"
    "PD\n"
    "buffer_load_dwordx4 v26, s[44:47], s57 offen offset:2048 lds\n"
    "v_mfma_f32_16x16x32_bf16 a[108:111], a[244:247], a[220:223], a[108:111]\n"
    "PD\n"
    "ds_read_b64_tr_b16 a[230:231], v8 offset:12288\n"
    "v_mfma_f32_16x16x32_bf16 a[108:111], a[240:243], a[204:207], a[108:111]\n"
    "PD\n"
    "s_waitcnt lgkmcnt(4)\n"
    "v_mfma_f32_16x16x32_bf16 a[124:127], a[248:251], a[204:207], a[124:127]\n"
    "PD\n"
    "ds_read_b64_tr_b16 a[232:233], v9 offset:0\n"
    "v_mfma_f32_16x16x32_bf16 a[124:127], a[252:255], a[220:223], a[124:127]\n"
    "PD\n"
    "buffer_load_dwordx4 v27, s[44:47], s57 offen offset:3072 lds\n"
    "v_mfma_f32_16x16x32_bf16 a[120:123], a[252:255], a[216:219], a[120:123]\n"
    "PD\n"
    "ds_read_b64_tr_b16 a[234:235], v9 offset:4096\n"
    "v_mfma_f32_16x16x32_bf16 a[120:123], a[248:251], a[200:203], a[120:123]\n"
    "PD\n"
    "s_add_u32 s57, s57, s59\n"
    "v_mfma_f32_16x16x32_bf16 a[116:119], a[248:251], a[196:199], a[116:119]\n"
    "PD\n"
    "ds_read_b64_tr_b16 a[236:237], v9 offset:8192\n"
    "v_mfma_f32_16x16x32_bf16 a[116:119], a[252:255], a[212:215], a[116:119]\n"
    "PD\n"
    "v_mfma_f32_16x16x32_bf16 a[112:115], a[252:255], a[208:211], a[112:115]\n"
    "PD\n"
    "ds_read_b64_tr_b16 a[238:239], v9 offset:12288\n"
    "v_mfma_f32_16x16x32_bf16 a[112:115], a[248:251], a[192:195], a[112:115]\n"
    "PD\n"
    "s_waitcnt lgkmcnt(4)\n"
    "s_branch L_U\n"
    "F_O:\n"
    "PP\n"
    "s_waitcnt vmcnt(20) lgkmcnt(4)\n"
    "v_mfma_f32_16x16x32_bf16 v[128:131], a[224:227], a[128:131], 0\n"
    "PD\n"
    "ds_read_b64_tr_b16 a[240:241], v10 offset:0\n"
    "v_mfma_f32_16x16x32_bf16 v[128:131], a[228:231], a[144:147], v[128:131]\n"
    "PD\n"
    "s_mov_b32 s37, 0\n"
    "s_xor_b32 s60, s61, 0x10000\n"
    "v_mfma_f32_16x16x32_bf16 v[132:135], a[228:231], a[148:151], 0\n"
    "PD\n"
    "ds_read_b64_tr_b16 a[242:243], v10 offset:4096\n"
    "v_accvgpr_read_b32 v40, a96\n"
    "v_mfma_f32_16x16x32_bf16 v[132:135], a[224:227], a[132:135], v[132:135]\n"
    "PD\n"
    "s_movk_i32 s56, 24576\n"
    "s_lshr_b32 s38, s30, 8\n"
    "v_accvgpr_read_b32 v41, a97\n"
    "v_mfma_f32_16x16x32_bf16 v[136:139], a[224:227], a[136:139], 0\n"
    "PD\n"
    "ds_read_b64_tr_b16 a[244:245], v10 offset:8192\n"
    "v_accvgpr_read_b32 v42, a98\n"
    "v_mfma_f32_16x16x32_bf16 v[136:139], a[228:231], a[152:155], v[136:139]\n"
    "PD\n"
    "s_sub_u32 s38, s38, 2\n"
    "s_mov_b32 s39, 1\n"
    "v_accvgpr_read_b32 v43, a99\n"
    "v_mfma_f32_16x16x32_bf16 v[140:143], a[228:231], a[156:159], 0\n"
    "PD\n"
    "ds_read_b64_tr_b16 a[246:247], v10 offset:12288\n"
    "v_accvgpr_read_b32 v44, a112\n"
    "v_mfma_f32_16x16x32_bf16 v[140:143], a[224:227], a[140:143], v[140:143]\n"
    "PD\n"
    "s_waitcnt lgkmcnt(4)\n"
    "v_accvgpr_read_b32 v45, a113\n"
    "v_accvgpr_read_b32 v46, a114\n"
    "v_mfma_f32_16x16x32_bf16 v[156:159], a[232:235], a[140:143], 0\n"
    "PD\n"
    "ds_read_b64_tr_b16 a[248:249], v11 offset:0\n"
    "v_accvgpr_read_b32 v47, a115\n"
    "v_mfma_f32_16x16x32_bf16 v[156:159], a[236:239], a[156:159], v[156:159]\n"
    "PD\n"
    "s_movk_i32 s56, 0x6000\n"
    "s_mov_b32 s37, 1\n"
    "v_mul_f32 v40, v40, v56\n"
    "v_mfma_f32_16x16x32_bf16 v[152:155], a[236:239], a[152:155], 0\n"
    "PD\n"
    "ds_read_b64_tr_b16 a[250:251], v11 offset:4096\n"
    "v_mul_f32 v41, v41, v56\n"
    "v_mfma_f32_16x16x32_bf16 v[152:155], a[232:235], a[136:139], v[152:155]\n"
    "PD\n"
    "s_mov_b32 s39, 2\n"
    "s_mov_b64 vcc, -1\n"
    "v_mul_f32 v42, v42, v56\n"
    "v_mfma_f32_16x16x32_bf16 v[148:151], a[232:235], a[132:135], 0\n"
    "PD\n"
    "ds_read_b64_tr_b16 a[252:253], v11 offset:8192\n"
    "v_mul_f32 v43, v43, v56\n"
    "v_mfma_f32_16x16x32_bf16 v[148:151], a[236:239], a[148:151], v[148:151]\n"
    "PD\n"
    "s_add_u32 m0, s60, 16384\n"
    "v_mul_f32 v44, v44, v56\n"
    "v_mul_f32 v45, v45, v56\n"
    "v_mfma_f32_16x16x32_bf16 v[144:147], a[236:239], a[144:147], 0\n"
    "PD\n"
    "ds_read_b64_tr_b16 a[254:255], v11 offset:12288\n"
    "v_mul_f32 v46, v46, v56\n"
    "v_mfma_f32_16x16x32_bf16 v[144:147], a[232:235], a[128:131], v[144:147]\n"
    "PD\n"
    "s_waitcnt lgkmcnt(4)\n"
    "v_mul_f32 v47, v47, v56\n"
    "v_cvt_pk_bf16_f32 v112, v40, v41\n"
    "v_mfma_f32_16x16x32_bf16 v[160:163], a[240:243], a[128:131], 0\n"
    "PD\n"
    "ds_read_b64_tr_b16 a[224:225], v12 offset:0\n"
    "v_cvt_pk_bf16_f32 v113, v42, v43\n"
    "v_mfma_f32_16x16x32_bf16 v[160:163], a[244:247], a[144:147], v[160:163]\n"
    "PD\n"
    "buffer_load_dwordx4 v28, s[48:51], s58 offen offset:0 lds\n"
    "v_cvt_pk_bf16_f32 v114, v44, v45\n"
    "v_cvt_pk_bf16_f32 v115, v46, v47\n"
    "v_mfma_f32_16x16x32_bf16 v[164:167], a[244:247], a[148:151], 0\n"
    "PD\n"
    "buffer_store_dwordx4 v[112:115], v4, s[52:55], s69 offen offset:1024 sc1\n"
    "ds_read_b64_tr_b16 a[226:227], v12 offset:4096\n"
    "v_mfma_f32_16x16x32_bf16 v[164:167], a[240:243], a[132:135], v[164:167]\n"
    "PD\n"
    "v_accvgpr_read_b32 v48, a100\n"
    "v_accvgpr_read_b32 v49, a101\n"
    "v_mfma_f32_16x16x32_bf16 v[168:171], a[240:243], a[136:139], 0\n"
    "PD\n"
    "ds_read_b64_tr_b16 a[228:229], v12 offset:8192\n"
    "v_accvgpr_read_b32 v50, a102\n"
    "v_mfma_f32_16x16x32_bf16 v[168:171], a[244:247], a[152:155], v[168:171]\n"
    "PD\n"
    "buffer_load_dwordx4 v29, s[48:51], s58 offen offset:1024 lds\n"
    "v_accvgpr_read_b32 v51, a103\n"
    "v_accvgpr_read_b32 v52, a116\n"
    "v_mfma_f32_16x16x32_bf16 v[172:175], a[244:247], a[156:159], 0\n"
    "PD\n"
    "ds_read_b64_tr_b16 a[230:231], v12 offset:12288\n"
    "v_accvgpr_read_b32 v53, a117\n"
    "v_mfma_f32_16x16x32_bf16 v[172:175], a[240:243], a[140:143], v[172:175]\n"
    "PD\n"
    "s_waitcnt lgkmcnt(4)\n"
    "v_accvgpr_read_b32 v54, a118\n"
    "v_accvgpr_read_b32 v55, a119\n"
    "v_mfma_f32_16x16x32_bf16 v[188:191], a[248:251], a[140:143], 0\n"
    "PD\n"
    "ds_read_b64_tr_b16 a[232:233], v13 offset:0\n"
    "v_mul_f32 v48, v48, v57\n"
    "v_mfma_f32_16x16x32_bf16 v[188:191], a[252:255], a[156:159], v[188:191]\n"
    "PD\n"
    "buffer_load_dwordx4 v30, s[48:51], s58 offen offset:2048 lds\n"
    "v_mul_f32 v49, v49, v57\n"
    "v_mul_f32 v50, v50, v57\n"
    "v_mfma_f32_16x16x32_bf16 v[184:187], a[252:255], a[152:155], 0\n"
    "PD\n"
    "ds_read_b64_tr_b16 a[234:235], v13 offset:4096\n"
    "v_mul_f32 v51, v51, v57\n"
    "v_mfma_f32_16x16x32_bf16 v[184:187], a[248:251], a[136:139], v[184:187]\n"
    "PD\n"
    "v_mul_f32 v52, v52, v57\n"
    "v_mul_f32 v53, v53, v57\n"
    "v_mfma_f32_16x16x32_bf16 v[180:183], a[248:251], a[132:135], 0\n"
    "PD\n"
    "ds_read_b64_tr_b16 a[236:237], v13 offset:8192\n"
    "v_mul_f32 v54, v54, v57\n"
    "v_mfma_f32_16x16x32_bf16 v[180:183], a[252:255], a[148:151], v[180:183]\n"
    "PD\n"
    "buffer_load_dwordx4 v31, s[48:51], s58 offen offset:3072 lds\n"
    "v_mul_f32 v55, v55, v57\n"
    "v_cvt_pk_bf16_f32 v116, v48, v49\n"
    "v_mfma_f32_16x16x32_bf16 v[176:179], a[252:255], a[144:147], 0\n"
    "PD\n"
    "ds_read_b64_tr_b16 a[238:239], v13 offset:12288\n"
    "v_cvt_pk_bf16_f32 v117, v50, v51\n"
    "v_mfma_f32_16x16x32_bf16 v[176:179], a[248:251], a[128:131], v[176:179]\n"
    "PD\n"
    "s_waitcnt lgkmcnt(4)\n"
    "v_cvt_pk_bf16_f32 v118, v52, v53\n"
    "v_cvt_pk_bf16_f32 v119, v54, v55\n"
    "v_mfma_f32_16x16x32_bf16 v[192:195], a[224:227], a[128:131], 0\n"
    "PD\n"
    "ds_read_b64_tr_b16 a[240:241], v14 offset:0\n"
    "v_accvgpr_read_b32 v40, a104\n"
    "v_mfma_f32_16x16x32_bf16 v[192:195], a[228:231], a[144:147], v[192:195]\n"
    "PD\n"
    "s_add_u32 s58, s58, s59\n"
    "v_accvgpr_read_b32 v41, a105\n"
    "v_accvgpr_read_b32 v42, a106\n"
    "v_mfma_f32_16x16x32_bf16 v[196:199], a[228:231], a[148:151], 0\n"
    "PD\n"
    "ds_read_b64_tr_b16 a[242:243], v14 offset:4096\n"
    "v_accvgpr_read_b32 v43, a107\n"
    "v_mfma_f32_16x16x32_bf16 v[196:199], a[224:227], a[132:135], v[196:199]\n"
    "PD\n"
    "s_add_u32 m0, s60, 32768\n"
    "v_accvgpr_read_b32 v44, a120\n"
    "v_accvgpr_read_b32 v45, a121\n"
    "v_mfma_f32_16x16x32_bf16 v[200:203], a[224:227], a[136:139], 0\n"
    "PD\n"
    "ds_read_b64_tr_b16 a[244:245], v14 offset:8192\n"
    "v_accvgpr_read_b32 v46, a122\n"
    "v_mfma_f32_16x16x32_bf16 v[200:203], a[228:231], a[152:155], v[200:203]\n"
    "PD\n"
    "buffer_load_dwordx4 v24, s[44:47], s57 offen offset:0 lds\n"
    "v_accvgpr_read_b32 v47, a123\n"
    "v_mul_f32 v40, v40, v58\n"
    "v_mfma_f32_16x16x32_bf16 v[204:207], a[228:231], a[156:159], 0\n"
    "PD\n"
    "buffer_store_dwordx4 v[116:119], v4, s[52:55], s69 offen offset:3072 sc1\n"
    "ds_read_b64_tr_b16 a[246:247], v14 offset:12288\n"
    "v_mfma_f32_16x16x32_bf16 v[204:207], a[224:227], a[140:143], v[204:207]\n"
    "PD\n"
    "s_waitcnt lgkmcnt(4)\n"
    "v_mul_f32 v41, v41, v58\n"
    "v_mul_f32 v42, v42, v58\n"
    "v_mfma_f32_16x16x32_bf16 v[220:223], a[232:235], a[140:143], 0\n"
    "PD\n"
    "ds_read_b64_tr_b16 a[248:249], v15 offset:0\n"
    "v_mul_f32 v43, v43, v58\n"
    "v_mfma_f32_16x16x32_bf16 v[220:223], a[236:239], a[156:159], v[220:223]\n"
    "PD\n"
    "buffer_load_dwordx4 v25, s[44:47], s57 offen offset:1024 lds\n"
    "v_mul_f32 v44, v44, v58\n"
    "v_mul_f32 v45, v45, v58\n"
    "v_mfma_f32_16x16x32_bf16 v[216:219], a[236:239], a[152:155], 0\n"
    "PD\n"
    "ds_read_b64_tr_b16 a[250:251], v15 offset:4096\n"
    "v_mul_f32 v46, v46, v58\n"
    "v_mfma_f32_16x16x32_bf16 v[216:219], a[232:235], a[136:139], v[216:219]\n"
    "PD\n"
    "v_mul_f32 v47, v47, v58\n"
    "v_cvt_pk_bf16_f32 v120, v40, v41\n"
    "v_mfma_f32_16x16x32_bf16 v[212:215], a[232:235], a[132:135], 0\n"
    "PD\n"
    "ds_read_b64_tr_b16 a[252:253], v15 offset:8192\n"
    "v_cvt_pk_bf16_f32 v121, v42, v43\n"
    "v_mfma_f32_16x16x32_bf16 v[212:215], a[236:239], a[148:151], v[212:215]\n"
    "PD\n"
    "buffer_load_dwordx4 v26, s[44:47], s57 offen offset:2048 lds\n"
    "v_cvt_pk_bf16_f32 v122, v44, v45\n"
    "v_cvt_pk_bf16_f32 v123, v46, v47\n"
    "v_mfma_f32_16x16x32_bf16 v[208:211], a[236:239], a[144:147], 0\n"
    "PD\n"
    "ds_read_b64_tr_b16 a[254:255], v15 offset:12288\n"
    "v_accvgpr_read_b32 v48, a108\n"
    "v_mfma_f32_16x16x32_bf16 v[208:211], a[232:235], a[128:131], v[208:211]\n"
    "PD\n"
    "s_waitcnt lgkmcnt(4)\n"
    "v_accvgpr_read_b32 v49, a109\n"
    "v_accvgpr_read_b32 v50, a110\n"
    "v_mfma_f32_16x16x32_bf16 v[224:227], a[240:243], a[128:131], 0\n"
    "PD\n"
    "ds_read_b64_tr_b16 a[224:225], v8 offset:16384\n"
    "v_accvgpr_read_b32 v51, a111\n"
    "v_mfma_f32_16x16x32_bf16 v[224:227], a[244:247], a[144:147], v[224:227]\n"
    "PD\n"
    "buffer_load_dwordx4 v27, s[44:47], s57 offen offset:3072 lds\n"
    "v_accvgpr_read_b32 v52, a124\n"
    "v_accvgpr_read_b32 v53, a125\n"
    "v_mfma_f32_16x16x32_bf16 v[228:231], a[244:247], a[148:151], 0\n"
    "PD\n"
    "ds_read_b64_tr_b16 a[226:227], v8 offset:20480\n"
    "v_accvgpr_read_b32 v54, a126\n"
    "v_mfma_f32_16x16x32_bf16 v[228:231], a[240:243], a[132:135], v[228:231]\n"
    "PD\n"
    "s_add_u32 s57, s57, s59\n"
    "v_accvgpr_read_b32 v55, a127\n"
    "v_mul_f32 v48, v48, v59\n"
    "v_mfma_f32_16x16x32_bf16 v[232:235], a[240:243], a[136:139], 0\n"
    "PD\n"
    "ds_read_b64_tr_b16 a[228:229], v8 offset:24576\n"
    "v_mul_f32 v49, v49, v59\n"
    "v_mfma_f32_16x16x32_bf16 v[232:235], a[244:247], a[152:155], v[232:235]\n"
    "PD\n"
    "v_mul_f32 v50, v50, v59\n"
    "v_mul_f32 v51, v51, v59\n"
    "v_mfma_f32_16x16x32_bf16 v[236:239], a[244:247], a[156:159], 0\n"
    "PD\n"
    "ds_read_b64_tr_b16 a[230:231], v8 offset:28672\n"
    "v_mul_f32 v52, v52, v59\n"
    "v_mfma_f32_16x16x32_bf16 v[236:239], a[240:243], a[140:143], v[236:239]\n"
    "PD\n"
    "s_waitcnt lgkmcnt(4)\n"
    "v_mul_f32 v53, v53, v59\n"
    "v_mul_f32 v54, v54, v59\n"
    "v_mfma_f32_16x16x32_bf16 v[252:255], a[248:251], a[140:143], 0\n"
    "PD\n"
    "ds_read_b64_tr_b16 a[232:233], v9 offset:16384\n"
    "v_mul_f32 v55, v55, v59\n"
    "v_mfma_f32_16x16x32_bf16 v[252:255], a[252:255], a[156:159], v[252:255]\n"
    "PD\n"
    "v_cvt_pk_bf16_f32 v124, v48, v49\n"
    "v_cvt_pk_bf16_f32 v125, v50, v51\n"
    "v_mfma_f32_16x16x32_bf16 v[248:251], a[252:255], a[152:155], 0\n"
    "PD\n"
    "buffer_store_dwordx4 v[120:123], v5, s[52:55], s69 offen offset:1024 sc1\n"
    "ds_read_b64_tr_b16 a[234:235], v9 offset:20480\n"
    "v_mfma_f32_16x16x32_bf16 v[248:251], a[248:251], a[136:139], v[248:251]\n"
    "PD\n"
    "buffer_load_dwordx4 a[96:99], v20, s[40:43], s56 offen offset:0 sc1\n"
    "v_cvt_pk_bf16_f32 v126, v52, v53\n"
    "v_cvt_pk_bf16_f32 v127, v54, v55\n"
    "v_mfma_f32_16x16x32_bf16 v[244:247], a[248:251], a[132:135], 0\n"
    "PD\n"
    "ds_read_b64_tr_b16 a[236:237], v9 offset:24576\n"
    "v_accvgpr_read_b32 v40, a0\n"
    "v_mfma_f32_16x16x32_bf16 v[244:247], a[252:255], a[148:151], v[244:247]\n"
    "PD\n"
    "v_accvgpr_read_b32 v41, a1\n"
    "v_accvgpr_read_b32 v42, a2\n"
    "v_mfma_f32_16x16x32_bf16 v[240:243], a[252:255], a[144:147], 0\n"
    "PD\n"
    "ds_read_b64_tr_b16 a[238:239], v9 offset:28672\n"
    "v_accvgpr_read_b32 v43, a3\n"
    "v_mfma_f32_16x16x32_bf16 v[240:243], a[248:251], a[128:131], v[240:243]\n"
    "PD\n"
    "s_waitcnt vmcnt(24) lgkmcnt(4)\n"
    "v_accvgpr_read_b32 v44, a16\n"
    "v_accvgpr_read_b32 v45, a17\n"
    "v_mfma_f32_16x16x32_bf16 v[128:131], a[224:227], a[160:163], v[128:131]\n"
    "PD\n"
    "ds_read_b64_tr_b16 a[240:241], v10 offset:16384\n"
    "v_accvgpr_read_b32 v46, a18\n"
    "v_mfma_f32_16x16x32_bf16 v[128:131], a[228:231], a[176:179], v[128:131]\n"
    "PD\n"
    "buffer_load_dwordx4 a[112:115], v20, s[40:43], s56 offen offset:1024 sc1\n"
    "v_accvgpr_read_b32 v47, a19\n"
    "v_mul_f32 v40, v40, v56\n"
    "v_mfma_f32_16x16x32_bf16 v[132:135], a[228:231], a[180:183], v[132:135]\n"
    "PD\n"
    "ds_read_b64_tr_b16 a[242:243], v10 offset:20480\n"
    "v_mul_f32 v41, v41, v56\n"
    "v_mfma_f32_16x16x32_bf16 v[132:135], a[224:227], a[164:167], v[132:135]\n"
    "PD\n"
    "v_mul_f32 v42, v42, v56\n"
    "v_mul_f32 v43, v43, v56\n"
    "v_mfma_f32_16x16x32_bf16 v[136:139], a[224:227], a[168:171], v[136:139]\n"
    "PD\n"
    "ds_read_b64_tr_b16 a[244:245], v10 offset:24576\n"
    "v_mul_f32 v44, v44, v56\n"
    "v_mfma_f32_16x16x32_bf16 v[136:139], a[228:231], a[184:187], v[136:139]\n"
    "PD\n"
    "buffer_load_dwordx4 a[100:103], v21, s[40:43], s56 offen offset:0 sc1\n"
    "v_mul_f32 v45, v45, v56\n"
    "v_mul_f32 v46, v46, v56\n"
    "v_mfma_f32_16x16x32_bf16 v[140:143], a[228:231], a[188:191], v[140:143]\n"
    "PD\n"
    "ds_read_b64_tr_b16 a[246:247], v10 offset:28672\n"
    "v_mul_f32 v47, v47, v56\n"
    "v_mfma_f32_16x16x32_bf16 v[140:143], a[224:227], a[172:175], v[140:143]\n"
    "PD\n"
    "s_waitcnt lgkmcnt(4)\n"
    "v_cvt_pk_bf16_f32 v64, v40, v41\n"
    "v_cvt_pk_bf16_f32 v65, v42, v43\n"
    "v_mfma_f32_16x16x32_bf16 v[156:159], a[232:235], a[172:175], v[156:159]\n"
    "PD\n"
    "ds_read_b64_tr_b16 a[248:249], v11 offset:16384\n"
    "v_cvt_pk_bf16_f32 v66, v44, v45\n"
    "v_mfma_f32_16x16x32_bf16 v[156:159], a[236:239], a[188:191], v[156:159]\n"
    "PD\n"
    "buffer_load_dwordx4 a[116:119], v21, s[40:43], s56 offen offset:1024 sc1\n"
    "v_cvt_pk_bf16_f32 v67, v46, v47\n"
    "v_accvgpr_read_b32 v48, a4\n"
    "v_mfma_f32_16x16x32_bf16 v[152:155], a[236:239], a[184:187], v[152:155]\n"
    "PD\n"
    "ds_read_b64_tr_b16 a[250:251], v11 offset:20480\n"
    "v_accvgpr_read_b32 v49, a5\n"
    "v_mfma_f32_16x16x32_bf16 v[152:155], a[232:235], a[168:171], v[152:155]\n"
    "PD\n"
    "v_accvgpr_read_b32 v50, a6\n"
    "v_accvgpr_read_b32 v51, a7\n"
    "v_mfma_f32_16x16x32_bf16 v[148:151], a[232:235], a[164:167], v[148:151]\n"
    "PD\n"
    "ds_read_b64_tr_b16 a[252:253], v11 offset:24576\n"
    "v_accvgpr_read_b32 v52, a20\n"
    "v_mfma_f32_16x16x32_bf16 v[148:151], a[236:239], a[180:183], v[148:151]\n"
    "PD\n"
    "buffer_load_dwordx4 a[104:107], v22, s[40:43], s56 offen offset:0 sc1\n"
    "v_accvgpr_read_b32 v53, a21\n"
    "v_accvgpr_read_b32 v54, a22\n"
    "v_mfma_f32_16x16x32_bf16 v[144:147], a[236:239], a[176:179], v[144:147]\n"
    "PD\n"
    "buffer_store_dwordx4 v[124:127], v5, s[52:55], s69 offen offset:3072 sc1\n"
    "ds_read_b64_tr_b16 a[254:255], v11 offset:28672\n"
    "v_mfma_f32_16x16x32_bf16 v[144:147], a[232:235], a[160:163], v[144:147]\n"
    "PD\n"
    "s_waitcnt lgkmcnt(4)\n"
    "v_accvgpr_read_b32 v55, a23\n"
    "v_mul_f32 v48, v48, v57\n"
    "v_mfma_f32_16x16x32_bf16 v[160:163], a[240:243], a[160:163], v[160:163]\n"
    "PD\n"
    "ds_read_b64_tr_b16 a[224:225], v12 offset:16384\n"
    "v_mul_f32 v49, v49, v57\n"
    "v_mfma_f32_16x16x32_bf16 v[160:163], a[244:247], a[176:179], v[160:163]\n"
    "PD\n"
    "buffer_load_dwordx4 a[120:123], v22, s[40:43], s56 offen offset:1024 sc1\n"
    "v_mul_f32 v50, v50, v57\n"
    "v_mul_f32 v51, v51, v57\n"
    "v_mfma_f32_16x16x32_bf16 v[164:167], a[244:247], a[180:183], v[164:167]\n"
    "PD\n"
    "ds_read_b64_tr_b16 a[226:227], v12 offset:20480\n"
    "v_mul_f32 v52, v52, v57\n"
    "v_mfma_f32_16x16x32_bf16 v[164:167], a[240:243], a[164:167], v[164:167]\n"
    "PD\n"
    "v_mul_f32 v53, v53, v57\n"
    "v_mul_f32 v54, v54, v57\n"
    "v_mfma_f32_16x16x32_bf16 v[168:171], a[240:243], a[168:171], v[168:171]\n"
    "PD\n"
    "ds_read_b64_tr_b16 a[228:229], v12 offset:24576\n"
    "v_mul_f32 v55, v55, v57\n"
    "v_mfma_f32_16x16x32_bf16 v[168:171], a[244:247], a[184:187], v[168:171]\n"
    "PD\n"
    "buffer_load_dwordx4 a[108:111], v23, s[40:43], s56 offen offset:0 sc1\n"
    "v_cvt_pk_bf16_f32 v68, v48, v49\n"
    "v_cvt_pk_bf16_f32 v69, v50, v51\n"
    "v_mfma_f32_16x16x32_bf16 v[172:175], a[244:247], a[188:191], v[172:175]\n"
    "PD\n"
    "ds_read_b64_tr_b16 a[230:231], v12 offset:28672\n"
    "v_cvt_pk_bf16_f32 v70, v52, v53\n"
    "v_mfma_f32_16x16x32_bf16 v[172:175], a[240:243], a[172:175], v[172:175]\n"
    "PD\n"
    "s_waitcnt lgkmcnt(4)\n"
    "v_cvt_pk_bf16_f32 v71, v54, v55\n"
    "v_accvgpr_read_b32 v40, a8\n"
    "v_mfma_f32_16x16x32_bf16 v[188:191], a[248:251], a[172:175], v[188:191]\n"
    "PD\n"
    "ds_read_b64_tr_b16 a[232:233], v13 offset:16384\n"
    "v_accvgpr_read_b32 v41, a9\n"
    "v_mfma_f32_16x16x32_bf16 v[188:191], a[252:255], a[188:191], v[188:191]\n"
    "PD\n"
    "buffer_load_dwordx4 a[124:127], v23, s[40:43], s56 offen offset:1024 sc1\n"
    "v_accvgpr_read_b32 v42, a10\n"
    "v_accvgpr_read_b32 v43, a11\n"
    "v_mfma_f32_16x16x32_bf16 v[184:187], a[252:255], a[184:187], v[184:187]\n"
    "PD\n"
    "ds_read_b64_tr_b16 a[234:235], v13 offset:20480\n"
    "v_accvgpr_read_b32 v44, a24\n"
    "v_mfma_f32_16x16x32_bf16 v[184:187], a[248:251], a[168:171], v[184:187]\n"
    "PD\n"
    "s_add_u32 s56, s56, s69\n"
    "v_accvgpr_read_b32 v45, a25\n"
    "v_accvgpr_read_b32 v46, a26\n"
    "v_mfma_f32_16x16x32_bf16 v[180:183], a[248:251], a[164:167], v[180:183]\n"
    "PD\n"
    "ds_read_b64_tr_b16 a[236:237], v13 offset:24576\n"
    "v_accvgpr_read_b32 v47, a27\n"
    "v_mfma_f32_16x16x32_bf16 v[180:183], a[252:255], a[180:183], v[180:183]\n"
    "PD\n"
    "buffer_load_dwordx4 a[128:131], v20, s[40:43], s56 offen offset:0 sc1\n"
    "v_mul_f32 v40, v40, v58\n"
    "v_mul_f32 v41, v41, v58\n"
    "v_mfma_f32_16x16x32_bf16 v[176:179], a[252:255], a[176:179], v[176:179]\n"
    "PD\n"
    "ds_read_b64_tr_b16 a[238:239], v13 offset:28672\n"
    "v_mul_f32 v42, v42, v58\n"
    "v_mfma_f32_16x16x32_bf16 v[176:179], a[248:251], a[160:163], v[176:179]\n"
    "PD\n"
    "s_waitcnt lgkmcnt(4)\n"
    "v_mul_f32 v43, v43, v58\n"
    "v_mul_f32 v44, v44, v58\n"
    "v_mfma_f32_16x16x32_bf16 v[192:195], a[224:227], a[160:163], v[192:195]\n"
    "PD\n"
    "ds_read_b64_tr_b16 a[240:241], v14 offset:16384\n"
    "v_mul_f32 v45, v45, v58\n"
    "v_mfma_f32_16x16x32_bf16 v[192:195], a[228:231], a[176:179], v[192:195]\n"
    "PD\n"
    "buffer_load_dwordx4 a[144:147], v20, s[40:43], s56 offen offset:1024 sc1\n"
    "v_mul_f32 v46, v46, v58\n"
    "v_mul_f32 v47, v47, v58\n"
    "v_mfma_f32_16x16x32_bf16 v[196:199], a[228:231], a[180:183], v[196:199]\n"
    "PD\n"
    "buffer_store_dwordx4 v[64:67], v4, s[52:55], 0 offen offset:0 sc1\n"
    "ds_read_b64_tr_b16 a[242:243], v14 offset:20480\n"
    "v_mfma_f32_16x16x32_bf16 v[196:199], a[224:227], a[164:167], v[196:199]\n"
    "PD\n"
    "v_cvt_pk_bf16_f32 v72, v40, v41\n"
    "v_cvt_pk_bf16_f32 v73, v42, v43\n"
    "v_mfma_f32_16x16x32_bf16 v[200:203], a[224:227], a[168:171], v[200:203]\n"
    "PD\n"
    "ds_read_b64_tr_b16 a[244:245], v14 offset:24576\n"
    "v_cvt_pk_bf16_f32 v74, v44, v45\n"
    "v_mfma_f32_16x16x32_bf16 v[200:203], a[228:231], a[184:187], v[200:203]\n"
    "PD\n"
    "buffer_load_dwordx4 a[132:135], v21, s[40:43], s56 offen offset:0 sc1\n"
    "v_cvt_pk_bf16_f32 v75, v46, v47\n"
    "v_accvgpr_read_b32 v48, a12\n"
    "v_mfma_f32_16x16x32_bf16 v[204:207], a[228:231], a[188:191], v[204:207]\n"
    "PD\n"
    "ds_read_b64_tr_b16 a[246:247], v14 offset:28672\n"
    "v_accvgpr_read_b32 v49, a13\n"
    "v_mfma_f32_16x16x32_bf16 v[204:207], a[224:227], a[172:175], v[204:207]\n"
    "PD\n"
    "s_waitcnt lgkmcnt(4)\n"
    "v_accvgpr_read_b32 v50, a14\n"
    "v_accvgpr_read_b32 v51, a15\n"
    "v_mfma_f32_16x16x32_bf16 v[220:223], a[232:235], a[172:175], v[220:223]\n"
    "PD\n"
    "ds_read_b64_tr_b16 a[248:249], v15 offset:16384\n"
    "v_accvgpr_read_b32 v52, a28\n"
    "v_mfma_f32_16x16x32_bf16 v[220:223], a[236:239], a[188:191], v[220:223]\n"
    "PD\n"
    "buffer_load_dwordx4 a[148:151], v21, s[40:43], s56 offen offset:1024 sc1\n"
    "v_accvgpr_read_b32 v53, a29\n"
    "v_accvgpr_read_b32 v54, a30\n"
    "v_mfma_f32_16x16x32_bf16 v[216:219], a[236:239], a[184:187], v[216:219]\n"
    "PD\n"
    "ds_read_b64_tr_b16 a[250:251], v15 offset:20480\n"
    "v_accvgpr_read_b32 v55, a31\n"
    "v_mfma_f32_16x16x32_bf16 v[216:219], a[232:235], a[168:171], v[216:219]\n"
    "PD\n"
    "v_mul_f32 v48, v48, v59\n"
    "v_mul_f32 v49, v49, v59\n"
    "v_mfma_f32_16x16x32_bf16 v[212:215], a[232:235], a[164:167], v[212:215]\n"
    "PD\n"
    "ds_read_b64_tr_b16 a[252:253], v15 offset:24576\n"
    "v_mul_f32 v50, v50, v59\n"
    "v_mfma_f32_16x16x32_bf16 v[212:215], a[236:239], a[180:183], v[212:215]\n"
    "PD\n"
    "buffer_load_dwordx4 a[136:139], v22, s[40:43], s56 offen offset:0 sc1\n"
    "v_mul_f32 v51, v51, v59\n"
    "v_mul_f32 v52, v52, v59\n"
    "v_mfma_f32_16x16x32_bf16 v[208:211], a[236:239], a[176:179], v[208:211]\n"
    "PD\n"
    "ds_read_b64_tr_b16 a[254:255], v15 offset:28672\n"
    "v_mul_f32 v53, v53, v59\n"
    "v_mfma_f32_16x16x32_bf16 v[208:211], a[232:235], a[160:163], v[208:211]\n"
    "PD\n"
    "s_waitcnt lgkmcnt(4)\n"
    "s_barrier\n"
    "v_mul_f32 v54, v54, v59\n"
    "v_mfma_f32_16x16x32_bf16 v[224:227], a[240:243], a[160:163], v[224:227]\n"
    "PD\n"
    "ds_read_b64_tr_b16 a[224:225], v8 offset:32768\n"
    "v_mul_f32 v55, v55, v59\n"
    "v_mfma_f32_16x16x32_bf16 v[224:227], a[244:247], a[176:179], v[224:227]\n"
    "PD\n"
    "buffer_load_dwordx4 a[152:155], v22, s[40:43], s56 offen offset:1024 sc1\n"
    "v_cvt_pk_bf16_f32 v76, v48, v49\n"
    "v_cvt_pk_bf16_f32 v77, v50, v51\n"
    "v_mfma_f32_16x16x32_bf16 v[228:231], a[244:247], a[180:183], v[228:231]\n"
    "PD\n"
    "ds_read_b64_tr_b16 a[226:227], v8 offset:36864\n"
    "v_cvt_pk_bf16_f32 v78, v52, v53\n"
    "v_mfma_f32_16x16x32_bf16 v[228:231], a[240:243], a[164:167], v[228:231]\n"
    "PD\n"
    "v_cvt_pk_bf16_f32 v79, v54, v55\n"
    "v_accvgpr_read_b32 v40, a32\n"
    "v_mfma_f32_16x16x32_bf16 v[232:235], a[240:243], a[168:171], v[232:235]\n"
    "PD\n"
    "ds_read_b64_tr_b16 a[228:229], v8 offset:40960\n"
    "v_accvgpr_read_b32 v41, a33\n"
    "v_mfma_f32_16x16x32_bf16 v[232:235], a[244:247], a[184:187], v[232:235]\n"
    "PD\n"
    "buffer_load_dwordx4 a[140:143], v23, s[40:43], s56 offen offset:0 sc1\n"
    "v_accvgpr_read_b32 v42, a34\n"
    "v_accvgpr_read_b32 v43, a35\n"
    "v_mfma_f32_16x16x32_bf16 v[236:239], a[244:247], a[188:191], v[236:239]\n"
    "PD\n"
    "buffer_store_dwordx4 v[68:71], v4, s[52:55], 0 offen offset:2048 sc1\n"
    "ds_read_b64_tr_b16 a[230:231], v8 offset:45056\n"
    "v_mfma_f32_16x16x32_bf16 v[236:239], a[240:243], a[172:175], v[236:239]\n"
    "PD\n"
    "s_waitcnt lgkmcnt(4)\n"
    "v_accvgpr_read_b32 v44, a48\n"
    "v_accvgpr_read_b32 v45, a49\n"
    "v_mfma_f32_16x16x32_bf16 v[252:255], a[248:251], a[172:175], v[252:255]\n"
    "PD\n"
    "ds_read_b64_tr_b16 a[232:233], v9 offset:32768\n"
    "v_accvgpr_read_b32 v46, a50\n"
    "v_mfma_f32_16x16x32_bf16 v[252:255], a[252:255], a[188:191], v[252:255]\n"
    "PD\n"
    "buffer_load_dwordx4 a[156:159], v23, s[40:43], s56 offen offset:1024 sc1\n"
    "v_accvgpr_read_b32 v47, a51\n"
    "v_mul_f32 v40, v40, v56\n"
    "v_mfma_f32_16x16x32_bf16 v[248:251], a[252:255], a[184:187], v[248:251]\n"
    "PD\n"
    "ds_read_b64_tr_b16 a[234:235], v9 offset:36864\n"
    "v_mul_f32 v41, v41, v56\n"
    "v_mfma_f32_16x16x32_bf16 v[248:251], a[248:251], a[168:171], v[248:251]\n"
    "PD\n"
    "s_add_u32 s56, s56, s69\n"
    "v_mul_f32 v42, v42, v56\n"
    "v_mul_f32 v43, v43, v56\n"
    "v_mfma_f32_16x16x32_bf16 v[244:247], a[248:251], a[164:167], v[244:247]\n"
    "PD\n"
    "ds_read_b64_tr_b16 a[236:237], v9 offset:40960\n"
    "v_mul_f32 v44, v44, v56\n"
    "v_mfma_f32_16x16x32_bf16 v[244:247], a[252:255], a[180:183], v[244:247]\n"
    "PD\n"
    "s_add_u32 m0, s60, 49152\n"
    "v_mul_f32 v45, v45, v56\n"
    "v_mul_f32 v46, v46, v56\n"
    "v_mfma_f32_16x16x32_bf16 v[240:243], a[252:255], a[176:179], v[240:243]\n"
    "PD\n"
    "ds_read_b64_tr_b16 a[238:239], v9 offset:45056\n"
    "v_mul_f32 v47, v47, v56\n"
    "v_mfma_f32_16x16x32_bf16 v[240:243], a[248:251], a[160:163], v[240:243]\n"
    "PD\n"
    "s_waitcnt vmcnt(34) lgkmcnt(4)\n"
    "v_cvt_pk_bf16_f32 v80, v40, v41\n"
    "v_cvt_pk_bf16_f32 v81, v42, v43\n"
    "v_mfma_f32_16x16x32_bf16 v[128:131], a[224:227], a[192:195], v[128:131]\n"
    "PD\n"
    "ds_read_b64_tr_b16 a[240:241], v10 offset:32768\n"
    "v_cvt_pk_bf16_f32 v82, v44, v45\n"
    "v_mfma_f32_16x16x32_bf16 v[128:131], a[228:231], a[208:211], v[128:131]\n"
    "PD\n"
    "buffer_load_dwordx4 v28, s[48:51], s58 offen offset:0 lds\n"
    "v_cvt_pk_bf16_f32 v83, v46, v47\n"
    "v_accvgpr_read_b32 v48, a36\n"
    "v_mfma_f32_16x16x32_bf16 v[132:135], a[228:231], a[212:215], v[132:135]\n"
    "PD\n"
    "ds_read_b64_tr_b16 a[242:243], v10 offset:36864\n"
    "v_accvgpr_read_b32 v49, a37\n"
    "v_mfma_f32_16x16x32_bf16 v[132:135], a[224:227], a[196:199], v[132:135]\n"
    "PD\n"
    "v_accvgpr_read_b32 v50, a38\n"
    "v_accvgpr_read_b32 v51, a39\n"
    "v_mfma_f32_16x16x32_bf16 v[136:139], a[224:227], a[200:203], v[136:139]\n"
    "PD\n"
    "ds_read_b64_tr_b16 a[244:245], v10 offset:40960\n"
    "v_accvgpr_read_b32 v52, a52\n"
    "v_mfma_f32_16x16x32_bf16 v[136:139], a[228:231], a[216:219], v[136:139]\n"
    "PD\n"
    "buffer_load_dwordx4 v29, s[48:51], s58 offen offset:1024 lds\n"
    "v_accvgpr_read_b32 v53, a53\n"
    "v_accvgpr_read_b32 v54, a54\n"
    "v_mfma_f32_16x16x32_bf16 v[140:143], a[228:231], a[220:223], v[140:143]\n"
    "PD\n"
    "ds_read_b64_tr_b16 a[246:247], v10 offset:45056\n"
    "v_accvgpr_read_b32 v55, a55\n"
    "v_mfma_f32_16x16x32_bf16 v[140:143], a[224:227], a[204:207], v[140:143]\n"
    "PD\n"
    "s_waitcnt lgkmcnt(4)\n"
    "v_mul_f32 v48, v48, v57\n"
    "v_mul_f32 v49, v49, v57\n"
    "v_mfma_f32_16x16x32_bf16 v[156:159], a[232:235], a[204:207], v[156:159]\n"
    "PD\n"
    "ds_read_b64_tr_b16 a[248:249], v11 offset:32768\n"
    "v_mul_f32 v50, v50, v57\n"
    "v_mfma_f32_16x16x32_bf16 v[156:159], a[236:239], a[220:223], v[156:159]\n"
    "PD\n"
    "buffer_load_dwordx4 v30, s[48:51], s58 offen offset:2048 lds\n"
    "v_mul_f32 v51, v51, v57\n"
    "v_mul_f32 v52, v52, v57\n"
    "v_mfma_f32_16x16x32_bf16 v[152:155], a[236:239], a[216:219], v[152:155]\n"
    "PD\n"
    "buffer_store_dwordx4 v[72:75], v5, s[52:55], 0 offen offset:0 sc1\n"
    "ds_read_b64_tr_b16 a[250:251], v11 offset:36864\n"
    "v_mfma_f32_16x16x32_bf16 v[152:155], a[232:235], a[200:203], v[152:155]\n"
    "PD\n"
    "v_mul_f32 v53, v53, v57\n"
    "v_mul_f32 v54, v54, v57\n"
    "v_mfma_f32_16x16x32_bf16 v[148:151], a[232:235], a[196:199], v[148:151]\n"
    "PD\n"
    "ds_read_b64_tr_b16 a[252:253], v11 offset:40960\n"
    "v_mul_f32 v55, v55, v57\n"
    "v_mfma_f32_16x16x32_bf16 v[148:151], a[236:239], a[212:215], v[148:151]\n"
    "PD\n"
    "buffer_load_dwordx4 v31, s[48:51], s58 offen offset:3072 lds\n"
    "v_cvt_pk_bf16_f32 v84, v48, v49\n"
    "v_cvt_pk_bf16_f32 v85, v50, v51\n"
    "v_mfma_f32_16x16x32_bf16 v[144:147], a[236:239], a[208:211], v[144:147]\n"
    "PD\n"
    "ds_read_b64_tr_b16 a[254:255], v11 offset:45056\n"
    "v_cvt_pk_bf16_f32 v86, v52, v53\n"
    "v_mfma_f32_16x16x32_bf16 v[144:147], a[232:235], a[192:195], v[144:147]\n"
    "PD\n"
    "s_waitcnt lgkmcnt(4)\n"
    "v_cvt_pk_bf16_f32 v87, v54, v55\n"
    "v_accvgpr_read_b32 v40, a40\n"
    "v_mfma_f32_16x16x32_bf16 v[160:163], a[240:243], a[192:195], v[160:163]\n"
    "PD\n"
    "ds_read_b64_tr_b16 a[224:225], v12 offset:32768\n"
    "v_accvgpr_read_b32 v41, a41\n"
    "v_mfma_f32_16x16x32_bf16 v[160:163], a[244:247], a[208:211], v[160:163]\n"
    "PD\n"
    "s_add_u32 s58, s58, s59\n"
    "v_accvgpr_read_b32 v42, a42\n"
    "v_accvgpr_read_b32 v43, a43\n"
    "v_mfma_f32_16x16x32_bf16 v[164:167], a[244:247], a[212:215], v[164:167]\n"
    "PD\n"
    "ds_read_b64_tr_b16 a[226:227], v12 offset:36864\n"
    "v_accvgpr_read_b32 v44, a56\n"
    "v_mfma_f32_16x16x32_bf16 v[164:167], a[240:243], a[196:199], v[164:167]\n"
    "PD\n"
    "buffer_load_dwordx4 a[160:163], v20, s[40:43], s56 offen offset:0 sc1\n"
    "v_accvgpr_read_b32 v45, a57\n"
    "v_accvgpr_read_b32 v46, a58\n"
    "v_mfma_f32_16x16x32_bf16 v[168:171], a[240:243], a[200:203], v[168:171]\n"
    "PD\n"
    "ds_read_b64_tr_b16 a[228:229], v12 offset:40960\n"
    "v_accvgpr_read_b32 v47, a59\n"
    "v_mfma_f32_16x16x32_bf16 v[168:171], a[244:247], a[216:219], v[168:171]\n"
    "PD\n"
    "v_mul_f32 v40, v40, v58\n"
    "v_mul_f32 v41, v41, v58\n"
    "v_mfma_f32_16x16x32_bf16 v[172:175], a[244:247], a[220:223], v[172:175]\n"
    "PD\n"
    "ds_read_b64_tr_b16 a[230:231], v12 offset:45056\n"
    "v_mul_f32 v42, v42, v58\n"
    "v_mfma_f32_16x16x32_bf16 v[172:175], a[240:243], a[204:207], v[172:175]\n"
    "PD\n"
    "s_waitcnt lgkmcnt(4)\n"
    "v_mul_f32 v43, v43, v58\n"
    "v_mul_f32 v44, v44, v58\n"
    "v_mfma_f32_16x16x32_bf16 v[188:191], a[248:251], a[204:207], v[188:191]\n"
    "PD\n"
    "ds_read_b64_tr_b16 a[232:233], v13 offset:32768\n"
    "v_mul_f32 v45, v45, v58\n"
    "v_mfma_f32_16x16x32_bf16 v[188:191], a[252:255], a[220:223], v[188:191]\n"
    "PD\n"
    "buffer_load_dwordx4 a[176:179], v20, s[40:43], s56 offen offset:1024 sc1\n"
    "v_mul_f32 v46, v46, v58\n"
    "v_mul_f32 v47, v47, v58\n"
    "v_mfma_f32_16x16x32_bf16 v[184:187], a[252:255], a[216:219], v[184:187]\n"
    "PD\n"
    "ds_read_b64_tr_b16 a[234:235], v13 offset:36864\n"
    "v_cvt_pk_bf16_f32 v88, v40, v41\n"
    "v_mfma_f32_16x16x32_bf16 v[184:187], a[248:251], a[200:203], v[184:187]\n"
    "PD\n"
    "v_cvt_pk_bf16_f32 v89, v42, v43\n"
    "v_cvt_pk_bf16_f32 v90, v44, v45\n"
    "v_mfma_f32_16x16x32_bf16 v[180:183], a[248:251], a[196:199], v[180:183]\n"
    "PD\n"
    "ds_read_b64_tr_b16 a[236:237], v13 offset:40960\n"
    "v_cvt_pk_bf16_f32 v91, v46, v47\n"
    "v_mfma_f32_16x16x32_bf16 v[180:183], a[252:255], a[212:215], v[180:183]\n"
    "PD\n"
    "buffer_load_dwordx4 a[164:167], v21, s[40:43], s56 offen offset:0 sc1\n"
    "v_accvgpr_read_b32 v48, a44\n"
    "v_accvgpr_read_b32 v49, a45\n"
    "v_mfma_f32_16x16x32_bf16 v[176:179], a[252:255], a[208:211], v[176:179]\n"
    "PD\n"
    "buffer_store_dwordx4 v[76:79], v5, s[52:55], 0 offen offset:2048 sc1\n"
    "ds_read_b64_tr_b16 a[238:239], v13 offset:45056\n"
    "v_mfma_f32_16x16x32_bf16 v[176:179], a[248:251], a[192:195], v[176:179]\n"
    "PD\n"
    "s_waitcnt lgkmcnt(4)\n"
    "v_accvgpr_read_b32 v50, a46\n"
    "v_accvgpr_read_b32 v51, a47\n"
    "v_mfma_f32_16x16x32_bf16 v[192:195], a[224:227], a[192:195], v[192:195]\n"
    "PD\n"
    "ds_read_b64_tr_b16 a[240:241], v14 offset:32768\n"
    "v_accvgpr_read_b32 v52, a60\n"
    "v_mfma_f32_16x16x32_bf16 v[192:195], a[228:231], a[208:211], v[192:195]\n"
    "PD\n"
    "buffer_load_dwordx4 a[180:183], v21, s[40:43], s56 offen offset:1024 sc1\n"
    "v_accvgpr_read_b32 v53, a61\n"
    "v_accvgpr_read_b32 v54, a62\n"
    "v_mfma_f32_16x16x32_bf16 v[196:199], a[228:231], a[212:215], v[196:199]\n"
    "PD\n"
    "ds_read_b64_tr_b16 a[242:243], v14 offset:36864\n"
    "v_accvgpr_read_b32 v55, a63\n"
    "v_mfma_f32_16x16x32_bf16 v[196:199], a[224:227], a[196:199], v[196:199]\n"
    "PD\n"
    "v_mul_f32 v48, v48, v59\n"
    "v_mul_f32 v49, v49, v59\n"
    "v_mfma_f32_16x16x32_bf16 v[200:203], a[224:227], a[200:203], v[200:203]\n"
    "PD\n"
    "ds_read_b64_tr_b16 a[244:245], v14 offset:40960\n"
    "v_mul_f32 v50, v50, v59\n"
    "v_mfma_f32_16x16x32_bf16 v[200:203], a[228:231], a[216:219], v[200:203]\n"
    "PD\n"
    "buffer_load_dwordx4 a[168:171], v22, s[40:43], s56 offen offset:0 sc1\n"
    "v_mul_f32 v51, v51, v59\n"
    "v_mul_f32 v52, v52, v59\n"
    "v_mfma_f32_16x16x32_bf16 v[204:207], a[228:231], a[220:223], v[204:207]\n"
    "PD\n"
    "ds_read_b64_tr_b16 a[246:247], v14 offset:45056\n"
    "v_mul_f32 v53, v53, v59\n"
    "v_mfma_f32_16x16x32_bf16 v[204:207], a[224:227], a[204:207], v[204:207]\n"
    "PD\n"
    "s_waitcnt lgkmcnt(4)\n"
    "v_mul_f32 v54, v54, v59\n"
    "v_mul_f32 v55, v55, v59\n"
    "v_mfma_f32_16x16x32_bf16 v[220:223], a[232:235], a[204:207], v[220:223]\n"
    "PD\n"
    "ds_read_b64_tr_b16 a[248:249], v15 offset:32768\n"
    "v_cvt_pk_bf16_f32 v92, v48, v49\n"
    "v_mfma_f32_16x16x32_bf16 v[220:223], a[236:239], a[220:223], v[220:223]\n"
    "PD\n"
    "buffer_load_dwordx4 a[184:187], v22, s[40:43], s56 offen offset:1024 sc1\n"
    "v_cvt_pk_bf16_f32 v93, v50, v51\n"
    "v_cvt_pk_bf16_f32 v94, v52, v53\n"
    "v_mfma_f32_16x16x32_bf16 v[216:219], a[236:239], a[216:219], v[216:219]\n"
    "PD\n"
    "ds_read_b64_tr_b16 a[250:251], v15 offset:36864\n"
    "v_cvt_pk_bf16_f32 v95, v54, v55\n"
    "v_mfma_f32_16x16x32_bf16 v[216:219], a[232:235], a[200:203], v[216:219]\n"
    "PD\n"
    "v_accvgpr_read_b32 v40, a64\n"
    "v_accvgpr_read_b32 v41, a65\n"
    "v_mfma_f32_16x16x32_bf16 v[212:215], a[232:235], a[196:199], v[212:215]\n"
    "PD\n"
    "ds_read_b64_tr_b16 a[252:253], v15 offset:40960\n"
    "v_accvgpr_read_b32 v42, a66\n"
    "v_mfma_f32_16x16x32_bf16 v[212:215], a[236:239], a[212:215], v[212:215]\n"
    "PD\n"
    "buffer_load_dwordx4 a[172:175], v23, s[40:43], s56 offen offset:0 sc1\n"
    "v_accvgpr_read_b32 v43, a67\n"
    "v_accvgpr_read_b32 v44, a80\n"
    "v_mfma_f32_16x16x32_bf16 v[208:211], a[236:239], a[208:211], v[208:211]\n"
    "PD\n"
    "ds_read_b64_tr_b16 a[254:255], v15 offset:45056\n"
    "v_accvgpr_read_b32 v45, a81\n"
    "v_mfma_f32_16x16x32_bf16 v[208:211], a[232:235], a[192:195], v[208:211]\n"
    "PD\n"
    "s_waitcnt lgkmcnt(4)\n"
    "v_accvgpr_read_b32 v46, a82\n"
    "v_accvgpr_read_b32 v47, a83\n"
    "v_mfma_f32_16x16x32_bf16 v[224:227], a[240:243], a[192:195], v[224:227]\n"
    "PD\n"
    "ds_read_b64_tr_b16 a[224:225], v8 offset:49152\n"
    "v_mul_f32 v40, v40, v56\n"
    "v_mfma_f32_16x16x32_bf16 v[224:227], a[244:247], a[208:211], v[224:227]\n"
    "PD\n"
    "buffer_load_dwordx4 a[188:191], v23, s[40:43], s56 offen offset:1024 sc1\n"
    "v_mul_f32 v41, v41, v56\n"
    "v_mul_f32 v42, v42, v56\n"
    "v_mfma_f32_16x16x32_bf16 v[228:231], a[244:247], a[212:215], v[228:231]\n"
    "PD\n"
    "buffer_store_dwordx4 v[80:83], v4, s[52:55], 0 offen offset:1024 sc1\n"
    "ds_read_b64_tr_b16 a[226:227], v8 offset:53248\n"
    "v_mfma_f32_16x16x32_bf16 v[228:231], a[240:243], a[196:199], v[228:231]\n"
    "PD\n"
    "s_add_u32 s56, s56, s69\n"
    "v_mul_f32 v43, v43, v56\n"
    "v_mul_f32 v44, v44, v56\n"
    "v_mfma_f32_16x16x32_bf16 v[232:235], a[240:243], a[200:203], v[232:235]\n"
    "PD\n"
    "ds_read_b64_tr_b16 a[228:229], v8 offset:57344\n"
    "v_mul_f32 v45, v45, v56\n"
    "v_mfma_f32_16x16x32_bf16 v[232:235], a[244:247], a[216:219], v[232:235]\n"
    "PD\n"
    "s_xor_b32 m0, s60, 65536\n"
    "s_xor_b32 s60, s60, 0x10000\n"
    "v_mul_f32 v46, v46, v56\n"
    "v_mfma_f32_16x16x32_bf16 v[236:239], a[244:247], a[220:223], v[236:239]\n"
    "PD\n"
    "ds_read_b64_tr_b16 a[230:231], v8 offset:61440\n"
    "v_xor_b32 v8, 0x10000, v8\n"
    "v_mfma_f32_16x16x32_bf16 v[236:239], a[240:243], a[204:207], v[236:239]\n"
    "PD\n"
    "s_waitcnt lgkmcnt(4)\n"
    "v_mul_f32 v47, v47, v56\n"
    "v_cvt_pk_bf16_f32 v96, v40, v41\n"
    "v_mfma_f32_16x16x32_bf16 v[252:255], a[248:251], a[204:207], v[252:255]\n"
    "PD\n"
    "ds_read_b64_tr_b16 a[232:233], v9 offset:49152\n"
    "v_cvt_pk_bf16_f32 v97, v42, v43\n"
    "v_mfma_f32_16x16x32_bf16 v[252:255], a[252:255], a[220:223], v[252:255]\n"
    "PD\n"
    "buffer_load_dwordx4 v24, s[44:47], s57 offen offset:0 lds\n"
    "v_cvt_pk_bf16_f32 v98, v44, v45\n"
    "v_cvt_pk_bf16_f32 v99, v46, v47\n"
    "v_mfma_f32_16x16x32_bf16 v[248:251], a[252:255], a[216:219], v[248:251]\n"
    "PD\n"
    "ds_read_b64_tr_b16 a[234:235], v9 offset:53248\n"
    "v_accvgpr_read_b32 v48, a68\n"
    "v_mfma_f32_16x16x32_bf16 v[248:251], a[248:251], a[200:203], v[248:251]\n"
    "PD\n"
    "v_accvgpr_read_b32 v49, a69\n"
    "v_accvgpr_read_b32 v50, a70\n"
    "v_mfma_f32_16x16x32_bf16 v[244:247], a[248:251], a[196:199], v[244:247]\n"
    "PD\n"
    "ds_read_b64_tr_b16 a[236:237], v9 offset:57344\n"
    "v_accvgpr_read_b32 v51, a71\n"
    "v_mfma_f32_16x16x32_bf16 v[244:247], a[252:255], a[212:215], v[244:247]\n"
    "PD\n"
    "buffer_load_dwordx4 v25, s[44:47], s57 offen offset:1024 lds\n"
    "v_accvgpr_read_b32 v52, a84\n"
    "v_accvgpr_read_b32 v53, a85\n"
    "v_mfma_f32_16x16x32_bf16 v[240:243], a[252:255], a[208:211], v[240:243]\n"
    "PD\n"
    "ds_read_b64_tr_b16 a[238:239], v9 offset:61440\n"
    "v_xor_b32 v9, 0x10000, v9\n"
    "v_mfma_f32_16x16x32_bf16 v[240:243], a[248:251], a[192:195], v[240:243]\n"
    "PD\n"
    "s_waitcnt vmcnt(27) lgkmcnt(4)\n"
    "v_accvgpr_read_b32 v54, a86\n"
    "v_accvgpr_read_b32 v55, a87\n"
    "v_mfma_f32_16x16x32_bf16 v[128:131], a[224:227], a[96:99], v[128:131]\n"
    "PD\n"
    "ds_read_b64_tr_b16 a[240:241], v10 offset:49152\n"
    "v_mul_f32 v48, v48, v57\n"
    "v_mfma_f32_16x16x32_bf16 v[128:131], a[228:231], a[112:115], v[128:131]\n"
    "PD\n"
    "buffer_load_dwordx4 v26, s[44:47], s57 offen offset:2048 lds\n"
    "v_mul_f32 v49, v49, v57\n"
    "v_mul_f32 v50, v50, v57\n"
    "v_mfma_f32_16x16x32_bf16 v[132:135], a[228:231], a[116:119], v[132:135]\n"
    "PD\n"
    "ds_read_b64_tr_b16 a[242:243], v10 offset:53248\n"
    "v_mul_f32 v51, v51, v57\n"
    "v_mfma_f32_16x16x32_bf16 v[132:135], a[224:227], a[100:103], v[132:135]\n"
    "PD\n"
    "v_mul_f32 v52, v52, v57\n"
    "v_mul_f32 v53, v53, v57\n"
    "v_mfma_f32_16x16x32_bf16 v[136:139], a[224:227], a[104:107], v[136:139]\n"
    "PD\n"
    "ds_read_b64_tr_b16 a[244:245], v10 offset:57344\n"
    "v_mul_f32 v54, v54, v57\n"
    "v_mfma_f32_16x16x32_bf16 v[136:139], a[228:231], a[120:123], v[136:139]\n"
    "PD\n"
    "buffer_load_dwordx4 v27, s[44:47], s57 offen offset:3072 lds\n"
    "v_mul_f32 v55, v55, v57\n"
    "v_cvt_pk_bf16_f32 v100, v48, v49\n"
    "v_mfma_f32_16x16x32_bf16 v[140:143], a[228:231], a[124:127], v[140:143]\n"
    "PD\n"
    "ds_read_b64_tr_b16 a[246:247], v10 offset:61440\n"
    "v_xor_b32 v10, 0x10000, v10\n"
    "v_mfma_f32_16x16x32_bf16 v[140:143], a[224:227], a[108:111], v[140:143]\n"
    "PD\n"
    "s_waitcnt lgkmcnt(4)\n"
    "buffer_store_dwordx4 v[84:87], v4, s[52:55], 0 offen offset:3072 sc1\n"
    "v_cvt_pk_bf16_f32 v101, v50, v51\n"
    "v_mfma_f32_16x16x32_bf16 v[156:159], a[232:235], a[108:111], v[156:159]\n"
    "PD\n"
    "ds_read_b64_tr_b16 a[248:249], v11 offset:49152\n"
    "v_cvt_pk_bf16_f32 v102, v52, v53\n"
    "v_mfma_f32_16x16x32_bf16 v[156:159], a[236:239], a[124:127], v[156:159]\n"
    "PD\n"
    "s_add_u32 s57, s57, s59\n"
    "v_cvt_pk_bf16_f32 v103, v54, v55\n"
    "v_accvgpr_read_b32 v40, a72\n"
    "v_mfma_f32_16x16x32_bf16 v[152:155], a[236:239], a[120:123], v[152:155]\n"
    "PD\n"
    "ds_read_b64_tr_b16 a[250:251], v11 offset:53248\n"
    "v_accvgpr_read_b32 v41, a73\n"
    "v_mfma_f32_16x16x32_bf16 v[152:155], a[232:235], a[104:107], v[152:155]\n"
    "PD\n"
    "buffer_load_dwordx4 a[192:195], v20, s[40:43], s56 offen offset:0 sc1\n"
    "v_accvgpr_read_b32 v42, a74\n"
    "v_accvgpr_read_b32 v43, a75\n"
    "v_mfma_f32_16x16x32_bf16 v[148:151], a[232:235], a[100:103], v[148:151]\n"
    "PD\n"
    "ds_read_b64_tr_b16 a[252:253], v11 offset:57344\n"
    "v_accvgpr_read_b32 v44, a88\n"
    "v_mfma_f32_16x16x32_bf16 v[148:151], a[236:239], a[116:119], v[148:151]\n"
    "PD\n"
    "v_accvgpr_read_b32 v45, a89\n"
    "v_accvgpr_read_b32 v46, a90\n"
    "v_mfma_f32_16x16x32_bf16 v[144:147], a[236:239], a[112:115], v[144:147]\n"
    "PD\n"
    "ds_read_b64_tr_b16 a[254:255], v11 offset:61440\n"
    "v_xor_b32 v11, 0x10000, v11\n"
    "v_mfma_f32_16x16x32_bf16 v[144:147], a[232:235], a[96:99], v[144:147]\n"
    "PD\n"
    "s_waitcnt lgkmcnt(4)\n"
    "v_accvgpr_read_b32 v47, a91\n"
    "v_mul_f32 v40, v40, v58\n"
    "v_mfma_f32_16x16x32_bf16 v[160:163], a[240:243], a[96:99], v[160:163]\n"
    "PD\n"
    "ds_read_b64_tr_b16 a[224:225], v12 offset:49152\n"
    "v_mul_f32 v41, v41, v58\n"
    "v_mfma_f32_16x16x32_bf16 v[160:163], a[244:247], a[112:115], v[160:163]\n"
    "PD\n"
    "buffer_load_dwordx4 a[208:211], v20, s[40:43], s56 offen offset:1024 sc1\n"
    "v_mul_f32 v42, v42, v58\n"
    "v_mul_f32 v43, v43, v58\n"
    "v_mfma_f32_16x16x32_bf16 v[164:167], a[244:247], a[116:119], v[164:167]\n"
    "PD\n"
    "ds_read_b64_tr_b16 a[226:227], v12 offset:53248\n"
    "v_mul_f32 v44, v44, v58\n"
    "v_mfma_f32_16x16x32_bf16 v[164:167], a[240:243], a[100:103], v[164:167]\n"
    "PD\n"
    "v_mul_f32 v45, v45, v58\n"
    "v_mul_f32 v46, v46, v58\n"
    "v_mfma_f32_16x16x32_bf16 v[168:171], a[240:243], a[104:107], v[168:171]\n"
    "PD\n"
    "ds_read_b64_tr_b16 a[228:229], v12 offset:57344\n"
    "v_mul_f32 v47, v47, v58\n"
    "v_mfma_f32_16x16x32_bf16 v[168:171], a[244:247], a[120:123], v[168:171]\n"
    "PD\n"
    "buffer_load_dwordx4 a[196:199], v21, s[40:43], s56 offen offset:0 sc1\n"
    "v_cvt_pk_bf16_f32 v104, v40, v41\n"
    "v_cvt_pk_bf16_f32 v105, v42, v43\n"
    "v_mfma_f32_16x16x32_bf16 v[172:175], a[244:247], a[124:127], v[172:175]\n"
    "PD\n"
    "ds_read_b64_tr_b16 a[230:231], v12 offset:61440\n"
    "v_xor_b32 v12, 0x10000, v12\n"
    "v_mfma_f32_16x16x32_bf16 v[172:175], a[240:243], a[108:111], v[172:175]\n"
    "PD\n"
    "s_waitcnt lgkmcnt(4)\n"
    "v_cvt_pk_bf16_f32 v106, v44, v45\n"
    "v_cvt_pk_bf16_f32 v107, v46, v47\n"
    "v_mfma_f32_16x16x32_bf16 v[188:191], a[248:251], a[108:111], v[188:191]\n"
    "PD\n"
    "ds_read_b64_tr_b16 a[232:233], v13 offset:49152\n"
    "v_accvgpr_read_b32 v48, a76\n"
    "v_mfma_f32_16x16x32_bf16 v[188:191], a[252:255], a[124:127], v[188:191]\n"
    "PD\n"
    "buffer_load_dwordx4 a[212:215], v21, s[40:43], s56 offen offset:1024 sc1\n"
    "v_accvgpr_read_b32 v49, a77\n"
    "v_accvgpr_read_b32 v50, a78\n"
    "v_mfma_f32_16x16x32_bf16 v[184:187], a[252:255], a[120:123], v[184:187]\n"
    "PD\n"
    "ds_read_b64_tr_b16 a[234:235], v13 offset:53248\n"
    "v_accvgpr_read_b32 v51, a79\n"
    "v_mfma_f32_16x16x32_bf16 v[184:187], a[248:251], a[104:107], v[184:187]\n"
    "PD\n"
    "buffer_store_dwordx4 v[88:91], v5, s[52:55], 0 offen offset:1024 sc1\n"
    "v_accvgpr_read_b32 v52, a92\n"
    "v_accvgpr_read_b32 v53, a93\n"
    "v_mfma_f32_16x16x32_bf16 v[180:183], a[248:251], a[100:103], v[180:183]\n"
    "PD\n"
    "ds_read_b64_tr_b16 a[236:237], v13 offset:57344\n"
    "v_accvgpr_read_b32 v54, a94\n"
    "v_mfma_f32_16x16x32_bf16 v[180:183], a[252:255], a[116:119], v[180:183]\n"
    "PD\n"
    "buffer_load_dwordx4 a[200:203], v22, s[40:43], s56 offen offset:0 sc1\n"
    "v_accvgpr_read_b32 v55, a95\n"
    "v_mul_f32 v48, v48, v59\n"
    "v_mfma_f32_16x16x32_bf16 v[176:179], a[252:255], a[112:115], v[176:179]\n"
    "PD\n"
    "ds_read_b64_tr_b16 a[238:239], v13 offset:61440\n"
    "v_xor_b32 v13, 0x10000, v13\n"
    "v_mfma_f32_16x16x32_bf16 v[176:179], a[248:251], a[96:99], v[176:179]\n"
    "PD\n"
    "s_waitcnt lgkmcnt(4)\n"
    "v_mul_f32 v49, v49, v59\n"
    "v_mul_f32 v50, v50, v59\n"
    "v_mfma_f32_16x16x32_bf16 v[192:195], a[224:227], a[96:99], v[192:195]\n"
    "PD\n"
    "ds_read_b64_tr_b16 a[240:241], v14 offset:49152\n"
    "v_mul_f32 v51, v51, v59\n"
    "v_mfma_f32_16x16x32_bf16 v[192:195], a[228:231], a[112:115], v[192:195]\n"
    "PD\n"
    "buffer_load_dwordx4 a[216:219], v22, s[40:43], s56 offen offset:1024 sc1\n"
    "v_mul_f32 v52, v52, v59\n"
    "v_mul_f32 v53, v53, v59\n"
    "v_mfma_f32_16x16x32_bf16 v[196:199], a[228:231], a[116:119], v[196:199]\n"
    "PD\n"
    "ds_read_b64_tr_b16 a[242:243], v14 offset:53248\n"
    "v_mul_f32 v54, v54, v59\n"
    "v_mfma_f32_16x16x32_bf16 v[196:199], a[224:227], a[100:103], v[196:199]\n"
    "PD\n"
    "v_mul_f32 v55, v55, v59\n"
    "v_cvt_pk_bf16_f32 v108, v48, v49\n"
    "v_mfma_f32_16x16x32_bf16 v[200:203], a[224:227], a[104:107], v[200:203]\n"
    "PD\n"
    "ds_read_b64_tr_b16 a[244:245], v14 offset:57344\n"
    "v_cvt_pk_bf16_f32 v109, v50, v51\n"
    "v_mfma_f32_16x16x32_bf16 v[200:203], a[228:231], a[120:123], v[200:203]\n"
    "PD\n"
    "buffer_load_dwordx4 a[204:207], v23, s[40:43], s56 offen offset:0 sc1\n"
    "v_cvt_pk_bf16_f32 v110, v52, v53\n"
    "v_cvt_pk_bf16_f32 v111, v54, v55\n"
    "v_mfma_f32_16x16x32_bf16 v[204:207], a[228:231], a[124:127], v[204:207]\n"
    "PD\n"
    "ds_read_b64_tr_b16 a[246:247], v14 offset:61440\n"
    "v_xor_b32 v14, 0x10000, v14\n"
    "v_mfma_f32_16x16x32_bf16 v[204:207], a[224:227], a[108:111], v[204:207]\n"
    "PD\n"
    "s_waitcnt lgkmcnt(4)\n"
    "v_mfma_f32_16x16x32_bf16 v[220:223], a[232:235], a[108:111], v[220:223]\n"
    "PD\n"
    "ds_read_b64_tr_b16 a[248:249], v15 offset:49152\n"
    "v_mfma_f32_16x16x32_bf16 v[220:223], a[236:239], a[124:127], v[220:223]\n"
    "PD\n"
    "buffer_load_dwordx4 a[220:223], v23, s[40:43], s56 offen offset:1024 sc1\n"
    "v_mfma_f32_16x16x32_bf16 v[216:219], a[236:239], a[120:123], v[216:219]\n"
    "PD\n"
    "ds_read_b64_tr_b16 a[250:251], v15 offset:53248\n"
    "v_mfma_f32_16x16x32_bf16 v[216:219], a[232:235], a[104:107], v[216:219]\n"
    "PD\n"
    "s_add_u32 s56, s56, s69\n"
    "v_mfma_f32_16x16x32_bf16 v[212:215], a[232:235], a[100:103], v[212:215]\n"
    "PD\n"
    "ds_read_b64_tr_b16 a[252:253], v15 offset:57344\n"
    "v_mfma_f32_16x16x32_bf16 v[212:215], a[236:239], a[116:119], v[212:215]\n"
    "PD\n"
    "v_mfma_f32_16x16x32_bf16 v[208:211], a[236:239], a[112:115], v[208:211]\n"
    "PD\n"
    "ds_read_b64_tr_b16 a[254:255], v15 offset:61440\n"
    "v_xor_b32 v15, 0x10000, v15\n"
    "v_mfma_f32_16x16x32_bf16 v[208:211], a[232:235], a[96:99], v[208:211]\n"
    "PD\n"
    "s_waitcnt lgkmcnt(4)\n"
    "s_barrier\n"
    "v_mfma_f32_16x16x32_bf16 v[224:227], a[240:243], a[96:99], v[224:227]\n"
    "PD\n"
    "buffer_store_dwordx4 v[92:95], v5, s[52:55], 0 offen offset:3072 sc1\n"
    "ds_read_b64_tr_b16 a[224:225], v8 offset:0\n"
    "v_mfma_f32_16x16x32_bf16 v[224:227], a[244:247], a[112:115], v[224:227]\n"
    "PD\n"
    "v_mfma_f32_16x16x32_bf16 v[228:231], a[244:247], a[116:119], v[228:231]\n"
    "PD\n"
    "ds_read_b64_tr_b16 a[226:227], v8 offset:4096\n"
    "v_mfma_f32_16x16x32_bf16 v[228:231], a[240:243], a[100:103], v[228:231]\n"
    "PD\n"
    "v_mfma_f32_16x16x32_bf16 v[232:235], a[240:243], a[104:107], v[232:235]\n"
    "PD\n"
    "ds_read_b64_tr_b16 a[228:229], v8 offset:8192\n"
    "v_mfma_f32_16x16x32_bf16 v[232:235], a[244:247], a[120:123], v[232:235]\n"
    "PD\n"
    "v_mfma_f32_16x16x32_bf16 v[236:239], a[244:247], a[124:127], v[236:239]\n"
    "PD\n"
    "ds_read_b64_tr_b16 a[230:231], v8 offset:12288\n"
    "v_mfma_f32_16x16x32_bf16 v[236:239], a[240:243], a[108:111], v[236:239]\n"
    "PD\n"
    "s_waitcnt lgkmcnt(4)\n"
    "v_mfma_f32_16x16x32_bf16 v[252:255], a[248:251], a[108:111], v[252:255]\n"
    "PD\n"
    "ds_read_b64_tr_b16 a[232:233], v9 offset:0\n"
    "v_mfma_f32_16x16x32_bf16 v[252:255], a[252:255], a[124:127], v[252:255]\n"
    "PD\n"
    "v_mfma_f32_16x16x32_bf16 v[248:251], a[252:255], a[120:123], v[248:251]\n"
    "PD\n"
    "ds_read_b64_tr_b16 a[234:235], v9 offset:4096\n"
    "v_mfma_f32_16x16x32_bf16 v[248:251], a[248:251], a[104:107], v[248:251]\n"
    "PD\n"
    "v_mfma_f32_16x16x32_bf16 v[244:247], a[248:251], a[100:103], v[244:247]\n"
    "PD\n"
    "ds_read_b64_tr_b16 a[236:237], v9 offset:8192\n"
    "v_mfma_f32_16x16x32_bf16 v[244:247], a[252:255], a[116:119], v[244:247]\n"
    "PD\n"
    "v_mfma_f32_16x16x32_bf16 v[240:243], a[252:255], a[112:115], v[240:243]\n"
    "PD\n"
    "ds_read_b64_tr_b16 a[238:239], v9 offset:12288\n"
    "v_mfma_f32_16x16x32_bf16 v[240:243], a[248:251], a[96:99], v[240:243]\n"
    "PD\n"
    "s_waitcnt vmcnt(30) lgkmcnt(4)\n"
    "s_branch L_O\n"
    "E_O:\n"
    "PP\n"
    "s_mov_b64 vcc, -1\n"
    "s_mov_b32 s37, 0\n"
    "s_xor_b32 s60, s61, 0x10000\n"
    "s_movk_i32 s56, 24576\n"
    "PD\n"
    "s_lshr_b32 s38, s30, 8\n"
    "PD\n"
    "s_sub_u32 s38, s38, 2\n"
    "PD\n"
    "s_mov_b32 s39, 1\n"
    "PD\n"
    "s_waitcnt vmcnt(24) lgkmcnt(4)\n"
    "L_O:\n"
    "PD\n"
    "v_mfma_f32_16x16x32_bf16 v[128:131], a[224:227], a[128:131], v[128:131]\n"
    "PD\n"
    "ds_read_b64_tr_b16 a[240:241], v10 offset:0\n"
    "s_bitcmp1_b32 12, s37\n"
    "v_mfma_f32_16x16x32_bf16 v[128:131], a[228:231], a[144:147], v[128:131]\n"
    "PD\n"
    "s_cbranch_scc1 H_Ot\n"
    "R_Ot:\n"
    "buffer_load_dwordx4 a[96:99], v20, s[40:43], s56 offen offset:0 sc1\n"
    "v_mfma_f32_16x16x32_bf16 v[132:135], a[228:231], a[148:151], v[132:135]\n"
    "PD\n"
    "ds_read_b64_tr_b16 a[242:243], v10 offset:4096\n"
    "v_mfma_f32_16x16x32_bf16 v[132:135], a[224:227], a[132:135], v[132:135]\n"
    "PD\n"
    "v_mfma_f32_16x16x32_bf16 v[136:139], a[224:227], a[136:139], v[136:139]\n"
    "PD\n"
    "ds_read_b64_tr_b16 a[244:245], v10 offset:8192\n"
    "v_mfma_f32_16x16x32_bf16 v[136:139], a[228:231], a[152:155], v[136:139]\n"
    "PD\n"
    "buffer_load_dwordx4 a[112:115], v20, s[40:43], s56 offen offset:1024 sc1\n"
    "v_mfma_f32_16x16x32_bf16 v[140:143], a[228:231], a[156:159], v[140:143]\n"
    "PD\n"
    "ds_read_b64_tr_b16 a[246:247], v10 offset:12288\n"
    "v_mfma_f32_16x16x32_bf16 v[140:143], a[224:227], a[140:143], v[140:143]\n"
    "PD\n"
    "s_waitcnt lgkmcnt(4)\n"
    "v_mfma_f32_16x16x32_bf16 v[156:159], a[232:235], a[140:143], v[156:159]\n"
    "PD\n"
    "ds_read_b64_tr_b16 a[248:249], v11 offset:0\n"
    "v_mfma_f32_16x16x32_bf16 v[156:159], a[236:239], a[156:159], v[156:159]\n"
    "PD\n"
    "buffer_load_dwordx4 a[100:103], v21, s[40:43], s56 offen offset:0 sc1\n"
    "v_mfma_f32_16x16x32_bf16 v[152:155], a[236:239], a[152:155], v[152:155]\n"
    "PD\n"
    "ds_read_b64_tr_b16 a[250:251], v11 offset:4096\n"
    "v_mfma_f32_16x16x32_bf16 v[152:155], a[232:235], a[136:139], v[152:155]\n"
    "PD\n"
    "s_cbranch_vccnz H_Os00\n"
    "R_Os00:\n"
    "v_mfma_f32_16x16x32_bf16 v[148:151], a[232:235], a[132:135], v[148:151]\n"
    "PD\n"
    "ds_read_b64_tr_b16 a[252:253], v11 offset:8192\n"
    "v_mfma_f32_16x16x32_bf16 v[148:151], a[236:239], a[148:151], v[148:151]\n"
    "PD\n"
    "buffer_load_dwordx4 a[116:119], v21, s[40:43], s56 offen offset:1024 sc1\n"
    "v_mfma_f32_16x16x32_bf16 v[144:147], a[236:239], a[144:147], v[144:147]\n"
    "PD\n"
    "ds_read_b64_tr_b16 a[254:255], v11 offset:12288\n"
    "v_mfma_f32_16x16x32_bf16 v[144:147], a[232:235], a[128:131], v[144:147]\n"
    "PD\n"
    "s_waitcnt lgkmcnt(4)\n"
    "v_mfma_f32_16x16x32_bf16 v[160:163], a[240:243], a[128:131], v[160:163]\n"
    "PD\n"
    "ds_read_b64_tr_b16 a[224:225], v12 offset:0\n"
    "v_mfma_f32_16x16x32_bf16 v[160:163], a[244:247], a[144:147], v[160:163]\n"
    "PD\n"
    "buffer_load_dwordx4 a[104:107], v22, s[40:43], s56 offen offset:0 sc1\n"
    "v_mfma_f32_16x16x32_bf16 v[164:167], a[244:247], a[148:151], v[164:167]\n"
    "PD\n"
    "ds_read_b64_tr_b16 a[226:227], v12 offset:4096\n"
    "v_mfma_f32_16x16x32_bf16 v[164:167], a[240:243], a[132:135], v[164:167]\n"
    "PD\n"
    "v_mfma_f32_16x16x32_bf16 v[168:171], a[240:243], a[136:139], v[168:171]\n"
    "PD\n"
    "ds_read_b64_tr_b16 a[228:229], v12 offset:8192\n"
    "v_mfma_f32_16x16x32_bf16 v[168:171], a[244:247], a[152:155], v[168:171]\n"
    "PD\n"
    "buffer_load_dwordx4 a[120:123], v22, s[40:43], s56 offen offset:1024 sc1\n"
    "v_mfma_f32_16x16x32_bf16 v[172:175], a[244:247], a[156:159], v[172:175]\n"
    "PD\n"
    "ds_read_b64_tr_b16 a[230:231], v12 offset:12288\n"
    "v_mfma_f32_16x16x32_bf16 v[172:175], a[240:243], a[140:143], v[172:175]\n"
    "PD\n"
    "s_waitcnt lgkmcnt(4)\n"
    "v_mfma_f32_16x16x32_bf16 v[188:191], a[248:251], a[140:143], v[188:191]\n"
    "PD\n"
    "ds_read_b64_tr_b16 a[232:233], v13 offset:0\n"
    "v_mfma_f32_16x16x32_bf16 v[188:191], a[252:255], a[156:159], v[188:191]\n"
    "PD\n"
    "buffer_load_dwordx4 a[108:111], v23, s[40:43], s56 offen offset:0 sc1\n"
    "v_mfma_f32_16x16x32_bf16 v[184:187], a[252:255], a[152:155], v[184:187]\n"
    "PD\n"
    "ds_read_b64_tr_b16 a[234:235], v13 offset:4096\n"
    "v_mfma_f32_16x16x32_bf16 v[184:187], a[248:251], a[136:139], v[184:187]\n"
    "PD\n"
    "v_mfma_f32_16x16x32_bf16 v[180:183], a[248:251], a[132:135], v[180:183]\n"
    "PD\n"
    "ds_read_b64_tr_b16 a[236:237], v13 offset:8192\n"
    "v_mfma_f32_16x16x32_bf16 v[180:183], a[252:255], a[148:151], v[180:183]\n"
    "PD\n"
    "buffer_load_dwordx4 a[124:127], v23, s[40:43], s56 offen offset:1024 sc1\n"
    "v_mfma_f32_16x16x32_bf16 v[176:179], a[252:255], a[144:147], v[176:179]\n"
    "PD\n"
    "ds_read_b64_tr_b16 a[238:239], v13 offset:12288\n"
    "v_mfma_f32_16x16x32_bf16 v[176:179], a[248:251], a[128:131], v[176:179]\n"
    "PD\n"
    "s_waitcnt lgkmcnt(4)\n"
    "v_mfma_f32_16x16x32_bf16 v[192:195], a[224:227], a[128:131], v[192:195]\n"
    "PD\n"
    "ds_read_b64_tr_b16 a[240:241], v14 offset:0\n"
    "v_mfma_f32_16x16x32_bf16 v[192:195], a[228:231], a[144:147], v[192:195]\n"
    "PD\n"
    "s_add_u32 s56, s56, s69\n"
    "s_add_u32 m0, s60, 16384\n"
    "v_mfma_f32_16x16x32_bf16 v[196:199], a[228:231], a[148:151], v[196:199]\n"
    "PD\n"
    "ds_read_b64_tr_b16 a[242:243], v14 offset:4096\n"
    "v_mfma_f32_16x16x32_bf16 v[196:199], a[224:227], a[132:135], v[196:199]\n"
    "PD\n"
    "v_mfma_f32_16x16x32_bf16 v[200:203], a[224:227], a[136:139], v[200:203]\n"
    "PD\n"
    "ds_read_b64_tr_b16 a[244:245], v14 offset:8192\n"
    "v_mfma_f32_16x16x32_bf16 v[200:203], a[228:231], a[152:155], v[200:203]\n"
    "PD\n"
    "buffer_load_dwordx4 v28, s[48:51], s58 offen offset:0 lds\n"
    "v_mfma_f32_16x16x32_bf16 v[204:207], a[228:231], a[156:159], v[204:207]\n"
    "PD\n"
    "ds_read_b64_tr_b16 a[246:247], v14 offset:12288\n"
    "v_mfma_f32_16x16x32_bf16 v[204:207], a[224:227], a[140:143], v[204:207]\n"
    "PD\n"
    "s_waitcnt lgkmcnt(4)\n"
    "v_mfma_f32_16x16x32_bf16 v[220:223], a[232:235], a[140:143], v[220:223]\n"
    "PD\n"
    "ds_read_b64_tr_b16 a[248:249], v15 offset:0\n"
    "v_mfma_f32_16x16x32_bf16 v[220:223], a[236:239], a[156:159], v[220:223]\n"
    "PD\n"
    "buffer_load_dwordx4 v29, s[48:51], s58 offen offset:1024 lds\n"
    "v_mfma_f32_16x16x32_bf16 v[216:219], a[236:239], a[152:155], v[216:219]\n"
    "PD\n"
    "ds_read_b64_tr_b16 a[250:251], v15 offset:4096\n"
    "v_mfma_f32_16x16x32_bf16 v[216:219], a[232:235], a[136:139], v[216:219]\n"
    "PD\n"
    "s_cbranch_vccnz H_Os01\n"
    "R_Os01:\n"
    "v_mfma_f32_16x16x32_bf16 v[212:215], a[232:235], a[132:135], v[212:215]\n"
    "PD\n"
    "ds_read_b64_tr_b16 a[252:253], v15 offset:8192\n"
    "v_mfma_f32_16x16x32_bf16 v[212:215], a[236:239], a[148:151], v[212:215]\n"
    "PD\n"
    "v_mfma_f32_16x16x32_bf16 v[208:211], a[236:239], a[144:147], v[208:211]\n"
    "PD\n"
    "ds_read_b64_tr_b16 a[254:255], v15 offset:12288\n"
    "v_mfma_f32_16x16x32_bf16 v[208:211], a[232:235], a[128:131], v[208:211]\n"
    "PD\n"
    "s_waitcnt lgkmcnt(4)\n"
    "v_mfma_f32_16x16x32_bf16 v[224:227], a[240:243], a[128:131], v[224:227]\n"
    "PD\n"
    "ds_read_b64_tr_b16 a[224:225], v8 offset:16384\n"
    "v_mfma_f32_16x16x32_bf16 v[224:227], a[244:247], a[144:147], v[224:227]\n"
    "PD\n"
    "buffer_load_dwordx4 v30, s[48:51], s58 offen offset:2048 lds\n"
    "v_mfma_f32_16x16x32_bf16 v[228:231], a[244:247], a[148:151], v[228:231]\n"
    "PD\n"
    "ds_read_b64_tr_b16 a[226:227], v8 offset:20480\n"
    "v_mfma_f32_16x16x32_bf16 v[228:231], a[240:243], a[132:135], v[228:231]\n"
    "PD\n"
    "v_mfma_f32_16x16x32_bf16 v[232:235], a[240:243], a[136:139], v[232:235]\n"
    "PD\n"
    "ds_read_b64_tr_b16 a[228:229], v8 offset:24576\n"
    "v_mfma_f32_16x16x32_bf16 v[232:235], a[244:247], a[152:155], v[232:235]\n"
    "PD\n"
    "v_mfma_f32_16x16x32_bf16 v[236:239], a[244:247], a[156:159], v[236:239]\n"
    "PD\n"
    "ds_read_b64_tr_b16 a[230:231], v8 offset:28672\n"
    "v_mfma_f32_16x16x32_bf16 v[236:239], a[240:243], a[140:143], v[236:239]\n"
    "PD\n"
    "s_waitcnt lgkmcnt(4)\n"
    "v_mfma_f32_16x16x32_bf16 v[252:255], a[248:251], a[140:143], v[252:255]\n"
    "PD\n"
    "ds_read_b64_tr_b16 a[232:233], v9 offset:16384\n"
    "v_mfma_f32_16x16x32_bf16 v[252:255], a[252:255], a[156:159], v[252:255]\n"
    "PD\n"
    "buffer_load_dwordx4 v31, s[48:51], s58 offen offset:3072 lds\n"
    "v_mfma_f32_16x16x32_bf16 v[248:251], a[252:255], a[152:155], v[248:251]\n"
    "PD\n"
    "ds_read_b64_tr_b16 a[234:235], v9 offset:20480\n"
    "v_mfma_f32_16x16x32_bf16 v[248:251], a[248:251], a[136:139], v[248:251]\n"
    "PD\n"
    "s_add_u32 s58, s58, s59\n"
    "v_mfma_f32_16x16x32_bf16 v[244:247], a[248:251], a[132:135], v[244:247]\n"
    "PD\n"
    "ds_read_b64_tr_b16 a[236:237], v9 offset:24576\n"
    "v_mfma_f32_16x16x32_bf16 v[244:247], a[252:255], a[148:151], v[244:247]\n"
    "PD\n"
    "v_mfma_f32_16x16x32_bf16 v[240:243], a[252:255], a[144:147], v[240:243]\n"
    "PD\n"
    "ds_read_b64_tr_b16 a[238:239], v9 offset:28672\n"
    "v_mfma_f32_16x16x32_bf16 v[240:243], a[248:251], a[128:131], v[240:243]\n"
    "PD\n"
    "s_waitcnt vmcnt(20) lgkmcnt(4)\n"
    "v_mfma_f32_16x16x32_bf16 v[128:131], a[224:227], a[160:163], v[128:131]\n"
    "PD\n"
    "ds_read_b64_tr_b16 a[240:241], v10 offset:16384\n"
    "v_mfma_f32_16x16x32_bf16 v[128:131], a[228:231], a[176:179], v[128:131]\n"
    "PD\n"
    "buffer_load_dwordx4 a[128:131], v20, s[40:43], s56 offen offset:0 sc1\n"
    "v_mfma_f32_16x16x32_bf16 v[132:135], a[228:231], a[180:183], v[132:135]\n"
    "PD\n"
    "ds_read_b64_tr_b16 a[242:243], v10 offset:20480\n"
    "v_mfma_f32_16x16x32_bf16 v[132:135], a[224:227], a[164:167], v[132:135]\n"
    "PD\n"
    "v_mfma_f32_16x16x32_bf16 v[136:139], a[224:227], a[168:171], v[136:139]\n"
    "PD\n"
    "ds_read_b64_tr_b16 a[244:245], v10 offset:24576\n"
    "v_mfma_f32_16x16x32_bf16 v[136:139], a[228:231], a[184:187], v[136:139]\n"
    "PD\n"
    "buffer_load_dwordx4 a[144:147], v20, s[40:43], s56 offen offset:1024 sc1\n"
    "v_mfma_f32_16x16x32_bf16 v[140:143], a[228:231], a[188:191], v[140:143]\n"
    "PD\n"
    "ds_read_b64_tr_b16 a[246:247], v10 offset:28672\n"
    "v_mfma_f32_16x16x32_bf16 v[140:143], a[224:227], a[172:175], v[140:143]\n"
    "PD\n"
    "s_waitcnt lgkmcnt(4)\n"
    "v_mfma_f32_16x16x32_bf16 v[156:159], a[232:235], a[172:175], v[156:159]\n"
    "PD\n"
    "ds_read_b64_tr_b16 a[248:249], v11 offset:16384\n"
    "v_mfma_f32_16x16x32_bf16 v[156:159], a[236:239], a[188:191], v[156:159]\n"
    "PD\n"
    "buffer_load_dwordx4 a[132:135], v21, s[40:43], s56 offen offset:0 sc1\n"
    "v_mfma_f32_16x16x32_bf16 v[152:155], a[236:239], a[184:187], v[152:155]\n"
    "PD\n"
    "ds_read_b64_tr_b16 a[250:251], v11 offset:20480\n"
    "v_mfma_f32_16x16x32_bf16 v[152:155], a[232:235], a[168:171], v[152:155]\n"
    "PD\n"
    "s_cbranch_vccnz H_Os10\n"
    "R_Os10:\n"
    "v_mfma_f32_16x16x32_bf16 v[148:151], a[232:235], a[164:167], v[148:151]\n"
    "PD\n"
    "ds_read_b64_tr_b16 a[252:253], v11 offset:24576\n"
    "v_mfma_f32_16x16x32_bf16 v[148:151], a[236:239], a[180:183], v[148:151]\n"
    "PD\n"
    "buffer_load_dwordx4 a[148:151], v21, s[40:43], s56 offen offset:1024 sc1\n"
    "v_mfma_f32_16x16x32_bf16 v[144:147], a[236:239], a[176:179], v[144:147]\n"
    "PD\n"
    "ds_read_b64_tr_b16 a[254:255], v11 offset:28672\n"
    "v_mfma_f32_16x16x32_bf16 v[144:147], a[232:235], a[160:163], v[144:147]\n"
    "PD\n"
    "s_waitcnt lgkmcnt(4)\n"
    "v_mfma_f32_16x16x32_bf16 v[160:163], a[240:243], a[160:163], v[160:163]\n"
    "PD\n"
    "ds_read_b64_tr_b16 a[224:225], v12 offset:16384\n"
    "v_mfma_f32_16x16x32_bf16 v[160:163], a[244:247], a[176:179], v[160:163]\n"
    "PD\n"
    "buffer_load_dwordx4 a[136:139], v22, s[40:43], s56 offen offset:0 sc1\n"
    "v_mfma_f32_16x16x32_bf16 v[164:167], a[244:247], a[180:183], v[164:167]\n"
    "PD\n"
    "ds_read_b64_tr_b16 a[226:227], v12 offset:20480\n"
    "v_mfma_f32_16x16x32_bf16 v[164:167], a[240:243], a[164:167], v[164:167]\n"
    "PD\n"
    "v_mfma_f32_16x16x32_bf16 v[168:171], a[240:243], a[168:171], v[168:171]\n"
    "PD\n"
    "ds_read_b64_tr_b16 a[228:229], v12 offset:24576\n"
    "v_mfma_f32_16x16x32_bf16 v[168:171], a[244:247], a[184:187], v[168:171]\n"
    "PD\n"
    "buffer_load_dwordx4 a[152:155], v22, s[40:43], s56 offen offset:1024 sc1\n"
    "v_mfma_f32_16x16x32_bf16 v[172:175], a[244:247], a[188:191], v[172:175]\n"
    "PD\n"
    "ds_read_b64_tr_b16 a[230:231], v12 offset:28672\n"
    "v_mfma_f32_16x16x32_bf16 v[172:175], a[240:243], a[172:175], v[172:175]\n"
    "PD\n"
    "s_waitcnt lgkmcnt(4)\n"
    "v_mfma_f32_16x16x32_bf16 v[188:191], a[248:251], a[172:175], v[188:191]\n"
    "PD\n"
    "ds_read_b64_tr_b16 a[232:233], v13 offset:16384\n"
    "v_mfma_f32_16x16x32_bf16 v[188:191], a[252:255], a[188:191], v[188:191]\n"
    "PD\n"
    "buffer_load_dwordx4 a[140:143], v23, s[40:43], s56 offen offset:0 sc1\n"
    "v_mfma_f32_16x16x32_bf16 v[184:187], a[252:255], a[184:187], v[184:187]\n"
    "PD\n"
    "ds_read_b64_tr_b16 a[234:235], v13 offset:20480\n"
    "v_mfma_f32_16x16x32_bf16 v[184:187], a[248:251], a[168:171], v[184:187]\n"
    "PD\n"
    "v_mfma_f32_16x16x32_bf16 v[180:183], a[248:251], a[164:167], v[180:183]\n"
    "PD\n"
    "ds_read_b64_tr_b16 a[236:237], v13 offset:24576\n"
    "v_mfma_f32_16x16x32_bf16 v[180:183], a[252:255], a[180:183], v[180:183]\n"
    "PD\n"
    "buffer_load_dwordx4 a[156:159], v23, s[40:43], s56 offen offset:1024 sc1\n"
    "v_mfma_f32_16x16x32_bf16 v[176:179], a[252:255], a[176:179], v[176:179]\n"
    "PD\n"
    "ds_read_b64_tr_b16 a[238:239], v13 offset:28672\n"
    "v_mfma_f32_16x16x32_bf16 v[176:179], a[248:251], a[160:163], v[176:179]\n"
    "PD\n"
    "s_waitcnt lgkmcnt(4)\n"
    "v_mfma_f32_16x16x32_bf16 v[192:195], a[224:227], a[160:163], v[192:195]\n"
    "PD\n"
    "ds_read_b64_tr_b16 a[240:241], v14 offset:16384\n"
    "v_mfma_f32_16x16x32_bf16 v[192:195], a[228:231], a[176:179], v[192:195]\n"
    "PD\n"
    "s_add_u32 s56, s56, s69\n"
    "s_add_u32 m0, s60, 32768\n"
    "v_mfma_f32_16x16x32_bf16 v[196:199], a[228:231], a[180:183], v[196:199]\n"
    "PD\n"
    "ds_read_b64_tr_b16 a[242:243], v14 offset:20480\n"
    "v_mfma_f32_16x16x32_bf16 v[196:199], a[224:227], a[164:167], v[196:199]\n"
    "PD\n"
    "v_mfma_f32_16x16x32_bf16 v[200:203], a[224:227], a[168:171], v[200:203]\n"
    "PD\n"
    "ds_read_b64_tr_b16 a[244:245], v14 offset:24576\n"
    "v_mfma_f32_16x16x32_bf16 v[200:203], a[228:231], a[184:187], v[200:203]\n"
    "PD\n"
    "buffer_load_dwordx4 v24, s[44:47], s57 offen offset:0 lds\n"
    "v_mfma_f32_16x16x32_bf16 v[204:207], a[228:231], a[188:191], v[204:207]\n"
    "PD\n"
    "ds_read_b64_tr_b16 a[246:247], v14 offset:28672\n"
    "v_mfma_f32_16x16x32_bf16 v[204:207], a[224:227], a[172:175], v[204:207]\n"
    "PD\n"
    "s_waitcnt lgkmcnt(4)\n"
    "v_mfma_f32_16x16x32_bf16 v[220:223], a[232:235], a[172:175], v[220:223]\n"
    "PD\n"
    "ds_read_b64_tr_b16 a[248:249], v15 offset:16384\n"
    "v_mfma_f32_16x16x32_bf16 v[220:223], a[236:239], a[188:191], v[220:223]\n"
    "PD\n"
    "buffer_load_dwordx4 v25, s[44:47], s57 offen offset:1024 lds\n"
    "v_mfma_f32_16x16x32_bf16 v[216:219], a[236:239], a[184:187], v[216:219]\n"
    "PD\n"
    "ds_read_b64_tr_b16 a[250:251], v15 offset:20480\n"
    "v_mfma_f32_16x16x32_bf16 v[216:219], a[232:235], a[168:171], v[216:219]\n"
    "PD\n"
    "s_cbranch_vccnz H_Os11\n"
    "R_Os11:\n"
    "v_mfma_f32_16x16x32_bf16 v[212:215], a[232:235], a[164:167], v[212:215]\n"
    "PD\n"
    "ds_read_b64_tr_b16 a[252:253], v15 offset:24576\n"
    "v_mfma_f32_16x16x32_bf16 v[212:215], a[236:239], a[180:183], v[212:215]\n"
    "PD\n"
    "v_mfma_f32_16x16x32_bf16 v[208:211], a[236:239], a[176:179], v[208:211]\n"
    "PD\n"
    "ds_read_b64_tr_b16 a[254:255], v15 offset:28672\n"
    "v_mfma_f32_16x16x32_bf16 v[208:211], a[232:235], a[160:163], v[208:211]\n"
    "PD\n"
    "s_waitcnt lgkmcnt(4)\n"
    "s_barrier\n"
    "v_mfma_f32_16x16x32_bf16 v[224:227], a[240:243], a[160:163], v[224:227]\n"
    "PD\n"
    "ds_read_b64_tr_b16 a[224:225], v8 offset:32768\n"
    "v_mfma_f32_16x16x32_bf16 v[224:227], a[244:247], a[176:179], v[224:227]\n"
    "PD\n"
    "buffer_load_dwordx4 v26, s[44:47], s57 offen offset:2048 lds\n"
    "v_mfma_f32_16x16x32_bf16 v[228:231], a[244:247], a[180:183], v[228:231]\n"
    "PD\n"
    "ds_read_b64_tr_b16 a[226:227], v8 offset:36864\n"
    "v_mfma_f32_16x16x32_bf16 v[228:231], a[240:243], a[164:167], v[228:231]\n"
    "PD\n"
    "v_mfma_f32_16x16x32_bf16 v[232:235], a[240:243], a[168:171], v[232:235]\n"
    "PD\n"
    "ds_read_b64_tr_b16 a[228:229], v8 offset:40960\n"
    "v_mfma_f32_16x16x32_bf16 v[232:235], a[244:247], a[184:187], v[232:235]\n"
    "PD\n"
    "v_mfma_f32_16x16x32_bf16 v[236:239], a[244:247], a[188:191], v[236:239]\n"
    "PD\n"
    "ds_read_b64_tr_b16 a[230:231], v8 offset:45056\n"
    "v_mfma_f32_16x16x32_bf16 v[236:239], a[240:243], a[172:175], v[236:239]\n"
    "PD\n"
    "s_waitcnt lgkmcnt(4)\n"
    "v_mfma_f32_16x16x32_bf16 v[252:255], a[248:251], a[172:175], v[252:255]\n"
    "PD\n"
    "ds_read_b64_tr_b16 a[232:233], v9 offset:32768\n"
    "v_mfma_f32_16x16x32_bf16 v[252:255], a[252:255], a[188:191], v[252:255]\n"
    "PD\n"
    "buffer_load_dwordx4 v27, s[44:47], s57 offen offset:3072 lds\n"
    "v_mfma_f32_16x16x32_bf16 v[248:251], a[252:255], a[184:187], v[248:251]\n"
    "PD\n"
    "ds_read_b64_tr_b16 a[234:235], v9 offset:36864\n"
    "v_mfma_f32_16x16x32_bf16 v[248:251], a[248:251], a[168:171], v[248:251]\n"
    "PD\n"
    "s_add_u32 s57, s57, s59\n"
    "v_mfma_f32_16x16x32_bf16 v[244:247], a[248:251], a[164:167], v[244:247]\n"
    "PD\n"
    "ds_read_b64_tr_b16 a[236:237], v9 offset:40960\n"
    "v_mfma_f32_16x16x32_bf16 v[244:247], a[252:255], a[180:183], v[244:247]\n"
    "PD\n"
    "v_mfma_f32_16x16x32_bf16 v[240:243], a[252:255], a[176:179], v[240:243]\n"
    "PD\n"
    "ds_read_b64_tr_b16 a[238:239], v9 offset:45056\n"
    "v_mfma_f32_16x16x32_bf16 v[240:243], a[248:251], a[160:163], v[240:243]\n"
    "PD\n"
    "s_waitcnt vmcnt(24) lgkmcnt(4)\n"
    "v_mfma_f32_16x16x32_bf16 v[128:131], a[224:227], a[192:195], v[128:131]\n"
    "PD\n"
    "ds_read_b64_tr_b16 a[240:241], v10 offset:32768\n"
    "s_bitcmp1_b32 6, s37\n"
    "v_mfma_f32_16x16x32_bf16 v[128:131], a[228:231], a[208:211], v[128:131]\n"
    "PD\n"
    "s_cbranch_scc1 H_Om\n"
    "R_Om:\n"
    "buffer_load_dwordx4 a[160:163], v20, s[40:43], s56 offen offset:0 sc1\n"
    "v_mfma_f32_16x16x32_bf16 v[132:135], a[228:231], a[212:215], v[132:135]\n"
    "PD\n"
    "ds_read_b64_tr_b16 a[242:243], v10 offset:36864\n"
    "v_mfma_f32_16x16x32_bf16 v[132:135], a[224:227], a[196:199], v[132:135]\n"
    "PD\n"
    "v_mfma_f32_16x16x32_bf16 v[136:139], a[224:227], a[200:203], v[136:139]\n"
    "PD\n"
    "ds_read_b64_tr_b16 a[244:245], v10 offset:40960\n"
    "v_mfma_f32_16x16x32_bf16 v[136:139], a[228:231], a[216:219], v[136:139]\n"
    "PD\n"
    "buffer_load_dwordx4 a[176:179], v20, s[40:43], s56 offen offset:1024 sc1\n"
    "v_mfma_f32_16x16x32_bf16 v[140:143], a[228:231], a[220:223], v[140:143]\n"
    "PD\n"
    "ds_read_b64_tr_b16 a[246:247], v10 offset:45056\n"
    "v_mfma_f32_16x16x32_bf16 v[140:143], a[224:227], a[204:207], v[140:143]\n"
    "PD\n"
    "s_waitcnt lgkmcnt(4)\n"
    "v_mfma_f32_16x16x32_bf16 v[156:159], a[232:235], a[204:207], v[156:159]\n"
    "PD\n"
    "ds_read_b64_tr_b16 a[248:249], v11 offset:32768\n"
    "v_mfma_f32_16x16x32_bf16 v[156:159], a[236:239], a[220:223], v[156:159]\n"
    "PD\n"
    "buffer_load_dwordx4 a[164:167], v21, s[40:43], s56 offen offset:0 sc1\n"
    "v_mfma_f32_16x16x32_bf16 v[152:155], a[236:239], a[216:219], v[152:155]\n"
    "PD\n"
    "ds_read_b64_tr_b16 a[250:251], v11 offset:36864\n"
    "v_mfma_f32_16x16x32_bf16 v[152:155], a[232:235], a[200:203], v[152:155]\n"
    "PD\n"
    "s_cbranch_vccnz H_Os20\n"
    "R_Os20:\n"
    "v_mfma_f32_16x16x32_bf16 v[148:151], a[232:235], a[196:199], v[148:151]\n"
    "PD\n"
    "ds_read_b64_tr_b16 a[252:253], v11 offset:40960\n"
    "v_mfma_f32_16x16x32_bf16 v[148:151], a[236:239], a[212:215], v[148:151]\n"
    "PD\n"
    "buffer_load_dwordx4 a[180:183], v21, s[40:43], s56 offen offset:1024 sc1\n"
    "v_mfma_f32_16x16x32_bf16 v[144:147], a[236:239], a[208:211], v[144:147]\n"
    "PD\n"
    "ds_read_b64_tr_b16 a[254:255], v11 offset:45056\n"
    "v_mfma_f32_16x16x32_bf16 v[144:147], a[232:235], a[192:195], v[144:147]\n"
    "PD\n"
    "s_waitcnt lgkmcnt(4)\n"
    "v_mfma_f32_16x16x32_bf16 v[160:163], a[240:243], a[192:195], v[160:163]\n"
    "PD\n"
    "ds_read_b64_tr_b16 a[224:225], v12 offset:32768\n"
    "v_mfma_f32_16x16x32_bf16 v[160:163], a[244:247], a[208:211], v[160:163]\n"
    "PD\n"
    "buffer_load_dwordx4 a[168:171], v22, s[40:43], s56 offen offset:0 sc1\n"
    "v_mfma_f32_16x16x32_bf16 v[164:167], a[244:247], a[212:215], v[164:167]\n"
    "PD\n"
    "ds_read_b64_tr_b16 a[226:227], v12 offset:36864\n"
    "v_mfma_f32_16x16x32_bf16 v[164:167], a[240:243], a[196:199], v[164:167]\n"
    "PD\n"
    "v_mfma_f32_16x16x32_bf16 v[168:171], a[240:243], a[200:203], v[168:171]\n"
    "PD\n"
    "ds_read_b64_tr_b16 a[228:229], v12 offset:40960\n"
    "v_mfma_f32_16x16x32_bf16 v[168:171], a[244:247], a[216:219], v[168:171]\n"
    "PD\n"
    "buffer_load_dwordx4 a[184:187], v22, s[40:43], s56 offen offset:1024 sc1\n"
    "v_mfma_f32_16x16x32_bf16 v[172:175], a[244:247], a[220:223], v[172:175]\n"
    "PD\n"
    "ds_read_b64_tr_b16 a[230:231], v12 offset:45056\n"
    "v_mfma_f32_16x16x32_bf16 v[172:175], a[240:243], a[204:207], v[172:175]\n"
    "PD\n"
    "s_waitcnt lgkmcnt(4)\n"
    "v_mfma_f32_16x16x32_bf16 v[188:191], a[248:251], a[204:207], v[188:191]\n"
    "PD\n"
    "ds_read_b64_tr_b16 a[232:233], v13 offset:32768\n"
    "v_mfma_f32_16x16x32_bf16 v[188:191], a[252:255], a[220:223], v[188:191]\n"
    "PD\n"
    "buffer_load_dwordx4 a[172:175], v23, s[40:43], s56 offen offset:0 sc1\n"
    "v_mfma_f32_16x16x32_bf16 v[184:187], a[252:255], a[216:219], v[184:187]\n"
    "PD\n"
    "ds_read_b64_tr_b16 a[234:235], v13 offset:36864\n"
    "v_mfma_f32_16x16x32_bf16 v[184:187], a[248:251], a[200:203], v[184:187]\n"
    "PD\n"
    "v_mfma_f32_16x16x32_bf16 v[180:183], a[248:251], a[196:199], v[180:183]\n"
    "PD\n"
    "ds_read_b64_tr_b16 a[236:237], v13 offset:40960\n"
    "v_mfma_f32_16x16x32_bf16 v[180:183], a[252:255], a[212:215], v[180:183]\n"
    "PD\n"
    "buffer_load_dwordx4 a[188:191], v23, s[40:43], s56 offen offset:1024 sc1\n"
    "v_mfma_f32_16x16x32_bf16 v[176:179], a[252:255], a[208:211], v[176:179]\n"
    "PD\n"
    "ds_read_b64_tr_b16 a[238:239], v13 offset:45056\n"
    "v_mfma_f32_16x16x32_bf16 v[176:179], a[248:251], a[192:195], v[176:179]\n"
    "PD\n"
    "s_waitcnt lgkmcnt(4)\n"
    "v_mfma_f32_16x16x32_bf16 v[192:195], a[224:227], a[192:195], v[192:195]\n"
    "PD\n"
    "ds_read_b64_tr_b16 a[240:241], v14 offset:32768\n"
    "v_mfma_f32_16x16x32_bf16 v[192:195], a[228:231], a[208:211], v[192:195]\n"
    "PD\n"
    "s_add_u32 s56, s56, s69\n"
    "s_add_u32 m0, s60, 49152\n"
    "v_mfma_f32_16x16x32_bf16 v[196:199], a[228:231], a[212:215], v[196:199]\n"
    "PD\n"
    "ds_read_b64_tr_b16 a[242:243], v14 offset:36864\n"
    "v_mfma_f32_16x16x32_bf16 v[196:199], a[224:227], a[196:199], v[196:199]\n"
    "PD\n"
    "v_mfma_f32_16x16x32_bf16 v[200:203], a[224:227], a[200:203], v[200:203]\n"
    "PD\n"
    "ds_read_b64_tr_b16 a[244:245], v14 offset:40960\n"
    "v_mfma_f32_16x16x32_bf16 v[200:203], a[228:231], a[216:219], v[200:203]\n"
    "PD\n"
    "buffer_load_dwordx4 v28, s[48:51], s58 offen offset:0 lds\n"
    "v_mfma_f32_16x16x32_bf16 v[204:207], a[228:231], a[220:223], v[204:207]\n"
    "PD\n"
    "ds_read_b64_tr_b16 a[246:247], v14 offset:45056\n"
    "v_mfma_f32_16x16x32_bf16 v[204:207], a[224:227], a[204:207], v[204:207]\n"
    "PD\n"
    "s_waitcnt lgkmcnt(4)\n"
    "v_mfma_f32_16x16x32_bf16 v[220:223], a[232:235], a[204:207], v[220:223]\n"
    "PD\n"
    "ds_read_b64_tr_b16 a[248:249], v15 offset:32768\n"
    "v_mfma_f32_16x16x32_bf16 v[220:223], a[236:239], a[220:223], v[220:223]\n"
    "PD\n"
    "buffer_load_dwordx4 v29, s[48:51], s58 offen offset:1024 lds\n"
    "v_mfma_f32_16x16x32_bf16 v[216:219], a[236:239], a[216:219], v[216:219]\n"
    "PD\n"
    "ds_read_b64_tr_b16 a[250:251], v15 offset:36864\n"
    "v_mfma_f32_16x16x32_bf16 v[216:219], a[232:235], a[200:203], v[216:219]\n"
    "PD\n"
    "s_cbranch_vccnz H_Os21\n"
    "R_Os21:\n"
    "v_mfma_f32_16x16x32_bf16 v[212:215], a[232:235], a[196:199], v[212:215]\n"
    "PD\n"
    "ds_read_b64_tr_b16 a[252:253], v15 offset:40960\n"
    "v_mfma_f32_16x16x32_bf16 v[212:215], a[236:239], a[212:215], v[212:215]\n"
    "PD\n"
    "v_mfma_f32_16x16x32_bf16 v[208:211], a[236:239], a[208:211], v[208:211]\n"
    "PD\n"
    "ds_read_b64_tr_b16 a[254:255], v15 offset:45056\n"
    "v_mfma_f32_16x16x32_bf16 v[208:211], a[232:235], a[192:195], v[208:211]\n"
    "PD\n"
    "s_waitcnt lgkmcnt(4)\n"
    "v_mfma_f32_16x16x32_bf16 v[224:227], a[240:243], a[192:195], v[224:227]\n"
    "PD\n"
    "ds_read_b64_tr_b16 a[224:225], v8 offset:49152\n"
    "v_mfma_f32_16x16x32_bf16 v[224:227], a[244:247], a[208:211], v[224:227]\n"
    "PD\n"
    "buffer_load_dwordx4 v30, s[48:51], s58 offen offset:2048 lds\n"
    "v_mfma_f32_16x16x32_bf16 v[228:231], a[244:247], a[212:215], v[228:231]\n"
    "PD\n"
    "ds_read_b64_tr_b16 a[226:227], v8 offset:53248\n"
    "v_mfma_f32_16x16x32_bf16 v[228:231], a[240:243], a[196:199], v[228:231]\n"
    "PD\n"
    "v_mfma_f32_16x16x32_bf16 v[232:235], a[240:243], a[200:203], v[232:235]\n"
    "PD\n"
    "ds_read_b64_tr_b16 a[228:229], v8 offset:57344\n"
    "v_mfma_f32_16x16x32_bf16 v[232:235], a[244:247], a[216:219], v[232:235]\n"
    "PD\n"
    "v_mfma_f32_16x16x32_bf16 v[236:239], a[244:247], a[220:223], v[236:239]\n"
    "PD\n"
    "ds_read_b64_tr_b16 a[230:231], v8 offset:61440\n"
    "v_xor_b32 v8, 0x10000, v8\n"
    "v_mfma_f32_16x16x32_bf16 v[236:239], a[240:243], a[204:207], v[236:239]\n"
    "PD\n"
    "s_waitcnt lgkmcnt(4)\n"
    "v_mfma_f32_16x16x32_bf16 v[252:255], a[248:251], a[204:207], v[252:255]\n"
    "PD\n"
    "ds_read_b64_tr_b16 a[232:233], v9 offset:49152\n"
    "v_mfma_f32_16x16x32_bf16 v[252:255], a[252:255], a[220:223], v[252:255]\n"
    "PD\n"
    "buffer_load_dwordx4 v31, s[48:51], s58 offen offset:3072 lds\n"
    "v_mfma_f32_16x16x32_bf16 v[248:251], a[252:255], a[216:219], v[248:251]\n"
    "PD\n"
    "ds_read_b64_tr_b16 a[234:235], v9 offset:53248\n"
    "v_mfma_f32_16x16x32_bf16 v[248:251], a[248:251], a[200:203], v[248:251]\n"
    "PD\n"
    "s_add_u32 s58, s58, s59\n"
    "v_mfma_f32_16x16x32_bf16 v[244:247], a[248:251], a[196:199], v[244:247]\n"
    "PD\n"
    "ds_read_b64_tr_b16 a[236:237], v9 offset:57344\n"
    "v_mfma_f32_16x16x32_bf16 v[244:247], a[252:255], a[212:215], v[244:247]\n"
    "PD\n"
    "v_mfma_f32_16x16x32_bf16 v[240:243], a[252:255], a[208:211], v[240:243]\n"
    "PD\n"
    "ds_read_b64_tr_b16 a[238:239], v9 offset:61440\n"
    "v_xor_b32 v9, 0x10000, v9\n"
    "v_mfma_f32_16x16x32_bf16 v[240:243], a[248:251], a[192:195], v[240:243]\n"
    "PD\n"
    "s_waitcnt vmcnt(28) lgkmcnt(4)\n"
    "v_mfma_f32_16x16x32_bf16 v[128:131], a[224:227], a[96:99], v[128:131]\n"
    "PD\n"
    "ds_read_b64_tr_b16 a[240:241], v10 offset:49152\n"
    "v_mfma_f32_16x16x32_bf16 v[128:131], a[228:231], a[112:115], v[128:131]\n"
    "PD\n"
    "buffer_load_dwordx4 a[192:195], v20, s[40:43], s56 offen offset:0 sc1\n"
    "v_mfma_f32_16x16x32_bf16 v[132:135], a[228:231], a[116:119], v[132:135]\n"
    "PD\n"
    "ds_read_b64_tr_b16 a[242:243], v10 offset:53248\n"
    "v_mfma_f32_16x16x32_bf16 v[132:135], a[224:227], a[100:103], v[132:135]\n"
    "PD\n"
    "v_mfma_f32_16x16x32_bf16 v[136:139], a[224:227], a[104:107], v[136:139]\n"
    "PD\n"
    "ds_read_b64_tr_b16 a[244:245], v10 offset:57344\n"
    "v_mfma_f32_16x16x32_bf16 v[136:139], a[228:231], a[120:123], v[136:139]\n"
    "PD\n"
    "buffer_load_dwordx4 a[208:211], v20, s[40:43], s56 offen offset:1024 sc1\n"
    "v_mfma_f32_16x16x32_bf16 v[140:143], a[228:231], a[124:127], v[140:143]\n"
    "PD\n"
    "ds_read_b64_tr_b16 a[246:247], v10 offset:61440\n"
    "v_xor_b32 v10, 0x10000, v10\n"
    "v_mfma_f32_16x16x32_bf16 v[140:143], a[224:227], a[108:111], v[140:143]\n"
    "PD\n"
    "s_waitcnt lgkmcnt(4)\n"
    "v_mfma_f32_16x16x32_bf16 v[156:159], a[232:235], a[108:111], v[156:159]\n"
    "PD\n"
    "ds_read_b64_tr_b16 a[248:249], v11 offset:49152\n"
    "v_mfma_f32_16x16x32_bf16 v[156:159], a[236:239], a[124:127], v[156:159]\n"
    "PD\n"
    "buffer_load_dwordx4 a[196:199], v21, s[40:43], s56 offen offset:0 sc1\n"
    "v_mfma_f32_16x16x32_bf16 v[152:155], a[236:239], a[120:123], v[152:155]\n"
    "PD\n"
    "ds_read_b64_tr_b16 a[250:251], v11 offset:53248\n"
    "v_mfma_f32_16x16x32_bf16 v[152:155], a[232:235], a[104:107], v[152:155]\n"
    "PD\n"
    "s_cbranch_vccnz H_Os30\n"
    "R_Os30:\n"
    "v_mfma_f32_16x16x32_bf16 v[148:151], a[232:235], a[100:103], v[148:151]\n"
    "PD\n"
    "ds_read_b64_tr_b16 a[252:253], v11 offset:57344\n"
    "v_mfma_f32_16x16x32_bf16 v[148:151], a[236:239], a[116:119], v[148:151]\n"
    "PD\n"
    "buffer_load_dwordx4 a[212:215], v21, s[40:43], s56 offen offset:1024 sc1\n"
    "v_mfma_f32_16x16x32_bf16 v[144:147], a[236:239], a[112:115], v[144:147]\n"
    "PD\n"
    "ds_read_b64_tr_b16 a[254:255], v11 offset:61440\n"
    "v_xor_b32 v11, 0x10000, v11\n"
    "v_mfma_f32_16x16x32_bf16 v[144:147], a[232:235], a[96:99], v[144:147]\n"
    "PD\n"
    "s_waitcnt lgkmcnt(4)\n"
    "v_mfma_f32_16x16x32_bf16 v[160:163], a[240:243], a[96:99], v[160:163]\n"
    "PD\n"
    "ds_read_b64_tr_b16 a[224:225], v12 offset:49152\n"
    "v_mfma_f32_16x16x32_bf16 v[160:163], a[244:247], a[112:115], v[160:163]\n"
    "PD\n"
    "buffer_load_dwordx4 a[200:203], v22, s[40:43], s56 offen offset:0 sc1\n"
    "v_mfma_f32_16x16x32_bf16 v[164:167], a[244:247], a[116:119], v[164:167]\n"
    "PD\n"
    "ds_read_b64_tr_b16 a[226:227], v12 offset:53248\n"
    "v_mfma_f32_16x16x32_bf16 v[164:167], a[240:243], a[100:103], v[164:167]\n"
    "PD\n"
    "v_mfma_f32_16x16x32_bf16 v[168:171], a[240:243], a[104:107], v[168:171]\n"
    "PD\n"
    "ds_read_b64_tr_b16 a[228:229], v12 offset:57344\n"
    "v_mfma_f32_16x16x32_bf16 v[168:171], a[244:247], a[120:123], v[168:171]\n"
    "PD\n"
    "buffer_load_dwordx4 a[216:219], v22, s[40:43], s56 offen offset:1024 sc1\n"
    "v_mfma_f32_16x16x32_bf16 v[172:175], a[244:247], a[124:127], v[172:175]\n"
    "PD\n"
    "ds_read_b64_tr_b16 a[230:231], v12 offset:61440\n"
    "v_xor_b32 v12, 0x10000, v12\n"
    "v_mfma_f32_16x16x32_bf16 v[172:175], a[240:243], a[108:111], v[172:175]\n"
    "PD\n"
    "s_waitcnt lgkmcnt(4)\n"
    "v_mfma_f32_16x16x32_bf16 v[188:191], a[248:251], a[108:111], v[188:191]\n"
    "PD\n"
    "ds_read_b64_tr_b16 a[232:233], v13 offset:49152\n"
    "v_mfma_f32_16x16x32_bf16 v[188:191], a[252:255], a[124:127], v[188:191]\n"
    "PD\n"
    "buffer_load_dwordx4 a[204:207], v23, s[40:43], s56 offen offset:0 sc1\n"
    "v_mfma_f32_16x16x32_bf16 v[184:187], a[252:255], a[120:123], v[184:187]\n"
    "PD\n"
    "ds_read_b64_tr_b16 a[234:235], v13 offset:53248\n"
    "v_mfma_f32_16x16x32_bf16 v[184:187], a[248:251], a[104:107], v[184:187]\n"
    "PD\n"
    "v_mfma_f32_16x16x32_bf16 v[180:183], a[248:251], a[100:103], v[180:183]\n"
    "PD\n"
    "ds_read_b64_tr_b16 a[236:237], v13 offset:57344\n"
    "v_mfma_f32_16x16x32_bf16 v[180:183], a[252:255], a[116:119], v[180:183]\n"
    "PD\n"
    "buffer_load_dwordx4 a[220:223], v23, s[40:43], s56 offen offset:1024 sc1\n"
    "v_mfma_f32_16x16x32_bf16 v[176:179], a[252:255], a[112:115], v[176:179]\n"
    "PD\n"
    "ds_read_b64_tr_b16 a[238:239], v13 offset:61440\n"
    "v_xor_b32 v13, 0x10000, v13\n"
    "v_mfma_f32_16x16x32_bf16 v[176:179], a[248:251], a[96:99], v[176:179]\n"
    "PD\n"
    "s_waitcnt lgkmcnt(4)\n"
    "v_mfma_f32_16x16x32_bf16 v[192:195], a[224:227], a[96:99], v[192:195]\n"
    "PD\n"
    "ds_read_b64_tr_b16 a[240:241], v14 offset:49152\n"
    "v_mfma_f32_16x16x32_bf16 v[192:195], a[228:231], a[112:115], v[192:195]\n"
    "PD\n"
    "s_add_u32 s56, s56, s69\n"
    "s_xor_b32 m0, s60, 0x10000\n"
    "v_mfma_f32_16x16x32_bf16 v[196:199], a[228:231], a[116:119], v[196:199]\n"
    "PD\n"
    "ds_read_b64_tr_b16 a[242:243], v14 offset:53248\n"
    "v_mfma_f32_16x16x32_bf16 v[196:199], a[224:227], a[100:103], v[196:199]\n"
    "PD\n"
    "s_add_u32 s37, s37, 1\n"
    "v_mfma_f32_16x16x32_bf16 v[200:203], a[224:227], a[104:107], v[200:203]\n"
    "PD\n"
    "ds_read_b64_tr_b16 a[244:245], v14 offset:57344\n"
    "v_mfma_f32_16x16x32_bf16 v[200:203], a[228:231], a[120:123], v[200:203]\n"
    "PD\n"
    "buffer_load_dwordx4 v24, s[44:47], s57 offen offset:0 lds\n"
    "v_mfma_f32_16x16x32_bf16 v[204:207], a[228:231], a[124:127], v[204:207]\n"
    "PD\n"
    "ds_read_b64_tr_b16 a[246:247], v14 offset:61440\n"
    "v_xor_b32 v14, 0x10000, v14\n"
    "v_mfma_f32_16x16x32_bf16 v[204:207], a[224:227], a[108:111], v[204:207]\n"
    "PD\n"
    "s_waitcnt lgkmcnt(4)\n"
    "v_mfma_f32_16x16x32_bf16 v[220:223], a[232:235], a[108:111], v[220:223]\n"
    "PD\n"
    "ds_read_b64_tr_b16 a[248:249], v15 offset:49152\n"
    "v_mfma_f32_16x16x32_bf16 v[220:223], a[236:239], a[124:127], v[220:223]\n"
    "PD\n"
    "buffer_load_dwordx4 v25, s[44:47], s57 offen offset:1024 lds\n"
    "v_mfma_f32_16x16x32_bf16 v[216:219], a[236:239], a[120:123], v[216:219]\n"
    "PD\n"
    "ds_read_b64_tr_b16 a[250:251], v15 offset:53248\n"
    "v_mfma_f32_16x16x32_bf16 v[216:219], a[232:235], a[104:107], v[216:219]\n"
    "PD\n"
    "s_cbranch_vccnz H_Os31\n"
    "R_Os31:\n"
    "v_mfma_f32_16x16x32_bf16 v[212:215], a[232:235], a[100:103], v[212:215]\n"
    "PD\n"
    "ds_read_b64_tr_b16 a[252:253], v15 offset:57344\n"
    "v_mfma_f32_16x16x32_bf16 v[212:215], a[236:239], a[116:119], v[212:215]\n"
    "PD\n"
    "v_mfma_f32_16x16x32_bf16 v[208:211], a[236:239], a[112:115], v[208:211]\n"
    "PD\n"
    "ds_read_b64_tr_b16 a[254:255], v15 offset:61440\n"
    "v_xor_b32 v15, 0x10000, v15\n"
    "v_mfma_f32_16x16x32_bf16 v[208:211], a[232:235], a[96:99], v[208:211]\n"
    "PD\n"
    "s_waitcnt lgkmcnt(4)\n"
    "s_waitcnt vmcnt(34)\n"
    "s_barrier\n"
    "v_mfma_f32_16x16x32_bf16 v[224:227], a[240:243], a[96:99], v[224:227]\n"
    "PD\n"
    "ds_read_b64_tr_b16 a[224:225], v8 offset:0\n"
    "v_mfma_f32_16x16x32_bf16 v[224:227], a[244:247], a[112:115], v[224:227]\n"
    "PD\n"
    "buffer_load_dwordx4 v26, s[44:47], s57 offen offset:2048 lds\n"
    "v_mfma_f32_16x16x32_bf16 v[228:231], a[244:247], a[116:119], v[228:231]\n"
    "PD\n"
    "ds_read_b64_tr_b16 a[226:227], v8 offset:4096\n"
    "v_mfma_f32_16x16x32_bf16 v[228:231], a[240:243], a[100:103], v[228:231]\n"
    "PD\n"
    "s_xor_b32 s60, s60, 0x10000\n"
    "v_mfma_f32_16x16x32_bf16 v[232:235], a[240:243], a[104:107], v[232:235]\n"
    "PD\n"
    "ds_read_b64_tr_b16 a[228:229], v8 offset:8192\n"
    "v_mfma_f32_16x16x32_bf16 v[232:235], a[244:247], a[120:123], v[232:235]\n"
    "PD\n"
    "v_mfma_f32_16x16x32_bf16 v[236:239], a[244:247], a[124:127], v[236:239]\n"
    "PD\n"
    "ds_read_b64_tr_b16 a[230:231], v8 offset:12288\n"
    "v_mfma_f32_16x16x32_bf16 v[236:239], a[240:243], a[108:111], v[236:239]\n"
    "PD\n"
    "s_waitcnt lgkmcnt(4)\n"
    "v_mfma_f32_16x16x32_bf16 v[252:255], a[248:251], a[108:111], v[252:255]\n"
    "PD\n"
    "ds_read_b64_tr_b16 a[232:233], v9 offset:0\n"
    "v_mfma_f32_16x16x32_bf16 v[252:255], a[252:255], a[124:127], v[252:255]\n"
    "PD\n"
    "buffer_load_dwordx4 v27, s[44:47], s57 offen offset:3072 lds\n"
    "v_mfma_f32_16x16x32_bf16 v[248:251], a[252:255], a[120:123], v[248:251]\n"
    "PD\n"
    "ds_read_b64_tr_b16 a[234:235], v9 offset:4096\n"
    "v_mfma_f32_16x16x32_bf16 v[248:251], a[248:251], a[104:107], v[248:251]\n"
    "PD\n"
    "s_add_u32 s57, s57, s59\n"
    "v_mfma_f32_16x16x32_bf16 v[244:247], a[248:251], a[100:103], v[244:247]\n"
    "PD\n"
    "ds_read_b64_tr_b16 a[236:237], v9 offset:8192\n"
    "s_mov_b64 vcc, 0\n"
    "v_mfma_f32_16x16x32_bf16 v[244:247], a[252:255], a[116:119], v[244:247]\n"
    "PD\n"
    "v_mfma_f32_16x16x32_bf16 v[240:243], a[252:255], a[112:115], v[240:243]\n"
    "PD\n"
    "ds_read_b64_tr_b16 a[238:239], v9 offset:12288\n"
    "s_cmp_lg_u32 s37, s38\n"
    "v_mfma_f32_16x16x32_bf16 v[240:243], a[248:251], a[96:99], v[240:243]\n"
    "PD\n"
    "s_waitcnt vmcnt(28) lgkmcnt(4)\n"
    "s_cbranch_scc1 L_O\n"
    "PP\n"
    "v_mfma_f32_16x16x32_bf16 v[136:139], a[224:227], a[136:139], v[136:139]\n"
    "PD\n"
    "ds_read_b64_tr_b16 a[240:241], v10 offset:0\n"
    "ds_read_b64_tr_b16 a[242:243], v10 offset:4096\n"
    "v_mfma_f32_16x16x32_bf16 v[136:139], a[228:231], a[152:155], v[136:139]\n"
    "PD\n"
    "ds_read_b64_tr_b16 a[244:245], v10 offset:8192\n"
    "ds_read_b64_tr_b16 a[246:247], v10 offset:12288\n"
    "v_mfma_f32_16x16x32_bf16 v[140:143], a[228:231], a[156:159], v[140:143]\n"
    "PD\n"
    "ds_read_b64_tr_b16 a[248:249], v11 offset:0\n"
    "ds_read_b64_tr_b16 a[250:251], v11 offset:4096\n"
    "v_mfma_f32_16x16x32_bf16 v[140:143], a[224:227], a[140:143], v[140:143]\n"
    "PD\n"
    "ds_read_b64_tr_b16 a[252:253], v11 offset:8192\n"
    "ds_read_b64_tr_b16 a[254:255], v11 offset:12288\n"
    "s_waitcnt lgkmcnt(8)\n"
    "v_mfma_f32_16x16x32_bf16 v[156:159], a[232:235], a[140:143], v[156:159]\n"
    "PD\n"
    "buffer_load_dwordx4 a[104:107], v22, s[40:43], s56 offen offset:0 sc1\n"
    "ds_read_b64_tr_b16 a[224:225], v12 offset:0\n"
    "v_mfma_f32_16x16x32_bf16 v[156:159], a[236:239], a[156:159], v[156:159]\n"
    "PD\n"
    "ds_read_b64_tr_b16 a[226:227], v12 offset:4096\n"
    "v_mfma_f32_16x16x32_bf16 v[152:155], a[236:239], a[152:155], v[152:155]\n"
    "PD\n"
    "ds_read_b64_tr_b16 a[228:229], v12 offset:8192\n"
    "v_mfma_f32_16x16x32_bf16 v[152:155], a[232:235], a[136:139], v[152:155]\n"
    "PD\n"
    "ds_read_b64_tr_b16 a[230:231], v12 offset:12288\n"
    "s_waitcnt lgkmcnt(8)\n"
    "v_mfma_f32_16x16x32_bf16 v[168:171], a[240:243], a[136:139], v[168:171]\n"
    "PD\n"
    "buffer_load_dwordx4 a[120:123], v22, s[40:43], s56 offen offset:1024 sc1\n"
    "ds_read_b64_tr_b16 a[232:233], v13 offset:0\n"
    "v_mfma_f32_16x16x32_bf16 v[168:171], a[244:247], a[152:155], v[168:171]\n"
    "PD\n"
    "ds_read_b64_tr_b16 a[234:235], v13 offset:4096\n"
    "v_mfma_f32_16x16x32_bf16 v[172:175], a[244:247], a[156:159], v[172:175]\n"
    "PD\n"
    "ds_read_b64_tr_b16 a[236:237], v13 offset:8192\n"
    "v_mfma_f32_16x16x32_bf16 v[172:175], a[240:243], a[140:143], v[172:175]\n"
    "PD\n"
    "ds_read_b64_tr_b16 a[238:239], v13 offset:12288\n"
    "s_waitcnt lgkmcnt(8)\n"
    "v_mfma_f32_16x16x32_bf16 v[188:191], a[248:251], a[140:143], v[188:191]\n"
    "PD\n"
    "buffer_load_dwordx4 a[108:111], v23, s[40:43], s56 offen offset:0 sc1\n"
    "ds_read_b64_tr_b16 a[240:241], v14 offset:0\n"
    "v_mfma_f32_16x16x32_bf16 v[188:191], a[252:255], a[156:159], v[188:191]\n"
    "PD\n"
    "ds_read_b64_tr_b16 a[242:243], v14 offset:4096\n"
    "v_mfma_f32_16x16x32_bf16 v[184:187], a[252:255], a[152:155], v[184:187]\n"
    "PD\n"
    "ds_read_b64_tr_b16 a[244:245], v14 offset:8192\n"
    "v_mfma_f32_16x16x32_bf16 v[184:187], a[248:251], a[136:139], v[184:187]\n"
    "PD\n"
    "ds_read_b64_tr_b16 a[246:247], v14 offset:12288\n"
    "s_waitcnt lgkmcnt(8)\n"
    "v_mfma_f32_16x16x32_bf16 v[200:203], a[224:227], a[136:139], v[200:203]\n"
    "PD\n"
    "buffer_load_dwordx4 a[124:127], v23, s[40:43], s56 offen offset:1024 sc1\n"
    "ds_read_b64_tr_b16 a[248:249], v15 offset:0\n"
    "v_mfma_f32_16x16x32_bf16 v[200:203], a[228:231], a[152:155], v[200:203]\n"
    "PD\n"
    "s_add_u32 s56, s56, s69\n"
    "ds_read_b64_tr_b16 a[250:251], v15 offset:4096\n"
    "v_mfma_f32_16x16x32_bf16 v[204:207], a[228:231], a[156:159], v[204:207]\n"
    "PD\n"
    "s_add_u32 m0, s60, 16384\n"
    "ds_read_b64_tr_b16 a[252:253], v15 offset:8192\n"
    "v_mfma_f32_16x16x32_bf16 v[204:207], a[224:227], a[140:143], v[204:207]\n"
    "PD\n"
    "ds_read_b64_tr_b16 a[254:255], v15 offset:12288\n"
    "s_waitcnt lgkmcnt(8)\n"
    "v_mfma_f32_16x16x32_bf16 v[220:223], a[232:235], a[140:143], v[220:223]\n"
    "PD\n"
    "buffer_load_dwordx4 v28, s[48:51], s58 offen offset:0 lds\n"
    "ds_read_b64_tr_b16 a[224:225], v8 offset:16384\n"
    "v_mfma_f32_16x16x32_bf16 v[220:223], a[236:239], a[156:159], v[220:223]\n"
    "PD\n"
    "ds_read_b64_tr_b16 a[226:227], v8 offset:20480\n"
    "v_mfma_f32_16x16x32_bf16 v[216:219], a[236:239], a[152:155], v[216:219]\n"
    "PD\n"
    "ds_read_b64_tr_b16 a[228:229], v8 offset:24576\n"
    "v_mfma_f32_16x16x32_bf16 v[216:219], a[232:235], a[136:139], v[216:219]\n"
    "PD\n"
    "ds_read_b64_tr_b16 a[230:231], v8 offset:28672\n"
    "s_waitcnt lgkmcnt(8)\n"
    "v_mfma_f32_16x16x32_bf16 v[232:235], a[240:243], a[136:139], v[232:235]\n"
    "PD\n"
    "buffer_load_dwordx4 v29, s[48:51], s58 offen offset:1024 lds\n"
    "ds_read_b64_tr_b16 a[232:233], v9 offset:16384\n"
    "v_mfma_f32_16x16x32_bf16 v[232:235], a[244:247], a[152:155], v[232:235]\n"
    "PD\n"
    "ds_read_b64_tr_b16 a[234:235], v9 offset:20480\n"
    "v_mfma_f32_16x16x32_bf16 v[236:239], a[244:247], a[156:159], v[236:239]\n"
    "PD\n"
    "ds_read_b64_tr_b16 a[236:237], v9 offset:24576\n"
    "v_mfma_f32_16x16x32_bf16 v[236:239], a[240:243], a[140:143], v[236:239]\n"
    "PD\n"
    "ds_read_b64_tr_b16 a[238:239], v9 offset:28672\n"
    "s_waitcnt lgkmcnt(8)\n"
    "v_mfma_f32_16x16x32_bf16 v[252:255], a[248:251], a[140:143], v[252:255]\n"
    "PD\n"
    "buffer_load_dwordx4 v30, s[48:51], s58 offen offset:2048 lds\n"
    "ds_read_b64_tr_b16 a[240:241], v10 offset:16384\n"
    "v_mfma_f32_16x16x32_bf16 v[252:255], a[252:255], a[156:159], v[252:255]\n"
    "PD\n"
    "ds_read_b64_tr_b16 a[242:243], v10 offset:20480\n"
    "v_mfma_f32_16x16x32_bf16 v[248:251], a[252:255], a[152:155], v[248:251]\n"
    "PD\n"
    "ds_read_b64_tr_b16 a[244:245], v10 offset:24576\n"
    "v_mfma_f32_16x16x32_bf16 v[248:251], a[248:251], a[136:139], v[248:251]\n"
    "PD\n"
    "ds_read_b64_tr_b16 a[246:247], v10 offset:28672\n"
    "s_waitcnt vmcnt(23) lgkmcnt(8)\n"
    "v_mfma_f32_16x16x32_bf16 v[136:139], a[224:227], a[168:171], v[136:139]\n"
    "PD\n"
    "buffer_load_dwordx4 v31, s[48:51], s58 offen offset:3072 lds\n"
    "ds_read_b64_tr_b16 a[248:249], v11 offset:16384\n"
    "v_mfma_f32_16x16x32_bf16 v[136:139], a[228:231], a[184:187], v[136:139]\n"
    "PD\n"
    "s_add_u32 s58, s58, s59\n"
    "ds_read_b64_tr_b16 a[250:251], v11 offset:20480\n"
    "v_mfma_f32_16x16x32_bf16 v[140:143], a[228:231], a[188:191], v[140:143]\n"
    "PD\n"
    "ds_read_b64_tr_b16 a[252:253], v11 offset:24576\n"
    "v_mfma_f32_16x16x32_bf16 v[140:143], a[224:227], a[172:175], v[140:143]\n"
    "PD\n"
    "ds_read_b64_tr_b16 a[254:255], v11 offset:28672\n"
    "s_waitcnt lgkmcnt(8)\n"
    "v_mfma_f32_16x16x32_bf16 v[156:159], a[232:235], a[172:175], v[156:159]\n"
    "PD\n"
    "buffer_load_dwordx4 a[8:11], v22, s[40:43], s56 offen offset:0 sc1\n"
    "ds_read_b64_tr_b16 a[224:225], v12 offset:16384\n"
    "v_mfma_f32_16x16x32_bf16 v[156:159], a[236:239], a[188:191], v[156:159]\n"
    "PD\n"
    "ds_read_b64_tr_b16 a[226:227], v12 offset:20480\n"
    "v_mfma_f32_16x16x32_bf16 v[152:155], a[236:239], a[184:187], v[152:155]\n"
    "PD\n"
    "ds_read_b64_tr_b16 a[228:229], v12 offset:24576\n"
    "v_mfma_f32_16x16x32_bf16 v[152:155], a[232:235], a[168:171], v[152:155]\n"
    "PD\n"
    "ds_read_b64_tr_b16 a[230:231], v12 offset:28672\n"
    "s_waitcnt lgkmcnt(8)\n"
    "v_mfma_f32_16x16x32_bf16 v[168:171], a[240:243], a[168:171], v[168:171]\n"
    "PD\n"
    "buffer_load_dwordx4 a[24:27], v22, s[40:43], s56 offen offset:1024 sc1\n"
    "ds_read_b64_tr_b16 a[232:233], v13 offset:16384\n"
    "v_mfma_f32_16x16x32_bf16 v[168:171], a[244:247], a[184:187], v[168:171]\n"
    "PD\n"
    "ds_read_b64_tr_b16 a[234:235], v13 offset:20480\n"
    "v_mfma_f32_16x16x32_bf16 v[172:175], a[244:247], a[188:191], v[172:175]\n"
    "PD\n"
    "ds_read_b64_tr_b16 a[236:237], v13 offset:24576\n"
    "v_mfma_f32_16x16x32_bf16 v[172:175], a[240:243], a[172:175], v[172:175]\n"
    "PD\n"
    "ds_read_b64_tr_b16 a[238:239], v13 offset:28672\n"
    "s_waitcnt lgkmcnt(8)\n"
    "v_mfma_f32_16x16x32_bf16 v[188:191], a[248:251], a[172:175], v[188:191]\n"
    "PD\n"
    "buffer_load_dwordx4 a[12:15], v23, s[40:43], s56 offen offset:0 sc1\n"
    "ds_read_b64_tr_b16 a[240:241], v14 offset:16384\n"
    "v_mfma_f32_16x16x32_bf16 v[188:191], a[252:255], a[188:191], v[188:191]\n"
    "PD\n"
    "ds_read_b64_tr_b16 a[242:243], v14 offset:20480\n"
    "v_mfma_f32_16x16x32_bf16 v[184:187], a[252:255], a[184:187], v[184:187]\n"
    "PD\n"
    "ds_read_b64_tr_b16 a[244:245], v14 offset:24576\n"
    "v_mfma_f32_16x16x32_bf16 v[184:187], a[248:251], a[168:171], v[184:187]\n"
    "PD\n"
    "ds_read_b64_tr_b16 a[246:247], v14 offset:28672\n"
    "s_waitcnt lgkmcnt(8)\n"
    "v_mfma_f32_16x16x32_bf16 v[200:203], a[224:227], a[168:171], v[200:203]\n"
    "PD\n"
    "buffer_load_dwordx4 a[28:31], v23, s[40:43], s56 offen offset:1024 sc1\n"
    "ds_read_b64_tr_b16 a[248:249], v15 offset:16384\n"
    "v_mfma_f32_16x16x32_bf16 v[200:203], a[228:231], a[184:187], v[200:203]\n"
    "PD\n"
    "s_add_u32 s56, s56, s69\n"
    "ds_read_b64_tr_b16 a[250:251], v15 offset:20480\n"
    "v_mfma_f32_16x16x32_bf16 v[204:207], a[228:231], a[188:191], v[204:207]\n"
    "PD\n"
    "ds_read_b64_tr_b16 a[252:253], v15 offset:24576\n"
    "v_mfma_f32_16x16x32_bf16 v[204:207], a[224:227], a[172:175], v[204:207]\n"
    "PD\n"
    "ds_read_b64_tr_b16 a[254:255], v15 offset:28672\n"
    "s_waitcnt lgkmcnt(8)\n"
    "s_waitcnt vmcnt(24)\n"
    "s_barrier\n"
    "v_mfma_f32_16x16x32_bf16 v[220:223], a[232:235], a[172:175], v[220:223]\n"
    "PD\n"
    "buffer_load_dwordx4 a[40:43], v22, s[40:43], s56 offen offset:0 sc1\n"
    "ds_read_b64_tr_b16 a[224:225], v8 offset:32768\n"
    "v_mfma_f32_16x16x32_bf16 v[220:223], a[236:239], a[188:191], v[220:223]\n"
    "PD\n"
    "ds_read_b64_tr_b16 a[226:227], v8 offset:36864\n"
    "v_mfma_f32_16x16x32_bf16 v[216:219], a[236:239], a[184:187], v[216:219]\n"
    "PD\n"
    "ds_read_b64_tr_b16 a[228:229], v8 offset:40960\n"
    "v_mfma_f32_16x16x32_bf16 v[216:219], a[232:235], a[168:171], v[216:219]\n"
    "PD\n"
    "ds_read_b64_tr_b16 a[230:231], v8 offset:45056\n"
    "s_waitcnt lgkmcnt(8)\n"
    "v_mfma_f32_16x16x32_bf16 v[232:235], a[240:243], a[168:171], v[232:235]\n"
    "PD\n"
    "buffer_load_dwordx4 a[56:59], v22, s[40:43], s56 offen offset:1024 sc1\n"
    "ds_read_b64_tr_b16 a[232:233], v9 offset:32768\n"
    "v_mfma_f32_16x16x32_bf16 v[232:235], a[244:247], a[184:187], v[232:235]\n"
    "PD\n"
    "ds_read_b64_tr_b16 a[234:235], v9 offset:36864\n"
    "v_mfma_f32_16x16x32_bf16 v[236:239], a[244:247], a[188:191], v[236:239]\n"
    "PD\n"
    "ds_read_b64_tr_b16 a[236:237], v9 offset:40960\n"
    "v_mfma_f32_16x16x32_bf16 v[236:239], a[240:243], a[172:175], v[236:239]\n"
    "PD\n"
    "ds_read_b64_tr_b16 a[238:239], v9 offset:45056\n"
    "s_waitcnt lgkmcnt(8)\n"
    "v_mfma_f32_16x16x32_bf16 v[252:255], a[248:251], a[172:175], v[252:255]\n"
    "PD\n"
    "buffer_load_dwordx4 a[44:47], v23, s[40:43], s56 offen offset:0 sc1\n"
    "ds_read_b64_tr_b16 a[240:241], v10 offset:32768\n"
    "v_mfma_f32_16x16x32_bf16 v[252:255], a[252:255], a[188:191], v[252:255]\n"
    "PD\n"
    "ds_read_b64_tr_b16 a[242:243], v10 offset:36864\n"
    "v_mfma_f32_16x16x32_bf16 v[248:251], a[252:255], a[184:187], v[248:251]\n"
    "PD\n"
    "ds_read_b64_tr_b16 a[244:245], v10 offset:40960\n"
    "v_mfma_f32_16x16x32_bf16 v[248:251], a[248:251], a[168:171], v[248:251]\n"
    "PD\n"
    "ds_read_b64_tr_b16 a[246:247], v10 offset:45056\n"
    "s_waitcnt vmcnt(19) lgkmcnt(8)\n"
    "v_mfma_f32_16x16x32_bf16 v[136:139], a[224:227], a[200:203], v[136:139]\n"
    "PD\n"
    "buffer_load_dwordx4 a[60:63], v23, s[40:43], s56 offen offset:1024 sc1\n"
    "ds_read_b64_tr_b16 a[248:249], v11 offset:32768\n"
    "v_mfma_f32_16x16x32_bf16 v[136:139], a[228:231], a[216:219], v[136:139]\n"
    "PD\n"
    "s_add_u32 s56, s56, s69\n"
    "ds_read_b64_tr_b16 a[250:251], v11 offset:36864\n"
    "v_mfma_f32_16x16x32_bf16 v[140:143], a[228:231], a[220:223], v[140:143]\n"
    "PD\n"
    "s_add_u32 m0, s60, 32768\n"
    "ds_read_b64_tr_b16 a[252:253], v11 offset:40960\n"
    "v_mfma_f32_16x16x32_bf16 v[140:143], a[224:227], a[204:207], v[140:143]\n"
    "PD\n"
    "ds_read_b64_tr_b16 a[254:255], v11 offset:45056\n"
    "s_waitcnt lgkmcnt(8)\n"
    "v_mfma_f32_16x16x32_bf16 v[156:159], a[232:235], a[204:207], v[156:159]\n"
    "PD\n"
    "buffer_load_dwordx4 v24, s[44:47], s57 offen offset:0 lds\n"
    "ds_read_b64_tr_b16 a[224:225], v12 offset:32768\n"
    "v_mfma_f32_16x16x32_bf16 v[156:159], a[236:239], a[220:223], v[156:159]\n"
    "PD\n"
    "ds_read_b64_tr_b16 a[226:227], v12 offset:36864\n"
    "v_mfma_f32_16x16x32_bf16 v[152:155], a[236:239], a[216:219], v[152:155]\n"
    "PD\n"
    "ds_read_b64_tr_b16 a[228:229], v12 offset:40960\n"
    "v_mfma_f32_16x16x32_bf16 v[152:155], a[232:235], a[200:203], v[152:155]\n"
    "PD\n"
    "ds_read_b64_tr_b16 a[230:231], v12 offset:45056\n"
    "s_waitcnt lgkmcnt(8)\n"
    "v_mfma_f32_16x16x32_bf16 v[168:171], a[240:243], a[200:203], v[168:171]\n"
    "PD\n"
    "buffer_load_dwordx4 v25, s[44:47], s57 offen offset:1024 lds\n"
    "ds_read_b64_tr_b16 a[232:233], v13 offset:32768\n"
    "v_mfma_f32_16x16x32_bf16 v[168:171], a[244:247], a[216:219], v[168:171]\n"
    "PD\n"
    "ds_read_b64_tr_b16 a[234:235], v13 offset:36864\n"
    "v_mfma_f32_16x16x32_bf16 v[172:175], a[244:247], a[220:223], v[172:175]\n"
    "PD\n"
    "ds_read_b64_tr_b16 a[236:237], v13 offset:40960\n"
    "v_mfma_f32_16x16x32_bf16 v[172:175], a[240:243], a[204:207], v[172:175]\n"
    "PD\n"
    "ds_read_b64_tr_b16 a[238:239], v13 offset:45056\n"
    "s_waitcnt lgkmcnt(8)\n"
    "v_mfma_f32_16x16x32_bf16 v[188:191], a[248:251], a[204:207], v[188:191]\n"
    "PD\n"
    "buffer_load_dwordx4 v26, s[44:47], s57 offen offset:2048 lds\n"
    "ds_read_b64_tr_b16 a[240:241], v14 offset:32768\n"
    "v_mfma_f32_16x16x32_bf16 v[188:191], a[252:255], a[220:223], v[188:191]\n"
    "PD\n"
    "ds_read_b64_tr_b16 a[242:243], v14 offset:36864\n"
    "v_mfma_f32_16x16x32_bf16 v[184:187], a[252:255], a[216:219], v[184:187]\n"
    "PD\n"
    "ds_read_b64_tr_b16 a[244:245], v14 offset:40960\n"
    "v_mfma_f32_16x16x32_bf16 v[184:187], a[248:251], a[200:203], v[184:187]\n"
    "PD\n"
    "ds_read_b64_tr_b16 a[246:247], v14 offset:45056\n"
    "s_waitcnt lgkmcnt(8)\n"
    "v_mfma_f32_16x16x32_bf16 v[200:203], a[224:227], a[200:203], v[200:203]\n"
    "PD\n"
    "buffer_load_dwordx4 v27, s[44:47], s57 offen offset:3072 lds\n"
    "ds_read_b64_tr_b16 a[248:249], v15 offset:32768\n"
    "v_mfma_f32_16x16x32_bf16 v[200:203], a[228:231], a[216:219], v[200:203]\n"
    "PD\n"
    "s_add_u32 s57, s57, s59\n"
    "ds_read_b64_tr_b16 a[250:251], v15 offset:36864\n"
    "v_mfma_f32_16x16x32_bf16 v[204:207], a[228:231], a[220:223], v[204:207]\n"
    "PD\n"
    "s_add_u32 m0, s60, 49152\n"
    "ds_read_b64_tr_b16 a[252:253], v15 offset:40960\n"
    "v_mfma_f32_16x16x32_bf16 v[204:207], a[224:227], a[204:207], v[204:207]\n"
    "PD\n"
    "ds_read_b64_tr_b16 a[254:255], v15 offset:45056\n"
    "s_waitcnt lgkmcnt(8)\n"
    "v_mfma_f32_16x16x32_bf16 v[220:223], a[232:235], a[204:207], v[220:223]\n"
    "PD\n"
    "buffer_load_dwordx4 v28, s[48:51], s58 offen offset:0 lds\n"
    "ds_read_b64_tr_b16 a[224:225], v8 offset:49152\n"
    "v_mfma_f32_16x16x32_bf16 v[220:223], a[236:239], a[220:223], v[220:223]\n"
    "PD\n"
    "ds_read_b64_tr_b16 a[226:227], v8 offset:53248\n"
    "v_mfma_f32_16x16x32_bf16 v[216:219], a[236:239], a[216:219], v[216:219]\n"
    "PD\n"
    "ds_read_b64_tr_b16 a[228:229], v8 offset:57344\n"
    "v_mfma_f32_16x16x32_bf16 v[216:219], a[232:235], a[200:203], v[216:219]\n"
    "PD\n"
    "ds_read_b64_tr_b16 a[230:231], v8 offset:61440\n"
    "s_waitcnt lgkmcnt(8)\n"
    "v_mfma_f32_16x16x32_bf16 v[232:235], a[240:243], a[200:203], v[232:235]\n"
    "PD\n"
    "buffer_load_dwordx4 v29, s[48:51], s58 offen offset:1024 lds\n"
    "ds_read_b64_tr_b16 a[232:233], v9 offset:49152\n"
    "v_mfma_f32_16x16x32_bf16 v[232:235], a[244:247], a[216:219], v[232:235]\n"
    "PD\n"
    "ds_read_b64_tr_b16 a[234:235], v9 offset:53248\n"
    "v_xor_b32 v8, 0x10000, v8\n"
    "v_mfma_f32_16x16x32_bf16 v[236:239], a[244:247], a[220:223], v[236:239]\n"
    "PD\n"
    "ds_read_b64_tr_b16 a[236:237], v9 offset:57344\n"
    "v_mfma_f32_16x16x32_bf16 v[236:239], a[240:243], a[204:207], v[236:239]\n"
    "PD\n"
    "ds_read_b64_tr_b16 a[238:239], v9 offset:61440\n"
    "s_waitcnt lgkmcnt(8)\n"
    "v_mfma_f32_16x16x32_bf16 v[252:255], a[248:251], a[204:207], v[252:255]\n"
    "PD\n"
    "buffer_load_dwordx4 v30, s[48:51], s58 offen offset:2048 lds\n"
    "ds_read_b64_tr_b16 a[240:241], v10 offset:49152\n"
    "v_mfma_f32_16x16x32_bf16 v[252:255], a[252:255], a[220:223], v[252:255]\n"
    "PD\n"
    "ds_read_b64_tr_b16 a[242:243], v10 offset:53248\n"
    "v_xor_b32 v9, 0x10000, v9\n"
    "v_mfma_f32_16x16x32_bf16 v[248:251], a[252:255], a[216:219], v[248:251]\n"
    "PD\n"
    "ds_read_b64_tr_b16 a[244:245], v10 offset:57344\n"
    "v_mfma_f32_16x16x32_bf16 v[248:251], a[248:251], a[200:203], v[248:251]\n"
    "PD\n"
    "ds_read_b64_tr_b16 a[246:247], v10 offset:61440\n"
    "s_waitcnt vmcnt(19) lgkmcnt(8)\n"
    "v_mfma_f32_16x16x32_bf16 v[136:139], a[224:227], a[104:107], v[136:139]\n"
    "PD\n"
    "buffer_load_dwordx4 v31, s[48:51], s58 offen offset:3072 lds\n"
    "ds_read_b64_tr_b16 a[248:249], v11 offset:49152\n"
    "v_mfma_f32_16x16x32_bf16 v[136:139], a[228:231], a[120:123], v[136:139]\n"
    "PD\n"
    "ds_read_b64_tr_b16 a[250:251], v11 offset:53248\n"
    "v_xor_b32 v10, 0x10000, v10\n"
    "v_mfma_f32_16x16x32_bf16 v[140:143], a[228:231], a[124:127], v[140:143]\n"
    "PD\n"
    "s_add_u32 s58, s58, s59\n"
    "ds_read_b64_tr_b16 a[252:253], v11 offset:57344\n"
    "v_mfma_f32_16x16x32_bf16 v[140:143], a[224:227], a[108:111], v[140:143]\n"
    "PD\n"
    "ds_read_b64_tr_b16 a[254:255], v11 offset:61440\n"
    "s_waitcnt lgkmcnt(8)\n"
    "v_mfma_f32_16x16x32_bf16 v[156:159], a[232:235], a[108:111], v[156:159]\n"
    "PD\n"
    "buffer_load_dwordx4 a[72:75], v22, s[40:43], s56 offen offset:0 sc1\n"
    "ds_read_b64_tr_b16 a[224:225], v12 offset:49152\n"
    "v_mfma_f32_16x16x32_bf16 v[156:159], a[236:239], a[124:127], v[156:159]\n"
    "PD\n"
    "ds_read_b64_tr_b16 a[226:227], v12 offset:53248\n"
    "v_xor_b32 v11, 0x10000, v11\n"
    "v_mfma_f32_16x16x32_bf16 v[152:155], a[236:239], a[120:123], v[152:155]\n"
    "PD\n"
    "s_mov_b32 s54, 53248\n"
    "ds_read_b64_tr_b16 a[228:229], v12 offset:57344\n"
    "v_mfma_f32_16x16x32_bf16 v[152:155], a[232:235], a[104:107], v[152:155]\n"
    "PD\n"
    "ds_read_b64_tr_b16 a[230:231], v12 offset:61440\n"
    "s_waitcnt lgkmcnt(8)\n"
    "v_mfma_f32_16x16x32_bf16 v[168:171], a[240:243], a[104:107], v[168:171]\n"
    "PD\n"
    "buffer_load_dwordx4 a[88:91], v22, s[40:43], s56 offen offset:1024 sc1\n"
    "ds_read_b64_tr_b16 a[232:233], v13 offset:49152\n"
    "v_mfma_f32_16x16x32_bf16 v[168:171], a[244:247], a[120:123], v[168:171]\n"
    "PD\n"
    "ds_read_b64_tr_b16 a[234:235], v13 offset:53248\n"
    "v_xor_b32 v12, 0x10000, v12\n"
    "v_mfma_f32_16x16x32_bf16 v[172:175], a[244:247], a[124:127], v[172:175]\n"
    "PD\n"
    "s_mov_b64 s[52:53], s[88:89]\n"
    "ds_read_b64_tr_b16 a[236:237], v13 offset:57344\n"
    "v_mfma_f32_16x16x32_bf16 v[172:175], a[240:243], a[108:111], v[172:175]\n"
    "PD\n"
    "ds_read_b64_tr_b16 a[238:239], v13 offset:61440\n"
    "s_waitcnt lgkmcnt(8)\n"
    "v_mfma_f32_16x16x32_bf16 v[188:191], a[248:251], a[108:111], v[188:191]\n"
    "PD\n"
    "buffer_load_dwordx4 a[76:79], v23, s[40:43], s56 offen offset:0 sc1\n"
    "ds_read_b64_tr_b16 a[240:241], v14 offset:49152\n"
    "v_mfma_f32_16x16x32_bf16 v[188:191], a[252:255], a[124:127], v[188:191]\n"
    "PD\n"
    "ds_read_b64_tr_b16 a[242:243], v14 offset:53248\n"
    "v_xor_b32 v13, 0x10000, v13\n"
    "v_mfma_f32_16x16x32_bf16 v[184:187], a[252:255], a[120:123], v[184:187]\n"
    "PD\n"
    "s_mul_i32 s70, s29, 13312\n"
    "ds_read_b64_tr_b16 a[244:245], v14 offset:57344\n"
    "v_mfma_f32_16x16x32_bf16 v[184:187], a[248:251], a[104:107], v[184:187]\n"
    "PD\n"
    "ds_read_b64_tr_b16 a[246:247], v14 offset:61440\n"
    "s_waitcnt lgkmcnt(8)\n"
    "v_mfma_f32_16x16x32_bf16 v[200:203], a[224:227], a[104:107], v[200:203]\n"
    "PD\n"
    "buffer_load_dwordx4 a[92:95], v23, s[40:43], s56 offen offset:1024 sc1\n"
    "ds_read_b64_tr_b16 a[248:249], v15 offset:49152\n"
    "v_mfma_f32_16x16x32_bf16 v[200:203], a[228:231], a[120:123], v[200:203]\n"
    "PD\n"
    "ds_read_b64_tr_b16 a[250:251], v15 offset:53248\n"
    "v_xor_b32 v14, 0x10000, v14\n"
    "v_mfma_f32_16x16x32_bf16 v[204:207], a[228:231], a[124:127], v[204:207]\n"
    "PD\n"
    "s_add_u32 s56, s56, s69\n"
    "ds_read_b64_tr_b16 a[252:253], v15 offset:57344\n"
    "v_mfma_f32_16x16x32_bf16 v[204:207], a[224:227], a[108:111], v[204:207]\n"
    "PD\n"
    "ds_read_b64_tr_b16 a[254:255], v15 offset:61440\n"
    "s_waitcnt lgkmcnt(8)\n"
    "s_waitcnt vmcnt(20)\n"
    "s_barrier\n"
    "v_mfma_f32_16x16x32_bf16 v[220:223], a[232:235], a[108:111], v[220:223]\n"
    "PD\n"
    "buffer_load_dwordx4 a[136:139], v22, s[40:43], s56 offen offset:0 sc1\n"
    "ds_read_b64_tr_b16 a[224:225], v8 offset:0\n"
    "v_mfma_f32_16x16x32_bf16 v[220:223], a[236:239], a[124:127], v[220:223]\n"
    "PD\n"
    "ds_read_b64_tr_b16 a[226:227], v8 offset:4096\n"
    "v_xor_b32 v15, 0x10000, v15\n"
    "v_mfma_f32_16x16x32_bf16 v[216:219], a[236:239], a[120:123], v[216:219]\n"
    "PD\n"
    "s_add_u32 s71, s70, 0x1000\n"
    "ds_read_b64_tr_b16 a[228:229], v8 offset:8192\n"
    "v_mfma_f32_16x16x32_bf16 v[216:219], a[232:235], a[104:107], v[216:219]\n"
    "PD\n"
    "ds_read_b64_tr_b16 a[230:231], v8 offset:12288\n"
    "s_waitcnt lgkmcnt(8)\n"
    "v_mfma_f32_16x16x32_bf16 v[232:235], a[240:243], a[104:107], v[232:235]\n"
    "PD\n"
    "buffer_load_dwordx4 a[152:155], v22, s[40:43], s56 offen offset:1024 sc1\n"
    "ds_read_b64_tr_b16 a[232:233], v9 offset:0\n"
    "v_mfma_f32_16x16x32_bf16 v[232:235], a[244:247], a[120:123], v[232:235]\n"
    "PD\n"
    "s_add_u32 s72, s70, 0x2000\n"
    "ds_read_b64_tr_b16 a[234:235], v9 offset:4096\n"
    "v_mfma_f32_16x16x32_bf16 v[236:239], a[244:247], a[124:127], v[236:239]\n"
    "PD\n"
    "s_or_b32 s85, s24, s29\n"
    "ds_read_b64_tr_b16 a[236:237], v9 offset:8192\n"
    "v_mfma_f32_16x16x32_bf16 v[236:239], a[240:243], a[108:111], v[236:239]\n"
    "PD\n"
    "ds_read_b64_tr_b16 a[238:239], v9 offset:12288\n"
    "s_waitcnt lgkmcnt(8)\n"
    "v_mfma_f32_16x16x32_bf16 v[252:255], a[248:251], a[108:111], v[252:255]\n"
    "PD\n"
    "buffer_load_dwordx4 a[140:143], v23, s[40:43], s56 offen offset:0 sc1\n"
    "ds_read_b64_tr_b16 a[240:241], v10 offset:0\n"
    "v_mfma_f32_16x16x32_bf16 v[252:255], a[252:255], a[124:127], v[252:255]\n"
    "PD\n"
    "s_lshl2_add_u32 s62, s26, s29\n"
    "ds_read_b64_tr_b16 a[242:243], v10 offset:4096\n"
    "v_mfma_f32_16x16x32_bf16 v[248:251], a[252:255], a[120:123], v[248:251]\n"
    "PD\n"
    "s_lshl_b32 s62, s62, 15\n"
    "ds_read_b64_tr_b16 a[244:245], v10 offset:8192\n"
    "v_mfma_f32_16x16x32_bf16 v[248:251], a[248:251], a[104:107], v[248:251]\n"
    "PD\n"
    "ds_read_b64_tr_b16 a[246:247], v10 offset:12288\n"
    "s_waitcnt vmcnt(19) lgkmcnt(8)\n"
    "v_mfma_f32_16x16x32_bf16 v[136:139], a[224:227], a[8:11], v[136:139]\n"
    "PD\n"
    "buffer_load_dwordx4 a[156:159], v23, s[40:43], s56 offen offset:1024 sc1\n"
    "ds_read_b64_tr_b16 a[248:249], v11 offset:0\n"
    "v_mfma_f32_16x16x32_bf16 v[136:139], a[228:231], a[24:27], v[136:139]\n"
    "PD\n"
    "s_lshl_b32 s63, s24, 8\n"
    "ds_read_b64_tr_b16 a[250:251], v11 offset:4096\n"
    "v_mfma_f32_16x16x32_bf16 v[140:143], a[228:231], a[28:31], v[140:143]\n"
    "PD\n"
    "s_add_u32 s62, s62, s63\n"
    "ds_read_b64_tr_b16 a[252:253], v11 offset:8192\n"
    "v_mfma_f32_16x16x32_bf16 v[140:143], a[224:227], a[12:15], v[140:143]\n"
    "PD\n"
    "ds_read_b64_tr_b16 a[254:255], v11 offset:12288\n"
    "s_waitcnt lgkmcnt(8)\n"
    "v_mfma_f32_16x16x32_bf16 v[156:159], a[232:235], a[12:15], v[156:159]\n"
    "PD\n"
    "buffer_load_dwordx4 a[172:175], v21, s[40:43], s56 offen offset:0 sc1\n"
    "ds_read_b64_tr_b16 a[224:225], v12 offset:0\n"
    "v_mfma_f32_16x16x32_bf16 v[156:159], a[236:239], a[28:31], v[156:159]\n"
    "PD\n"
    "s_add_u32 s48, s4, s62\n"
    "s_addc_u32 s49, s5, 0\n"
    "ds_read_b64_tr_b16 a[226:227], v12 offset:4096\n"
    "v_mfma_f32_16x16x32_bf16 v[152:155], a[236:239], a[24:27], v[152:155]\n"
    "PD\n"
    "s_add_u32 s44, s20, s62\n"
    "s_addc_u32 s45, s21, 0\n"
    "ds_read_b64_tr_b16 a[228:229], v12 offset:8192\n"
    "v_mfma_f32_16x16x32_bf16 v[152:155], a[232:235], a[8:11], v[152:155]\n"
    "PD\n"
    "ds_read_b64_tr_b16 a[230:231], v12 offset:12288\n"
    "s_waitcnt lgkmcnt(8)\n"
    "v_mfma_f32_16x16x32_bf16 v[168:171], a[240:243], a[8:11], v[168:171]\n"
    "PD\n"
    "buffer_load_dwordx4 a[188:191], v21, s[40:43], s56 offen offset:1024 sc1\n"
    "ds_read_b64_tr_b16 a[232:233], v13 offset:0\n"
    "v_mfma_f32_16x16x32_bf16 v[168:171], a[244:247], a[24:27], v[168:171]\n"
    "PD\n"
    "s_mov_b32 s50, 0x7900\n"
    "ds_read_b64_tr_b16 a[234:235], v13 offset:4096\n"
    "v_mfma_f32_16x16x32_bf16 v[172:175], a[244:247], a[28:31], v[172:175]\n"
    "PD\n"
    "s_mov_b32 s46, 0x7900\n"
    "ds_read_b64_tr_b16 a[236:237], v13 offset:8192\n"
    "v_mfma_f32_16x16x32_bf16 v[172:175], a[240:243], a[12:15], v[172:175]\n"
    "PD\n"
    "ds_read_b64_tr_b16 a[238:239], v13 offset:12288\n"
    "s_waitcnt lgkmcnt(8)\n"
    "v_mfma_f32_16x16x32_bf16 v[188:191], a[248:251], a[12:15], v[188:191]\n"
    "PD\n"
    "buffer_load_dwordx4 a[168:171], v20, s[40:43], s56 offen offset:0 sc1\n"
    "ds_read_b64_tr_b16 a[240:241], v14 offset:0\n"
    "v_mfma_f32_16x16x32_bf16 v[188:191], a[252:255], a[28:31], v[188:191]\n"
    "PD\n"
    "ds_read_b64_tr_b16 a[242:243], v14 offset:4096\n"
    "v_and_b32 v56, 15, v0\n"
    "v_mfma_f32_16x16x32_bf16 v[184:187], a[252:255], a[24:27], v[184:187]\n"
    "PD\n"
    "ds_read_b64_tr_b16 a[244:245], v14 offset:8192\n"
    "v_bfe_u32 v57, v0, 5, 1\n"
    "v_mfma_f32_16x16x32_bf16 v[184:187], a[248:251], a[8:11], v[184:187]\n"
    "PD\n"
    "ds_read_b64_tr_b16 a[246:247], v14 offset:12288\n"
    "s_waitcnt lgkmcnt(8)\n"
    "v_mfma_f32_16x16x32_bf16 v[200:203], a[224:227], a[8:11], v[200:203]\n"
    "PD\n"
    "buffer_load_dwordx4 a[184:187], v20, s[40:43], s56 offen offset:1024 sc1\n"
    "ds_read_b64_tr_b16 a[248:249], v15 offset:0\n"
    "v_mfma_f32_16x16x32_bf16 v[200:203], a[228:231], a[24:27], v[200:203]\n"
    "PD\n"
    "s_sub_u32 s56, s56, 0x8000\n"
    "ds_read_b64_tr_b16 a[250:251], v15 offset:4096\n"
    "v_mfma_f32_16x16x32_bf16 v[204:207], a[228:231], a[28:31], v[204:207]\n"
    "PD\n"
    "ds_read_b64_tr_b16 a[252:253], v15 offset:8192\n"
    "v_bfe_u32 v58, v0, 4, 1\n"
    "v_mfma_f32_16x16x32_bf16 v[204:207], a[224:227], a[12:15], v[204:207]\n"
    "PD\n"
    "ds_read_b64_tr_b16 a[254:255], v15 offset:12288\n"
    "s_waitcnt lgkmcnt(8)\n"
    "v_mfma_f32_16x16x32_bf16 v[220:223], a[232:235], a[12:15], v[220:223]\n"
    "PD\n"
    "buffer_load_dwordx4 a[96:99], v20, s[40:43], s56 offen offset:0 sc1\n"
    "ds_read_b64_tr_b16 a[224:225], v8 offset:16384\n"
    "v_mfma_f32_16x16x32_bf16 v[220:223], a[236:239], a[28:31], v[220:223]\n"
    "PD\n"
    "ds_read_b64_tr_b16 a[226:227], v8 offset:20480\n"
    "v_lshl_add_u32 v57, v58, 1, v57\n"
    "v_mfma_f32_16x16x32_bf16 v[216:219], a[236:239], a[24:27], v[216:219]\n"
    "PD\n"
    "ds_read_b64_tr_b16 a[228:229], v8 offset:24576\n"
    "v_lshlrev_b32 v57, 4, v57\n"
    "v_mfma_f32_16x16x32_bf16 v[216:219], a[232:235], a[8:11], v[216:219]\n"
    "PD\n"
    "ds_read_b64_tr_b16 a[230:231], v8 offset:28672\n"
    "s_waitcnt lgkmcnt(8)\n"
    "v_mfma_f32_16x16x32_bf16 v[232:235], a[240:243], a[8:11], v[232:235]\n"
    "PD\n"
    "buffer_load_dwordx4 a[112:115], v20, s[40:43], s56 offen offset:1024 sc1\n"
    "ds_read_b64_tr_b16 a[232:233], v9 offset:16384\n"
    "v_mfma_f32_16x16x32_bf16 v[232:235], a[244:247], a[24:27], v[232:235]\n"
    "PD\n"
    "ds_read_b64_tr_b16 a[234:235], v9 offset:20480\n"
    "v_lshl_add_u32 v1, v56, 11, v57\n"
    "v_mfma_f32_16x16x32_bf16 v[236:239], a[244:247], a[28:31], v[236:239]\n"
    "PD\n"
    "s_mov_b32 s66, 0xbfb8aa3b\n"
    "ds_read_b64_tr_b16 a[236:237], v9 offset:24576\n"
    "v_mfma_f32_16x16x32_bf16 v[236:239], a[240:243], a[12:15], v[236:239]\n"
    "PD\n"
    "ds_read_b64_tr_b16 a[238:239], v9 offset:28672\n"
    "s_waitcnt lgkmcnt(8)\n"
    "v_mfma_f32_16x16x32_bf16 v[252:255], a[248:251], a[12:15], v[252:255]\n"
    "PD\n"
    "buffer_load_dwordx4 a[100:103], v21, s[40:43], s56 offen offset:0 sc1\n"
    "ds_read_b64_tr_b16 a[240:241], v10 offset:16384\n"
    "v_mfma_f32_16x16x32_bf16 v[252:255], a[252:255], a[28:31], v[252:255]\n"
    "PD\n"
    "s_mov_b32 s68, 0xffff0000\n"
    "ds_read_b64_tr_b16 a[242:243], v10 offset:20480\n"
    "v_mfma_f32_16x16x32_bf16 v[248:251], a[252:255], a[24:27], v[248:251]\n"
    "PD\n"
    "ds_read_b64_tr_b16 a[244:245], v10 offset:24576\n"
    "v_mfma_f32_16x16x32_bf16 v[248:251], a[248:251], a[8:11], v[248:251]\n"
    "PD\n"
    "ds_read_b64_tr_b16 a[246:247], v10 offset:28672\n"
    "s_waitcnt vmcnt(23) lgkmcnt(8)\n"
    "v_mfma_f32_16x16x32_bf16 v[136:139], a[224:227], a[40:43], v[136:139]\n"
    "PD\n"
    "buffer_load_dwordx4 a[116:119], v21, s[40:43], s56 offen offset:1024 sc1\n"
    "ds_read_b64_tr_b16 a[248:249], v11 offset:16384\n"
    "v_mfma_f32_16x16x32_bf16 v[136:139], a[228:231], a[56:59], v[136:139]\n"
    "PD\n"
    "s_add_u32 s56, s56, s69\n"
    "ds_read_b64_tr_b16 a[250:251], v11 offset:20480\n"
    "v_mfma_f32_16x16x32_bf16 v[140:143], a[228:231], a[60:63], v[140:143]\n"
    "PD\n"
    "ds_read_b64_tr_b16 a[252:253], v11 offset:24576\n"
    "v_mfma_f32_16x16x32_bf16 v[140:143], a[224:227], a[44:47], v[140:143]\n"
    "PD\n"
    "ds_read_b64_tr_b16 a[254:255], v11 offset:28672\n"
    "s_waitcnt lgkmcnt(8)\n"
    "v_mfma_f32_16x16x32_bf16 v[156:159], a[232:235], a[44:47], v[156:159]\n"
    "PD\n"
    "buffer_load_dwordx4 a[0:3], v20, s[40:43], s56 offen offset:0 sc1\n"
    "ds_read_b64_tr_b16 a[224:225], v12 offset:16384\n"
    "v_mfma_f32_16x16x32_bf16 v[156:159], a[236:239], a[60:63], v[156:159]\n"
    "PD\n"
    "ds_read_b64_tr_b16 a[226:227], v12 offset:20480\n"
    "v_mfma_f32_16x16x32_bf16 v[152:155], a[236:239], a[56:59], v[152:155]\n"
    "PD\n"
    "ds_read_b64_tr_b16 a[228:229], v12 offset:24576\n"
    "v_mfma_f32_16x16x32_bf16 v[152:155], a[232:235], a[40:43], v[152:155]\n"
    "PD\n"
    "ds_read_b64_tr_b16 a[230:231], v12 offset:28672\n"
    "s_waitcnt lgkmcnt(8)\n"
    "v_mfma_f32_16x16x32_bf16 v[168:171], a[240:243], a[40:43], v[168:171]\n"
    "PD\n"
    "buffer_load_dwordx4 a[16:19], v20, s[40:43], s56 offen offset:1024 sc1\n"
    "ds_read_b64_tr_b16 a[232:233], v13 offset:16384\n"
    "v_mfma_f32_16x16x32_bf16 v[168:171], a[244:247], a[56:59], v[168:171]\n"
    "PD\n"
    "ds_read_b64_tr_b16 a[234:235], v13 offset:20480\n"
    "v_mfma_f32_16x16x32_bf16 v[172:175], a[244:247], a[60:63], v[172:175]\n"
    "PD\n"
    "ds_read_b64_tr_b16 a[236:237], v13 offset:24576\n"
    "v_mfma_f32_16x16x32_bf16 v[172:175], a[240:243], a[44:47], v[172:175]\n"
    "PD\n"
    "ds_read_b64_tr_b16 a[238:239], v13 offset:28672\n"
    "s_waitcnt lgkmcnt(8)\n"
    "v_mfma_f32_16x16x32_bf16 v[188:191], a[248:251], a[44:47], v[188:191]\n"
    "PD\n"
    "buffer_load_dwordx4 a[4:7], v21, s[40:43], s56 offen offset:0 sc1\n"
    "ds_read_b64_tr_b16 a[240:241], v14 offset:16384\n"
    "v_mfma_f32_16x16x32_bf16 v[188:191], a[252:255], a[60:63], v[188:191]\n"
    "PD\n"
    "ds_read_b64_tr_b16 a[242:243], v14 offset:20480\n"
    "v_mfma_f32_16x16x32_bf16 v[184:187], a[252:255], a[56:59], v[184:187]\n"
    "PD\n"
    "ds_read_b64_tr_b16 a[244:245], v14 offset:24576\n"
    "v_mfma_f32_16x16x32_bf16 v[184:187], a[248:251], a[40:43], v[184:187]\n"
    "PD\n"
    "ds_read_b64_tr_b16 a[246:247], v14 offset:28672\n"
    "s_waitcnt lgkmcnt(8)\n"
    "v_mfma_f32_16x16x32_bf16 v[200:203], a[224:227], a[40:43], v[200:203]\n"
    "PD\n"
    "buffer_load_dwordx4 a[20:23], v21, s[40:43], s56 offen offset:1024 sc1\n"
    "ds_read_b64_tr_b16 a[248:249], v15 offset:16384\n"
    "v_mfma_f32_16x16x32_bf16 v[200:203], a[228:231], a[56:59], v[200:203]\n"
    "PD\n"
    "s_add_u32 s56, s56, s69\n"
    "ds_read_b64_tr_b16 a[250:251], v15 offset:20480\n"
    "v_mfma_f32_16x16x32_bf16 v[204:207], a[228:231], a[60:63], v[204:207]\n"
    "PD\n"
    "ds_read_b64_tr_b16 a[252:253], v15 offset:24576\n"
    "v_mfma_f32_16x16x32_bf16 v[204:207], a[224:227], a[44:47], v[204:207]\n"
    "PD\n"
    "ds_read_b64_tr_b16 a[254:255], v15 offset:28672\n"
    "s_waitcnt lgkmcnt(8)\n"
    "s_waitcnt vmcnt(20)\n"
    "s_barrier\n"
    "v_mfma_f32_16x16x32_bf16 v[220:223], a[232:235], a[44:47], v[220:223]\n"
    "PD\n"
    "buffer_load_dwordx4 a[32:35], v20, s[40:43], s56 offen offset:0 sc1\n"
    "ds_read_b64_tr_b16 a[224:225], v8 offset:32768\n"
    "v_mfma_f32_16x16x32_bf16 v[220:223], a[236:239], a[60:63], v[220:223]\n"
    "PD\n"
    "ds_read_b64_tr_b16 a[226:227], v8 offset:36864\n"
    "v_mfma_f32_16x16x32_bf16 v[216:219], a[236:239], a[56:59], v[216:219]\n"
    "PD\n"
    "ds_read_b64_tr_b16 a[228:229], v8 offset:40960\n"
    "v_mfma_f32_16x16x32_bf16 v[216:219], a[232:235], a[40:43], v[216:219]\n"
    "PD\n"
    "ds_read_b64_tr_b16 a[230:231], v8 offset:45056\n"
    "s_waitcnt lgkmcnt(8)\n"
    "v_mfma_f32_16x16x32_bf16 v[232:235], a[240:243], a[40:43], v[232:235]\n"
    "PD\n"
    "buffer_load_dwordx4 a[48:51], v20, s[40:43], s56 offen offset:1024 sc1\n"
    "ds_read_b64_tr_b16 a[232:233], v9 offset:32768\n"
    "v_mfma_f32_16x16x32_bf16 v[232:235], a[244:247], a[56:59], v[232:235]\n"
    "PD\n"
    "ds_read_b64_tr_b16 a[234:235], v9 offset:36864\n"
    "v_mfma_f32_16x16x32_bf16 v[236:239], a[244:247], a[60:63], v[236:239]\n"
    "PD\n"
    "ds_read_b64_tr_b16 a[236:237], v9 offset:40960\n"
    "v_mfma_f32_16x16x32_bf16 v[236:239], a[240:243], a[44:47], v[236:239]\n"
    "PD\n"
    "ds_read_b64_tr_b16 a[238:239], v9 offset:45056\n"
    "s_waitcnt lgkmcnt(8)\n"
    "v_mfma_f32_16x16x32_bf16 v[252:255], a[248:251], a[44:47], v[252:255]\n"
    "PD\n"
    "buffer_load_dwordx4 a[36:39], v21, s[40:43], s56 offen offset:0 sc1\n"
    "ds_read_b64_tr_b16 a[240:241], v10 offset:32768\n"
    "v_mfma_f32_16x16x32_bf16 v[252:255], a[252:255], a[60:63], v[252:255]\n"
    "PD\n"
    "ds_read_b64_tr_b16 a[242:243], v10 offset:36864\n"
    "v_mfma_f32_16x16x32_bf16 v[248:251], a[252:255], a[56:59], v[248:251]\n"
    "PD\n"
    "ds_read_b64_tr_b16 a[244:245], v10 offset:40960\n"
    "v_mfma_f32_16x16x32_bf16 v[248:251], a[248:251], a[40:43], v[248:251]\n"
    "PD\n"
    "ds_read_b64_tr_b16 a[246:247], v10 offset:45056\n"
    "s_waitcnt vmcnt(19) lgkmcnt(8)\n"
    "v_mfma_f32_16x16x32_bf16 v[136:139], a[224:227], a[72:75], v[136:139]\n"
    "PD\n"
    "buffer_load_dwordx4 a[52:55], v21, s[40:43], s56 offen offset:1024 sc1\n"
    "ds_read_b64_tr_b16 a[248:249], v11 offset:32768\n"
    "v_mfma_f32_16x16x32_bf16 v[136:139], a[228:231], a[88:91], v[136:139]\n"
    "PD\n"
    "s_add_u32 s56, s56, s69\n"
    "ds_read_b64_tr_b16 a[250:251], v11 offset:36864\n"
    "v_mfma_f32_16x16x32_bf16 v[140:143], a[228:231], a[92:95], v[140:143]\n"
    "PD\n"
    "ds_read_b64_tr_b16 a[252:253], v11 offset:40960\n"
    "v_mfma_f32_16x16x32_bf16 v[140:143], a[224:227], a[76:79], v[140:143]\n"
    "PD\n"
    "ds_read_b64_tr_b16 a[254:255], v11 offset:45056\n"
    "s_waitcnt lgkmcnt(8)\n"
    "v_mfma_f32_16x16x32_bf16 v[156:159], a[232:235], a[76:79], v[156:159]\n"
    "PD\n"
    "buffer_load_dwordx4 a[64:67], v20, s[40:43], s56 offen offset:0 sc1\n"
    "ds_read_b64_tr_b16 a[224:225], v12 offset:32768\n"
    "v_mfma_f32_16x16x32_bf16 v[156:159], a[236:239], a[92:95], v[156:159]\n"
    "PD\n"
    "ds_read_b64_tr_b16 a[226:227], v12 offset:36864\n"
    "v_mfma_f32_16x16x32_bf16 v[152:155], a[236:239], a[88:91], v[152:155]\n"
    "PD\n"
    "ds_read_b64_tr_b16 a[228:229], v12 offset:40960\n"
    "v_mfma_f32_16x16x32_bf16 v[152:155], a[232:235], a[72:75], v[152:155]\n"
    "PD\n"
    "ds_read_b64_tr_b16 a[230:231], v12 offset:45056\n"
    "s_waitcnt lgkmcnt(8)\n"
    "v_mfma_f32_16x16x32_bf16 v[168:171], a[240:243], a[72:75], v[168:171]\n"
    "PD\n"
    "buffer_load_dwordx4 a[80:83], v20, s[40:43], s56 offen offset:1024 sc1\n"
    "ds_read_b64_tr_b16 a[232:233], v13 offset:32768\n"
    "v_mfma_f32_16x16x32_bf16 v[168:171], a[244:247], a[88:91], v[168:171]\n"
    "PD\n"
    "ds_read_b64_tr_b16 a[234:235], v13 offset:36864\n"
    "v_mfma_f32_16x16x32_bf16 v[172:175], a[244:247], a[92:95], v[172:175]\n"
    "PD\n"
    "ds_read_b64_tr_b16 a[236:237], v13 offset:40960\n"
    "v_mfma_f32_16x16x32_bf16 v[172:175], a[240:243], a[76:79], v[172:175]\n"
    "PD\n"
    "ds_read_b64_tr_b16 a[238:239], v13 offset:45056\n"
    "s_waitcnt lgkmcnt(8)\n"
    "v_mfma_f32_16x16x32_bf16 v[188:191], a[248:251], a[76:79], v[188:191]\n"
    "PD\n"
    "buffer_load_dwordx4 a[68:71], v21, s[40:43], s56 offen offset:0 sc1\n"
    "ds_read_b64_tr_b16 a[240:241], v14 offset:32768\n"
    "v_mfma_f32_16x16x32_bf16 v[188:191], a[252:255], a[92:95], v[188:191]\n"
    "PD\n"
    "ds_read_b64_tr_b16 a[242:243], v14 offset:36864\n"
    "v_mfma_f32_16x16x32_bf16 v[184:187], a[252:255], a[88:91], v[184:187]\n"
    "PD\n"
    "ds_read_b64_tr_b16 a[244:245], v14 offset:40960\n"
    "v_mfma_f32_16x16x32_bf16 v[184:187], a[248:251], a[72:75], v[184:187]\n"
    "PD\n"
    "ds_read_b64_tr_b16 a[246:247], v14 offset:45056\n"
    "s_waitcnt lgkmcnt(8)\n"
    "v_mfma_f32_16x16x32_bf16 v[200:203], a[224:227], a[72:75], v[200:203]\n"
    "PD\n"
    "buffer_load_dwordx4 a[84:87], v21, s[40:43], s56 offen offset:1024 sc1\n"
    "ds_read_b64_tr_b16 a[248:249], v15 offset:32768\n"
    "v_mfma_f32_16x16x32_bf16 v[200:203], a[228:231], a[88:91], v[200:203]\n"
    "PD\n"
    "s_add_u32 s56, s56, s69\n"
    "ds_read_b64_tr_b16 a[250:251], v15 offset:36864\n"
    "v_mfma_f32_16x16x32_bf16 v[204:207], a[228:231], a[92:95], v[204:207]\n"
    "PD\n"
    "ds_read_b64_tr_b16 a[252:253], v15 offset:40960\n"
    "v_mfma_f32_16x16x32_bf16 v[204:207], a[224:227], a[76:79], v[204:207]\n"
    "PD\n"
    "ds_read_b64_tr_b16 a[254:255], v15 offset:45056\n"
    "s_waitcnt lgkmcnt(8)\n"
    "v_mfma_f32_16x16x32_bf16 v[220:223], a[232:235], a[76:79], v[220:223]\n"
    "PD\n"
    "ds_read_b64_tr_b16 a[200:201], v8 offset:49152\n"
    "v_mfma_f32_16x16x32_bf16 v[220:223], a[236:239], a[92:95], v[220:223]\n"
    "PD\n"
    "ds_read_b64_tr_b16 a[202:203], v8 offset:53248\n"
    "v_mfma_f32_16x16x32_bf16 v[216:219], a[236:239], a[88:91], v[216:219]\n"
    "PD\n"
    "ds_read_b64_tr_b16 a[204:205], v8 offset:57344\n"
    "v_mfma_f32_16x16x32_bf16 v[216:219], a[232:235], a[72:75], v[216:219]\n"
    "PD\n"
    "ds_read_b64_tr_b16 a[206:207], v8 offset:61440\n"
    "s_waitcnt lgkmcnt(8)\n"
    "v_mfma_f32_16x16x32_bf16 v[232:235], a[240:243], a[72:75], v[232:235]\n"
    "PD\n"
    "ds_read_b64_tr_b16 a[216:217], v9 offset:49152\n"
    "v_mfma_f32_16x16x32_bf16 v[232:235], a[244:247], a[88:91], v[232:235]\n"
    "PD\n"
    "ds_read_b64_tr_b16 a[218:219], v9 offset:53248\n"
    "v_xor_b32 v8, 0x10000, v8\n"
    "v_mfma_f32_16x16x32_bf16 v[236:239], a[244:247], a[92:95], v[236:239]\n"
    "PD\n"
    "ds_read_b64_tr_b16 a[220:221], v9 offset:57344\n"
    "v_mfma_f32_16x16x32_bf16 v[236:239], a[240:243], a[76:79], v[236:239]\n"
    "PD\n"
    "ds_read_b64_tr_b16 a[222:223], v9 offset:61440\n"
    "s_waitcnt lgkmcnt(8)\n"
    "v_mfma_f32_16x16x32_bf16 v[252:255], a[248:251], a[76:79], v[252:255]\n"
    "PD\n"
    "ds_read_b64_tr_b16 a[104:105], v10 offset:49152\n"
    "v_mfma_f32_16x16x32_bf16 v[252:255], a[252:255], a[92:95], v[252:255]\n"
    "PD\n"
    "ds_read_b64_tr_b16 a[106:107], v10 offset:53248\n"
    "v_xor_b32 v9, 0x10000, v9\n"
    "v_mfma_f32_16x16x32_bf16 v[248:251], a[252:255], a[88:91], v[248:251]\n"
    "PD\n"
    "ds_read_b64_tr_b16 a[108:109], v10 offset:57344\n"
    "v_mfma_f32_16x16x32_bf16 v[248:251], a[248:251], a[72:75], v[248:251]\n"
    "PD\n"
    "ds_read_b64_tr_b16 a[110:111], v10 offset:61440\n"
    "s_waitcnt vmcnt(20) lgkmcnt(8)\n"
    "v_mfma_f32_16x16x32_bf16 v[136:139], a[200:203], a[136:139], v[136:139]\n"
    "PD\n"
    "ds_read_b64_tr_b16 a[120:121], v11 offset:49152\n"
    "v_mfma_f32_16x16x32_bf16 v[136:139], a[204:207], a[152:155], v[136:139]\n"
    "PD\n"
    "ds_read_b64_tr_b16 a[122:123], v11 offset:53248\n"
    "v_xor_b32 v10, 0x10000, v10\n"
    "v_mfma_f32_16x16x32_bf16 v[140:143], a[204:207], a[156:159], v[140:143]\n"
    "PD\n"
    "ds_read_b64_tr_b16 a[124:125], v11 offset:57344\n"
    "v_mfma_f32_16x16x32_bf16 v[140:143], a[200:203], a[140:143], v[140:143]\n"
    "PD\n"
    "ds_read_b64_tr_b16 a[126:127], v11 offset:61440\n"
    "s_waitcnt lgkmcnt(8)\n"
    "v_mfma_f32_16x16x32_bf16 v[156:159], a[216:219], a[140:143], v[156:159]\n"
    "PD\n"
    "ds_read_b64_tr_b16 a[8:9], v12 offset:49152\n"
    "v_mfma_f32_16x16x32_bf16 v[156:159], a[220:223], a[156:159], v[156:159]\n"
    "PD\n"
    "ds_read_b64_tr_b16 a[10:11], v12 offset:53248\n"
    "v_xor_b32 v11, 0x10000, v11\n"
    "v_mfma_f32_16x16x32_bf16 v[152:155], a[220:223], a[152:155], v[152:155]\n"
    "PD\n"
    "ds_read_b64_tr_b16 a[12:13], v12 offset:57344\n"
    "v_mfma_f32_16x16x32_bf16 v[152:155], a[216:219], a[136:139], v[152:155]\n"
    "PD\n"
    "ds_read_b64_tr_b16 a[14:15], v12 offset:61440\n"
    "s_waitcnt lgkmcnt(8)\n"
    "v_mfma_f32_16x16x32_bf16 v[168:171], a[104:107], a[136:139], v[168:171]\n"
    "PD\n"
    "ds_read_b64_tr_b16 a[24:25], v13 offset:49152\n"
    "v_mfma_f32_16x16x32_bf16 v[168:171], a[108:111], a[152:155], v[168:171]\n"
    "PD\n"
    "ds_read_b64_tr_b16 a[26:27], v13 offset:53248\n"
    "v_xor_b32 v12, 0x10000, v12\n"
    "v_mfma_f32_16x16x32_bf16 v[172:175], a[108:111], a[156:159], v[172:175]\n"
    "PD\n"
    "ds_read_b64_tr_b16 a[28:29], v13 offset:57344\n"
    "v_mfma_f32_16x16x32_bf16 v[172:175], a[104:107], a[140:143], v[172:175]\n"
    "PD\n"
    "ds_read_b64_tr_b16 a[30:31], v13 offset:61440\n"
    "s_waitcnt lgkmcnt(8)\n"
    "v_mfma_f32_16x16x32_bf16 v[188:191], a[120:123], a[140:143], v[188:191]\n"
    "PD\n"
    "ds_read_b64_tr_b16 a[40:41], v14 offset:49152\n"
    "v_mfma_f32_16x16x32_bf16 v[188:191], a[124:127], a[156:159], v[188:191]\n"
    "PD\n"
    "ds_read_b64_tr_b16 a[42:43], v14 offset:53248\n"
    "v_xor_b32 v13, 0x10000, v13\n"
    "v_mfma_f32_16x16x32_bf16 v[184:187], a[124:127], a[152:155], v[184:187]\n"
    "PD\n"
    "ds_read_b64_tr_b16 a[44:45], v14 offset:57344\n"
    "v_mfma_f32_16x16x32_bf16 v[184:187], a[120:123], a[136:139], v[184:187]\n"
    "PD\n"
    "ds_read_b64_tr_b16 a[46:47], v14 offset:61440\n"
    "s_waitcnt lgkmcnt(8)\n"
    "v_mfma_f32_16x16x32_bf16 v[200:203], a[8:11], a[136:139], v[200:203]\n"
    "PD\n"
    "ds_read_b64_tr_b16 a[56:57], v15 offset:49152\n"
    "v_mfma_f32_16x16x32_bf16 v[200:203], a[12:15], a[152:155], v[200:203]\n"
    "PD\n"
    "ds_read_b64_tr_b16 a[58:59], v15 offset:53248\n"
    "v_xor_b32 v14, 0x10000, v14\n"
    "v_mfma_f32_16x16x32_bf16 v[204:207], a[12:15], a[156:159], v[204:207]\n"
    "PD\n"
    "ds_read_b64_tr_b16 a[60:61], v15 offset:57344\n"
    "v_mfma_f32_16x16x32_bf16 v[204:207], a[8:11], a[140:143], v[204:207]\n"
    "PD\n"
    "ds_read_b64_tr_b16 a[62:63], v15 offset:61440\n"
    "s_waitcnt lgkmcnt(8)\n"
    "v_mfma_f32_16x16x32_bf16 v[220:223], a[24:27], a[140:143], v[220:223]\n"
    "PD\n"
    "ds_read_b64_tr_b16 a[224:225], v8 offset:0\n"
    "v_mfma_f32_16x16x32_bf16 v[220:223], a[28:31], a[156:159], v[220:223]\n"
    "PD\n"
    "ds_read_b64_tr_b16 a[226:227], v8 offset:4096\n"
    "v_xor_b32 v15, 0x10000, v15\n"
    "v_mfma_f32_16x16x32_bf16 v[216:219], a[28:31], a[152:155], v[216:219]\n"
    "PD\n"
    "ds_read_b64_tr_b16 a[228:229], v8 offset:8192\n"
    "v_mfma_f32_16x16x32_bf16 v[216:219], a[24:27], a[136:139], v[216:219]\n"
    "PD\n"
    "ds_read_b64_tr_b16 a[230:231], v8 offset:12288\n"
    "s_waitcnt lgkmcnt(8)\n"
    "v_mfma_f32_16x16x32_bf16 v[232:235], a[40:43], a[136:139], v[232:235]\n"
    "PD\n"
    "ds_read_b64_tr_b16 a[232:233], v9 offset:0\n"
    "v_mfma_f32_16x16x32_bf16 v[232:235], a[44:47], a[152:155], v[232:235]\n"
    "PD\n"
    "ds_read_b64_tr_b16 a[234:235], v9 offset:4096\n"
    "v_mfma_f32_16x16x32_bf16 v[236:239], a[44:47], a[156:159], v[236:239]\n"
    "PD\n"
    "ds_read_b64_tr_b16 a[236:237], v9 offset:8192\n"
    "v_mfma_f32_16x16x32_bf16 v[236:239], a[40:43], a[140:143], v[236:239]\n"
    "PD\n"
    "ds_read_b64_tr_b16 a[238:239], v9 offset:12288\n"
    "s_waitcnt lgkmcnt(8)\n"
    "v_mfma_f32_16x16x32_bf16 v[252:255], a[56:59], a[140:143], v[252:255]\n"
    "PD\n"
    "ds_read_b64_tr_b16 a[240:241], v10 offset:0\n"
    "v_mfma_f32_16x16x32_bf16 v[252:255], a[60:63], a[156:159], v[252:255]\n"
    "PD\n"
    "ds_read_b64_tr_b16 a[242:243], v10 offset:4096\n"
    "v_mfma_f32_16x16x32_bf16 v[248:251], a[60:63], a[152:155], v[248:251]\n"
    "PD\n"
    "ds_read_b64_tr_b16 a[244:245], v10 offset:8192\n"
    "v_mfma_f32_16x16x32_bf16 v[248:251], a[56:59], a[136:139], v[248:251]\n"
    "PD\n"
    "ds_read_b64_tr_b16 a[246:247], v10 offset:12288\n"
    "s_waitcnt vmcnt(18)\n"
    "v_mfma_f32_16x16x32_bf16 v[132:135], a[200:203], a[172:175], v[132:135]\n"
    "PD\n"
    "buffer_load_dwordx4 v[40:43], v1, s[48:51], 0 offen offset:0\n"
    "v_mfma_f32_16x16x32_bf16 v[132:135], a[204:207], a[188:191], v[132:135]\n"
    "PD\n"
    "v_mfma_f32_16x16x32_bf16 v[148:151], a[220:223], a[188:191], v[148:151]\n"
    "PD\n"
    "v_mfma_f32_16x16x32_bf16 v[148:151], a[216:219], a[172:175], v[148:151]\n"
    "PD\n"
    "v_mfma_f32_16x16x32_bf16 v[164:167], a[104:107], a[172:175], v[164:167]\n"
    "PD\n"
    "buffer_load_dwordx4 v[44:47], v1, s[48:51], 0 offen offset:64\n"
    "v_mfma_f32_16x16x32_bf16 v[164:167], a[108:111], a[188:191], v[164:167]\n"
    "PD\n"
    "v_mfma_f32_16x16x32_bf16 v[180:183], a[124:127], a[188:191], v[180:183]\n"
    "PD\n"
    "v_mfma_f32_16x16x32_bf16 v[180:183], a[120:123], a[172:175], v[180:183]\n"
    "PD\n"
    "v_mfma_f32_16x16x32_bf16 v[196:199], a[8:11], a[172:175], v[196:199]\n"
    "PD\n"
    "buffer_load_dwordx4 v[48:51], v1, s[48:51], 0 offen offset:128\n"
    "v_mfma_f32_16x16x32_bf16 v[196:199], a[12:15], a[188:191], v[196:199]\n"
    "PD\n"
    "v_mfma_f32_16x16x32_bf16 v[212:215], a[28:31], a[188:191], v[212:215]\n"
    "PD\n"
    "v_mfma_f32_16x16x32_bf16 v[212:215], a[24:27], a[172:175], v[212:215]\n"
    "PD\n"
    "v_mfma_f32_16x16x32_bf16 v[228:231], a[40:43], a[172:175], v[228:231]\n"
    "PD\n"
    "buffer_load_dwordx4 v[52:55], v1, s[48:51], 0 offen offset:192\n"
    "v_cvt_pk_bf16_f32 v64, v136, v137\n"
    "v_cvt_pk_bf16_f32 v65, v138, v139\n"
    "v_mfma_f32_16x16x32_bf16 v[228:231], a[44:47], a[188:191], v[228:231]\n"
    "PD\n"
    "v_cvt_pk_bf16_f32 v66, v152, v153\n"
    "v_cvt_pk_bf16_f32 v67, v154, v155\n"
    "v_mfma_f32_16x16x32_bf16 v[244:247], a[60:63], a[188:191], v[244:247]\n"
    "PD\n"
    "v_min_u32 v64, -2, v64\n"
    "v_min_u32 v65, -2, v65\n"
    "v_mfma_f32_16x16x32_bf16 v[244:247], a[56:59], a[172:175], v[244:247]\n"
    "PD\n"
    "s_waitcnt lgkmcnt(8)\n"
    "v_min_u32 v66, -2, v66\n"
    "v_min_u32 v67, -2, v67\n"
    "v_mfma_f32_16x16x32_bf16 v[128:131], a[224:227], a[128:131], v[128:131]\n"
    "PD\n"
    "buffer_store_dwordx4 v[64:67], v4, s[52:55], s0 offen offset:0 sc1\n"
    "ds_read_b64_tr_b16 a[248:249], v11 offset:0\n"
    "v_mfma_f32_16x16x32_bf16 v[128:131], a[228:231], a[144:147], v[128:131]\n"
    "PD\n"
    "ds_read_b64_tr_b16 a[250:251], v11 offset:4096\n"
    "v_cvt_pk_bf16_f32 v80, v140, v141\n"
    "v_mfma_f32_16x16x32_bf16 v[132:135], a[228:231], a[148:151], v[132:135]\n"
    "PD\n"
    "ds_read_b64_tr_b16 a[252:253], v11 offset:8192\n"
    "v_cvt_pk_bf16_f32 v81, v142, v143\n"
    "v_mfma_f32_16x16x32_bf16 v[132:135], a[224:227], a[132:135], v[132:135]\n"
    "PD\n"
    "ds_read_b64_tr_b16 a[254:255], v11 offset:12288\n"
    "s_waitcnt lgkmcnt(8)\n"
    "v_mfma_f32_16x16x32_bf16 v[148:151], a[232:235], a[132:135], v[148:151]\n"
    "PD\n"
    "ds_read_b64_tr_b16 a[224:225], v12 offset:0\n"
    "v_cvt_pk_bf16_f32 v82, v156, v157\n"
    "v_mfma_f32_16x16x32_bf16 v[148:151], a[236:239], a[148:151], v[148:151]\n"
    "PD\n"
    "ds_read_b64_tr_b16 a[226:227], v12 offset:4096\n"
    "v_cvt_pk_bf16_f32 v83, v158, v159\n"
    "v_mfma_f32_16x16x32_bf16 v[144:147], a[236:239], a[144:147], v[144:147]\n"
    "PD\n"
    "ds_read_b64_tr_b16 a[228:229], v12 offset:8192\n"
    "v_min_u32 v80, -2, v80\n"
    "v_mfma_f32_16x16x32_bf16 v[144:147], a[232:235], a[128:131], v[144:147]\n"
    "PD\n"
    "ds_read_b64_tr_b16 a[230:231], v12 offset:12288\n"
    "s_waitcnt lgkmcnt(8)\n"
    "v_mfma_f32_16x16x32_bf16 v[160:163], a[240:243], a[128:131], v[160:163]\n"
    "PD\n"
    "ds_read_b64_tr_b16 a[232:233], v13 offset:0\n"
    "v_min_u32 v81, -2, v81\n"
    "v_mfma_f32_16x16x32_bf16 v[160:163], a[244:247], a[144:147], v[160:163]\n"
    "PD\n"
    "ds_read_b64_tr_b16 a[234:235], v13 offset:4096\n"
    "v_min_u32 v82, -2, v82\n"
    "v_mfma_f32_16x16x32_bf16 v[164:167], a[244:247], a[148:151], v[164:167]\n"
    "PD\n"
    "ds_read_b64_tr_b16 a[236:237], v13 offset:8192\n"
    "v_min_u32 v83, -2, v83\n"
    "v_mfma_f32_16x16x32_bf16 v[164:167], a[240:243], a[132:135], v[164:167]\n"
    "PD\n"
    "ds_read_b64_tr_b16 a[238:239], v13 offset:12288\n"
    "s_waitcnt lgkmcnt(8)\n"
    "v_mfma_f32_16x16x32_bf16 v[180:183], a[248:251], a[132:135], v[180:183]\n"
    "PD\n"
    "buffer_store_dwordx4 v[80:83], v4, s[52:55], s1 offen offset:0 sc1\n"
    "ds_read_b64_tr_b16 a[240:241], v14 offset:0\n"
    "v_mfma_f32_16x16x32_bf16 v[180:183], a[252:255], a[148:151], v[180:183]\n"
    "PD\n"
    "ds_read_b64_tr_b16 a[242:243], v14 offset:4096\n"
    "v_cvt_pk_bf16_f32 v68, v168, v169\n"
    "v_mfma_f32_16x16x32_bf16 v[176:179], a[252:255], a[144:147], v[176:179]\n"
    "PD\n"
    "ds_read_b64_tr_b16 a[244:245], v14 offset:8192\n"
    "v_cvt_pk_bf16_f32 v69, v170, v171\n"
    "v_mfma_f32_16x16x32_bf16 v[176:179], a[248:251], a[128:131], v[176:179]\n"
    "PD\n"
    "ds_read_b64_tr_b16 a[246:247], v14 offset:12288\n"
    "s_waitcnt lgkmcnt(8)\n"
    "v_mfma_f32_16x16x32_bf16 v[192:195], a[224:227], a[128:131], v[192:195]\n"
    "PD\n"
    "ds_read_b64_tr_b16 a[248:249], v15 offset:0\n"
    "v_cvt_pk_bf16_f32 v70, v184, v185\n"
    "v_mfma_f32_16x16x32_bf16 v[192:195], a[228:231], a[144:147], v[192:195]\n"
    "PD\n"
    "ds_read_b64_tr_b16 a[250:251], v15 offset:4096\n"
    "v_cvt_pk_bf16_f32 v71, v186, v187\n"
    "v_mfma_f32_16x16x32_bf16 v[196:199], a[228:231], a[148:151], v[196:199]\n"
    "PD\n"
    "ds_read_b64_tr_b16 a[252:253], v15 offset:8192\n"
    "v_min_u32 v68, -2, v68\n"
    "v_mfma_f32_16x16x32_bf16 v[196:199], a[224:227], a[132:135], v[196:199]\n"
    "PD\n"
    "ds_read_b64_tr_b16 a[254:255], v15 offset:12288\n"
    "s_waitcnt lgkmcnt(8)\n"
    "v_mfma_f32_16x16x32_bf16 v[212:215], a[232:235], a[132:135], v[212:215]\n"
    "PD\n"
    "ds_read_b64_tr_b16 a[224:225], v8 offset:16384\n"
    "v_min_u32 v69, -2, v69\n"
    "v_mfma_f32_16x16x32_bf16 v[212:215], a[236:239], a[148:151], v[212:215]\n"
    "PD\n"
    "ds_read_b64_tr_b16 a[226:227], v8 offset:20480\n"
    "v_min_u32 v70, -2, v70\n"
    "v_mfma_f32_16x16x32_bf16 v[208:211], a[236:239], a[144:147], v[208:211]\n"
    "PD\n"
    "ds_read_b64_tr_b16 a[228:229], v8 offset:24576\n"
    "v_min_u32 v71, -2, v71\n"
    "v_mfma_f32_16x16x32_bf16 v[208:211], a[232:235], a[128:131], v[208:211]\n"
    "PD\n"
    "ds_read_b64_tr_b16 a[230:231], v8 offset:28672\n"
    "s_waitcnt lgkmcnt(8)\n"
    "v_mfma_f32_16x16x32_bf16 v[224:227], a[240:243], a[128:131], v[224:227]\n"
    "PD\n"
    "buffer_store_dwordx4 v[68:71], v4, s[52:55], s0 offen offset:1024 sc1\n"
    "ds_read_b64_tr_b16 a[232:233], v9 offset:16384\n"
    "v_mfma_f32_16x16x32_bf16 v[224:227], a[244:247], a[144:147], v[224:227]\n"
    "PD\n"
    "ds_read_b64_tr_b16 a[234:235], v9 offset:20480\n"
    "v_cvt_pk_bf16_f32 v84, v172, v173\n"
    "v_mfma_f32_16x16x32_bf16 v[228:231], a[244:247], a[148:151], v[228:231]\n"
    "PD\n"
    "ds_read_b64_tr_b16 a[236:237], v9 offset:24576\n"
    "v_cvt_pk_bf16_f32 v85, v174, v175\n"
    "v_mfma_f32_16x16x32_bf16 v[228:231], a[240:243], a[132:135], v[228:231]\n"
    "PD\n"
    "ds_read_b64_tr_b16 a[238:239], v9 offset:28672\n"
    "s_waitcnt lgkmcnt(8)\n"
    "v_mfma_f32_16x16x32_bf16 v[244:247], a[248:251], a[132:135], v[244:247]\n"
    "PD\n"
    "ds_read_b64_tr_b16 a[240:241], v10 offset:16384\n"
    "v_cvt_pk_bf16_f32 v86, v188, v189\n"
    "v_mfma_f32_16x16x32_bf16 v[244:247], a[252:255], a[148:151], v[244:247]\n"
    "PD\n"
    "ds_read_b64_tr_b16 a[242:243], v10 offset:20480\n"
    "v_cvt_pk_bf16_f32 v87, v190, v191\n"
    "v_mfma_f32_16x16x32_bf16 v[240:243], a[252:255], a[144:147], v[240:243]\n"
    "PD\n"
    "ds_read_b64_tr_b16 a[244:245], v10 offset:24576\n"
    "v_min_u32 v84, -2, v84\n"
    "v_mfma_f32_16x16x32_bf16 v[240:243], a[248:251], a[128:131], v[240:243]\n"
    "PD\n"
    "ds_read_b64_tr_b16 a[246:247], v10 offset:28672\n"
    "s_waitcnt lgkmcnt(8)\n"
    "v_mfma_f32_16x16x32_bf16 v[128:131], a[224:227], a[160:163], v[128:131]\n"
    "PD\n"
    "ds_read_b64_tr_b16 a[248:249], v11 offset:16384\n"
    "v_min_u32 v85, -2, v85\n"
    "v_mfma_f32_16x16x32_bf16 v[128:131], a[228:231], a[176:179], v[128:131]\n"
    "PD\n"
    "ds_read_b64_tr_b16 a[250:251], v11 offset:20480\n"
    "v_min_u32 v86, -2, v86\n"
    "v_mfma_f32_16x16x32_bf16 v[132:135], a[228:231], a[180:183], v[132:135]\n"
    "PD\n"
    "ds_read_b64_tr_b16 a[252:253], v11 offset:24576\n"
    "v_min_u32 v87, -2, v87\n"
    "v_mfma_f32_16x16x32_bf16 v[132:135], a[224:227], a[164:167], v[132:135]\n"
    "PD\n"
    "ds_read_b64_tr_b16 a[254:255], v11 offset:28672\n"
    "s_waitcnt lgkmcnt(8)\n"
    "v_mfma_f32_16x16x32_bf16 v[148:151], a[232:235], a[164:167], v[148:151]\n"
    "PD\n"
    "buffer_store_dwordx4 v[84:87], v4, s[52:55], s1 offen offset:1024 sc1\n"
    "ds_read_b64_tr_b16 a[224:225], v12 offset:16384\n"
    "v_mfma_f32_16x16x32_bf16 v[148:151], a[236:239], a[180:183], v[148:151]\n"
    "PD\n"
    "ds_read_b64_tr_b16 a[226:227], v12 offset:20480\n"
    "v_cvt_pk_bf16_f32 v72, v200, v201\n"
    "v_mfma_f32_16x16x32_bf16 v[144:147], a[236:239], a[176:179], v[144:147]\n"
    "PD\n"
    "ds_read_b64_tr_b16 a[228:229], v12 offset:24576\n"
    "v_cvt_pk_bf16_f32 v73, v202, v203\n"
    "v_mfma_f32_16x16x32_bf16 v[144:147], a[232:235], a[160:163], v[144:147]\n"
    "PD\n"
    "ds_read_b64_tr_b16 a[230:231], v12 offset:28672\n"
    "s_waitcnt lgkmcnt(8)\n"
    "v_mfma_f32_16x16x32_bf16 v[160:163], a[240:243], a[160:163], v[160:163]\n"
    "PD\n"
    "ds_read_b64_tr_b16 a[232:233], v13 offset:16384\n"
    "v_cvt_pk_bf16_f32 v74, v216, v217\n"
    "v_mfma_f32_16x16x32_bf16 v[160:163], a[244:247], a[176:179], v[160:163]\n"
    "PD\n"
    "ds_read_b64_tr_b16 a[234:235], v13 offset:20480\n"
    "v_cvt_pk_bf16_f32 v75, v218, v219\n"
    "v_mfma_f32_16x16x32_bf16 v[164:167], a[244:247], a[180:183], v[164:167]\n"
    "PD\n"
    "ds_read_b64_tr_b16 a[236:237], v13 offset:24576\n"
    "v_min_u32 v72, -2, v72\n"
    "v_mfma_f32_16x16x32_bf16 v[164:167], a[240:243], a[164:167], v[164:167]\n"
    "PD\n"
    "ds_read_b64_tr_b16 a[238:239], v13 offset:28672\n"
    "s_waitcnt lgkmcnt(8)\n"
    "v_mfma_f32_16x16x32_bf16 v[180:183], a[248:251], a[164:167], v[180:183]\n"
    "PD\n"
    "ds_read_b64_tr_b16 a[240:241], v14 offset:16384\n"
    "v_min_u32 v73, -2, v73\n"
    "v_mfma_f32_16x16x32_bf16 v[180:183], a[252:255], a[180:183], v[180:183]\n"
    "PD\n"
    "ds_read_b64_tr_b16 a[242:243], v14 offset:20480\n"
    "v_min_u32 v74, -2, v74\n"
    "v_mfma_f32_16x16x32_bf16 v[176:179], a[252:255], a[176:179], v[176:179]\n"
    "PD\n"
    "ds_read_b64_tr_b16 a[244:245], v14 offset:24576\n"
    "v_min_u32 v75, -2, v75\n"
    "v_mfma_f32_16x16x32_bf16 v[176:179], a[248:251], a[160:163], v[176:179]\n"
    "PD\n"
    "ds_read_b64_tr_b16 a[246:247], v14 offset:28672\n"
    "s_waitcnt lgkmcnt(8)\n"
    "v_mfma_f32_16x16x32_bf16 v[192:195], a[224:227], a[160:163], v[192:195]\n"
    "PD\n"
    "buffer_store_dwordx4 v[72:75], v4, s[52:55], s0 offen offset:2048 sc1\n"
    "ds_read_b64_tr_b16 a[248:249], v15 offset:16384\n"
    "v_mfma_f32_16x16x32_bf16 v[192:195], a[228:231], a[176:179], v[192:195]\n"
    "PD\n"
    "ds_read_b64_tr_b16 a[250:251], v15 offset:20480\n"
    "v_cvt_pk_bf16_f32 v88, v204, v205\n"
    "v_mfma_f32_16x16x32_bf16 v[196:199], a[228:231], a[180:183], v[196:199]\n"
    "PD\n"
    "ds_read_b64_tr_b16 a[252:253], v15 offset:24576\n"
    "v_cvt_pk_bf16_f32 v89, v206, v207\n"
    "v_mfma_f32_16x16x32_bf16 v[196:199], a[224:227], a[164:167], v[196:199]\n"
    "PD\n"
    "ds_read_b64_tr_b16 a[254:255], v15 offset:28672\n"
    "s_waitcnt lgkmcnt(8)\n"
    "v_mfma_f32_16x16x32_bf16 v[212:215], a[232:235], a[164:167], v[212:215]\n"
    "PD\n"
    "ds_read_b64_tr_b16 a[224:225], v8 offset:32768\n"
    "v_cvt_pk_bf16_f32 v90, v220, v221\n"
    "v_mfma_f32_16x16x32_bf16 v[212:215], a[236:239], a[180:183], v[212:215]\n"
    "PD\n"
    "ds_read_b64_tr_b16 a[226:227], v8 offset:36864\n"
    "v_cvt_pk_bf16_f32 v91, v222, v223\n"
    "v_mfma_f32_16x16x32_bf16 v[208:211], a[236:239], a[176:179], v[208:211]\n"
    "PD\n"
    "ds_read_b64_tr_b16 a[228:229], v8 offset:40960\n"
    "v_min_u32 v88, -2, v88\n"
    "v_mfma_f32_16x16x32_bf16 v[208:211], a[232:235], a[160:163], v[208:211]\n"
    "PD\n"
    "ds_read_b64_tr_b16 a[230:231], v8 offset:45056\n"
    "s_waitcnt lgkmcnt(8)\n"
    "v_mfma_f32_16x16x32_bf16 v[224:227], a[240:243], a[160:163], v[224:227]\n"
    "PD\n"
    "ds_read_b64_tr_b16 a[232:233], v9 offset:32768\n"
    "v_min_u32 v89, -2, v89\n"
    "v_mfma_f32_16x16x32_bf16 v[224:227], a[244:247], a[176:179], v[224:227]\n"
    "PD\n"
    "ds_read_b64_tr_b16 a[234:235], v9 offset:36864\n"
    "v_min_u32 v90, -2, v90\n"
    "v_mfma_f32_16x16x32_bf16 v[228:231], a[244:247], a[180:183], v[228:231]\n"
    "PD\n"
    "ds_read_b64_tr_b16 a[236:237], v9 offset:40960\n"
    "v_min_u32 v91, -2, v91\n"
    "v_mfma_f32_16x16x32_bf16 v[228:231], a[240:243], a[164:167], v[228:231]\n"
    "PD\n"
    "ds_read_b64_tr_b16 a[238:239], v9 offset:45056\n"
    "s_waitcnt lgkmcnt(8)\n"
    "v_mfma_f32_16x16x32_bf16 v[244:247], a[248:251], a[164:167], v[244:247]\n"
    "PD\n"
    "buffer_store_dwordx4 v[88:91], v4, s[52:55], s1 offen offset:2048 sc1\n"
    "ds_read_b64_tr_b16 a[240:241], v10 offset:32768\n"
    "v_mfma_f32_16x16x32_bf16 v[244:247], a[252:255], a[180:183], v[244:247]\n"
    "PD\n"
    "ds_read_b64_tr_b16 a[242:243], v10 offset:36864\n"
    "v_cvt_pk_bf16_f32 v76, v232, v233\n"
    "v_mfma_f32_16x16x32_bf16 v[240:243], a[252:255], a[176:179], v[240:243]\n"
    "PD\n"
    "ds_read_b64_tr_b16 a[244:245], v10 offset:40960\n"
    "v_cvt_pk_bf16_f32 v77, v234, v235\n"
    "v_mfma_f32_16x16x32_bf16 v[240:243], a[248:251], a[160:163], v[240:243]\n"
    "PD\n"
    "ds_read_b64_tr_b16 a[246:247], v10 offset:45056\n"
    "s_waitcnt lgkmcnt(8)\n"
    "v_mfma_f32_16x16x32_bf16 v[128:131], a[224:227], a[192:195], v[128:131]\n"
    "PD\n"
    "ds_read_b64_tr_b16 a[248:249], v11 offset:32768\n"
    "v_cvt_pk_bf16_f32 v78, v248, v249\n"
    "v_mfma_f32_16x16x32_bf16 v[128:131], a[228:231], a[208:211], v[128:131]\n"
    "PD\n"
    "ds_read_b64_tr_b16 a[250:251], v11 offset:36864\n"
    "v_cvt_pk_bf16_f32 v79, v250, v251\n"
    "v_mfma_f32_16x16x32_bf16 v[132:135], a[228:231], a[212:215], v[132:135]\n"
    "PD\n"
    "ds_read_b64_tr_b16 a[252:253], v11 offset:40960\n"
    "v_min_u32 v76, -2, v76\n"
    "v_mfma_f32_16x16x32_bf16 v[132:135], a[224:227], a[196:199], v[132:135]\n"
    "PD\n"
    "ds_read_b64_tr_b16 a[254:255], v11 offset:45056\n"
    "s_waitcnt lgkmcnt(8)\n"
    "v_mfma_f32_16x16x32_bf16 v[148:151], a[232:235], a[196:199], v[148:151]\n"
    "PD\n"
    "ds_read_b64_tr_b16 a[224:225], v12 offset:32768\n"
    "v_min_u32 v77, -2, v77\n"
    "v_mfma_f32_16x16x32_bf16 v[148:151], a[236:239], a[212:215], v[148:151]\n"
    "PD\n"
    "ds_read_b64_tr_b16 a[226:227], v12 offset:36864\n"
    "v_min_u32 v78, -2, v78\n"
    "v_mfma_f32_16x16x32_bf16 v[144:147], a[236:239], a[208:211], v[144:147]\n"
    "PD\n"
    "ds_read_b64_tr_b16 a[228:229], v12 offset:40960\n"
    "v_min_u32 v79, -2, v79\n"
    "v_mfma_f32_16x16x32_bf16 v[144:147], a[232:235], a[192:195], v[144:147]\n"
    "PD\n"
    "ds_read_b64_tr_b16 a[230:231], v12 offset:45056\n"
    "s_waitcnt lgkmcnt(8)\n"
    "v_mfma_f32_16x16x32_bf16 v[160:163], a[240:243], a[192:195], v[160:163]\n"
    "PD\n"
    "buffer_store_dwordx4 v[76:79], v4, s[52:55], s0 offen offset:3072 sc1\n"
    "ds_read_b64_tr_b16 a[232:233], v13 offset:32768\n"
    "v_mfma_f32_16x16x32_bf16 v[160:163], a[244:247], a[208:211], v[160:163]\n"
    "PD\n"
    "ds_read_b64_tr_b16 a[234:235], v13 offset:36864\n"
    "v_cvt_pk_bf16_f32 v92, v236, v237\n"
    "v_mfma_f32_16x16x32_bf16 v[164:167], a[244:247], a[212:215], v[164:167]\n"
    "PD\n"
    "ds_read_b64_tr_b16 a[236:237], v13 offset:40960\n"
    "v_cvt_pk_bf16_f32 v93, v238, v239\n"
    "v_mfma_f32_16x16x32_bf16 v[164:167], a[240:243], a[196:199], v[164:167]\n"
    "PD\n"
    "ds_read_b64_tr_b16 a[238:239], v13 offset:45056\n"
    "s_waitcnt lgkmcnt(8)\n"
    "v_mfma_f32_16x16x32_bf16 v[180:183], a[248:251], a[196:199], v[180:183]\n"
    "PD\n"
    "ds_read_b64_tr_b16 a[240:241], v14 offset:32768\n"
    "v_cvt_pk_bf16_f32 v94, v252, v253\n"
    "v_mfma_f32_16x16x32_bf16 v[180:183], a[252:255], a[212:215], v[180:183]\n"
    "PD\n"
    "ds_read_b64_tr_b16 a[242:243], v14 offset:36864\n"
    "v_cvt_pk_bf16_f32 v95, v254, v255\n"
    "v_mfma_f32_16x16x32_bf16 v[176:179], a[252:255], a[208:211], v[176:179]\n"
    "PD\n"
    "ds_read_b64_tr_b16 a[244:245], v14 offset:40960\n"
    "v_min_u32 v92, -2, v92\n"
    "v_mfma_f32_16x16x32_bf16 v[176:179], a[248:251], a[192:195], v[176:179]\n"
    "PD\n"
    "ds_read_b64_tr_b16 a[246:247], v14 offset:45056\n"
    "s_waitcnt lgkmcnt(8)\n"
    "v_mfma_f32_16x16x32_bf16 v[192:195], a[224:227], a[192:195], v[192:195]\n"
    "PD\n"
    "ds_read_b64_tr_b16 a[248:249], v15 offset:32768\n"
    "v_min_u32 v93, -2, v93\n"
    "v_mfma_f32_16x16x32_bf16 v[192:195], a[228:231], a[208:211], v[192:195]\n"
    "PD\n"
    "ds_read_b64_tr_b16 a[250:251], v15 offset:36864\n"
    "v_min_u32 v94, -2, v94\n"
    "v_mfma_f32_16x16x32_bf16 v[196:199], a[228:231], a[212:215], v[196:199]\n"
    "PD\n"
    "ds_read_b64_tr_b16 a[252:253], v15 offset:40960\n"
    "v_min_u32 v95, -2, v95\n"
    "v_mfma_f32_16x16x32_bf16 v[196:199], a[224:227], a[196:199], v[196:199]\n"
    "PD\n"
    "ds_read_b64_tr_b16 a[254:255], v15 offset:45056\n"
    "s_waitcnt lgkmcnt(8)\n"
    "v_mfma_f32_16x16x32_bf16 v[212:215], a[232:235], a[196:199], v[212:215]\n"
    "PD\n"
    "buffer_store_dwordx4 v[92:95], v4, s[52:55], s1 offen offset:3072 sc1\n"
    "ds_read_b64_tr_b16 a[224:225], v8 offset:49152\n"
    "v_mfma_f32_16x16x32_bf16 v[212:215], a[236:239], a[212:215], v[212:215]\n"
    "PD\n"
    "ds_read_b64_tr_b16 a[226:227], v8 offset:53248\n"
    "s_waitcnt vmcnt(11)\n"
    "v_permlane16_swap_b32 v40, v42\n"
    "v_mfma_f32_16x16x32_bf16 v[208:211], a[236:239], a[208:211], v[208:211]\n"
    "PD\n"
    "ds_read_b64_tr_b16 a[228:229], v8 offset:57344\n"
    "v_permlane16_swap_b32 v41, v43\n"
    "v_mfma_f32_16x16x32_bf16 v[208:211], a[232:235], a[192:195], v[208:211]\n"
    "PD\n"
    "ds_read_b64_tr_b16 a[230:231], v8 offset:61440\n"
    "s_waitcnt lgkmcnt(8)\n"
    "v_mfma_f32_16x16x32_bf16 v[224:227], a[240:243], a[192:195], v[224:227]\n"
    "PD\n"
    "ds_read_b64_tr_b16 a[232:233], v9 offset:49152\n"
    "s_waitcnt vmcnt(10)\n"
    "v_permlane16_swap_b32 v44, v46\n"
    "v_mfma_f32_16x16x32_bf16 v[224:227], a[244:247], a[208:211], v[224:227]\n"
    "PD\n"
    "ds_read_b64_tr_b16 a[234:235], v9 offset:53248\n"
    "v_xor_b32 v8, 0x10000, v8\n"
    "v_mfma_f32_16x16x32_bf16 v[228:231], a[244:247], a[212:215], v[228:231]\n"
    "PD\n"
    "ds_read_b64_tr_b16 a[236:237], v9 offset:57344\n"
    "v_permlane16_swap_b32 v45, v47\n"
    "v_mfma_f32_16x16x32_bf16 v[228:231], a[240:243], a[196:199], v[228:231]\n"
    "PD\n"
    "ds_read_b64_tr_b16 a[238:239], v9 offset:61440\n"
    "s_waitcnt lgkmcnt(8)\n"
    "v_mfma_f32_16x16x32_bf16 v[244:247], a[248:251], a[196:199], v[244:247]\n"
    "PD\n"
    "ds_read_b64_tr_b16 a[240:241], v10 offset:49152\n"
    "s_waitcnt vmcnt(9)\n"
    "v_permlane16_swap_b32 v48, v50\n"
    "v_mfma_f32_16x16x32_bf16 v[244:247], a[252:255], a[212:215], v[244:247]\n"
    "PD\n"
    "ds_read_b64_tr_b16 a[242:243], v10 offset:53248\n"
    "v_xor_b32 v9, 0x10000, v9\n"
    "v_mfma_f32_16x16x32_bf16 v[240:243], a[252:255], a[208:211], v[240:243]\n"
    "PD\n"
    "ds_read_b64_tr_b16 a[244:245], v10 offset:57344\n"
    "v_permlane16_swap_b32 v49, v51\n"
    "v_mfma_f32_16x16x32_bf16 v[240:243], a[248:251], a[192:195], v[240:243]\n"
    "PD\n"
    "ds_read_b64_tr_b16 a[246:247], v10 offset:61440\n"
    "s_waitcnt lgkmcnt(8)\n"
    "v_mfma_f32_16x16x32_bf16 v[128:131], a[224:227], a[96:99], v[128:131]\n"
    "PD\n"
    "ds_read_b64_tr_b16 a[248:249], v11 offset:49152\n"
    "s_waitcnt vmcnt(8)\n"
    "v_permlane16_swap_b32 v52, v54\n"
    "v_mfma_f32_16x16x32_bf16 v[128:131], a[228:231], a[112:115], v[128:131]\n"
    "PD\n"
    "ds_read_b64_tr_b16 a[250:251], v11 offset:53248\n"
    "v_xor_b32 v10, 0x10000, v10\n"
    "v_mfma_f32_16x16x32_bf16 v[132:135], a[228:231], a[116:119], v[132:135]\n"
    "PD\n"
    "ds_read_b64_tr_b16 a[252:253], v11 offset:57344\n"
    "v_permlane16_swap_b32 v53, v55\n"
    "v_mfma_f32_16x16x32_bf16 v[132:135], a[224:227], a[100:103], v[132:135]\n"
    "PD\n"
    "ds_read_b64_tr_b16 a[254:255], v11 offset:61440\n"
    "s_waitcnt lgkmcnt(8)\n"
    "v_mfma_f32_16x16x32_bf16 v[148:151], a[232:235], a[100:103], v[148:151]\n"
    "PD\n"
    "ds_read_b64_tr_b16 a[224:225], v12 offset:49152\n"
    "v_lshlrev_b32 v140, 16, v40\n"
    "v_mfma_f32_16x16x32_bf16 v[148:151], a[236:239], a[116:119], v[148:151]\n"
    "PD\n"
    "ds_read_b64_tr_b16 a[226:227], v12 offset:53248\n"
    "v_xor_b32 v11, 0x10000, v11\n"
    "v_mfma_f32_16x16x32_bf16 v[144:147], a[236:239], a[112:115], v[144:147]\n"
    "PD\n"
    "ds_read_b64_tr_b16 a[228:229], v12 offset:57344\n"
    "v_and_b32 v141, s68, v40\n"
    "v_mfma_f32_16x16x32_bf16 v[144:147], a[232:235], a[96:99], v[144:147]\n"
    "PD\n"
    "ds_read_b64_tr_b16 a[230:231], v12 offset:61440\n"
    "s_waitcnt lgkmcnt(8)\n"
    "v_mfma_f32_16x16x32_bf16 v[160:163], a[240:243], a[96:99], v[160:163]\n"
    "PD\n"
    "ds_read_b64_tr_b16 a[232:233], v13 offset:49152\n"
    "v_lshlrev_b32 v142, 16, v41\n"
    "v_mfma_f32_16x16x32_bf16 v[160:163], a[244:247], a[112:115], v[160:163]\n"
    "PD\n"
    "ds_read_b64_tr_b16 a[234:235], v13 offset:53248\n"
    "v_xor_b32 v12, 0x10000, v12\n"
    "v_mfma_f32_16x16x32_bf16 v[164:167], a[244:247], a[116:119], v[164:167]\n"
    "PD\n"
    "ds_read_b64_tr_b16 a[236:237], v13 offset:57344\n"
    "v_and_b32 v143, s68, v41\n"
    "v_mfma_f32_16x16x32_bf16 v[164:167], a[240:243], a[100:103], v[164:167]\n"
    "PD\n"
    "ds_read_b64_tr_b16 a[238:239], v13 offset:61440\n"
    "s_waitcnt lgkmcnt(8)\n"
    "v_mfma_f32_16x16x32_bf16 v[180:183], a[248:251], a[100:103], v[180:183]\n"
    "PD\n"
    "ds_read_b64_tr_b16 a[240:241], v14 offset:49152\n"
    "v_lshlrev_b32 v156, 16, v42\n"
    "v_mfma_f32_16x16x32_bf16 v[180:183], a[252:255], a[116:119], v[180:183]\n"
    "PD\n"
    "ds_read_b64_tr_b16 a[242:243], v14 offset:53248\n"
    "v_xor_b32 v13, 0x10000, v13\n"
    "v_mfma_f32_16x16x32_bf16 v[176:179], a[252:255], a[112:115], v[176:179]\n"
    "PD\n"
    "ds_read_b64_tr_b16 a[244:245], v14 offset:57344\n"
    "v_and_b32 v157, s68, v42\n"
    "v_mfma_f32_16x16x32_bf16 v[176:179], a[248:251], a[96:99], v[176:179]\n"
    "PD\n"
    "ds_read_b64_tr_b16 a[246:247], v14 offset:61440\n"
    "s_waitcnt lgkmcnt(8)\n"
    "v_mfma_f32_16x16x32_bf16 v[192:195], a[224:227], a[96:99], v[192:195]\n"
    "PD\n"
    "ds_read_b64_tr_b16 a[248:249], v15 offset:49152\n"
    "v_lshlrev_b32 v158, 16, v43\n"
    "v_mfma_f32_16x16x32_bf16 v[192:195], a[228:231], a[112:115], v[192:195]\n"
    "PD\n"
    "ds_read_b64_tr_b16 a[250:251], v15 offset:53248\n"
    "v_xor_b32 v14, 0x10000, v14\n"
    "v_mfma_f32_16x16x32_bf16 v[196:199], a[228:231], a[116:119], v[196:199]\n"
    "PD\n"
    "ds_read_b64_tr_b16 a[252:253], v15 offset:57344\n"
    "v_and_b32 v159, s68, v43\n"
    "v_mfma_f32_16x16x32_bf16 v[196:199], a[224:227], a[100:103], v[196:199]\n"
    "PD\n"
    "ds_read_b64_tr_b16 a[254:255], v15 offset:61440\n"
    "s_waitcnt lgkmcnt(8)\n"
    "v_mfma_f32_16x16x32_bf16 v[212:215], a[232:235], a[100:103], v[212:215]\n"
    "PD\n"
    "ds_read_b64_tr_b16 a[224:225], v8 offset:0\n"
    "v_lshlrev_b32 v172, 16, v44\n"
    "v_mfma_f32_16x16x32_bf16 v[212:215], a[236:239], a[116:119], v[212:215]\n"
    "PD\n"
    "ds_read_b64_tr_b16 a[226:227], v8 offset:4096\n"
    "v_xor_b32 v15, 0x10000, v15\n"
    "v_mfma_f32_16x16x32_bf16 v[208:211], a[236:239], a[112:115], v[208:211]\n"
    "PD\n"
    "ds_read_b64_tr_b16 a[228:229], v8 offset:8192\n"
    "v_and_b32 v173, s68, v44\n"
    "v_mfma_f32_16x16x32_bf16 v[208:211], a[232:235], a[96:99], v[208:211]\n"
    "PD\n"
    "ds_read_b64_tr_b16 a[230:231], v8 offset:12288\n"
    "s_waitcnt lgkmcnt(8)\n"
    "v_mfma_f32_16x16x32_bf16 v[224:227], a[240:243], a[96:99], v[224:227]\n"
    "PD\n"
    "ds_read_b64_tr_b16 a[232:233], v9 offset:0\n"
    "v_lshlrev_b32 v174, 16, v45\n"
    "v_mfma_f32_16x16x32_bf16 v[224:227], a[244:247], a[112:115], v[224:227]\n"
    "PD\n"
    "ds_read_b64_tr_b16 a[234:235], v9 offset:4096\n"
    "v_and_b32 v175, s68, v45\n"
    "v_mfma_f32_16x16x32_bf16 v[228:231], a[244:247], a[116:119], v[228:231]\n"
    "PD\n"
    "ds_read_b64_tr_b16 a[236:237], v9 offset:8192\n"
    "v_lshlrev_b32 v188, 16, v46\n"
    "v_mfma_f32_16x16x32_bf16 v[228:231], a[240:243], a[100:103], v[228:231]\n"
    "PD\n"
    "ds_read_b64_tr_b16 a[238:239], v9 offset:12288\n"
    "s_waitcnt lgkmcnt(8)\n"
    "v_mfma_f32_16x16x32_bf16 v[244:247], a[248:251], a[100:103], v[244:247]\n"
    "PD\n"
    "ds_read_b64_tr_b16 a[240:241], v10 offset:0\n"
    "v_and_b32 v189, s68, v46\n"
    "v_mfma_f32_16x16x32_bf16 v[244:247], a[252:255], a[116:119], v[244:247]\n"
    "PD\n"
    "ds_read_b64_tr_b16 a[242:243], v10 offset:4096\n"
    "v_lshlrev_b32 v190, 16, v47\n"
    "v_mfma_f32_16x16x32_bf16 v[240:243], a[252:255], a[112:115], v[240:243]\n"
    "PD\n"
    "ds_read_b64_tr_b16 a[244:245], v10 offset:8192\n"
    "v_and_b32 v191, s68, v47\n"
    "v_mfma_f32_16x16x32_bf16 v[240:243], a[248:251], a[96:99], v[240:243]\n"
    "PD\n"
    "ds_read_b64_tr_b16 a[246:247], v10 offset:12288\n"
    "s_waitcnt lgkmcnt(8)\n"
    "v_mfma_f32_16x16x32_bf16 v[128:131], a[224:227], a[0:3], v[128:131]\n"
    "PD\n"
    "ds_read_b64_tr_b16 a[248:249], v11 offset:0\n"
    "v_lshlrev_b32 v204, 16, v48\n"
    "v_mfma_f32_16x16x32_bf16 v[128:131], a[228:231], a[16:19], v[128:131]\n"
    "PD\n"
    "ds_read_b64_tr_b16 a[250:251], v11 offset:4096\n"
    "v_and_b32 v205, s68, v48\n"
    "v_mfma_f32_16x16x32_bf16 v[132:135], a[228:231], a[20:23], v[132:135]\n"
    "PD\n"
    "ds_read_b64_tr_b16 a[252:253], v11 offset:8192\n"
    "v_lshlrev_b32 v206, 16, v49\n"
    "v_mfma_f32_16x16x32_bf16 v[132:135], a[224:227], a[4:7], v[132:135]\n"
    "PD\n"
    "ds_read_b64_tr_b16 a[254:255], v11 offset:12288\n"
    "s_waitcnt lgkmcnt(8)\n"
    "v_mfma_f32_16x16x32_bf16 v[148:151], a[232:235], a[4:7], v[148:151]\n"
    "PD\n"
    "ds_read_b64_tr_b16 a[224:225], v12 offset:0\n"
    "v_and_b32 v207, s68, v49\n"
    "v_mfma_f32_16x16x32_bf16 v[148:151], a[236:239], a[20:23], v[148:151]\n"
    "PD\n"
    "ds_read_b64_tr_b16 a[226:227], v12 offset:4096\n"
    "v_lshlrev_b32 v220, 16, v50\n"
    "v_mfma_f32_16x16x32_bf16 v[144:147], a[236:239], a[16:19], v[144:147]\n"
    "PD\n"
    "ds_read_b64_tr_b16 a[228:229], v12 offset:8192\n"
    "v_and_b32 v221, s68, v50\n"
    "v_mfma_f32_16x16x32_bf16 v[144:147], a[232:235], a[0:3], v[144:147]\n"
    "PD\n"
    "ds_read_b64_tr_b16 a[230:231], v12 offset:12288\n"
    "s_waitcnt lgkmcnt(8)\n"
    "v_mfma_f32_16x16x32_bf16 v[160:163], a[240:243], a[0:3], v[160:163]\n"
    "PD\n"
    "ds_read_b64_tr_b16 a[232:233], v13 offset:0\n"
    "v_lshlrev_b32 v222, 16, v51\n"
    "v_mfma_f32_16x16x32_bf16 v[160:163], a[244:247], a[16:19], v[160:163]\n"
    "PD\n"
    "ds_read_b64_tr_b16 a[234:235], v13 offset:4096\n"
    "v_and_b32 v223, s68, v51\n"
    "v_mfma_f32_16x16x32_bf16 v[164:167], a[244:247], a[20:23], v[164:167]\n"
    "PD\n"
    "ds_read_b64_tr_b16 a[236:237], v13 offset:8192\n"
    "v_lshlrev_b32 v236, 16, v52\n"
    "v_mfma_f32_16x16x32_bf16 v[164:167], a[240:243], a[4:7], v[164:167]\n"
    "PD\n"
    "ds_read_b64_tr_b16 a[238:239], v13 offset:12288\n"
    "s_waitcnt lgkmcnt(8)\n"
    "v_mfma_f32_16x16x32_bf16 v[180:183], a[248:251], a[4:7], v[180:183]\n"
    "PD\n"
    "ds_read_b64_tr_b16 a[240:241], v14 offset:0\n"
    "v_and_b32 v237, s68, v52\n"
    "v_mfma_f32_16x16x32_bf16 v[180:183], a[252:255], a[20:23], v[180:183]\n"
    "PD\n"
    "ds_read_b64_tr_b16 a[242:243], v14 offset:4096\n"
    "v_lshlrev_b32 v238, 16, v53\n"
    "v_mfma_f32_16x16x32_bf16 v[176:179], a[252:255], a[16:19], v[176:179]\n"
    "PD\n"
    "ds_read_b64_tr_b16 a[244:245], v14 offset:8192\n"
    "v_and_b32 v239, s68, v53\n"
    "v_mfma_f32_16x16x32_bf16 v[176:179], a[248:251], a[0:3], v[176:179]\n"
    "PD\n"
    "ds_read_b64_tr_b16 a[246:247], v14 offset:12288\n"
    "s_waitcnt lgkmcnt(8)\n"
    "v_mfma_f32_16x16x32_bf16 v[192:195], a[224:227], a[0:3], v[192:195]\n"
    "PD\n"
    "ds_read_b64_tr_b16 a[248:249], v15 offset:0\n"
    "v_lshlrev_b32 v252, 16, v54\n"
    "v_mfma_f32_16x16x32_bf16 v[192:195], a[228:231], a[16:19], v[192:195]\n"
    "PD\n"
    "ds_read_b64_tr_b16 a[250:251], v15 offset:4096\n"
    "v_and_b32 v253, s68, v54\n"
    "v_mfma_f32_16x16x32_bf16 v[196:199], a[228:231], a[20:23], v[196:199]\n"
    "PD\n"
    "ds_read_b64_tr_b16 a[252:253], v15 offset:8192\n"
    "v_lshlrev_b32 v254, 16, v55\n"
    "v_mfma_f32_16x16x32_bf16 v[196:199], a[224:227], a[4:7], v[196:199]\n"
    "PD\n"
    "ds_read_b64_tr_b16 a[254:255], v15 offset:12288\n"
    "s_waitcnt lgkmcnt(8)\n"
    "v_mfma_f32_16x16x32_bf16 v[212:215], a[232:235], a[4:7], v[212:215]\n"
    "PD\n"
    "ds_read_b64_tr_b16 a[224:225], v8 offset:16384\n"
    "v_and_b32 v255, s68, v55\n"
    "v_mfma_f32_16x16x32_bf16 v[212:215], a[236:239], a[20:23], v[212:215]\n"
    "PD\n"
    "buffer_load_dwordx4 v[64:67], v4, s[52:55], s71 offen offset:0 sc1\n"
    "ds_read_b64_tr_b16 a[226:227], v8 offset:20480\n"
    "v_mfma_f32_16x16x32_bf16 v[208:211], a[236:239], a[16:19], v[208:211]\n"
    "PD\n"
    "ds_read_b64_tr_b16 a[228:229], v8 offset:24576\n"
    "v_mfma_f32_16x16x32_bf16 v[208:211], a[232:235], a[0:3], v[208:211]\n"
    "PD\n"
    "ds_read_b64_tr_b16 a[230:231], v8 offset:28672\n"
    "s_waitcnt lgkmcnt(8)\n"
    "v_mfma_f32_16x16x32_bf16 v[224:227], a[240:243], a[0:3], v[224:227]\n"
    "PD\n"
    "ds_read_b64_tr_b16 a[232:233], v9 offset:16384\n"
    "v_mfma_f32_16x16x32_bf16 v[224:227], a[244:247], a[16:19], v[224:227]\n"
    "PD\n"
    "buffer_load_dwordx4 v[80:83], v4, s[52:55], s72 offen offset:0 sc1\n"
    "ds_read_b64_tr_b16 a[234:235], v9 offset:20480\n"
    "v_mfma_f32_16x16x32_bf16 v[228:231], a[244:247], a[20:23], v[228:231]\n"
    "PD\n"
    "ds_read_b64_tr_b16 a[236:237], v9 offset:24576\n"
    "v_mfma_f32_16x16x32_bf16 v[228:231], a[240:243], a[4:7], v[228:231]\n"
    "PD\n"
    "ds_read_b64_tr_b16 a[238:239], v9 offset:28672\n"
    "s_waitcnt lgkmcnt(8)\n"
    "v_mfma_f32_16x16x32_bf16 v[244:247], a[248:251], a[4:7], v[244:247]\n"
    "PD\n"
    "ds_read_b64_tr_b16 a[240:241], v10 offset:16384\n"
    "v_mfma_f32_16x16x32_bf16 v[244:247], a[252:255], a[20:23], v[244:247]\n"
    "PD\n"
    "buffer_load_dwordx4 v[68:71], v4, s[52:55], s71 offen offset:1024 sc1\n"
    "ds_read_b64_tr_b16 a[242:243], v10 offset:20480\n"
    "v_mfma_f32_16x16x32_bf16 v[240:243], a[252:255], a[16:19], v[240:243]\n"
    "PD\n"
    "ds_read_b64_tr_b16 a[244:245], v10 offset:24576\n"
    "v_mfma_f32_16x16x32_bf16 v[240:243], a[248:251], a[0:3], v[240:243]\n"
    "PD\n"
    "ds_read_b64_tr_b16 a[246:247], v10 offset:28672\n"
    "s_waitcnt lgkmcnt(8)\n"
    "v_mfma_f32_16x16x32_bf16 v[128:131], a[224:227], a[32:35], v[128:131]\n"
    "PD\n"
    "ds_read_b64_tr_b16 a[248:249], v11 offset:16384\n"
    "v_mfma_f32_16x16x32_bf16 v[128:131], a[228:231], a[48:51], v[128:131]\n"
    "PD\n"
    "buffer_load_dwordx4 v[84:87], v4, s[52:55], s72 offen offset:1024 sc1\n"
    "ds_read_b64_tr_b16 a[250:251], v11 offset:20480\n"
    "v_mfma_f32_16x16x32_bf16 v[132:135], a[228:231], a[52:55], v[132:135]\n"
    "PD\n"
    "ds_read_b64_tr_b16 a[252:253], v11 offset:24576\n"
    "v_mfma_f32_16x16x32_bf16 v[132:135], a[224:227], a[36:39], v[132:135]\n"
    "PD\n"
    "ds_read_b64_tr_b16 a[254:255], v11 offset:28672\n"
    "s_waitcnt lgkmcnt(8)\n"
    "v_mfma_f32_16x16x32_bf16 v[148:151], a[232:235], a[36:39], v[148:151]\n"
    "PD\n"
    "ds_read_b64_tr_b16 a[224:225], v12 offset:16384\n"
    "v_mfma_f32_16x16x32_bf16 v[148:151], a[236:239], a[52:55], v[148:151]\n"
    "PD\n"
    "buffer_load_dwordx4 v[72:75], v4, s[52:55], s71 offen offset:2048 sc1\n"
    "ds_read_b64_tr_b16 a[226:227], v12 offset:20480\n"
    "v_mfma_f32_16x16x32_bf16 v[144:147], a[236:239], a[48:51], v[144:147]\n"
    "PD\n"
    "ds_read_b64_tr_b16 a[228:229], v12 offset:24576\n"
    "v_mfma_f32_16x16x32_bf16 v[144:147], a[232:235], a[32:35], v[144:147]\n"
    "PD\n"
    "ds_read_b64_tr_b16 a[230:231], v12 offset:28672\n"
    "s_waitcnt lgkmcnt(8)\n"
    "v_mfma_f32_16x16x32_bf16 v[160:163], a[240:243], a[32:35], v[160:163]\n"
    "PD\n"
    "ds_read_b64_tr_b16 a[232:233], v13 offset:16384\n"
    "v_mfma_f32_16x16x32_bf16 v[160:163], a[244:247], a[48:51], v[160:163]\n"
    "PD\n"
    "buffer_load_dwordx4 v[88:91], v4, s[52:55], s72 offen offset:2048 sc1\n"
    "ds_read_b64_tr_b16 a[234:235], v13 offset:20480\n"
    "v_mfma_f32_16x16x32_bf16 v[164:167], a[244:247], a[52:55], v[164:167]\n"
    "PD\n"
    "ds_read_b64_tr_b16 a[236:237], v13 offset:24576\n"
    "v_mfma_f32_16x16x32_bf16 v[164:167], a[240:243], a[36:39], v[164:167]\n"
    "PD\n"
    "ds_read_b64_tr_b16 a[238:239], v13 offset:28672\n"
    "s_waitcnt lgkmcnt(8)\n"
    "v_mfma_f32_16x16x32_bf16 v[180:183], a[248:251], a[36:39], v[180:183]\n"
    "PD\n"
    "ds_read_b64_tr_b16 a[240:241], v14 offset:16384\n"
    "v_mfma_f32_16x16x32_bf16 v[180:183], a[252:255], a[52:55], v[180:183]\n"
    "PD\n"
    "buffer_load_dwordx4 v[76:79], v4, s[52:55], s71 offen offset:3072 sc1\n"
    "ds_read_b64_tr_b16 a[242:243], v14 offset:20480\n"
    "v_mfma_f32_16x16x32_bf16 v[176:179], a[252:255], a[48:51], v[176:179]\n"
    "PD\n"
    "ds_read_b64_tr_b16 a[244:245], v14 offset:24576\n"
    "v_mfma_f32_16x16x32_bf16 v[176:179], a[248:251], a[32:35], v[176:179]\n"
    "PD\n"
    "ds_read_b64_tr_b16 a[246:247], v14 offset:28672\n"
    "s_waitcnt lgkmcnt(8)\n"
    "v_mfma_f32_16x16x32_bf16 v[192:195], a[224:227], a[32:35], v[192:195]\n"
    "PD\n"
    "ds_read_b64_tr_b16 a[248:249], v15 offset:16384\n"
    "v_mfma_f32_16x16x32_bf16 v[192:195], a[228:231], a[48:51], v[192:195]\n"
    "PD\n"
    "buffer_load_dwordx4 v[92:95], v4, s[52:55], s72 offen offset:3072 sc1\n"
    "ds_read_b64_tr_b16 a[250:251], v15 offset:20480\n"
    "v_mfma_f32_16x16x32_bf16 v[196:199], a[228:231], a[52:55], v[196:199]\n"
    "PD\n"
    "ds_read_b64_tr_b16 a[252:253], v15 offset:24576\n"
    "v_mfma_f32_16x16x32_bf16 v[196:199], a[224:227], a[36:39], v[196:199]\n"
    "PD\n"
    "ds_read_b64_tr_b16 a[254:255], v15 offset:28672\n"
    "s_waitcnt lgkmcnt(8)\n"
    "v_mfma_f32_16x16x32_bf16 v[212:215], a[232:235], a[36:39], v[212:215]\n"
    "PD\n"
    "ds_read_b64_tr_b16 a[224:225], v8 offset:32768\n"
    "v_mfma_f32_16x16x32_bf16 v[212:215], a[236:239], a[52:55], v[212:215]\n"
    "PD\n"
    "ds_read_b64_tr_b16 a[226:227], v8 offset:36864\n"
    "v_mfma_f32_16x16x32_bf16 v[208:211], a[236:239], a[48:51], v[208:211]\n"
    "PD\n"
    "ds_read_b64_tr_b16 a[228:229], v8 offset:40960\n"
    "v_mfma_f32_16x16x32_bf16 v[208:211], a[232:235], a[32:35], v[208:211]\n"
    "PD\n"
    "ds_read_b64_tr_b16 a[230:231], v8 offset:45056\n"
    "s_waitcnt lgkmcnt(8)\n"
    "v_mfma_f32_16x16x32_bf16 v[224:227], a[240:243], a[32:35], v[224:227]\n"
    "PD\n"
    "ds_read_b64_tr_b16 a[232:233], v9 offset:32768\n"
    "v_mfma_f32_16x16x32_bf16 v[224:227], a[244:247], a[48:51], v[224:227]\n"
    "PD\n"
    "ds_read_b64_tr_b16 a[234:235], v9 offset:36864\n"
    "v_mfma_f32_16x16x32_bf16 v[228:231], a[244:247], a[52:55], v[228:231]\n"
    "PD\n"
    "ds_read_b64_tr_b16 a[236:237], v9 offset:40960\n"
    "v_mfma_f32_16x16x32_bf16 v[228:231], a[240:243], a[36:39], v[228:231]\n"
    "PD\n"
    "ds_read_b64_tr_b16 a[238:239], v9 offset:45056\n"
    "s_waitcnt lgkmcnt(8)\n"
    "v_mfma_f32_16x16x32_bf16 v[244:247], a[248:251], a[36:39], v[244:247]\n"
    "PD\n"
    "ds_read_b64_tr_b16 a[240:241], v10 offset:32768\n"
    "v_mfma_f32_16x16x32_bf16 v[244:247], a[252:255], a[52:55], v[244:247]\n"
    "PD\n"
    "ds_read_b64_tr_b16 a[242:243], v10 offset:36864\n"
    "v_mfma_f32_16x16x32_bf16 v[240:243], a[252:255], a[48:51], v[240:243]\n"
    "PD\n"
    "ds_read_b64_tr_b16 a[244:245], v10 offset:40960\n"
    "v_mfma_f32_16x16x32_bf16 v[240:243], a[248:251], a[32:35], v[240:243]\n"
    "PD\n"
    "ds_read_b64_tr_b16 a[246:247], v10 offset:45056\n"
    "s_waitcnt lgkmcnt(8)\n"
    "v_mfma_f32_16x16x32_bf16 v[128:131], a[224:227], a[64:67], v[128:131]\n"
    "PD\n"
    "ds_read_b64_tr_b16 a[248:249], v11 offset:32768\n"
    "v_mfma_f32_16x16x32_bf16 v[128:131], a[228:231], a[80:83], v[128:131]\n"
    "PD\n"
    "ds_read_b64_tr_b16 a[250:251], v11 offset:36864\n"
    "v_mfma_f32_16x16x32_bf16 v[132:135], a[228:231], a[84:87], v[132:135]\n"
    "PD\n"
    "ds_read_b64_tr_b16 a[252:253], v11 offset:40960\n"
    "v_mfma_f32_16x16x32_bf16 v[132:135], a[224:227], a[68:71], v[132:135]\n"
    "PD\n"
    "ds_read_b64_tr_b16 a[254:255], v11 offset:45056\n"
    "s_waitcnt lgkmcnt(8)\n"
    "v_mfma_f32_16x16x32_bf16 v[148:151], a[232:235], a[68:71], v[148:151]\n"
    "PD\n"
    "ds_read_b64_tr_b16 a[224:225], v12 offset:32768\n"
    "v_mfma_f32_16x16x32_bf16 v[148:151], a[236:239], a[84:87], v[148:151]\n"
    "PD\n"
    "ds_read_b64_tr_b16 a[226:227], v12 offset:36864\n"
    "v_mfma_f32_16x16x32_bf16 v[144:147], a[236:239], a[80:83], v[144:147]\n"
    "PD\n"
    "ds_read_b64_tr_b16 a[228:229], v12 offset:40960\n"
    "v_mfma_f32_16x16x32_bf16 v[144:147], a[232:235], a[64:67], v[144:147]\n"
    "PD\n"
    "ds_read_b64_tr_b16 a[230:231], v12 offset:45056\n"
    "s_waitcnt lgkmcnt(8)\n"
    "v_mfma_f32_16x16x32_bf16 v[160:163], a[240:243], a[64:67], v[160:163]\n"
    "PD\n"
    "ds_read_b64_tr_b16 a[232:233], v13 offset:32768\n"
    "v_mfma_f32_16x16x32_bf16 v[160:163], a[244:247], a[80:83], v[160:163]\n"
    "PD\n"
    "ds_read_b64_tr_b16 a[234:235], v13 offset:36864\n"
    "v_mfma_f32_16x16x32_bf16 v[164:167], a[244:247], a[84:87], v[164:167]\n"
    "PD\n"
    "ds_read_b64_tr_b16 a[236:237], v13 offset:40960\n"
    "v_mfma_f32_16x16x32_bf16 v[164:167], a[240:243], a[68:71], v[164:167]\n"
    "PD\n"
    "ds_read_b64_tr_b16 a[238:239], v13 offset:45056\n"
    "s_waitcnt lgkmcnt(8)\n"
    "v_mfma_f32_16x16x32_bf16 v[180:183], a[248:251], a[68:71], v[180:183]\n"
    "PD\n"
    "ds_read_b64_tr_b16 a[240:241], v14 offset:32768\n"
    "v_mfma_f32_16x16x32_bf16 v[180:183], a[252:255], a[84:87], v[180:183]\n"
    "PD\n"
    "ds_read_b64_tr_b16 a[242:243], v14 offset:36864\n"
    "v_cvt_pk_bf16_f32 v96, v132, v133\n"
    "v_mfma_f32_16x16x32_bf16 v[176:179], a[252:255], a[80:83], v[176:179]\n"
    "PD\n"
    "ds_read_b64_tr_b16 a[244:245], v14 offset:40960\n"
    "v_cvt_pk_bf16_f32 v97, v134, v135\n"
    "v_mfma_f32_16x16x32_bf16 v[176:179], a[248:251], a[64:67], v[176:179]\n"
    "PD\n"
    "ds_read_b64_tr_b16 a[246:247], v14 offset:45056\n"
    "s_waitcnt lgkmcnt(8)\n"
    "v_mfma_f32_16x16x32_bf16 v[192:195], a[224:227], a[64:67], v[192:195]\n"
    "PD\n"
    "ds_read_b64_tr_b16 a[248:249], v15 offset:32768\n"
    "v_cvt_pk_bf16_f32 v98, v148, v149\n"
    "v_mfma_f32_16x16x32_bf16 v[192:195], a[228:231], a[80:83], v[192:195]\n"
    "PD\n"
    "ds_read_b64_tr_b16 a[250:251], v15 offset:36864\n"
    "v_cvt_pk_bf16_f32 v99, v150, v151\n"
    "v_mfma_f32_16x16x32_bf16 v[196:199], a[228:231], a[84:87], v[196:199]\n"
    "PD\n"
    "ds_read_b64_tr_b16 a[252:253], v15 offset:40960\n"
    "v_min_u32 v96, -2, v96\n"
    "v_mfma_f32_16x16x32_bf16 v[196:199], a[224:227], a[68:71], v[196:199]\n"
    "PD\n"
    "ds_read_b64_tr_b16 a[254:255], v15 offset:45056\n"
    "s_waitcnt lgkmcnt(8)\n"
    "v_mfma_f32_16x16x32_bf16 v[212:215], a[232:235], a[68:71], v[212:215]\n"
    "PD\n"
    "v_min_u32 v97, -2, v97\n"
    "v_min_u32 v98, -2, v98\n"
    "v_mfma_f32_16x16x32_bf16 v[212:215], a[236:239], a[84:87], v[212:215]\n"
    "PD\n"
    "v_min_u32 v99, -2, v99\n"
    "v_mfma_f32_16x16x32_bf16 v[208:211], a[236:239], a[80:83], v[208:211]\n"
    "PD\n"
    "buffer_store_dwordx4 v[96:99], v4, s[52:55], s98 offen offset:0 sc1\n"
    "v_cvt_pk_bf16_f32 v100, v164, v165\n"
    "v_cvt_pk_bf16_f32 v101, v166, v167\n"
    "v_mfma_f32_16x16x32_bf16 v[208:211], a[232:235], a[64:67], v[208:211]\n"
    "PD\n"
    "s_waitcnt lgkmcnt(4)\n"
    "v_cvt_pk_bf16_f32 v102, v180, v181\n"
    "v_cvt_pk_bf16_f32 v103, v182, v183\n"
    "v_mfma_f32_16x16x32_bf16 v[224:227], a[240:243], a[64:67], v[224:227]\n"
    "PD\n"
    "v_min_u32 v100, -2, v100\n"
    "v_min_u32 v101, -2, v101\n"
    "v_mfma_f32_16x16x32_bf16 v[224:227], a[244:247], a[80:83], v[224:227]\n"
    "PD\n"
    "v_min_u32 v102, -2, v102\n"
    "v_min_u32 v103, -2, v103\n"
    "v_mfma_f32_16x16x32_bf16 v[228:231], a[244:247], a[84:87], v[228:231]\n"
    "PD\n"
    "buffer_store_dwordx4 v[100:103], v4, s[52:55], s98 offen offset:1024 sc1\n"
    "v_mfma_f32_16x16x32_bf16 v[228:231], a[240:243], a[68:71], v[228:231]\n"
    "PD\n"
    "s_waitcnt lgkmcnt(0)\n"
    "v_mfma_f32_16x16x32_bf16 v[244:247], a[248:251], a[68:71], v[244:247]\n"
    "PD\n"
    "v_mfma_f32_16x16x32_bf16 v[244:247], a[252:255], a[84:87], v[244:247]\n"
    "PD\n"
    "v_cvt_pk_bf16_f32 v104, v196, v197\n"
    "v_cvt_pk_bf16_f32 v105, v198, v199\n"
    "v_mfma_f32_16x16x32_bf16 v[240:243], a[252:255], a[80:83], v[240:243]\n"
    "PD\n"
    "v_cvt_pk_bf16_f32 v106, v212, v213\n"
    "v_cvt_pk_bf16_f32 v107, v214, v215\n"
    "v_mfma_f32_16x16x32_bf16 v[240:243], a[248:251], a[64:67], v[240:243]\n"
    "PD\n"
    "v_min_u32 v104, -2, v104\n"
    "v_min_u32 v105, -2, v105\n"
    "v_mfma_f32_16x16x32_bf16 v[128:131], a[200:203], a[168:171], v[128:131]\n"
    "PD\n"
    "v_min_u32 v106, -2, v106\n"
    "v_min_u32 v107, -2, v107\n"
    "v_mfma_f32_16x16x32_bf16 v[128:131], a[204:207], a[184:187], v[128:131]\n"
    "PD\n"
    "buffer_store_dwordx4 v[104:107], v4, s[52:55], s98 offen offset:2048 sc1\n"
    "v_mfma_f32_16x16x32_bf16 v[144:147], a[220:223], a[184:187], v[144:147]\n"
    "PD\n"
    "v_mfma_f32_16x16x32_bf16 v[144:147], a[216:219], a[168:171], v[144:147]\n"
    "PD\n"
    "v_mfma_f32_16x16x32_bf16 v[160:163], a[104:107], a[168:171], v[160:163]\n"
    "PD\n"
    "v_mfma_f32_16x16x32_bf16 v[160:163], a[108:111], a[184:187], v[160:163]\n"
    "PD\n"
    "v_cvt_pk_bf16_f32 v108, v228, v229\n"
    "v_cvt_pk_bf16_f32 v109, v230, v231\n"
    "v_mfma_f32_16x16x32_bf16 v[176:179], a[124:127], a[184:187], v[176:179]\n"
    "PD\n"
    "v_cvt_pk_bf16_f32 v110, v244, v245\n"
    "v_cvt_pk_bf16_f32 v111, v246, v247\n"
    "v_mfma_f32_16x16x32_bf16 v[176:179], a[120:123], a[168:171], v[176:179]\n"
    "PD\n"
    "v_min_u32 v108, -2, v108\n"
    "v_min_u32 v109, -2, v109\n"
    "v_mfma_f32_16x16x32_bf16 v[192:195], a[8:11], a[168:171], v[192:195]\n"
    "PD\n"
    "v_min_u32 v110, -2, v110\n"
    "v_min_u32 v111, -2, v111\n"
    "v_mfma_f32_16x16x32_bf16 v[192:195], a[12:15], a[184:187], v[192:195]\n"
    "PD\n"
    "buffer_store_dwordx4 v[108:111], v4, s[52:55], s98 offen offset:3072 sc1\n"
    "v_mfma_f32_16x16x32_bf16 v[208:211], a[28:31], a[184:187], v[208:211]\n"
    "PD\n"
    "v_mfma_f32_16x16x32_bf16 v[208:211], a[24:27], a[168:171], v[208:211]\n"
    "PD\n"
    "v_mfma_f32_16x16x32_bf16 v[224:227], a[40:43], a[168:171], v[224:227]\n"
    "PD\n"
    "v_mfma_f32_16x16x32_bf16 v[224:227], a[44:47], a[184:187], v[224:227]\n"
    "PD\n"
    "v_mfma_f32_16x16x32_bf16 v[240:243], a[60:63], a[184:187], v[240:243]\n"
    "PD\n"
    "v_mfma_f32_16x16x32_bf16 v[240:243], a[56:59], a[168:171], v[240:243]\n"
    "PD\n"
    "s_mov_b32 s87, 0x40000\n"
    "s_waitcnt vmcnt(4)\n"
    "L_x1a:\n"
    "v_max3_u32 v56, v64, v65, v66\n"
    "v_max3_u32 v57, v67, v68, v69\n"
    "v_max3_u32 v56, v56, v70, v71\n"
    "v_max3_u32 v57, v57, v72, v73\n"
    "PD\n"
    "v_max3_u32 v56, v56, v74, v75\n"
    "PD\n"
    "v_max3_u32 v57, v57, v76, v77\n"
    "PD\n"
    "v_max3_u32 v56, v56, v78, v79\n"
    "PD\n"
    "v_max3_u32 v57, v57, v80, v81\n"
    "PD\n"
    "v_max3_u32 v56, v56, v82, v83\n"
    "PD\n"
    "v_max3_u32 v57, v57, v84, v85\n"
    "PD\n"
    "v_max3_u32 v56, v56, v86, v87\n"
    "PD\n"
    "v_max3_u32 v57, v57, v88, v89\n"
    "PD\n"
    "v_max3_u32 v56, v56, v90, v91\n"
    "PD\n"
    "v_max3_u32 v57, v57, v92, v93\n"
    "PD\n"
    "v_max3_u32 v56, v56, v94, v95\n"
    "PD\n"
    "v_max_u32 v56, v56, v57\n"
    "PD\n"
    "v_cmp_eq_u32 vcc, -1, v56\n"
    "s_cbranch_vccz L_x1b\n"
    "PD\n"
    "s_sleep 2\n"
    "PD\n"
    "buffer_load_dwordx4 v[64:67], v4, s[52:55], s71 offen offset:0 sc1\n"
    "PD\n"
    "buffer_load_dwordx4 v[80:83], v4, s[52:55], s72 offen offset:0 sc1\n"
    "PD\n"
    "buffer_load_dwordx4 v[68:71], v4, s[52:55], s71 offen offset:1024 sc1\n"
    "PD\n"
    "buffer_load_dwordx4 v[84:87], v4, s[52:55], s72 offen offset:1024 sc1\n"
    "PD\n"
    "buffer_load_dwordx4 v[72:75], v4, s[52:55], s71 offen offset:2048 sc1\n"
    "PD\n"
    "buffer_load_dwordx4 v[88:91], v4, s[52:55], s72 offen offset:2048 sc1\n"
    "PD\n"
    "buffer_load_dwordx4 v[76:79], v4, s[52:55], s71 offen offset:3072 sc1\n"
    "PD\n"
    "buffer_load_dwordx4 v[92:95], v4, s[52:55], s72 offen offset:3072 sc1\n"
    "PD\n"
    "s_sub_u32 s87, s87, 1\n"
    "PD\n"
    "s_cmp_eq_u32 s87, 0\n"
    "PD\n"
    "s_waitcnt vmcnt(0)\n"
    "s_cbranch_scc0 L_x1a\n"
    "PD\n"
    "L_x1b:\n"
    "PD\n"
    "v_lshlrev_b32 v136, 16, v64\n"
    "PD\n"
    "v_and_b32 v137, s68, v64\n"
    "PD\n"
    "v_lshlrev_b32 v138, 16, v65\n"
    "PD\n"
    "v_and_b32 v139, s68, v65\n"
    "PD\n"
    "v_lshlrev_b32 v152, 16, v66\n"
    "PD\n"
    "v_and_b32 v153, s68, v66\n"
    "PD\n"
    "v_lshlrev_b32 v154, 16, v67\n"
    "PD\n"
    "v_and_b32 v155, s68, v67\n"
    "PD\n"
    "v_lshlrev_b32 v132, 16, v80\n"
    "PD\n"
    "v_and_b32 v133, s68, v80\n"
    "PD\n"
    "v_lshlrev_b32 v134, 16, v81\n"
    "PD\n"
    "v_and_b32 v135, s68, v81\n"
    "PD\n"
    "v_lshlrev_b32 v148, 16, v82\n"
    "PD\n"
    "v_and_b32 v149, s68, v82\n"
    "PD\n"
    "v_lshlrev_b32 v150, 16, v83\n"
    "PD\n"
    "v_and_b32 v151, s68, v83\n"
    "PD\n"
    "v_pk_add_f32 v[136:137], v[136:137], v[132:133]\n"
    "PD\n"
    "v_pk_add_f32 v[138:139], v[138:139], v[134:135]\n"
    "PD\n"
    "v_pk_add_f32 v[152:153], v[152:153], v[148:149]\n"
    "PD\n"
    "v_pk_add_f32 v[154:155], v[154:155], v[150:151]\n"
    "PD\n"
    "v_lshlrev_b32 v168, 16, v68\n"
    "PD\n"
    "v_and_b32 v169, s68, v68\n"
    "PD\n"
    "v_lshlrev_b32 v170, 16, v69\n"
    "PD\n"
    "v_and_b32 v171, s68, v69\n"
    "PD\n"
    "v_lshlrev_b32 v184, 16, v70\n"
    "PD\n"
    "v_and_b32 v185, s68, v70\n"
    "PD\n"
    "v_lshlrev_b32 v186, 16, v71\n"
    "PD\n"
    "v_and_b32 v187, s68, v71\n"
    "PD\n"
    "v_lshlrev_b32 v164, 16, v84\n"
    "PD\n"
    "v_and_b32 v165, s68, v84\n"
    "PD\n"
    "v_lshlrev_b32 v166, 16, v85\n"
    "PD\n"
    "v_and_b32 v167, s68, v85\n"
    "PD\n"
    "v_lshlrev_b32 v180, 16, v86\n"
    "PD\n"
    "v_and_b32 v181, s68, v86\n"
    "PD\n"
    "v_lshlrev_b32 v182, 16, v87\n"
    "PD\n"
    "v_and_b32 v183, s68, v87\n"
    "PD\n"
    "v_pk_add_f32 v[168:169], v[168:169], v[164:165]\n"
    "PD\n"
    "v_pk_add_f32 v[170:171], v[170:171], v[166:167]\n"
    "PD\n"
    "v_pk_add_f32 v[184:185], v[184:185], v[180:181]\n"
    "PD\n"
    "v_pk_add_f32 v[186:187], v[186:187], v[182:183]\n"
    "PD\n"
    "v_lshlrev_b32 v200, 16, v72\n"
    "PD\n"
    "v_and_b32 v201, s68, v72\n"
    "PD\n"
    "v_lshlrev_b32 v202, 16, v73\n"
    "PD\n"
    "v_and_b32 v203, s68, v73\n"
    "PD\n"
    "v_lshlrev_b32 v216, 16, v74\n"
    "PD\n"
    "v_and_b32 v217, s68, v74\n"
    "PD\n"
    "v_lshlrev_b32 v218, 16, v75\n"
    "PD\n"
    "v_and_b32 v219, s68, v75\n"
    "PD\n"
    "v_lshlrev_b32 v196, 16, v88\n"
    "PD\n"
    "v_and_b32 v197, s68, v88\n"
    "PD\n"
    "v_lshlrev_b32 v198, 16, v89\n"
    "PD\n"
    "v_and_b32 v199, s68, v89\n"
    "PD\n"
    "v_lshlrev_b32 v212, 16, v90\n"
    "PD\n"
    "v_and_b32 v213, s68, v90\n"
    "PD\n"
    "v_lshlrev_b32 v214, 16, v91\n"
    "PD\n"
    "v_and_b32 v215, s68, v91\n"
    "PD\n"
    "v_pk_add_f32 v[200:201], v[200:201], v[196:197]\n"
    "PD\n"
    "v_pk_add_f32 v[202:203], v[202:203], v[198:199]\n"
    "PD\n"
    "v_pk_add_f32 v[216:217], v[216:217], v[212:213]\n"
    "PD\n"
    "v_pk_add_f32 v[218:219], v[218:219], v[214:215]\n"
    "PD\n"
    "v_lshlrev_b32 v232, 16, v76\n"
    "PD\n"
    "v_and_b32 v233, s68, v76\n"
    "PD\n"
    "v_lshlrev_b32 v234, 16, v77\n"
    "PD\n"
    "v_and_b32 v235, s68, v77\n"
    "PD\n"
    "v_lshlrev_b32 v248, 16, v78\n"
    "PD\n"
    "v_and_b32 v249, s68, v78\n"
    "PD\n"
    "v_lshlrev_b32 v250, 16, v79\n"
    "PD\n"
    "v_and_b32 v251, s68, v79\n"
    "PD\n"
    "v_lshlrev_b32 v228, 16, v92\n"
    "PD\n"
    "v_and_b32 v229, s68, v92\n"
    "PD\n"
    "v_lshlrev_b32 v230, 16, v93\n"
    "PD\n"
    "v_and_b32 v231, s68, v93\n"
    "PD\n"
    "v_lshlrev_b32 v244, 16, v94\n"
    "PD\n"
    "v_and_b32 v245, s68, v94\n"
    "PD\n"
    "v_lshlrev_b32 v246, 16, v95\n"
    "PD\n"
    "v_and_b32 v247, s68, v95\n"
    "PD\n"
    "v_pk_add_f32 v[232:233], v[232:233], v[228:229]\n"
    "PD\n"
    "v_pk_add_f32 v[234:235], v[234:235], v[230:231]\n"
    "PD\n"
    "v_pk_add_f32 v[248:249], v[248:249], v[244:245]\n"
    "PD\n"
    "v_pk_add_f32 v[250:251], v[250:251], v[246:247]\n"
    "PD\n"
    "buffer_load_dwordx4 v[40:43], v4, s[52:55], s70 offen offset:0 sc1\n"
    "PD\n"
    "buffer_load_dwordx4 v[44:47], v4, s[52:55], s70 offen offset:1024 sc1\n"
    "PD\n"
    "buffer_load_dwordx4 v[48:51], v4, s[52:55], s70 offen offset:2048 sc1\n"
    "PD\n"
    "buffer_load_dwordx4 v[52:55], v4, s[52:55], s70 offen offset:3072 sc1\n"
    "PD\n"
    "s_cmp_ge_u32 s29, 2\n"
    "PD\n"
    "s_cbranch_scc1 L_ns1\n"
    "PD\n"
    "v_pk_mul_f32 v[132:133], v[136:137], s[66:67] op_sel_hi:[1,0]\n"
    "PD\n"
    "v_pk_mul_f32 v[134:135], v[138:139], s[66:67] op_sel_hi:[1,0]\n"
    "PD\n"
    "v_pk_mul_f32 v[148:149], v[152:153], s[66:67] op_sel_hi:[1,0]\n"
    "PD\n"
    "v_pk_mul_f32 v[150:151], v[154:155], s[66:67] op_sel_hi:[1,0]\n"
    "PD\n"
    "v_exp_f32 v132, v132\n"
    "PD\n"
    "v_exp_f32 v133, v133\n"
    "PD\n"
    "v_exp_f32 v134, v134\n"
    "PD\n"
    "v_exp_f32 v135, v135\n"
    "PD\n"
    "v_exp_f32 v148, v148\n"
    "PD\n"
    "v_exp_f32 v149, v149\n"
    "PD\n"
    "v_exp_f32 v150, v150\n"
    "PD\n"
    "v_exp_f32 v151, v151\n"
    "PD\n"
    "v_pk_mul_f32 v[164:165], v[168:169], s[66:67] op_sel_hi:[1,0]\n"
    "PD\n"
    "v_pk_mul_f32 v[166:167], v[170:171], s[66:67] op_sel_hi:[1,0]\n"
    "PD\n"
    "v_pk_mul_f32 v[180:181], v[184:185], s[66:67] op_sel_hi:[1,0]\n"
    "PD\n"
    "v_pk_mul_f32 v[182:183], v[186:187], s[66:67] op_sel_hi:[1,0]\n"
    "PD\n"
    "v_exp_f32 v164, v164\n"
    "PD\n"
    "v_exp_f32 v165, v165\n"
    "PD\n"
    "v_exp_f32 v166, v166\n"
    "PD\n"
    "v_exp_f32 v167, v167\n"
    "PD\n"
    "v_exp_f32 v180, v180\n"
    "PD\n"
    "v_exp_f32 v181, v181\n"
    "PD\n"
    "v_exp_f32 v182, v182\n"
    "PD\n"
    "v_exp_f32 v183, v183\n"
    "PD\n"
    "v_pk_mul_f32 v[196:197], v[200:201], s[66:67] op_sel_hi:[1,0]\n"
    "PD\n"
    "v_pk_mul_f32 v[198:199], v[202:203], s[66:67] op_sel_hi:[1,0]\n"
    "PD\n"
    "v_pk_mul_f32 v[212:213], v[216:217], s[66:67] op_sel_hi:[1,0]\n"
    "PD\n"
    "v_pk_mul_f32 v[214:215], v[218:219], s[66:67] op_sel_hi:[1,0]\n"
    "PD\n"
    "v_exp_f32 v196, v196\n"
    "PD\n"
    "v_exp_f32 v197, v197\n"
    "PD\n"
    "v_exp_f32 v198, v198\n"
    "PD\n"
    "v_exp_f32 v199, v199\n"
    "PD\n"
    "v_exp_f32 v212, v212\n"
    "PD\n"
    "v_exp_f32 v213, v213\n"
    "PD\n"
    "v_exp_f32 v214, v214\n"
    "PD\n"
    "v_exp_f32 v215, v215\n"
    "PD\n"
    "v_pk_mul_f32 v[228:229], v[232:233], s[66:67] op_sel_hi:[1,0]\n"
    "PD\n"
    "v_pk_mul_f32 v[230:231], v[234:235], s[66:67] op_sel_hi:[1,0]\n"
    "PD\n"
    "v_pk_mul_f32 v[244:245], v[248:249], s[66:67] op_sel_hi:[1,0]\n"
    "PD\n"
    "v_pk_mul_f32 v[246:247], v[250:251], s[66:67] op_sel_hi:[1,0]\n"
    "PD\n"
    "v_exp_f32 v228, v228\n"
    "PD\n"
    "v_exp_f32 v229, v229\n"
    "PD\n"
    "v_exp_f32 v230, v230\n"
    "PD\n"
    "v_exp_f32 v231, v231\n"
    "PD\n"
    "v_exp_f32 v244, v244\n"
    "PD\n"
    "v_exp_f32 v245, v245\n"
    "PD\n"
    "v_exp_f32 v246, v246\n"
    "PD\n"
    "v_exp_f32 v247, v247\n"
    "PD\n"
    "v_pk_add_f32 v[132:133], v[132:133], 1.0 op_sel_hi:[1,0]\n"
    "PD\n"
    "v_pk_add_f32 v[134:135], v[134:135], 1.0 op_sel_hi:[1,0]\n"
    "PD\n"
    "v_pk_add_f32 v[148:149], v[148:149], 1.0 op_sel_hi:[1,0]\n"
    "PD\n"
    "v_pk_add_f32 v[150:151], v[150:151], 1.0 op_sel_hi:[1,0]\n"
    "PD\n"
    "v_rcp_f32 v132, v132\n"
    "PD\n"
    "v_rcp_f32 v133, v133\n"
    "PD\n"
    "v_rcp_f32 v134, v134\n"
    "PD\n"
    "v_rcp_f32 v135, v135\n"
    "PD\n"
    "v_rcp_f32 v148, v148\n"
    "PD\n"
    "v_rcp_f32 v149, v149\n"
    "PD\n"
    "v_rcp_f32 v150, v150\n"
    "PD\n"
    "v_rcp_f32 v151, v151\n"
    "PD\n"
    "v_pk_add_f32 v[164:165], v[164:165], 1.0 op_sel_hi:[1,0]\n"
    "PD\n"
    "v_pk_add_f32 v[166:167], v[166:167], 1.0 op_sel_hi:[1,0]\n"
    "PD\n"
    "v_pk_add_f32 v[180:181], v[180:181], 1.0 op_sel_hi:[1,0]\n"
    "PD\n"
    "v_pk_add_f32 v[182:183], v[182:183], 1.0 op_sel_hi:[1,0]\n"
    "PD\n"
    "v_rcp_f32 v164, v164\n"
    "PD\n"
    "v_rcp_f32 v165, v165\n"
    "PD\n"
    "v_rcp_f32 v166, v166\n"
    "PD\n"
    "v_rcp_f32 v167, v167\n"
    "PD\n"
    "v_rcp_f32 v180, v180\n"
    "PD\n"
    "v_rcp_f32 v181, v181\n"
    "PD\n"
    "v_rcp_f32 v182, v182\n"
    "PD\n"
    "v_rcp_f32 v183, v183\n"
    "PD\n"
    "v_pk_add_f32 v[196:197], v[196:197], 1.0 op_sel_hi:[1,0]\n"
    "PD\n"
    "v_pk_add_f32 v[198:199], v[198:199], 1.0 op_sel_hi:[1,0]\n"
    "PD\n"
    "v_pk_add_f32 v[212:213], v[212:213], 1.0 op_sel_hi:[1,0]\n"
    "PD\n"
    "v_pk_add_f32 v[214:215], v[214:215], 1.0 op_sel_hi:[1,0]\n"
    "PD\n"
    "v_rcp_f32 v196, v196\n"
    "PD\n"
    "v_rcp_f32 v197, v197\n"
    "PD\n"
    "v_rcp_f32 v198, v198\n"
    "PD\n"
    "v_rcp_f32 v199, v199\n"
    "PD\n"
    "v_rcp_f32 v212, v212\n"
    "PD\n"
    "v_rcp_f32 v213, v213\n"
    "PD\n"
    "v_rcp_f32 v214, v214\n"
    "PD\n"
    "v_rcp_f32 v215, v215\n"
    "PD\n"
    "v_pk_add_f32 v[228:229], v[228:229], 1.0 op_sel_hi:[1,0]\n"
    "PD\n"
    "v_pk_add_f32 v[230:231], v[230:231], 1.0 op_sel_hi:[1,0]\n"
    "PD\n"
    "v_pk_add_f32 v[244:245], v[244:245], 1.0 op_sel_hi:[1,0]\n"
    "PD\n"
    "v_pk_add_f32 v[246:247], v[246:247], 1.0 op_sel_hi:[1,0]\n"
    "PD\n"
    "v_rcp_f32 v228, v228\n"
    "PD\n"
    "v_rcp_f32 v229, v229\n"
    "PD\n"
    "v_rcp_f32 v230, v230\n"
    "PD\n"
    "v_rcp_f32 v231, v231\n"
    "PD\n"
    "v_rcp_f32 v244, v244\n"
    "PD\n"
    "v_rcp_f32 v245, v245\n"
    "PD\n"
    "v_rcp_f32 v246, v246\n"
    "PD\n"
    "v_rcp_f32 v247, v247\n"
    "PD\n"
    "v_pk_mul_f32 v[136:137], v[136:137], v[132:133]\n"
    "PD\n"
    "v_pk_mul_f32 v[138:139], v[138:139], v[134:135]\n"
    "PD\n"
    "v_pk_mul_f32 v[152:153], v[152:153], v[148:149]\n"
    "PD\n"
    "v_pk_mul_f32 v[154:155], v[154:155], v[150:151]\n"
    "PD\n"
    "v_pk_mul_f32 v[168:169], v[168:169], v[164:165]\n"
    "PD\n"
    "v_pk_mul_f32 v[170:171], v[170:171], v[166:167]\n"
    "PD\n"
    "v_pk_mul_f32 v[184:185], v[184:185], v[180:181]\n"
    "PD\n"
    "v_pk_mul_f32 v[186:187], v[186:187], v[182:183]\n"
    "PD\n"
    "v_pk_mul_f32 v[200:201], v[200:201], v[196:197]\n"
    "PD\n"
    "v_pk_mul_f32 v[202:203], v[202:203], v[198:199]\n"
    "PD\n"
    "v_pk_mul_f32 v[216:217], v[216:217], v[212:213]\n"
    "PD\n"
    "v_pk_mul_f32 v[218:219], v[218:219], v[214:215]\n"
    "PD\n"
    "v_pk_mul_f32 v[232:233], v[232:233], v[228:229]\n"
    "PD\n"
    "v_pk_mul_f32 v[234:235], v[234:235], v[230:231]\n"
    "PD\n"
    "v_pk_mul_f32 v[248:249], v[248:249], v[244:245]\n"
    "PD\n"
    "v_pk_mul_f32 v[250:251], v[250:251], v[246:247]\n"
    "PD\n"
    "L_ns1:\n"
    "PD\n"
    "s_mov_b32 s87, 0x40000\n"
    "PD\n"
    "s_waitcnt vmcnt(0)\n"
    "L_x2a:\n"
    "PD\n"
    "v_max3_u32 v56, v40, v41, v42\n"
    "PD\n"
    "v_max3_u32 v57, v43, v44, v45\n"
    "PD\n"
    "v_max3_u32 v56, v56, v46, v47\n"
    "PD\n"
    "v_max3_u32 v57, v57, v48, v49\n"
    "PD\n"
    "v_max3_u32 v56, v56, v50, v51\n"
    "PD\n"
    "v_max3_u32 v57, v57, v52, v53\n"
    "PD\n"
    "v_max3_u32 v56, v56, v54, v55\n"
    "PD\n"
    "v_max_u32 v56, v56, v57\n"
    "PD\n"
    "v_cmp_eq_u32 vcc, -1, v56\n"
    "s_cbranch_vccz L_x2b\n"
    "PD\n"
    "s_sleep 2\n"
    "PD\n"
    "buffer_load_dwordx4 v[40:43], v4, s[52:55], s70 offen offset:0 sc1\n"
    "PD\n"
    "buffer_load_dwordx4 v[44:47], v4, s[52:55], s70 offen offset:1024 sc1\n"
    "PD\n"
    "buffer_load_dwordx4 v[48:51], v4, s[52:55], s70 offen offset:2048 sc1\n"
    "PD\n"
    "buffer_load_dwordx4 v[52:55], v4, s[52:55], s70 offen offset:3072 sc1\n"
    "PD\n"
    "s_sub_u32 s87, s87, 1\n"
    "PD\n"
    "s_cmp_eq_u32 s87, 0\n"
    "PD\n"
    "s_waitcnt vmcnt(0)\n"
    "s_cbranch_scc0 L_x2a\n"
    "PD\n"
    "L_x2b:\n"
    "PD\n"
    "s_cmp_lg_u32 s85, 0\n"
    "PD\n"
    "s_cbranch_scc1 L_nogen\n"
    "PD\n"
    "s_mov_b64 exec, 1\n"
    "buffer_store_dwordx2 v[34:35], off, s[80:83], 0 sc1\n"
    "PD\n"
    "s_mov_b64 exec, -1\n"
    "L_nogen:\n"
    "PD\n"
    "v_lshlrev_b32 v132, 16, v40\n"
    "PD\n"
    "v_and_b32 v133, s68, v40\n"
    "PD\n"
    "v_lshlrev_b32 v134, 16, v41\n"
    "PD\n"
    "v_and_b32 v135, s68, v41\n"
    "PD\n"
    "v_lshlrev_b32 v148, 16, v42\n"
    "PD\n"
    "v_and_b32 v149, s68, v42\n"
    "PD\n"
    "v_lshlrev_b32 v150, 16, v43\n"
    "PD\n"
    "v_and_b32 v151, s68, v43\n"
    "PD\n"
    "v_pk_add_f32 v[128:129], v[128:129], v[132:133]\n"
    "PD\n"
    "v_pk_add_f32 v[130:131], v[130:131], v[134:135]\n"
    "PD\n"
    "v_pk_add_f32 v[144:145], v[144:145], v[148:149]\n"
    "PD\n"
    "v_pk_add_f32 v[146:147], v[146:147], v[150:151]\n"
    "PD\n"
    "v_lshlrev_b32 v164, 16, v44\n"
    "PD\n"
    "v_and_b32 v165, s68, v44\n"
    "PD\n"
    "v_lshlrev_b32 v166, 16, v45\n"
    "PD\n"
    "v_and_b32 v167, s68, v45\n"
    "PD\n"
    "v_lshlrev_b32 v180, 16, v46\n"
    "PD\n"
    "v_and_b32 v181, s68, v46\n"
    "PD\n"
    "v_lshlrev_b32 v182, 16, v47\n"
    "PD\n"
    "v_and_b32 v183, s68, v47\n"
    "PD\n"
    "v_pk_add_f32 v[160:161], v[160:161], v[164:165]\n"
    "PD\n"
    "v_pk_add_f32 v[162:163], v[162:163], v[166:167]\n"
    "PD\n"
    "v_pk_add_f32 v[176:177], v[176:177], v[180:181]\n"
    "PD\n"
    "v_pk_add_f32 v[178:179], v[178:179], v[182:183]\n"
    "PD\n"
    "v_lshlrev_b32 v196, 16, v48\n"
    "PD\n"
    "v_and_b32 v197, s68, v48\n"
    "PD\n"
    "v_lshlrev_b32 v198, 16, v49\n"
    "PD\n"
    "v_and_b32 v199, s68, v49\n"
    "PD\n"
    "v_lshlrev_b32 v212, 16, v50\n"
    "PD\n"
    "v_and_b32 v213, s68, v50\n"
    "PD\n"
    "v_lshlrev_b32 v214, 16, v51\n"
    "PD\n"
    "v_and_b32 v215, s68, v51\n"
    "PD\n"
    "v_pk_add_f32 v[192:193], v[192:193], v[196:197]\n"
    "PD\n"
    "v_pk_add_f32 v[194:195], v[194:195], v[198:199]\n"
    "PD\n"
    "v_pk_add_f32 v[208:209], v[208:209], v[212:213]\n"
    "PD\n"
    "v_pk_add_f32 v[210:211], v[210:211], v[214:215]\n"
    "PD\n"
    "v_lshlrev_b32 v228, 16, v52\n"
    "PD\n"
    "v_and_b32 v229, s68, v52\n"
    "PD\n"
    "v_lshlrev_b32 v230, 16, v53\n"
    "PD\n"
    "v_and_b32 v231, s68, v53\n"
    "PD\n"
    "v_lshlrev_b32 v244, 16, v54\n"
    "PD\n"
    "v_and_b32 v245, s68, v54\n"
    "PD\n"
    "v_lshlrev_b32 v246, 16, v55\n"
    "PD\n"
    "v_and_b32 v247, s68, v55\n"
    "PD\n"
    "v_pk_add_f32 v[224:225], v[224:225], v[228:229]\n"
    "PD\n"
    "v_pk_add_f32 v[226:227], v[226:227], v[230:231]\n"
    "PD\n"
    "v_pk_add_f32 v[240:241], v[240:241], v[244:245]\n"
    "PD\n"
    "v_pk_add_f32 v[242:243], v[242:243], v[246:247]\n"
    "PD\n"
    "s_cmp_lt_u32 s29, 2\n"
    "PD\n"
    "s_cbranch_scc1 L_ns2\n"
    "PD\n"
    "v_pk_mul_f32 v[132:133], v[128:129], s[66:67] op_sel_hi:[1,0]\n"
    "PD\n"
    "v_pk_mul_f32 v[134:135], v[130:131], s[66:67] op_sel_hi:[1,0]\n"
    "PD\n"
    "v_pk_mul_f32 v[148:149], v[144:145], s[66:67] op_sel_hi:[1,0]\n"
    "PD\n"
    "v_pk_mul_f32 v[150:151], v[146:147], s[66:67] op_sel_hi:[1,0]\n"
    "PD\n"
    "v_exp_f32 v132, v132\n"
    "PD\n"
    "v_exp_f32 v133, v133\n"
    "PD\n"
    "v_exp_f32 v134, v134\n"
    "PD\n"
    "v_exp_f32 v135, v135\n"
    "PD\n"
    "v_exp_f32 v148, v148\n"
    "PD\n"
    "v_exp_f32 v149, v149\n"
    "PD\n"
    "v_exp_f32 v150, v150\n"
    "PD\n"
    "v_exp_f32 v151, v151\n"
    "PD\n"
    "v_pk_mul_f32 v[164:165], v[160:161], s[66:67] op_sel_hi:[1,0]\n"
    "PD\n"
    "v_pk_mul_f32 v[166:167], v[162:163], s[66:67] op_sel_hi:[1,0]\n"
    "PD\n"
    "v_pk_mul_f32 v[180:181], v[176:177], s[66:67] op_sel_hi:[1,0]\n"
    "PD\n"
    "v_pk_mul_f32 v[182:183], v[178:179], s[66:67] op_sel_hi:[1,0]\n"
    "PD\n"
    "v_exp_f32 v164, v164\n"
    "PD\n"
    "v_exp_f32 v165, v165\n"
    "PD\n"
    "v_exp_f32 v166, v166\n"
    "PD\n"
    "v_exp_f32 v167, v167\n"
    "PD\n"
    "v_exp_f32 v180, v180\n"
    "PD\n"
    "v_exp_f32 v181, v181\n"
    "PD\n"
    "v_exp_f32 v182, v182\n"
    "PD\n"
    "v_exp_f32 v183, v183\n"
    "PD\n"
    "v_pk_mul_f32 v[196:197], v[192:193], s[66:67] op_sel_hi:[1,0]\n"
    "PD\n"
    "v_pk_mul_f32 v[198:199], v[194:195], s[66:67] op_sel_hi:[1,0]\n"
    "PD\n"
    "v_pk_mul_f32 v[212:213], v[208:209], s[66:67] op_sel_hi:[1,0]\n"
    "PD\n"
    "v_pk_mul_f32 v[214:215], v[210:211], s[66:67] op_sel_hi:[1,0]\n"
    "PD\n"
    "v_exp_f32 v196, v196\n"
    "PD\n"
    "v_exp_f32 v197, v197\n"
    "PD\n"
    "v_exp_f32 v198, v198\n"
    "PD\n"
    "v_exp_f32 v199, v199\n"
    "PD\n"
    "v_exp_f32 v212, v212\n"
    "PD\n"
    "v_exp_f32 v213, v213\n"
    "PD\n"
    "v_exp_f32 v214, v214\n"
    "PD\n"
    "v_exp_f32 v215, v215\n"
    "PD\n"
    "v_pk_mul_f32 v[228:229], v[224:225], s[66:67] op_sel_hi:[1,0]\n"
    "PD\n"
    "v_pk_mul_f32 v[230:231], v[226:227], s[66:67] op_sel_hi:[1,0]\n"
    "PD\n"
    "v_pk_mul_f32 v[244:245], v[240:241], s[66:67] op_sel_hi:[1,0]\n"
    "PD\n"
    "v_pk_mul_f32 v[246:247], v[242:243], s[66:67] op_sel_hi:[1,0]\n"
    "PD\n"
    "v_exp_f32 v228, v228\n"
    "PD\n"
    "v_exp_f32 v229, v229\n"
    "PD\n"
    "v_exp_f32 v230, v230\n"
    "PD\n"
    "v_exp_f32 v231, v231\n"
    "PD\n"
    "v_exp_f32 v244, v244\n"
    "PD\n"
    "v_exp_f32 v245, v245\n"
    "PD\n"
    "v_exp_f32 v246, v246\n"
    "PD\n"
    "v_exp_f32 v247, v247\n"
    "PD\n"
    "v_pk_add_f32 v[132:133], v[132:133], 1.0 op_sel_hi:[1,0]\n"
    "PD\n"
    "v_pk_add_f32 v[134:135], v[134:135], 1.0 op_sel_hi:[1,0]\n"
    "PD\n"
    "v_pk_add_f32 v[148:149], v[148:149], 1.0 op_sel_hi:[1,0]\n"
    "PD\n"
    "v_pk_add_f32 v[150:151], v[150:151], 1.0 op_sel_hi:[1,0]\n"
    "PD\n"
    "v_rcp_f32 v132, v132\n"
    "PD\n"
    "v_rcp_f32 v133, v133\n"
    "PD\n"
    "v_rcp_f32 v134, v134\n"
    "PD\n"
    "v_rcp_f32 v135, v135\n"
    "PD\n"
    "v_rcp_f32 v148, v148\n"
    "PD\n"
    "v_rcp_f32 v149, v149\n"
    "PD\n"
    "v_rcp_f32 v150, v150\n"
    "PD\n"
    "v_rcp_f32 v151, v151\n"
    "PD\n"
    "v_pk_add_f32 v[164:165], v[164:165], 1.0 op_sel_hi:[1,0]\n"
    "PD\n"
    "v_pk_add_f32 v[166:167], v[166:167], 1.0 op_sel_hi:[1,0]\n"
    "PD\n"
    "v_pk_add_f32 v[180:181], v[180:181], 1.0 op_sel_hi:[1,0]\n"
    "PD\n"
    "v_pk_add_f32 v[182:183], v[182:183], 1.0 op_sel_hi:[1,0]\n"
    "PD\n"
    "v_rcp_f32 v164, v164\n"
    "PD\n"
    "v_rcp_f32 v165, v165\n"
    "PD\n"
    "v_rcp_f32 v166, v166\n"
    "PD\n"
    "v_rcp_f32 v167, v167\n"
    "PD\n"
    "v_rcp_f32 v180, v180\n"
    "PD\n"
    "v_rcp_f32 v181, v181\n"
    "PD\n"
    "v_rcp_f32 v182, v182\n"
    "PD\n"
    "v_rcp_f32 v183, v183\n"
    "PD\n"
    "v_pk_add_f32 v[196:197], v[196:197], 1.0 op_sel_hi:[1,0]\n"
    "PD\n"
    "v_pk_add_f32 v[198:199], v[198:199], 1.0 op_sel_hi:[1,0]\n"
    "PD\n"
    "v_pk_add_f32 v[212:213], v[212:213], 1.0 op_sel_hi:[1,0]\n"
    "PD\n"
    "v_pk_add_f32 v[214:215], v[214:215], 1.0 op_sel_hi:[1,0]\n"
    "PD\n"
    "v_rcp_f32 v196, v196\n"
    "PD\n"
    "v_rcp_f32 v197, v197\n"
    "PD\n"
    "v_rcp_f32 v198, v198\n"
    "PD\n"
    "v_rcp_f32 v199, v199\n"
    "PD\n"
    "v_rcp_f32 v212, v212\n"
    "PD\n"
    "v_rcp_f32 v213, v213\n"
    "PD\n"
    "v_rcp_f32 v214, v214\n"
    "PD\n"
    "v_rcp_f32 v215, v215\n"
    "PD\n"
    "v_pk_add_f32 v[228:229], v[228:229], 1.0 op_sel_hi:[1,0]\n"
    "PD\n"
    "v_pk_add_f32 v[230:231], v[230:231], 1.0 op_sel_hi:[1,0]\n"
    "PD\n"
    "v_pk_add_f32 v[244:245], v[244:245], 1.0 op_sel_hi:[1,0]\n"
    "PD\n"
    "v_pk_add_f32 v[246:247], v[246:247], 1.0 op_sel_hi:[1,0]\n"
    "PD\n"
    "v_rcp_f32 v228, v228\n"
    "PD\n"
    "v_rcp_f32 v229, v229\n"
    "PD\n"
    "v_rcp_f32 v230, v230\n"
    "PD\n"
    "v_rcp_f32 v231, v231\n"
    "PD\n"
    "v_rcp_f32 v244, v244\n"
    "PD\n"
    "v_rcp_f32 v245, v245\n"
    "PD\n"
    "v_rcp_f32 v246, v246\n"
    "PD\n"
    "v_rcp_f32 v247, v247\n"
    "PD\n"
    "v_pk_mul_f32 v[128:129], v[128:129], v[132:133]\n"
    "PD\n"
    "v_pk_mul_f32 v[130:131], v[130:131], v[134:135]\n"
    "PD\n"
    "v_pk_mul_f32 v[144:145], v[144:145], v[148:149]\n"
    "PD\n"
    "v_pk_mul_f32 v[146:147], v[146:147], v[150:151]\n"
    "PD\n"
    "v_pk_mul_f32 v[160:161], v[160:161], v[164:165]\n"
    "PD\n"
    "v_pk_mul_f32 v[162:163], v[162:163], v[166:167]\n"
    "PD\n"
    "v_pk_mul_f32 v[176:177], v[176:177], v[180:181]\n"
    "PD\n"
    "v_pk_mul_f32 v[178:179], v[178:179], v[182:183]\n"
    "PD\n"
    "v_pk_mul_f32 v[192:193], v[192:193], v[196:197]\n"
    "PD\n"
    "v_pk_mul_f32 v[194:195], v[194:195], v[198:199]\n"
    "PD\n"
    "v_pk_mul_f32 v[208:209], v[208:209], v[212:213]\n"
    "PD\n"
    "v_pk_mul_f32 v[210:211], v[210:211], v[214:215]\n"
    "PD\n"
    "v_pk_mul_f32 v[224:225], v[224:225], v[228:229]\n"
    "PD\n"
    "v_pk_mul_f32 v[226:227], v[226:227], v[230:231]\n"
    "PD\n"
    "v_pk_mul_f32 v[240:241], v[240:241], v[244:245]\n"
    "PD\n"
    "v_pk_mul_f32 v[242:243], v[242:243], v[246:247]\n"
    "PD\n"
    "L_ns2:\n"
    "PD\n"
    "v_pk_fma_f32 v[128:129], v[128:129], v[136:137], v[140:141]\n"
    "PD\n"
    "v_pk_fma_f32 v[130:131], v[130:131], v[138:139], v[142:143]\n"
    "PD\n"
    "v_pk_fma_f32 v[144:145], v[144:145], v[152:153], v[156:157]\n"
    "PD\n"
    "v_pk_fma_f32 v[146:147], v[146:147], v[154:155], v[158:159]\n"
    "PD\n"
    "v_cvt_pk_bf16_f32 v112, v128, v129\n"
    "PD\n"
    "v_cvt_pk_bf16_f32 v113, v130, v131\n"
    "PD\n"
    "v_cvt_pk_bf16_f32 v114, v144, v145\n"
    "PD\n"
    "v_cvt_pk_bf16_f32 v115, v146, v147\n"
    "PD\n"
    "s_nop 0\n"
    "PD\n"
    "v_permlane16_swap_b32 v112, v114\n"
    "PD\n"
    "v_permlane16_swap_b32 v113, v115\n"
    "PD\n"
    "buffer_store_dwordx4 v[112:115], v1, s[44:47], 0 offen offset:0 sc1\n"
    "PD\n"
    "v_pk_fma_f32 v[160:161], v[160:161], v[168:169], v[172:173]\n"
    "PD\n"
    "v_pk_fma_f32 v[162:163], v[162:163], v[170:171], v[174:175]\n"
    "PD\n"
    "v_pk_fma_f32 v[176:177], v[176:177], v[184:185], v[188:189]\n"
    "PD\n"
    "v_pk_fma_f32 v[178:179], v[178:179], v[186:187], v[190:191]\n"
    "PD\n"
    "v_cvt_pk_bf16_f32 v116, v160, v161\n"
    "PD\n"
    "v_cvt_pk_bf16_f32 v117, v162, v163\n"
    "PD\n"
    "v_cvt_pk_bf16_f32 v118, v176, v177\n"
    "PD\n"
    "v_cvt_pk_bf16_f32 v119, v178, v179\n"
    "PD\n"
    "s_nop 0\n"
    "PD\n"
    "v_permlane16_swap_b32 v116, v118\n"
    "PD\n"
    "v_permlane16_swap_b32 v117, v119\n"
    "PD\n"
    "buffer_store_dwordx4 v[116:119], v1, s[44:47], 0 offen offset:64 sc1\n"
    "PD\n"
    "v_pk_fma_f32 v[192:193], v[192:193], v[200:201], v[204:205]\n"
    "PD\n"
    "v_pk_fma_f32 v[194:195], v[194:195], v[202:203], v[206:207]\n"
    "PD\n"
    "v_pk_fma_f32 v[208:209], v[208:209], v[216:217], v[220:221]\n"
    "PD\n"
    "v_pk_fma_f32 v[210:211], v[210:211], v[218:219], v[222:223]\n"
    "PD\n"
    "v_cvt_pk_bf16_f32 v120, v192, v193\n"
    "PD\n"
    "v_cvt_pk_bf16_f32 v121, v194, v195\n"
    "PD\n"
    "v_cvt_pk_bf16_f32 v122, v208, v209\n"
    "PD\n"
    "v_cvt_pk_bf16_f32 v123, v210, v211\n"
    "PD\n"
    "s_nop 0\n"
    "PD\n"
    "v_permlane16_swap_b32 v120, v122\n"
    "PD\n"
    "v_permlane16_swap_b32 v121, v123\n"
    "PD\n"
    "buffer_store_dwordx4 v[120:123], v1, s[44:47], 0 offen offset:128 sc1\n"
    "PD\n"
    "v_pk_fma_f32 v[224:225], v[224:225], v[232:233], v[236:237]\n"
    "PD\n"
    "v_pk_fma_f32 v[226:227], v[226:227], v[234:235], v[238:239]\n"
    "PD\n"
    "v_pk_fma_f32 v[240:241], v[240:241], v[248:249], v[252:253]\n"
    "PD\n"
    "v_pk_fma_f32 v[242:243], v[242:243], v[250:251], v[254:255]\n"
    "PD\n"
    "v_cvt_pk_bf16_f32 v124, v224, v225\n"
    "PD\n"
    "v_cvt_pk_bf16_f32 v125, v226, v227\n"
    "PD\n"
    "v_cvt_pk_bf16_f32 v126, v240, v241\n"
    "PD\n"
    "v_cvt_pk_bf16_f32 v127, v242, v243\n"
    "PD\n"
    "s_nop 0\n"
    "PD\n"
    "v_permlane16_swap_b32 v124, v126\n"
    "PD\n"
    "v_permlane16_swap_b32 v125, v127\n"
    "PD\n"
    "buffer_store_dwordx4 v[124:127], v1, s[44:47], 0 offen offset:192 sc1\n"
    "PD\n"
    "s_endpgm\n"
    "PF\n"
    "H_Um:\n"
    "PF\n"
    "s_waitcnt vmcnt(16)\n"
    "buffer_store_dwordx2 v[34:35], off, s[80:83], s84 sc1\n"
    "PF\n"
    "s_branch R_Um\n"
    "PF\n"
    "H_Ux:\n"
    "PF\n"
    "buffer_load_dwordx2 v[36:37], v38, s[80:83], s86 offen sc1\n"
    "PF\n"
    "s_waitcnt vmcnt(0)\n"
    "s_mov_b32 s87, 0x40000\n"
    "PF\n"
    "L_Ux_pc:\n"
    "PF\n"
    "v_cmp_ne_u64 vcc, v[36:37], v[34:35]\n"
    "s_cbranch_vccz L_Ux_pd\n"
    "PF\n"
    "s_sleep 8\n"
    "PF\n"
    "buffer_load_dwordx2 v[36:37], v38, s[80:83], s86 offen sc1\n"
    "PF\n"
    "s_sub_u32 s87, s87, 1\n"
    "PF\n"
    "s_cmp_eq_u32 s87, 0\n"
    "PF\n"
    "s_waitcnt vmcnt(0)\n"
    "s_cbranch_scc0 L_Ux_pc\n"
    "PF\n"
    "s_mov_b64 vcc, 0\n"
    "PF\n"
    "L_Ux_pd:\n"
    "PF\n"
    "s_branch R_Ux\n"
    "PF\n"
    "H_Ax:\n"
    "PF\n"
    "buffer_load_dwordx2 v[36:37], v38, s[80:83], s86 offen sc1\n"
    "PF\n"
    "s_waitcnt vmcnt(0)\n"
    "s_mov_b32 s87, 0x40000\n"
    "PF\n"
    "L_Ax_pc:\n"
    "PF\n"
    "v_cmp_ne_u64 vcc, v[36:37], v[34:35]\n"
    "s_cbranch_vccz L_Ax_pd\n"
    "PF\n"
    "s_sleep 8\n"
    "PF\n"
    "buffer_load_dwordx2 v[36:37], v38, s[80:83], s86 offen sc1\n"
    "PF\n"
    "s_sub_u32 s87, s87, 1\n"
    "PF\n"
    "s_cmp_eq_u32 s87, 0\n"
    "PF\n"
    "s_waitcnt vmcnt(0)\n"
    "s_cbranch_scc0 L_Ax_pc\n"
    "PF\n"
    "s_mov_b64 vcc, 0\n"
    "PF\n"
    "L_Ax_pd:\n"
    "PF\n"
    "s_branch R_Ax\n"
    "PF\n"
    "H_fb:\n"
    "s_waitcnt vmcnt(0) lgkmcnt(0)\n"
    "s_barrier\n"
    "s_mov_b32 s39, 1\n"
    "s_mov_b32 s84, 0x40000000\n"
    "s_lshl_b32 s62, s25, 19\n"
    "s_add_u32 s62, s62, 393216\n"
    "s_add_u32 s44, s8, s62\n"
    "s_addc_u32 s45, s9, 0\n"
    "s_mov_b32 s46, 0x20000\n"
    "s_mov_b32 s57, 0\n"
    "s_movk_i32 s59, 0x4000\n"
    "v_lshl_add_u32 v24, v6, 8, v32\n"
    "v_lshl_add_u32 v25, v6, 8, v33\n"
    "v_mov_b32 v26, v24\n"
    "v_mov_b32 v27, v25\n"
    "s_lshl_b32 s62, s25, 19\n"
    "s_add_u32 s62, s62, 393216\n"
    "s_add_u32 s48, s6, s62\n"
    "s_addc_u32 s49, s7, 0\n"
    "s_mov_b32 s50, 0x20000\n"
    "s_mov_b32 s58, 0\n"
    "v_lshrrev_b32 v56, 4, v0\n"
    "v_and_b32 v57, 15, v0\n"
    "v_xor_b32 v56, v56, v57\n"
    "v_lshlrev_b32 v56, 4, v56\n"
    "v_lshl_add_u32 v28, v6, 8, v56\n"
    "v_xor_b32 v29, 64, v28\n"
    "v_xor_b32 v30, 128, v28\n"
    "v_xor_b32 v31, 192, v28\n"
    "v_mov_b32 v224, 0xff800000\n"
    "v_mov_b32 v225, 0xff800000\n"
    "v_mov_b32 v226, 0xff800000\n"
    "v_mov_b32 v227, 0xff800000\n"
    "v_mov_b32 v56, 0x3f803f80\n"
    "v_accvgpr_write_b32 a208, v56\n"
    "v_accvgpr_write_b32 a209, v56\n"
    "v_accvgpr_write_b32 a210, v56\n"
    "v_accvgpr_write_b32 a211, v56\n"
    "v_accvgpr_write_b32 a212, -1\n"
    "v_accvgpr_write_b32 a213, -1\n"
    "v_accvgpr_write_b32 a214, -1\n"
    "v_accvgpr_write_b32 a215, -1\n"
    "s_add_u32 m0, s61, 16384\n"
    "s_mov_b32 s64, 8\n"
    "H_fb1:\n"
    "buffer_load_dwordx4 v28, s[48:51], s58 offen offset:0 lds\n"
    "buffer_load_dwordx4 v29, s[48:51], s58 offen offset:1024 lds\n"
    "buffer_load_dwordx4 v30, s[48:51], s58 offen offset:2048 lds\n"
    "buffer_load_dwordx4 v31, s[48:51], s58 offen offset:3072 lds\n"
    "s_add_u32 s58, s58, s59\n"
    "s_waitcnt vmcnt(0)\n"
    "s_barrier\n"
    "ds_read_b128 a[128:131], v16 offset:16384\n"
    "ds_read_b128 a[132:135], v17 offset:16384\n"
    "ds_read_b128 a[136:139], v18 offset:16384\n"
    "ds_read_b128 a[140:143], v19 offset:16384\n"
    "s_waitcnt lgkmcnt(0)\n"
    "v_mfma_f32_16x16x32_bf16 v[128:131], a[128:131], v[64:67], 0\n"
    "v_mfma_f32_16x16x32_bf16 v[128:131], a[132:135], v[68:71], v[128:131]\n"
    "v_mfma_f32_16x16x32_bf16 v[128:131], a[136:139], v[72:75], v[128:131]\n"
    "v_mfma_f32_16x16x32_bf16 v[128:131], a[140:143], v[76:79], v[128:131]\n"
    "v_mfma_f32_16x16x32_bf16 v[132:135], a[140:143], v[92:95], 0\n"
    "v_mfma_f32_16x16x32_bf16 v[132:135], a[136:139], v[88:91], v[132:135]\n"
    "v_mfma_f32_16x16x32_bf16 v[132:135], a[132:135], v[84:87], v[132:135]\n"
    "v_mfma_f32_16x16x32_bf16 v[132:135], a[128:131], v[80:83], v[132:135]\n"
    "v_mfma_f32_16x16x32_bf16 v[136:139], a[128:131], v[96:99], 0\n"
    "v_mfma_f32_16x16x32_bf16 v[136:139], a[132:135], v[100:103], v[136:139]\n"
    "v_mfma_f32_16x16x32_bf16 v[136:139], a[136:139], v[104:107], v[136:139]\n"
    "v_mfma_f32_16x16x32_bf16 v[136:139], a[140:143], v[108:111], v[136:139]\n"
    "v_mfma_f32_16x16x32_bf16 v[140:143], a[140:143], v[124:127], 0\n"
    "v_mfma_f32_16x16x32_bf16 v[140:143], a[136:139], v[120:123], v[140:143]\n"
    "v_mfma_f32_16x16x32_bf16 v[140:143], a[132:135], v[116:119], v[140:143]\n"
    "v_mfma_f32_16x16x32_bf16 v[140:143], a[128:131], v[112:115], v[140:143]\n"
    "s_nop 7\n"
    "v_max3_f32 v224, v224, v128, v129\n"
    "v_max3_f32 v224, v224, v130, v131\n"
    "v_max3_f32 v225, v225, v132, v133\n"
    "v_max3_f32 v225, v225, v134, v135\n"
    "v_max3_f32 v226, v226, v136, v137\n"
    "v_max3_f32 v226, v226, v138, v139\n"
    "v_max3_f32 v227, v227, v140, v141\n"
    "v_max3_f32 v227, v227, v142, v143\n"
    "ds_read_b128 a[144:147], v16 offset:20480\n"
    "ds_read_b128 a[148:151], v17 offset:20480\n"
    "ds_read_b128 a[152:155], v18 offset:20480\n"
    "ds_read_b128 a[156:159], v19 offset:20480\n"
    "s_waitcnt lgkmcnt(0)\n"
    "v_mfma_f32_16x16x32_bf16 v[156:159], a[144:147], v[112:115], 0\n"
    "v_mfma_f32_16x16x32_bf16 v[156:159], a[148:151], v[116:119], v[156:159]\n"
    "v_mfma_f32_16x16x32_bf16 v[156:159], a[152:155], v[120:123], v[156:159]\n"
    "v_mfma_f32_16x16x32_bf16 v[156:159], a[156:159], v[124:127], v[156:159]\n"
    "v_mfma_f32_16x16x32_bf16 v[152:155], a[156:159], v[108:111], 0\n"
    "v_mfma_f32_16x16x32_bf16 v[152:155], a[152:155], v[104:107], v[152:155]\n"
    "v_mfma_f32_16x16x32_bf16 v[152:155], a[148:151], v[100:103], v[152:155]\n"
    "v_mfma_f32_16x16x32_bf16 v[152:155], a[144:147], v[96:99], v[152:155]\n"
    "v_mfma_f32_16x16x32_bf16 v[148:151], a[144:147], v[80:83], 0\n"
    "v_mfma_f32_16x16x32_bf16 v[148:151], a[148:151], v[84:87], v[148:151]\n"
    "v_mfma_f32_16x16x32_bf16 v[148:151], a[152:155], v[88:91], v[148:151]\n"
    "v_mfma_f32_16x16x32_bf16 v[148:151], a[156:159], v[92:95], v[148:151]\n"
    "v_mfma_f32_16x16x32_bf16 v[144:147], a[156:159], v[76:79], 0\n"
    "v_mfma_f32_16x16x32_bf16 v[144:147], a[152:155], v[72:75], v[144:147]\n"
    "v_mfma_f32_16x16x32_bf16 v[144:147], a[148:151], v[68:71], v[144:147]\n"
    "v_mfma_f32_16x16x32_bf16 v[144:147], a[144:147], v[64:67], v[144:147]\n"
    "s_nop 7\n"
    "v_max3_f32 v224, v224, v144, v145\n"
    "v_max3_f32 v224, v224, v146, v147\n"
    "v_max3_f32 v225, v225, v148, v149\n"
    "v_max3_f32 v225, v225, v150, v151\n"
    "v_max3_f32 v226, v226, v152, v153\n"
    "v_max3_f32 v226, v226, v154, v155\n"
    "v_max3_f32 v227, v227, v156, v157\n"
    "v_max3_f32 v227, v227, v158, v159\n"
    "ds_read_b128 a[128:131], v16 offset:24576\n"
    "ds_read_b128 a[132:135], v17 offset:24576\n"
    "ds_read_b128 a[136:139], v18 offset:24576\n"
    "ds_read_b128 a[140:143], v19 offset:24576\n"
    "s_waitcnt lgkmcnt(0)\n"
    "v_mfma_f32_16x16x32_bf16 v[160:163], a[128:131], v[64:67], 0\n"
    "v_mfma_f32_16x16x32_bf16 v[160:163], a[132:135], v[68:71], v[160:163]\n"
    "v_mfma_f32_16x16x32_bf16 v[160:163], a[136:139], v[72:75], v[160:163]\n"
    "v_mfma_f32_16x16x32_bf16 v[160:163], a[140:143], v[76:79], v[160:163]\n"
    "v_mfma_f32_16x16x32_bf16 v[164:167], a[140:143], v[92:95], 0\n"
    "v_mfma_f32_16x16x32_bf16 v[164:167], a[136:139], v[88:91], v[164:167]\n"
    "v_mfma_f32_16x16x32_bf16 v[164:167], a[132:135], v[84:87], v[164:167]\n"
    "v_mfma_f32_16x16x32_bf16 v[164:167], a[128:131], v[80:83], v[164:167]\n"
    "v_mfma_f32_16x16x32_bf16 v[168:171], a[128:131], v[96:99], 0\n"
    "v_mfma_f32_16x16x32_bf16 v[168:171], a[132:135], v[100:103], v[168:171]\n"
    "v_mfma_f32_16x16x32_bf16 v[168:171], a[136:139], v[104:107], v[168:171]\n"
    "v_mfma_f32_16x16x32_bf16 v[168:171], a[140:143], v[108:111], v[168:171]\n"
    "v_mfma_f32_16x16x32_bf16 v[172:175], a[140:143], v[124:127], 0\n"
    "v_mfma_f32_16x16x32_bf16 v[172:175], a[136:139], v[120:123], v[172:175]\n"
    "v_mfma_f32_16x16x32_bf16 v[172:175], a[132:135], v[116:119], v[172:175]\n"
    "v_mfma_f32_16x16x32_bf16 v[172:175], a[128:131], v[112:115], v[172:175]\n"
    "s_nop 7\n"
    "v_max3_f32 v224, v224, v160, v161\n"
    "v_max3_f32 v224, v224, v162, v163\n"
    "v_max3_f32 v225, v225, v164, v165\n"
    "v_max3_f32 v225, v225, v166, v167\n"
    "v_max3_f32 v226, v226, v168, v169\n"
    "v_max3_f32 v226, v226, v170, v171\n"
    "v_max3_f32 v227, v227, v172, v173\n"
    "v_max3_f32 v227, v227, v174, v175\n"
    "ds_read_b128 a[144:147], v16 offset:28672\n"
    "ds_read_b128 a[148:151], v17 offset:28672\n"
    "ds_read_b128 a[152:155], v18 offset:28672\n"
    "ds_read_b128 a[156:159], v19 offset:28672\n"
    "s_waitcnt lgkmcnt(0)\n"
    "v_mfma_f32_16x16x32_bf16 v[188:191], a[144:147], v[112:115], 0\n"
    "v_mfma_f32_16x16x32_bf16 v[188:191], a[148:151], v[116:119], v[188:191]\n"
    "v_mfma_f32_16x16x32_bf16 v[188:191], a[152:155], v[120:123], v[188:191]\n"
    "v_mfma_f32_16x16x32_bf16 v[188:191], a[156:159], v[124:127], v[188:191]\n"
    "v_mfma_f32_16x16x32_bf16 v[184:187], a[156:159], v[108:111], 0\n"
    "v_mfma_f32_16x16x32_bf16 v[184:187], a[152:155], v[104:107], v[184:187]\n"
    "v_mfma_f32_16x16x32_bf16 v[184:187], a[148:151], v[100:103], v[184:187]\n"
    "v_mfma_f32_16x16x32_bf16 v[184:187], a[144:147], v[96:99], v[184:187]\n"
    "v_mfma_f32_16x16x32_bf16 v[180:183], a[144:147], v[80:83], 0\n"
    "v_mfma_f32_16x16x32_bf16 v[180:183], a[148:151], v[84:87], v[180:183]\n"
    "v_mfma_f32_16x16x32_bf16 v[180:183], a[152:155], v[88:91], v[180:183]\n"
    "v_mfma_f32_16x16x32_bf16 v[180:183], a[156:159], v[92:95], v[180:183]\n"
    "v_mfma_f32_16x16x32_bf16 v[176:179], a[156:159], v[76:79], 0\n"
    "v_mfma_f32_16x16x32_bf16 v[176:179], a[152:155], v[72:75], v[176:179]\n"
    "v_mfma_f32_16x16x32_bf16 v[176:179], a[148:151], v[68:71], v[176:179]\n"
    "v_mfma_f32_16x16x32_bf16 v[176:179], a[144:147], v[64:67], v[176:179]\n"
    "s_nop 7\n"
    "v_max3_f32 v224, v224, v176, v177\n"
    "v_max3_f32 v224, v224, v178, v179\n"
    "v_max3_f32 v225, v225, v180, v181\n"
    "v_max3_f32 v225, v225, v182, v183\n"
    "v_max3_f32 v226, v226, v184, v185\n"
    "v_max3_f32 v226, v226, v186, v187\n"
    "v_max3_f32 v227, v227, v188, v189\n"
    "v_max3_f32 v227, v227, v190, v191\n"
    "s_barrier\n"
    "s_sub_u32 s64, s64, 1\n"
    "s_cmp_lg_u32 s64, 0\n"
    "s_cbranch_scc1 H_fb1\n"
    "s_mov_b32 s58, 0\n"
    "s_add_u32 m0, s61, 0\n"
    "s_nop 0\n"
    "buffer_load_dwordx4 v24, s[44:47], s57 offen offset:0 lds\n"
    "buffer_load_dwordx4 v25, s[44:47], s57 offen offset:1024 lds\n"
    "buffer_load_dwordx4 v26, s[44:47], s57 offen offset:2048 lds\n"
    "buffer_load_dwordx4 v27, s[44:47], s57 offen offset:3072 lds\n"
    "s_add_u32 s57, s57, s59\n"
    "s_add_u32 m0, s61, 16384\n"
    "s_nop 0\n"
    "buffer_load_dwordx4 v28, s[48:51], s58 offen offset:0 lds\n"
    "buffer_load_dwordx4 v29, s[48:51], s58 offen offset:1024 lds\n"
    "buffer_load_dwordx4 v30, s[48:51], s58 offen offset:2048 lds\n"
    "buffer_load_dwordx4 v31, s[48:51], s58 offen offset:3072 lds\n"
    "s_add_u32 s58, s58, s59\n"
    "s_add_u32 m0, s61, 32768\n"
    "s_nop 0\n"
    "buffer_load_dwordx4 v24, s[44:47], s57 offen offset:0 lds\n"
    "buffer_load_dwordx4 v25, s[44:47], s57 offen offset:1024 lds\n"
    "buffer_load_dwordx4 v26, s[44:47], s57 offen offset:2048 lds\n"
    "buffer_load_dwordx4 v27, s[44:47], s57 offen offset:3072 lds\n"
    "s_add_u32 s57, s57, s59\n"
    "s_add_u32 m0, s61, 49152\n"
    "s_nop 0\n"
    "buffer_load_dwordx4 v28, s[48:51], s58 offen offset:0 lds\n"
    "buffer_load_dwordx4 v29, s[48:51], s58 offen offset:1024 lds\n"
    "buffer_load_dwordx4 v30, s[48:51], s58 offen offset:2048 lds\n"
    "buffer_load_dwordx4 v31, s[48:51], s58 offen offset:3072 lds\n"
    "s_add_u32 s58, s58, s59\n"
    "s_add_u32 m0, s61, 65536\n"
    "s_nop 0\n"
    "buffer_load_dwordx4 v24, s[44:47], s57 offen offset:0 lds\n"
    "buffer_load_dwordx4 v25, s[44:47], s57 offen offset:1024 lds\n"
    "buffer_load_dwordx4 v26, s[44:47], s57 offen offset:2048 lds\n"
    "buffer_load_dwordx4 v27, s[44:47], s57 offen offset:3072 lds\n"
    "s_add_u32 s57, s57, s59\n"
    "s_add_u32 m0, s61, 81920\n"
    "s_nop 0\n"
    "buffer_load_dwordx4 v28, s[48:51], s58 offen offset:0 lds\n"
    "buffer_load_dwordx4 v29, s[48:51], s58 offen offset:1024 lds\n"
    "buffer_load_dwordx4 v30, s[48:51], s58 offen offset:2048 lds\n"
    "buffer_load_dwordx4 v31, s[48:51], s58 offen offset:3072 lds\n"
    "s_add_u32 s58, s58, s59\n"
    "s_waitcnt vmcnt(0)\n"
    "s_barrier\n"
    "ds_read_b128 a[128:131], v16 offset:16384\n"
    "ds_read_b128 a[132:135], v17 offset:16384\n"
    "ds_read_b128 a[136:139], v18 offset:16384\n"
    "ds_read_b128 a[140:143], v19 offset:16384\n"
    "s_waitcnt lgkmcnt(0)\n"
    "s_branch L_red\n"
    "PF\n"
    "H_Ot:\n"
    "PF\n"
    "s_cmp_lg_u32 s37, 2\n"
    "PF\n"
    "s_cbranch_scc1 H_Ot_0\n"
    "PF\n"
    "s_cmp_lg_u32 s39, s37\n"
    "PF\n"
    "s_cbranch_scc1 L_O2_ns\n"
    "PF\n"
    "s_waitcnt vmcnt(24)\n"
    "buffer_store_dwordx2 v[34:35], off, s[80:83], s84 sc1\n"
    "PF\n"
    "L_O2_ns:\n"
    "PF\n"
    "s_mov_b32 s86, 0\n"
    "PF\n"
    "v_mov_b32 v38, v61\n"
    "PF\n"
    "buffer_load_dwordx2 v[36:37], v38, s[80:83], s86 offen sc1\n"
    "PF\n"
    "s_branch R_Ot\n"
    "PF\n"
    "H_Ot_0:\n"
    "PF\n"
    "s_waitcnt vmcnt(24)\n"
    "s_mov_b32 s87, 0x40000\n"
    "PF\n"
    "L_Ot_pc:\n"
    "PF\n"
    "v_cmp_ne_u64 vcc, v[36:37], v[34:35]\n"
    "s_cbranch_vccz L_Ot_pd\n"
    "PF\n"
    "s_sleep 8\n"
    "PF\n"
    "buffer_load_dwordx2 v[36:37], v38, s[80:83], s86 offen sc1\n"
    "PF\n"
    "s_sub_u32 s87, s87, 1\n"
    "PF\n"
    "s_cmp_eq_u32 s87, 0\n"
    "PF\n"
    "s_waitcnt vmcnt(0)\n"
    "s_cbranch_scc0 L_Ot_pc\n"
    "PF\n"
    "s_mov_b64 vcc, 0\n"
    "PF\n"
    "L_Ot_pd:\n"
    "PF\n"
    "s_branch R_Ot\n"
    "PF\n"
    "H_Om:\n"
    "PF\n"
    "s_cmp_lg_u32 s37, 1\n"
    "PF\n"
    "s_cbranch_scc1 H_Om_0\n"
    "PF\n"
    "s_mov_b64 vcc, 0\n"
    "PF\n"
    "s_cmp_lg_u32 s39, s37\n"
    "PF\n"
    "s_cbranch_scc1 L_O1_ns\n"
    "PF\n"
    "s_waitcnt vmcnt(24)\n"
    "buffer_store_dwordx2 v[34:35], off, s[80:83], s84 sc1\n"
    "PF\n"
    "L_O1_ns:\n"
    "PF\n"
    "s_branch R_Om\n"
    "PF\n"
    "H_Om_0:\n"
    "PF\n"
    "s_waitcnt vmcnt(24)\n"
    "s_mov_b32 s87, 0x40000\n"
    "PF\n"
    "L_Om_pc:\n"
    "PF\n"
    "v_cmp_ne_u64 vcc, v[36:37], v[34:35]\n"
    "s_cbranch_vccz L_Om_pd\n"
    "PF\n"
    "s_sleep 8\n"
    "PF\n"
    "buffer_load_dwordx2 v[36:37], v38, s[80:83], s86 offen sc1\n"
    "PF\n"
    "s_sub_u32 s87, s87, 1\n"
    "PF\n"
    "s_cmp_eq_u32 s87, 0\n"
    "PF\n"
    "s_waitcnt vmcnt(0)\n"
    "s_cbranch_scc0 L_Om_pc\n"
    "PF\n"
    "s_mov_b64 vcc, 0\n"
    "PF\n"
    "L_Om_pd:\n"
    "PF\n"
    "v_mov_b32 v38, v3\n"
    "PF\n"
    "buffer_load_dwordx2 v[36:37], v38, s[80:83], s86 offen sc1\n"
    "PF\n"
    "s_branch R_Om\n"
    "PF\n"
    "H_Os00:\n"
    "PF\n"
    "buffer_store_dwordx4 v[96:99], v4, s[52:55], s69 offen offset:0 sc1\n"
    "PF\n"
    "s_branch R_Os00\n"
    "PF\n"
    "H_Os01:\n"
    "PF\n"
    "buffer_store_dwordx4 v[100:103], v4, s[52:55], s69 offen offset:2048 sc1\n"
    "PF\n"
    "s_branch R_Os01\n"
    "PF\n"
    "H_Os10:\n"
    "PF\n"
    "buffer_store_dwordx4 v[104:107], v5, s[52:55], s69 offen offset:0 sc1\n"
    "PF\n"
    "s_branch R_Os10\n"
    "PF\n"
    "H_Os11:\n"
    "PF\n"
    "buffer_store_dwordx4 v[108:111], v5, s[52:55], s69 offen offset:2048 sc1\n"
    "PF\n"
    "s_branch R_Os11\n"
    "PF\n"
    "H_Os20:\n"
    "PF\n"
    "buffer_store_dwordx4 v[112:115], v4, s[52:55], s69 offen offset:1024 sc1\n"
    "PF\n"
    "s_branch R_Os20\n"
    "PF\n"
    "H_Os21:\n"
    "PF\n"
    "buffer_store_dwordx4 v[116:119], v4, s[52:55], s69 offen offset:3072 sc1\n"
    "PF\n"
    "s_branch R_Os21\n"
    "PF\n"
    "H_Os30:\n"
    "PF\n"
    "buffer_store_dwordx4 v[120:123], v5, s[52:55], s69 offen offset:1024 sc1\n"
    "PF\n"
    "s_branch R_Os30\n"
    "PF\n"
    "H_Os31:\n"
    "PF\n"
    "buffer_store_dwordx4 v[124:127], v5, s[52:55], s69 offen offset:3072 sc1\n"
    "PF\n"
    "s_branch R_Os31\n"
    "PF\n"
    "70:\n"
    "71:\n"
    "72:\n"
    "73:\n"
    "s_branch L_retA\n"
    "74:\n"
    "75:\n"
    "76:\n"
    "77:\n"
    "s_branch L_retB\n"
    "L_end:\n"
    : "+{v0}"(tid)
    : "{s[0:1]}"(__builtin_amdgcn_kernarg_segment_ptr()), "{s2}"(blockIdx.x), "s"((u32)(size_t)lds)
    : "v1","v2","v3","v4","v5","v6","v7","v8","v9","v10","v11","v12","v13","v14","v15","v16","v17","v18","v19","v20","v21","v22","v23","v24","v25","v26","v27","v28","v29","v30","v31","v32","v33","v34","v35","v36","v37","v38","v39","v40","v41","v42","v43","v44","v45","v46","v47","v48","v49","v50","v51","v52","v53","v54","v55","v56","v57","v58","v59","v60","v61","v62","v63","v64","v65","v66","v67","v68","v69","v70","v71","v72","v73","v74","v75","v76","v77","v78","v79","v80","v81","v82","v83","v84","v85","v86","v87","v88","v89","v90","v91","v92","v93","v94","v95","v96","v97","v98","v99","v100","v101","v102","v103","v104","v105","v106","v107","v108","v109","v110","v111","v112","v113","v114","v115","v116","v117","v118","v119","v120","v121","v122","v123","v124","v125","v126","v127","v128","v129","v130","v131","v132","v133","v134","v135","v136","v137","v138","v139","v140","v141","v142","v143","v144","v145","v146","v147","v148","v149","v150","v151","v152","v153","v154","v155","v156","v157","v158","v159","v160","v161","v162","v163","v164","v165","v166","v167","v168","v169","v170","v171","v172","v173","v174","v175","v176","v177","v178","v179","v180","v181","v182","v183","v184","v185","v186","v187","v188","v189","v190","v191","v192","v193","v194","v195","v196","v197","v198","v199","v200","v201","v202","v203","v204","v205","v206","v207","v208","v209","v210","v211","v212","v213","v214","v215","v216","v217","v218","v219","v220","v221","v222","v223","v224","v225","v226","v227","v228","v229","v230","v231","v232","v233","v234","v235","v236","v237","v238","v239","v240","v241","v242","v243","v244","v245","v246","v247","v248","v249","v250","v251","v252","v253","v254","v255","a0","a1","a2","a3","a4","a5","a6","a7","a8","a9","a10","a11","a12","a13","a14","a15","a16","a17","a18","a19","a20","a21","a22","a23","a24","a25","a26","a27","a28","a29","a30","a31","a32","a33","a34","a35","a36","a37","a38","a39","a40","a41","a42","a43","a44","a45","a46","a47","a48","a49","a50","a51","a52","a53","a54","a55","a56","a57","a58","a59","a60","a61","a62","a63","a64","a65","a66","a67","a68","a69","a70","a71","a72","a73","a74","a75","a76","a77","a78","a79","a80","a81","a82","a83","a84","a85","a86","a87","a88","a89","a90","a91","a92","a93","a94","a95","a96","a97","a98","a99","a100","a101","a102","a103","a104","a105","a106","a107","a108","a109","a110","a111","a112","a113","a114","a115","a116","a117","a118","a119","a120","a121","a122","a123","a124","a125","a126","a127","a128","a129","a130","a131","a132","a133","a134","a135","a136","a137","a138","a139","a140","a141","a142","a143","a144","a145","a146","a147","a148","a149","a150","a151","a152","a153","a154","a155","a156","a157","a158","a159","a160","a161","a162","a163","a164","a165","a166","a167","a168","a169","a170","a171","a172","a173","a174","a175","a176","a177","a178","a179","a180","a181","a182","a183","a184","a185","a186","a187","a188","a189","a190","a191","a192","a193","a194","a195","a196","a197","a198","a199","a200","a201","a202","a203","a204","a205","a206","a207","a208","a209","a210","a211","a212","a213","a214","a215","a216","a217","a218","a219","a220","a221","a222","a223","a224","a225","a226","a227","a228","a229","a230","a231","a232","a233","a234","a235","a236","a237","a238","a239","a240","a241","a242","a243","a244","a245","a246","a247","a248","a249","a250","a251","a252","a253","a254","a255","s4","s5","s6","s7","s8","s9","s10","s11","s12","s13","s14","s15","s16","s17","s18","s19","s20","s21","s22","s23","s24","s25","s26","s27","s28","s29","s30","s31","s34","s35","s36","s37","s38","s39","s40","s41","s42","s43","s44","s45","s46","s47","s48","s49","s50","s51","s52","s53","s54","s55","s56","s57","s58","s59","s60","s61","s62","s63","s64","s65","s66","s67","s68","s69","s70","s71","s72","s73","s74","s75","s76","s77","s78","s79","s80","s81","s82","s83","s84","s85","s86","s87","s88","s89","s90","s91","s92","s93","s94","s95","s96","s97","s98","s99","vcc","scc","memory");
}

extern "C" void kernel_launch(void* const* d_in, const int* in_sizes, int n_in, void* d_out, int out_size, void* d_ws, size_t ws_size, hipStream_t stream) {
  main_kernel<<<256, 256, 0, stream>>>(d_in[0], d_in[1], d_in[2], d_in[3], d_in[4], d_in[6], d_in[7], d_in[8], d_out, d_ws);
}
```
